```python
import torch

def _softmax(x, axis=-1):
    return torch.softmax(x, dim=axis)
def _gelu(x):
    return torch.nn.functional.gelu(x, approximate="tanh")
def _silu(x):
    return torch.nn.functional.silu(x)
def _sigmoid(x):
    return torch.sigmoid(x)
def _layernorm(x, axis=-1, eps=1e-5):
    m = x.mean(dim=axis, keepdim=True)
    v = x.var(dim=axis, keepdim=True, unbiased=False)
    return (x - m) / torch.sqrt(v + eps)
def _scatter_add(tgt, idx, src, axis=0):
    return tgt.clone().index_add_(axis, idx, src)
def _segment_sum(src, seg, n):
    out = torch.zeros((n,) + src.shape[1:], dtype=src.dtype)
    return out.index_add_(0, seg, src)
def _pad(x, pw):
    flat = tuple(v for pair in reversed(pw) for v in pair)
    return torch.nn.functional.pad(x, flat)

kernel_name = 'gated_mix__ssm_conv_s4d__ssm_conv_s5__prompt'

def setup_inputs():

    _w = (lambda a: (lambda t: t.to(torch.bfloat16) if t.is_floating_point() else t)(torch.as_tensor(a)))
    def _A_setup():
        Bsz, L, D, N = 1, 2048, 576, 128
        return {
            "u": _w((lambda *s: torch.randn(*s, dtype=torch.bfloat16) * 0.05)(Bsz, L, D)),
            "log_A_real": _w((lambda *s: torch.randn(*s, dtype=torch.bfloat16) * 3.2)(D, N) * 0.5),
            "A_imag": _w((lambda *s: torch.randn(*s, dtype=torch.bfloat16) * 0.05)(D, N) * 3.14),
            "C_real": _w((lambda *s: torch.randn(*s, dtype=torch.bfloat16) * 0.1)(D, N) / N ** 0.5),
            "C_imag": _w((lambda *s: torch.randn(*s, dtype=torch.bfloat16) * 0.1)(D, N) / N ** 0.5),
            "log_dt": _w((lambda *s: torch.randn(*s, dtype=torch.bfloat16) * 0.05)(D) * 0.5 - 4.0),
            "D_skip": _w((lambda *s: torch.randn(*s, dtype=torch.bfloat16) * 0.05)(D)),
        }
    _A_in = _A_setup()

    def _B_setup():
        B, S, D = 1, 2048, 576
        P = D
        Lambda_re = -0.5 * (lambda _x: torch.exp(torch.as_tensor(_x)))((lambda *s: torch.randn(*s, dtype=torch.bfloat16) * 0.2)(P))
        Lambda_im = (lambda *s: torch.randn(*s, dtype=torch.bfloat16) * 0.4)(P) * 1.0
        B_re = (lambda *s: torch.randn(*s, dtype=torch.bfloat16) * 0.2)(P, D) * (D ** -0.5)
        B_im = (lambda *s: torch.randn(*s, dtype=torch.bfloat16) * 0.2)(P, D) * (D ** -0.5)
        C_re = (lambda *s: torch.randn(*s, dtype=torch.bfloat16) * 0.2)(D, P) * (P ** -0.5)
        C_im = (lambda *s: torch.randn(*s, dtype=torch.bfloat16) * 0.2)(D, P) * (P ** -0.5)
        log_dt = (lambda *s: torch.randn(*s, dtype=torch.bfloat16) * 0.2)(P) * 0.3 - 2.0
        Dskip = (lambda *s: torch.randn(*s, dtype=torch.bfloat16) * 0.05)(D)
        return {
            "x": _w((lambda *s: torch.randn(*s, dtype=torch.bfloat16) * 0.05)(B, S, D)),
            "Lambda_re": _w(Lambda_re),
            "Lambda_im": _w(Lambda_im),
            "B_re": _w(B_re),
            "B_im": _w(B_im),
            "C_re": _w(C_re),
            "C_im": _w(C_im),
            "log_dt": _w(log_dt),
            "Dskip": _w(Dskip),
        }
    _B_in = _B_setup()

    return {
        "A_u": _A_in["u"],
        "A_log_A_real": _A_in["log_A_real"],
        "A_A_imag": _A_in["A_imag"],
        "A_C_real": _A_in["C_real"],
        "A_C_imag": _A_in["C_imag"],
        "A_log_dt": _A_in["log_dt"],
        "A_D_skip": _A_in["D_skip"],
        "B_x": _B_in["x"],
        "B_Lambda_re": _B_in["Lambda_re"],
        "B_Lambda_im": _B_in["Lambda_im"],
        "B_B_re": _B_in["B_re"],
        "B_B_im": _B_in["B_im"],
        "B_C_re": _B_in["C_re"],
        "B_C_im": _B_in["C_im"],
        "B_log_dt": _B_in["log_dt"],
        "B_Dskip": _B_in["Dskip"],
        "gate_w": _w((lambda *s: torch.randn(*s, dtype=torch.bfloat16) * 0.05)(576) * 16.0),
    }

def reference(A_u, A_log_A_real, A_A_imag, A_C_real, A_C_imag, A_log_dt, A_D_skip, B_x, B_Lambda_re, B_Lambda_im, B_B_re, B_B_im, B_C_re, B_C_im, B_log_dt, B_Dskip, gate_w):
    def _A_ref(u, log_A_real, A_imag, C_real, C_imag, log_dt, D_skip):
        L = u.shape[1]
        dt = torch.exp(log_dt)
        A = -torch.exp(log_A_real) + 1j * A_imag
        C = (C_real + 1j * C_imag) * dt[:, None]
        pos = (lambda x, dt: x.to(dt))(torch.arange(L), log_dt.dtype)
        P = torch.exp((A * dt[:, None])[:, :, None] * pos[None, None, :])
        K = 2.0 * torch.einsum("dn,dnl->dl", C, P).real
        L2 = 2 * L
        u_t = (lambda x, *d: x.permute(*d))(u, 0, 2, 1)
        Kf = (lambda x, *a, **k: torch.fft.rfft(x.float(), *a, **k))(K, L2)
        Uf = (lambda x, *a, **k: torch.fft.rfft(x.float(), *a, **k))(u_t, L2)
        y = (lambda x, *a, **k: torch.fft.irfft(x, *a, **k).to(torch.bfloat16))(Uf * Kf[None, :, :], L2)[:, :, :L]
        y = (lambda x, *d: x.permute(*d))(y, 0, 2, 1)
        return y + u * D_skip[None, None, :]

    def _B_ref(x, Lambda_re, Lambda_im, B_re, B_im, C_re, C_im, log_dt, Dskip):
        Bsz, L, H = x.shape
        Lam = Lambda_re + 1j * Lambda_im
        Bmat = B_re + 1j * B_im
        Cmat = C_re + 1j * C_im
        dt = torch.exp(log_dt)
        Lam_bar = torch.exp(dt * Lam)
        B_bar = ((Lam_bar - 1.0) / Lam)[:, None] * Bmat
        Bu = torch.einsum("bld,pd->blp", x + 0j, B_bar)
        h = torch.zeros_like(Bu[:, 0, :])
        hs = []
        for t in range(L):
            h = Lam_bar[None, :] * h + Bu[:, t, :]
            hs.append(h)
        hseq = torch.stack(hs, 1)
        y = 2.0 * torch.einsum("blp,dp->bld", hseq, Cmat).real + x * Dskip[None, None, :]
        return y

    x = A_u
    g = _sigmoid((x * gate_w).sum(-1, keepdims=True))
    a = _A_ref(x, A_log_A_real, A_A_imag, A_C_real, A_C_imag, A_log_dt, A_D_skip)
    b = _B_ref(x, B_Lambda_re, B_Lambda_im, B_B_re, B_B_im, B_C_re, B_C_im, B_log_dt, B_Dskip)
    return g * a + (1 - g) * b
```

```cpp
#include <hip/hip_runtime.h>
#include <hip/hip_bf16.h>
#include <stdint.h>

typedef unsigned short u16;
typedef unsigned int u32;
typedef unsigned long long u64;
typedef __attribute__((ext_vector_type(8))) __bf16 bf16x8;
typedef __attribute__((ext_vector_type(2))) __bf16 bf16x2;
typedef __attribute__((ext_vector_type(4))) short s16x4;
typedef __attribute__((ext_vector_type(8))) short s16x8;
typedef __attribute__((ext_vector_type(4))) float f32x4;
typedef __attribute__((ext_vector_type(2))) float f32x2;
typedef __attribute__((ext_vector_type(4))) unsigned u32x4;
typedef __attribute__((ext_vector_type(3))) unsigned u32x3;
typedef __attribute__((ext_vector_type(2))) unsigned u32x2;
#define LDS __attribute__((address_space(3)))
#define GLB __attribute__((address_space(1)))
#define DEV __device__ __forceinline__

constexpr int SL = 2048;
constexpr int DM = 576;

constexpr size_t WS_HT = 0;
constexpr size_t WS_AT = WS_HT + (size_t)2 * DM * SL * 2;
constexpr size_t WS_G = WS_AT + (size_t)DM * SL * 2;
constexpr size_t WS_GRAN = WS_G + (size_t)SL * 8;
constexpr size_t WS_EP = WS_GRAN + (size_t)9 * 16 * 2 * 64 * 8;

DEV float bf2f(u16 v) { return __uint_as_float((u32)v << 16); }
DEV float bflo(u32 v) { return __uint_as_float(v << 16); }
DEV float bfhi(u32 v) { return __uint_as_float(v & 0xffff0000u); }
DEV u32 pack_bf16(float lo, float hi) {
  f32x2 v = {lo, hi};
  bf16x2 b = __builtin_convertvector(v, bf16x2);
  return __builtin_bit_cast(u32, b);
}
DEV float rbf(float x) { return bflo(pack_bf16(x, x)); }
template <int N> DEV void wait_vm() { asm volatile("s_waitcnt vmcnt(%0)" ::"n"(N) : "memory"); }
DEV void bar() { asm volatile("s_waitcnt lgkmcnt(0)\n\ts_barrier" ::: "memory"); }
DEV void dma16(const void* src, LDS char* dst) {
  __builtin_amdgcn_global_load_lds((const GLB u32*)src, (LDS u32*)dst, 16, 0, 0);
}
DEV bf16x8 lds_frag(LDS const char* p) { return *(LDS const bf16x8*)p; }
DEV f32x4 mfma16(bf16x8 a, bf16x8 b, f32x4 c) { return __builtin_amdgcn_mfma_f32_16x16x32_bf16(a, b, c, 0, 0, 0); }
struct cf { float r, i; };
DEV cf cmul(cf a, cf b) { return {a.r * b.r - a.i * b.i, a.r * b.i + a.i * b.r}; }
DEV cf cfma(cf a, cf b, cf c) { return {fmaf(a.r, b.r, fmaf(-a.i, b.i, c.r)), fmaf(a.r, b.i, fmaf(a.i, b.r, c.i))}; }

struct IdxTab { u16 v[SL]; };
constexpr int round_pos(int j) {
  if (j < 256) return j;
  int sh = j < 512 ? 1 : (j < 1024 ? 2 : 3);
  int q = j >> sh, rem = j & ((1 << sh) - 1), half = 1 << (sh - 1);
  if (rem > half || (rem == half && (q & 1))) ++q;
  return q << sh;
}
constexpr IdxTab make_idx() {
  IdxTab t{};
  for (int j = 0; j < SL; ++j) {
    int pos = round_pos(j), row = 0, b = (pos & 127) >> 3;
    if (pos < 256) row = 8 * (pos >> 7) + (pos & 7);
    else if (pos < 512) row = 16 + ((pos & 7) >> 1) + 4 * ((pos - 256) >> 7);
    else if (pos < 1024) row = 24 + ((pos - 512) >> 7) + 4 * ((pos & 7) >> 2);
    else if (pos < 2048) row = 32 + ((pos - 1024) >> 7);
    else { row = 40; b = 0; }
    t.v[j] = (u16)(row * 16 + b);
  }
  return t;
}
__device__ const IdxTab g_idx = make_idx();

struct P1 {
  const u16 *x, *la, *aim, *cre, *cim, *ldtA;
  const u16 *lre, *lim, *bre, *bim, *ldtB, *gw;
  char* ws;
};
struct P2 {
  const u16 *x, *cre, *cim, *dskA, *dskB;
  const char* ws;
  float* out;
};

constexpr int G_NB = 4, G_STAGE = 32768;
constexpr int G_E = G_NB * G_STAGE;
constexpr int G_X = G_E + 4096;

DEV void role_g(const P1& p, LDS char* smem, int c, int g, int w, int lane) {
  GLB u32* ep = (GLB u32*)(p.ws + WS_EP + g * 256);
  GLB u64* gran = (GLB u64*)(p.ws + WS_GRAN) + (size_t)g * 16 * 128;
  if (w >= 8) {
    const int lw = w - 8;
    const u32 tag = __hip_atomic_load(ep, __ATOMIC_RELAXED, __HIP_MEMORY_SCOPE_AGENT) + 1u;
    u32 xo[4], bo[4];
#pragma unroll
    for (int i = 0; i < 4; ++i) {
      const int rho = 8 * (4 * lw + i) + (lane >> 3);
      const int hf = rho >> 6, m = (rho >> 4) & 3, r = rho & 15;
      const int tau = 64 * hf + 16 * (r >> 2) + 4 * m + (r & 3);
      const int ch = (lane & 7) ^ (rho & 7);
      xo[i] = (u32)(128 * c + tau) * 1152u + ch * 16;
      const int pp = 64 * g + 16 * (rho >> 5) + r;
      bo[i] = (u32)pp * 1152u + ch * 16;
    }
    auto issue = [&](int s) {
      LDS char* buf = smem + (s % G_NB) * G_STAGE;
#pragma unroll
      for (int i = 0; i < 4; ++i) dma16((const char*)p.x + xo[i] + s * 128, buf + (4 * lw + i) * 1024);
#pragma unroll
      for (int i = 0; i < 4; ++i) {
        const char* base = (const char*)((((4 * lw + i) >> 1) & 1) ? p.bim : p.bre);
        dma16(base + bo[i] + s * 128, buf + 16384 + (4 * lw + i) * 1024);
      }
    };
#pragma unroll
    for (int s = 0; s < G_NB - 1; ++s) issue(s);
#pragma unroll
    for (int s = 0; s < 9; ++s) {
      if (s <= 6) wait_vm<16>(); else if (s == 7) wait_vm<8>(); else wait_vm<0>();
      bar();
      if (s + G_NB - 1 < 9) issue(s + G_NB - 1);
    }
    bar();
    for (int cc = lw; cc < c; cc += 4) {
      GLB u64* gp = gran + cc * 128 + lane;
      u64 a, b;
      for (u32 spin = 0;; ++spin) {
        a = __hip_atomic_load(gp, __ATOMIC_RELAXED, __HIP_MEMORY_SCOPE_AGENT);
        b = __hip_atomic_load(gp + 64, __ATOMIC_RELAXED, __HIP_MEMORY_SCOPE_AGENT);
        const bool ok = (u32)(a >> 32) == tag && (u32)(b >> 32) == tag;
        if (__all(ok) || spin > (1u << 22)) break;
        __builtin_amdgcn_s_sleep(1);
      }
      f32x2 v = {__uint_as_float((u32)a), __uint_as_float((u32)b)};
      *(LDS f32x2*)(smem + G_X + (cc * 64 + lane) * 8) = v;
    }
    bar();
    if (c == 15 && lw == 0 && lane == 0) __hip_atomic_store(ep, tag, __ATOMIC_RELAXED, __HIP_MEMORY_SCOPE_AGENT);
    return;
  }
  const int hf = w >> 2, pq = w & 3, col = lane & 15, q = lane >> 4;
  const int pl = 16 * pq + col, pp = 64 * g + pl;
  const u32 tag = __hip_atomic_load(ep, __ATOMIC_RELAXED, __HIP_MEMORY_SCOPE_AGENT) + 1u;
  cf lam, coef2;
  {
    const float lre = bf2f(p.lre[pp]), lim = bf2f(p.lim[pp]);
    const float dt = rbf(expf(bf2f(p.ldtB[pp])));
    const float zr = dt * lre, zi = dt * lim;
    const float er = expf(zr);
    float sn, cs;
    sincosf(zi, &sn, &cs);
    lam = {er * cs, er * sn};
    const float nr = lam.r - 1.0f, ni = lam.i;
    const float den = 1.0f / (lre * lre + lim * lim);
    coef2 = {2.0f * (nr * lre + ni * lim) * den, 2.0f * (ni * lre - nr * lim) * den};
  }
  f32x4 are[4], aim[4];
#pragma unroll
  for (int m = 0; m < 4; ++m) { are[m] = f32x4{0, 0, 0, 0}; aim[m] = f32x4{0, 0, 0, 0}; }
  const int o0 = col * 128 + (((0 + q) ^ (col & 7)) << 4);
  const int o1 = col * 128 + (((4 + q) ^ (col & 7)) << 4);
  for (int s = 0; s < 9; ++s) {
    bar();
    LDS const char* buf = smem + (s % G_NB) * G_STAGE;
    LDS const char* ab = buf + hf * 8192;
    LDS const char* bb = buf + 16384 + pq * 4096;
#pragma unroll
    for (int ks = 0; ks < 2; ++ks) {
      const int o = ks ? o1 : o0;
      const bf16x8 br = lds_frag(bb + o), bi = lds_frag(bb + 2048 + o);
#pragma unroll
      for (int m = 0; m < 4; ++m) {
        const bf16x8 a = lds_frag(ab + m * 2048 + o);
        are[m] = mfma16(a, br, are[m]);
        aim[m] = mfma16(a, bi, aim[m]);
      }
    }
  }
  cf e = {0.f, 0.f};
#pragma unroll
  for (int t = 0; t < 16; ++t) e = cfma(lam, e, cf{are[t >> 2][t & 3], aim[t >> 2][t & 3]});
  const int seg = 4 * hf + q;
  *(LDS f32x2*)(smem + G_E + (seg * 64 + pl) * 8) = f32x2{e.r, e.i};
  cf l16 = cmul(lam, lam); l16 = cmul(l16, l16); l16 = cmul(l16, l16); l16 = cmul(l16, l16);
  cf l128 = cmul(l16, l16); l128 = cmul(l128, l128); l128 = cmul(l128, l128);
  bar();
  cf pre = {0.f, 0.f}, pw = {1.f, 0.f};
#pragma unroll
  for (int s = 0; s < 7; ++s) {
    const f32x2 v = *(LDS const f32x2*)(smem + G_E + (s * 64 + pl) * 8);
    const cf n = cfma(l16, pre, cf{v[0], v[1]});
    const cf np = cmul(pw, l16);
    if (s < seg) { pre = n; pw = np; }
  }
  if (seg == 7 && c < 15) {
    const cf tot = cfma(l16, pre, e);
    GLB u64* gp = gran + c * 128 + pl;
    __hip_atomic_store(gp, ((u64)tag << 32) | __float_as_uint(tot.r), __ATOMIC_RELAXED, __HIP_MEMORY_SCOPE_AGENT);
    __hip_atomic_store(gp + 64, ((u64)tag << 32) | __float_as_uint(tot.i), __ATOMIC_RELAXED, __HIP_MEMORY_SCOPE_AGENT);
  }
  bar();
  cf S = {0.f, 0.f};
  for (int cc = 0; cc < c; ++cc) {
    const f32x2 v = *(LDS const f32x2*)(smem + G_X + (cc * 64 + pl) * 8);
    S = cfma(l128, S, cf{v[0], v[1]});
  }
  cf r = cfma(pw, S, pre);
  u32 ore[8], oim[8];
#pragma unroll
  for (int t = 0; t < 16; t += 2) {
    r = cfma(lam, r, cf{are[t >> 2][t & 3], aim[t >> 2][t & 3]});
    const cf h0 = cmul(coef2, r);
    r = cfma(lam, r, cf{are[t >> 2][(t & 3) + 1], aim[t >> 2][(t & 3) + 1]});
    const cf h1 = cmul(coef2, r);
    ore[t >> 1] = pack_bf16(h0.r, h1.r);
    oim[t >> 1] = pack_bf16(-h0.i, -h1.i);
  }
  u16* hT = (u16*)(p.ws + WS_HT);
  const int l0 = 128 * c + 64 * hf + 16 * q;
  u32x4* dre = (u32x4*)(hT + (size_t)pp * SL + l0);
  u32x4* dim = (u32x4*)(hT + (size_t)(DM + pp) * SL + l0);
  dre[0] = u32x4{ore[0], ore[1], ore[2], ore[3]};
  dre[1] = u32x4{ore[4], ore[5], ore[6], ore[7]};
  dim[0] = u32x4{oim[0], oim[1], oim[2], oim[3]};
  dim[1] = u32x4{oim[4], oim[5], oim[6], oim[7]};
}

constexpr int S_CPY = 4160;
constexpr int S_X = 0;
constexpr int S_Y = 6 * 4 * S_CPY;
constexpr int S_YSZ = 41 * 64;
constexpr int S_Z = S_Y + 12 * S_YSZ;
constexpr int S_ZSZ = (256 + SL) * 2;
constexpr int K1_LDS = S_Z + 6 * S_ZSZ;

DEV cf cexp_k(float zr, float zi, float k) {
  const float mag = __builtin_amdgcn_exp2f(zr * k * 1.44269504088896341f);
  const float x = zi * k;
  const float hi = x * 0.15915494309189535f;
  const float lo = fmaf(x, 0.15915494309189535f, -hi) + x * 4.6017384e-10f;
  const float t = __builtin_amdgcn_fractf(hi) + lo;
  return {mag * __builtin_amdgcn_cosf(t), mag * __builtin_amdgcn_sinf(t)};
}

template <bool NEG>
DEV void octet(LDS char* dst, cf s0, cf m1, cf m2) {
  cf v[4];
  v[0] = s0; v[1] = cmul(v[0], m1); v[2] = cmul(v[1], m1); v[3] = cmul(v[2], m1);
#pragma unroll
  for (int i = 0; i < 4; ++i) {
    *(LDS u32*)(dst + i * 288) = pack_bf16(v[i].r, NEG ? -v[i].i : v[i].i);
    const cf t = cmul(v[i], m2);
    *(LDS u32*)(dst + (i + 4) * 288) = pack_bf16(t.r, NEG ? -t.i : t.i);
  }
}

template <int T0, int T1, int T2, int T3, int NF>
DEV void conv_unit(LDS const char* fb, LDS const char* gb, u16* dst) {
  f32x4 acc[4];
#pragma unroll
  for (int i = 0; i < 4; ++i) acc[i] = f32x4{0, 0, 0, 0};
#pragma unroll 1
  for (int rho = 0; rho < 8; ++rho) {
    bf16x8 F[NF], G[NF];
#pragma unroll
    for (int s = 0; s < NF; ++s) {
      const s16x4 lo = *(LDS const s16x4*)(fb + 512 * (7 - s));
      const s16x4 hi = *(LDS const s16x4*)(fb + 512 * (7 - s) + 8);
      F[s] = __builtin_bit_cast(bf16x8, __builtin_shufflevector(lo, hi, 0, 1, 2, 3, 4, 5, 6, 7));
      G[s] = lds_frag(gb + 512 * s);
    }
#pragma unroll
    for (int s = 0; s < NF; ++s) {
#pragma unroll
      for (int f = 0; f < NF; ++f) {
        const int t = s + f;
        if (t == T0) acc[0] = mfma16(F[s], G[f], acc[0]);
        if (t == T1) acc[1] = mfma16(F[s], G[f], acc[1]);
        if (t == T2) acc[2] = mfma16(F[s], G[f], acc[2]);
        if (t == T3) acc[3] = mfma16(F[s], G[f], acc[3]);
      }
    }
    fb -= 64;
    gb -= 64;
  }
  const int tiles[4] = {T0, T1, T2, T3};
#pragma unroll
  for (int i = 0; i < 4; ++i) {
    u32x2 o = {pack_bf16(acc[i][0], acc[i][1]), pack_bf16(acc[i][2], acc[i][3])};
    *(u32x2*)(dst + 256 * tiles[i]) = o;
  }
}

DEV void role_s(const P1& p, LDS char* smem, int sg, int w, int lane) {
  const int tid = w * 64 + lane;
  const int ch = w >> 1, hh = w & 1, d = 6 * sg + ch;
  u32x3 ur[4], up[3];
  if (w < 8) {
    const char* ub = (const char*)p.x + 12 * sg;
#pragma unroll
    for (int i = 0; i < 4; ++i) ur[i] = *(const u32x3*)(ub + (size_t)(4 * tid + i) * 1152);
#pragma unroll
    for (int i = 0; i < 3; ++i) {
      up[i] = u32x3{0, 0, 0};
      if (lane == 0 && w > 0) up[i] = *(const u32x3*)(ub + (size_t)(4 * tid - 3 + i) * 1152);
    }
  }
  const u32x4 ix0 = *(const u32x4*)(g_idx.v + 1024 * hh + 16 * lane);
  const u32x4 ix1 = *(const u32x4*)(g_idx.v + 1024 * hh + 16 * lane + 8);
  {
    const int n = d * 128 + 64 * hh + lane;
    const float dt = rbf(expf(bf2f(p.ldtA[d])));
    const float ar = -rbf(expf(bf2f(p.la[n])));
    const float zr = ar * dt, zi = bf2f(p.aim[n]) * dt;
    const cf C = {2.0f * (bf2f(p.cre[n]) * dt), 2.0f * (bf2f(p.cim[n]) * dt)};
    const cf w1 = cexp_k(zr, zi, 1.f), w8 = cexp_k(zr, zi, 8.f), w128 = cexp_k(zr, zi, 128.f);
    const cf w2 = cmul(w1, w1), w4 = cmul(w2, w2);
    const cf w16 = cmul(w8, w8), w32 = cmul(w16, w16), w64 = cmul(w32, w32);
    const cf w256 = cmul(w128, w128), w512 = cmul(w256, w256), w1024 = cmul(w512, w512), w2048 = cmul(w1024, w1024);
    LDS char* st = smem + S_X + w * 4608;
    LDS char* wr = st + 4 * lane;
    LDS const char* rd = st + (lane & 15) * 288 + (lane >> 4) * 16;
    bf16x8 B[4];
    octet<false>(wr, cf{1.f, 0.f}, w8, w32);
    octet<false>(wr + 8 * 288, w64, w8, w32);
    __builtin_amdgcn_fence(__ATOMIC_ACQ_REL, "wavefront");
#pragma unroll
    for (int ks = 0; ks < 4; ++ks) B[ks] = lds_frag(rd + 64 * ks);
    __builtin_amdgcn_fence(__ATOMIC_ACQ_REL, "wavefront");
    LDS char* yt = smem + S_Y + w * S_YSZ + ((lane >> 4) * 4 * 16 + (lane & 15)) * 4;
#pragma unroll
    for (int mt = 0; mt < 3; ++mt) {
      if (mt == 0) { octet<true>(wr, C, w1, w4); octet<true>(wr + 8 * 288, cmul(C, w128), w1, w4); }
      if (mt == 1) { octet<true>(wr, cmul(C, w256), w2, w128); octet<true>(wr + 8 * 288, cmul(C, w512), w128, w4); }
      if (mt == 2) { octet<true>(wr, cmul(C, w1024), w128, w512);
                     const cf t = cmul(C, w2048);
                     *(LDS u32*)(wr + 8 * 288) = pack_bf16(t.r, -t.i); }
      __builtin_amdgcn_fence(__ATOMIC_ACQ_REL, "wavefront");
      f32x4 acc = {0, 0, 0, 0};
#pragma unroll
      for (int ks = 0; ks < 4; ++ks) acc = mfma16(lds_frag(rd + 64 * ks), B[ks], acc);
      __builtin_amdgcn_fence(__ATOMIC_ACQ_REL, "wavefront");
#pragma unroll
      for (int r = 0; r < 4; ++r) {
        if (mt < 2 || (lane >> 4) < 2 || ((lane >> 4) == 2 && r == 0))
          *(LDS float*)(yt + (mt * 16 + r) * 64) = acc[r];
      }
    }
  }
  bar();
  if (w < 8) {
    u32x3 rm[3];
#pragma unroll
    for (int i = 0; i < 3; ++i)
#pragma unroll
      for (int k = 0; k < 3; ++k)
        rm[i][k] = (u32)__builtin_amdgcn_update_dpp((int)up[i][k], (int)ur[i + 1][k], 0x138, 0xf, 0xf, false);
#pragma unroll
    for (int c = 0; c < 6; ++c) {
      const u32 sel = (c & 1) ? 0x07060302u : 0x05040100u;
      const int k = c >> 1;
      const u32 P3 = __builtin_amdgcn_perm(ur[2][k], ur[3][k], sel);
      const u32 P2 = __builtin_amdgcn_perm(ur[1][k], ur[2][k], sel);
      const u32 P1_ = __builtin_amdgcn_perm(ur[0][k], ur[1][k], sel);
      const u32 P0 = __builtin_amdgcn_perm(rm[2][k], ur[0][k], sel);
      const u32 Pm1 = __builtin_amdgcn_perm(rm[1][k], rm[2][k], sel);
      const u32 Pm2 = __builtin_amdgcn_perm(rm[0][k], rm[1][k], sel);
      LDS char* xb = smem + S_X + c * 4 * S_CPY + 4088 - 8 * tid;
      *(LDS u32x2*)(xb) = u32x2{P3, P1_};
      *(LDS u32x2*)(xb + S_CPY) = u32x2{P2, P0};
      *(LDS u32x2*)(xb + 2 * S_CPY) = u32x2{P1_, Pm1};
      *(LDS u32x2*)(xb + 3 * S_CPY) = u32x2{P0, Pm2};
    }
  } else {
    const int t = tid - 512;
    if (t < 24 * 4) *(LDS u32x4*)(smem + S_X + (t >> 2) * S_CPY + 4096 + (t & 3) * 16) = u32x4{0, 0, 0, 0};
    if (t < 6 * 32) *(LDS u32x4*)(smem + S_Z + (t >> 5) * S_ZSZ + (t & 31) * 16) = u32x4{0, 0, 0, 0};
  }
  {
    LDS const char* y0 = smem + S_Y + (2 * ch) * S_YSZ;
    u32 o[8];
#pragma unroll
    for (int i = 0; i < 8; ++i) {
      const u32 pr = i < 4 ? ix0[i] : ix1[i - 4];
      const u32 a = (pr & 0xffffu) * 4, b = (pr >> 16) * 4;
      const float va = *(LDS const float*)(y0 + a) + *(LDS const float*)(y0 + S_YSZ + a);
      const float vb = *(LDS const float*)(y0 + b) + *(LDS const float*)(y0 + S_YSZ + b);
      o[i] = pack_bf16(va, vb);
    }
    LDS char* z = smem + S_Z + ch * S_ZSZ + (256 + 1024 * hh + 16 * lane) * 2;
    *(LDS u32x4*)z = u32x4{o[0], o[1], o[2], o[3]};
    *(LDS u32x4*)(z + 16) = u32x4{o[4], o[5], o[6], o[7]};
  }
  bar();
  {
    const int b = lane & 15, kq = lane >> 4, cp = (3 - b) & 3;
    LDS const char* fb = smem + S_X + ch * 4 * S_CPY + cp * S_CPY +
                         2 * (2047 - cp - b - 16 * (kq >> 1) + 8 * (kq & 1)) - 512 * 7;
    LDS const char* gb = smem + S_Z + ch * S_ZSZ + 512 + 32 * (b - (kq >> 1)) + 16 * (kq & 1);
    u16* dst = (u16*)(p.ws + WS_AT) + (size_t)d * SL + 16 * b + 4 * kq;
    if (hh == 0) conv_unit<0, 1, 6, 7, 8>(fb, gb, dst);
    else conv_unit<2, 3, 4, 5, 6>(fb, gb, dst);
  }
}

DEV void role_gate(const P1& p, int rb, int w, int lane) {
  const int sub = lane & 7;
  u32x4 wv[9];
#pragma unroll
  for (int i = 0; i < 9; ++i) wv[i] = *(const u32x4*)((const char*)p.gw + (i * 8 + sub) * 16);
  for (int r = w * 8 + (lane >> 3); r < 128; r += 96) {
    const int l = rb * 128 + r;
    float s = 0.f;
#pragma unroll
    for (int i = 0; i < 9; ++i) {
      const u32x4 xv = *(const u32x4*)((const char*)p.x + (size_t)l * 1152 + (i * 8 + sub) * 16);
#pragma unroll
      for (int k = 0; k < 4; ++k) {
        const u32 pr = pack_bf16(bflo(xv[k]) * bflo(wv[i][k]), bfhi(xv[k]) * bfhi(wv[i][k]));
        s += bflo(pr);
        s += bfhi(pr);
      }
    }
    s += __shfl_xor(s, 1); s += __shfl_xor(s, 2); s += __shfl_xor(s, 4);
    const float gv = rbf(1.0f / (1.0f + expf(-rbf(s))));
    if (sub == 0) *(f32x2*)(p.ws + WS_G + (size_t)l * 8) = f32x2{gv, rbf(1.0f - gv)};
  }
}

__global__ __launch_bounds__(768, 3) void k1_scan_conv(P1 p) {
  extern __shared__ __attribute__((aligned(16))) char smem_raw[];
  LDS char* smem = (LDS char*)smem_raw;
  const int w = __builtin_amdgcn_readfirstlane(threadIdx.x >> 6), lane = threadIdx.x & 63;
  const int xcd = blockIdx.x & 7, j = blockIdx.x >> 3;
  if (j < 18) role_g(p, smem, 2 * xcd + j / 9, j % 9, w, lane);
  else if (j < 30) role_s(p, smem, 12 * xcd + (j - 18), w, lane);
  else role_gate(p, 2 * xcd + (j - 30), w, lane);
}

constexpr int H_NB = 6, H_STAGE = 17408;
constexpr int H_AT = H_NB * H_STAGE + 1024;
constexpr int K2_LDS = H_AT + 9216;

template <int NDT, int DT0>
DEV void k2_compute(const P2& p, LDS char* smem, int l0, int d0, int w, int lane) {
  const int lh = (w >> 1) & 1, kh = w & 1;
  const int r = lane & 15, kq = lane >> 4;
  const int lt = 2 * lh + kh;
  const int l = l0 + 16 * lt + r;
  u32x2 xv[NDT], da[NDT], db[NDT];
#pragma unroll
  for (int t = 0; t < NDT; ++t) {
    int d = d0 + 16 * (DT0 + t) + 4 * kq;
    if (DT0 + t == 4 && kq >= 2) d = d0;
    xv[t] = *(const u32x2*)(p.x + (size_t)l * DM + d);
    da[t] = *(const u32x2*)(p.dskA + d);
    db[t] = *(const u32x2*)(p.dskB + d);
  }
  const f32x2 gg = *(const f32x2*)(p.ws + WS_G + (size_t)l * 8);

  f32x4 acc[2][NDT];
#pragma unroll
  for (int i = 0; i < 2; ++i)
#pragma unroll
    for (int t = 0; t < NDT; ++t) acc[i][t] = f32x4{0, 0, 0, 0};
  int ho[2][2];
#pragma unroll
  for (int i = 0; i < 2; ++i)
#pragma unroll
    for (int u = 0; u < 2; ++u) {
      const int row = 32 * kh + 8 * kq + 4 * u + (r >> 2);
      const int gsw = ((row >> 1) & 1) | (((row >> 3) & 1) << 1);
      ho[i][u] = row * 128 + (((2 * lh + i) ^ gsw) << 5) + (r & 3) * 8;
    }
  const int co = 8192 + (16 * DT0 + r) * 128 + (((4 * kh + kq) ^ (r & 7)) << 4);
  for (int s = 0; s < 18; ++s) {
    bar();
    LDS const char* buf = smem + (s % H_NB) * H_STAGE;
    bf16x8 hb[2];
#pragma unroll
    for (int i = 0; i < 2; ++i) {
      const s16x4 a = __builtin_amdgcn_ds_read_tr16_b64_v4i16((LDS s16x4*)(buf + ho[i][0]));
      const s16x4 b = __builtin_amdgcn_ds_read_tr16_b64_v4i16((LDS s16x4*)(buf + ho[i][1]));
      hb[i] = __builtin_bit_cast(bf16x8, __builtin_shufflevector(a, b, 0, 1, 2, 3, 4, 5, 6, 7));
    }
#pragma unroll
    for (int t = 0; t < NDT; ++t) {
      const bf16x8 ca = lds_frag(buf + co + t * 2048);
      acc[0][t] = mfma16(ca, hb[0], acc[0][t]);
      acc[1][t] = mfma16(ca, hb[1], acc[1][t]);
    }
  }
  bar();
  LDS char* ex = smem + w * 3072 + lane * 16;
#pragma unroll
  for (int t = 0; t < NDT; ++t) {
    const f32x4 a0 = acc[0][t], a1 = acc[1][t];
    *(LDS f32x4*)(ex + t * 1024) = kh ? +a0 : +a1;
  }
  bar();
  LDS const char* ey = smem + (w ^ 1) * 3072 + lane * 16;
#pragma unroll
  for (int t = 0; t < NDT; ++t) {
    const f32x4 o = *(LDS const f32x4*)(ey + t * 1024);
    const f32x4 a0 = acc[0][t], a1 = acc[1][t];
    const f32x4 y = (kh ? +a1 : +a0) + o;
    const s16x4 av = __builtin_amdgcn_ds_read_tr16_b64_v4i16(
        (LDS s16x4*)(smem + H_AT + (16 * (DT0 + t) + 4 * kq + (r >> 2)) * 128 + lt * 32 + (r & 3) * 8));
    f32x4 res;
#pragma unroll
    for (int i = 0; i < 4; ++i) {
      const u32 xw = xv[t][i >> 1], aw = da[t][i >> 1], bw = db[t][i >> 1];
      const float xf = (i & 1) ? bfhi(xw) : bflo(xw);
      const float sa = (i & 1) ? bfhi(aw) : bflo(aw);
      const float sb = (i & 1) ? bfhi(bw) : bflo(bw);
      const float yc = bf2f((u16)av[i]);
      const float a = rbf(yc + rbf(xf * sa));
      const float b = y[i] + rbf(xf * sb);
      res[i] = rbf(gg[0] * a) + gg[1] * b;
    }
    if (!(DT0 + t == 4 && kq >= 2)) *(f32x4*)(p.out + (size_t)l * DM + d0 + 16 * (DT0 + t) + 4 * kq) = res;
  }
}

__global__ __launch_bounds__(768, 3) void k2_out(P2 p) {
  extern __shared__ __attribute__((aligned(16))) char smem_raw[];
  LDS char* smem = (LDS char*)smem_raw;
  const int w = __builtin_amdgcn_readfirstlane(threadIdx.x >> 6), lane = threadIdx.x & 63;
  const int xcd = blockIdx.x & 7, j = blockIdx.x >> 3;
  const int l0 = 64 * (4 * xcd + (j >> 3)), d0 = 72 * (j & 7);
  if (w >= 8) {
    const int lw = w - 8;
    const char* hT = p.ws + WS_HT;
    u32 ho[2], co[2], c9;
#pragma unroll
    for (int i = 0; i < 2; ++i) {
      const int row = 8 * (2 * lw + i) + (lane >> 3), cp = lane & 7;
      const int gsw = ((row >> 1) & 1) | (((row >> 3) & 1) << 1);
      ho[i] = (u32)row * 4096u + l0 * 2 + (((((cp >> 1) ^ gsw) << 1) | (cp & 1)) << 4);
      co[i] = (u32)(d0 + row) * 1152u + ((cp ^ (row & 7)) << 4);
    }
    {
      const int row = 64 + 2 * lw + ((lane >> 3) & 1), cp = lane & 7;
      c9 = (u32)(d0 + row) * 1152u + ((cp ^ (row & 7)) << 4);
    }
    {
      const char* aT = p.ws + WS_AT;
#pragma unroll
      for (int i = 0; i < 3; ++i) {
        const int pc = lw + 4 * i;
        if (pc < 9) dma16(aT + (size_t)(d0 + 8 * pc + (lane >> 3)) * 4096 + l0 * 2 + (lane & 7) * 16, smem + H_AT + pc * 1024);
      }
    }
    auto issue = [&](int s) {
      LDS char* buf = smem + (s % H_NB) * H_STAGE;
      const char* cb = (const char*)(s < 9 ? p.cre : p.cim) + (s % 9) * 128;
#pragma unroll
      for (int i = 0; i < 2; ++i) dma16(hT + ho[i] + (size_t)s * 64 * 4096, buf + (2 * lw + i) * 1024);
#pragma unroll
      for (int i = 0; i < 2; ++i) dma16(cb + co[i], buf + 8192 + (2 * lw + i) * 1024);
      if (lane < 16) dma16(cb + c9, buf + 16384 + lw * 256);
    };
#pragma unroll
    for (int s = 0; s < H_NB - 1; ++s) issue(s);
#pragma unroll
    for (int s = 0; s < 18; ++s) {
      const int rem = (17 - s) < (H_NB - 2) ? (17 - s) : (H_NB - 2);
      if (rem == 4) wait_vm<20>(); else if (rem == 3) wait_vm<15>(); else if (rem == 2) wait_vm<10>();
      else if (rem == 1) wait_vm<5>(); else wait_vm<0>();
      bar();
      if (s + H_NB - 1 < 18) issue(s + H_NB - 1);
    }
    return;
  }
  if (w < 4) k2_compute<3, 0>(p, smem, l0, d0, w, lane);
  else k2_compute<2, 3>(p, smem, l0, d0, w, lane);
}

extern "C" void kernel_launch(void* const* d_in, const int* in_sizes, int n_in, void* d_out, int out_size,
                              void* d_ws, size_t ws_size, hipStream_t stream) {
  P1 a;
  a.x = (const u16*)d_in[0]; a.la = (const u16*)d_in[1]; a.aim = (const u16*)d_in[2];
  a.cre = (const u16*)d_in[3]; a.cim = (const u16*)d_in[4]; a.ldtA = (const u16*)d_in[5];
  a.lre = (const u16*)d_in[8]; a.lim = (const u16*)d_in[9]; a.bre = (const u16*)d_in[10];
  a.bim = (const u16*)d_in[11]; a.ldtB = (const u16*)d_in[14]; a.gw = (const u16*)d_in[16];
  a.ws = (char*)d_ws;
  P2 b;
  b.x = (const u16*)d_in[0]; b.cre = (const u16*)d_in[12]; b.cim = (const u16*)d_in[13];
  b.dskA = (const u16*)d_in[6]; b.dskB = (const u16*)d_in[15];
  b.ws = (const char*)d_ws; b.out = (float*)d_out;
  (void)hipFuncSetAttribute((const void*)k1_scan_conv, hipFuncAttributeMaxDynamicSharedMemorySize, K1_LDS);
  (void)hipFuncSetAttribute((const void*)k2_out, hipFuncAttributeMaxDynamicSharedMemorySize, K2_LDS);
  k1_scan_conv<<<256, 768, K1_LDS, stream>>>(a);
  k2_out<<<256, 768, K2_LDS, stream>>>(b);
}
```

```cpp
#include <hip/hip_runtime.h>
#include <hip/hip_bf16.h>
#include <stdint.h>
#include <type_traits>

typedef unsigned short u16;
typedef unsigned int u32;
typedef unsigned long long u64;
typedef __attribute__((ext_vector_type(8))) __bf16 bf16x8;
typedef __attribute__((ext_vector_type(2))) __bf16 bf16x2;
typedef __attribute__((ext_vector_type(4))) short s16x4;
typedef __attribute__((ext_vector_type(4))) float f32x4;
typedef __attribute__((ext_vector_type(2))) float f32x2;
typedef __attribute__((ext_vector_type(4))) unsigned u32x4;
typedef __attribute__((ext_vector_type(2))) unsigned u32x2;
#define LDS __attribute__((address_space(3)))
#define GLB __attribute__((address_space(1)))
#define DEV __device__ __forceinline__
#define RLX_AGENT __ATOMIC_RELAXED, __HIP_MEMORY_SCOPE_AGENT

constexpr int SL = 2048;
constexpr int DM = 576;

constexpr size_t WS_HB = 0;
constexpr size_t WS_AT = WS_HB + (size_t)2 * DM * SL * 2;
constexpr size_t WS_G = WS_AT + (size_t)DM * SL * 2;
constexpr size_t WS_GRAN = WS_G + (size_t)SL * 8;
constexpr size_t WS_EP = WS_GRAN + (size_t)9 * 16 * 2 * 64 * 8;
constexpr size_t WS_FL = WS_EP + 4096;
constexpr size_t WS_XT = WS_FL + (size_t)16 * DM * 8;
constexpr size_t WS_IN = WS_XT + (size_t)DM * SL * 2;
constexpr u64 FLAG_SET = 0x9E3779B97F4A7C15ull;
constexpr u32 SPIN_CAP = 1u << 17;
constexpr int IN_H = 0, IN_G = 36, IN_A = 40;

constexpr size_t WS_ST = (size_t)16 << 20;
#define STAMP(kern, idx) do {} while (0)
#define LSTAMP(kern, idx) do {} while (0)
#define LSTAMP2(kern, idx) do {} while (0)

DEV float bf2f(u16 v) { return __uint_as_float((u32)v << 16); }
DEV float bflo(u32 v) { return __uint_as_float(v << 16); }
DEV float bfhi(u32 v) { return __uint_as_float(v & 0xffff0000u); }
DEV u32 pack_bf16(float lo, float hi) {
  f32x2 v = {lo, hi};
  bf16x2 b = __builtin_convertvector(v, bf16x2);
  return __builtin_bit_cast(u32, b);
}
DEV float rbf(float x) { return bflo(pack_bf16(x, x)); }
template <int N> DEV void wait_vm() { asm volatile("s_waitcnt vmcnt(%0)" ::"n"(N) : "memory"); }
DEV void bar() { asm volatile("s_waitcnt lgkmcnt(0)\n\ts_barrier" ::: "memory"); }
DEV void wave_fence() { __builtin_amdgcn_fence(__ATOMIC_ACQ_REL, "wavefront"); }
template <int AUX> DEV void dma16(const void* src, LDS char* dst) {
  __builtin_amdgcn_global_load_lds((const GLB u32*)src, (LDS u32*)dst, 16, 0, AUX);
}
constexpr int SC1 = 16;
DEV bf16x8 lds_frag(LDS const char* p) { return *(LDS const bf16x8*)p; }
DEV s16x4 tr_read(LDS const char* a) { return __builtin_amdgcn_ds_read_tr16_b64_v4i16((LDS s16x4*)a); }
DEV bf16x8 tr_frag(LDS const char* a, LDS const char* b) {
  const s16x4 lo = tr_read(a), hi = tr_read(b);
  return __builtin_bit_cast(bf16x8, __builtin_shufflevector(lo, hi, 0, 1, 2, 3, 4, 5, 6, 7));
}
DEV f32x4 mfma16(bf16x8 a, bf16x8 b, f32x4 c) { return __builtin_amdgcn_mfma_f32_16x16x32_bf16(a, b, c, 0, 0, 0); }
DEV __amdgpu_buffer_rsrc_t mk_rsrc(const void* base, int bytes) {
  return __builtin_amdgcn_make_buffer_rsrc(const_cast<void*>(base), (short)0, bytes, 0x00020000);
}
struct cf { float r, i; };
DEV cf cmul(cf a, cf b) { return {a.r * b.r - a.i * b.i, a.r * b.i + a.i * b.r}; }
DEV cf cfma(cf a, cf b, cf c) { return {fmaf(a.r, b.r, fmaf(-a.i, b.i, c.r)), fmaf(a.r, b.i, fmaf(a.i, b.r, c.i))}; }
typedef f32x2 c2;
DEV c2 cm2(c2 a, c2 b) {
  c2 t, r;
  asm("v_pk_mul_f32 %0, %1, %2 op_sel:[0,0] op_sel_hi:[0,1]" : "=v"(t) : "v"(a), "v"(b));
  asm("v_pk_fma_f32 %0, %1, %2, %3 op_sel:[1,1,0] op_sel_hi:[1,0,1] neg_lo:[1,0,0]" : "=v"(r) : "v"(a), "v"(b), "v"(t));
  return r;
}
DEV f32x2 fma2(f32x2 a, f32x2 b, f32x2 c) { return __builtin_elementwise_fma(a, b, c); }
DEV f32x2 lo2(f32x4 v) { return __builtin_shufflevector(v, v, 0, 1); }
DEV f32x2 hi2(f32x4 v) { return __builtin_shufflevector(v, v, 2, 3); }
DEV f32x4 cat2(f32x2 a, f32x2 b) { return __builtin_shufflevector(a, b, 0, 1, 2, 3); }
DEV void wait_flags(GLB u64* word, bool need) {
  for (u32 spin = 0;; ++spin) {
    const u64 v = __hip_atomic_load(word, RLX_AGENT);
    if (__all(!need || v == FLAG_SET) || spin > SPIN_CAP) break;
    __builtin_amdgcn_s_sleep(2);
  }
}
DEV int tile_block(int lt, int idx) {
  const int xcd = lt >> 2, a = lt & 3;
  int j;
  if (idx < 5) { const int e = 5 * a + idx; j = e < 18 ? e : e + 12; }
  else j = 18 + 3 * a + (idx - 5);
  return (j << 3) | xcd;
}

struct IdxTab { u16 v[SL + 8]; };
constexpr int round_pos(int j) {
  if (j < 256) return j;
  int sh = j < 512 ? 1 : (j < 1024 ? 2 : 3);
  int q = j >> sh, rem = j & ((1 << sh) - 1), half = 1 << (sh - 1);
  if (rem > half || (rem == half && (q & 1))) ++q;
  return q << sh;
}
constexpr IdxTab make_idx() {
  IdxTab t{};
  for (int j = 0; j < SL; ++j) {
    int pos = round_pos(j), row = 0, b = (pos & 127) >> 3;
    if (pos < 256) row = 8 * (pos >> 7) + (pos & 7);
    else if (pos < 512) row = 16 + ((pos & 7) >> 1) + 4 * ((pos - 256) >> 7);
    else if (pos < 1024) row = 24 + ((pos - 512) >> 7) + 4 * ((pos & 7) >> 2);
    else if (pos < 2048) row = 32 + ((pos - 1024) >> 7);
    else { row = 40; b = 0; }
    t.v[j] = (u16)((row * 16 + b) * 4);
  }
  return t;
}
__device__ const IdxTab g_idx = make_idx();

struct P {
  const u16 *x, *la, *aim, *creA, *cimA, *ldtA, *dskA;
  const u16 *lre, *lim, *bre, *bim, *cre, *cim, *ldtB, *dskB, *gw;
  char* ws;
  float* out;
};

constexpr int G_NB = 4, G_STAGE = 32768;
constexpr int G_E = G_NB * G_STAGE;
constexpr int G_PF = G_E + 4096;
constexpr int G_L = G_PF + 4096;
constexpr int G_X = G_L + 1024;
constexpr int G_T = G_X + 2048;
constexpr int G_END = G_T + 8 * 16 * 144;

DEV void g_loader(const P& p, LDS char* smem, int c, int g, int lw, int lane) {
  [[maybe_unused]] const int w = lw + 8;
  GLB u32* ep = (GLB u32*)(p.ws + WS_EP + g * 256);
  GLB u64* gran = (GLB u64*)(p.ws + WS_GRAN) + (size_t)g * 16 * 128;
  const u32 tag = __hip_atomic_load(ep, RLX_AGENT) + 1u;
  u32 xo[4], bo[4];
#pragma unroll
  for (int i = 0; i < 4; ++i) {
    const int rho = 8 * (4 * lw + i) + (lane >> 3);
    const int hf = rho >> 6, m = (rho >> 4) & 3, r = rho & 15;
    const int tau = 64 * hf + 16 * (r >> 2) + 8 * (r & 1) + 2 * m + ((r >> 1) & 1);
    const int ch = (lane & 7) ^ (rho & 7);
    xo[i] = (u32)(128 * c + tau) * 1152u + ch * 16;
    const int pp = 64 * g + 16 * (rho >> 5) + r;
    bo[i] = (u32)pp * 1152u + ch * 16;
  }
  auto issue = [&](int s) {
    LDS char* buf = smem + (s & (G_NB - 1)) * G_STAGE + lw * 4096;
    int kb = g + s; kb = (kb >= 9 ? kb - 9 : kb) * 128;
#pragma unroll
    for (int i = 0; i < 4; ++i) dma16<0>((const char*)p.x + (xo[i] + kb), buf + i * 1024);
#pragma unroll
    for (int i = 0; i < 4; ++i) {
      const char* base = (const char*)((((4 * lw + i) >> 1) & 1) ? p.bim : p.bre);
      dma16<0>(base + (bo[i] + kb), buf + 16384 + i * 1024);
    }
  };
#pragma unroll
  for (int s = 0; s < G_NB - 1; ++s) issue(s);
#pragma unroll 1
  for (int s = 0; s < 6; ++s) { wait_vm<16>(); LSTAMP(0, 16 + s); bar(); issue(s + G_NB - 1); LSTAMP(0, 32 + s); }
  wait_vm<16>(); bar();
  wait_vm<8>(); bar();
  wait_vm<0>(); bar();
  STAMP(0, 1);
  bar();
  STAMP(0, 2);
  if (lw == 3) {
    const f32x2 lv = *(LDS const f32x2*)(smem + G_L + lane * 8);
    const cf l16 = {lv[0], lv[1]};
    f32x2 ev[8];
#pragma unroll
    for (int s = 0; s < 8; ++s) ev[s] = *(LDS const f32x2*)(smem + G_E + (s * 64 + lane) * 8);
    cf pre = {0.f, 0.f};
#pragma unroll
    for (int s = 0; s < 8; ++s) {
      *(LDS f32x2*)(smem + G_PF + (s * 64 + lane) * 8) = f32x2{pre.r, pre.i};
      pre = cfma(l16, pre, cf{ev[s][0], ev[s][1]});
    }
    if (c < 15) {
      __hip_atomic_store(gran + c * 128 + lane, ((u64)tag << 32) | __float_as_uint(pre.r), RLX_AGENT);
      __hip_atomic_store(gran + c * 128 + 64 + lane, ((u64)tag << 32) | __float_as_uint(pre.i), RLX_AGENT);
    }
  }
  const f32x2 lv = *(LDS const f32x2*)(smem + G_L + 512 + lane * 8);
  const cf l128 = {lv[0], lv[1]};
  const cf l256 = cmul(l128, l128), l512 = cmul(l256, l256);
  cf acc = {0.f, 0.f};
  if (lw < c) {
    const int nc = (c - lw + 3) >> 2;
    u64 a[4], b[4];
    for (u32 spin = 0;; ++spin) {
      bool ok = true;
#pragma unroll
      for (int i = 0; i < 4; ++i) {
        const int cc = lw + 4 * (i < nc ? i : 0);
        a[i] = __hip_atomic_load(gran + cc * 128 + lane, RLX_AGENT);
        b[i] = __hip_atomic_load(gran + cc * 128 + 64 + lane, RLX_AGENT);
      }
#pragma unroll
      for (int i = 0; i < 4; ++i) ok = ok && (u32)(a[i] >> 32) == tag && (u32)(b[i] >> 32) == tag;
      if (__all(ok) || spin > SPIN_CAP) break;
      __builtin_amdgcn_s_sleep(1);
    }
#pragma unroll
    for (int i = 0; i < 4; ++i)
      if (i < nc) acc = cfma(l512, acc, cf{__uint_as_float((u32)a[i]), __uint_as_float((u32)b[i])});
    const int e = c - 1 - (lw + 4 * (nc - 1));
    if (e & 1) acc = cmul(acc, l128);
    if (e & 2) acc = cmul(acc, l256);
  }
  *(LDS f32x2*)(smem + G_X + (lw * 64 + lane) * 8) = f32x2{acc.r, acc.i};
  STAMP(0, 3);
  bar();
  if (c == 15 && lw == 0 && lane == 0) __hip_atomic_store(ep, tag, RLX_AGENT);
}

template <int HF>
DEV void g_compute(const P& p, LDS char* smem, int c, int g, int w, int lane) {
  const int pq = w & 3, col = lane & 15, q = lane >> 4;
  const int pl = 16 * pq + col, pp = 64 * g + pl;
  cf lam, coef2;
  {
    const float lre = bf2f(p.lre[pp]), lim = bf2f(p.lim[pp]);
    const float dt = rbf(expf(bf2f(p.ldtB[pp])));
    const float zr = dt * lre, zi = dt * lim;
    const float er = expf(zr);
    float sn, cs;
    sincosf(zi, &sn, &cs);
    lam = {er * cs, er * sn};
    const float nr = lam.r - 1.0f, ni = lam.i;
    const float den = 1.0f / (lre * lre + lim * lim);
    coef2 = {2.0f * (nr * lre + ni * lim) * den, 2.0f * (ni * lre - nr * lim) * den};
  }
  cf l8 = cmul(lam, lam); l8 = cmul(l8, l8); l8 = cmul(l8, l8);
  const cf l16 = cmul(l8, l8);
  const cf l32 = cmul(l16, l16), l64 = cmul(l32, l32);
  if (HF == 0 && q == 0) {
    const cf l128 = cmul(l64, l64);
    *(LDS f32x2*)(smem + G_L + pl * 8) = f32x2{l16.r, l16.i};
    *(LDS f32x2*)(smem + G_L + 512 + pl * 8) = f32x2{l128.r, l128.i};
  }
  cf pw = HF ? l64 : cf{1.f, 0.f};
  { const cf t = cmul(pw, l16); if (q & 1) pw = t; }
  { const cf t = cmul(pw, l32); if (q & 2) pw = t; }
  asm volatile("" : "+v"(lam.r), "+v"(lam.i), "+v"(coef2.r), "+v"(coef2.i), "+v"(l8.r), "+v"(l8.i), "+v"(pw.r), "+v"(pw.i));
  f32x4 are[4], aim[4];
#pragma unroll
  for (int m = 0; m < 4; ++m) { are[m] = f32x4{0, 0, 0, 0}; aim[m] = f32x4{0, 0, 0, 0}; }
  const int o0 = col * 128 + (((0 + q) ^ (col & 7)) << 4);
  const int o1 = col * 128 + (((4 + q) ^ (col & 7)) << 4);
  bf16x8 br[2], bi[2], a[2][4];
#pragma unroll
  for (int ks = 0; ks < 2; ++ks) {
    br[ks] = bf16x8{}; bi[ks] = bf16x8{};
#pragma unroll
    for (int m = 0; m < 4; ++m) a[ks][m] = bf16x8{};
  }
  auto read_frags = [&](int s) {
    LDS const char* buf = smem + (s & (G_NB - 1)) * G_STAGE;
    LDS const char* ab = buf + HF * 8192;
    LDS const char* bb = buf + 16384 + pq * 4096;
#pragma unroll
    for (int ks = 0; ks < 2; ++ks) {
      const int o = ks ? o1 : o0;
      br[ks] = lds_frag(bb + o); bi[ks] = lds_frag(bb + 2048 + o);
#pragma unroll
      for (int m = 0; m < 4; ++m) a[ks][m] = lds_frag(ab + m * 2048 + o);
    }
  };
  auto mma = [&]() {
#pragma unroll
    for (int ks = 0; ks < 2; ++ks)
#pragma unroll
      for (int m = 0; m < 4; ++m) {
        are[m] = mfma16(a[ks][m], br[ks], are[m]);
        aim[m] = mfma16(a[ks][m], bi[ks], aim[m]);
      }
  };
  STAMP(0, 1);
#pragma unroll 1
  for (int s = 0; s < 9; ++s) {
    if (s == 2) wait_vm<0>();
    bar();
    if (s == 0) STAMP(0, 2);
    LSTAMP(0, 16 + s);
    if (HF == 0 && s == 2 && pq == 0)
      __hip_atomic_store((GLB u64*)(p.ws + WS_FL) + c * DM + 64 * g + lane, FLAG_SET, RLX_AGENT);
    if (HF == 1) { mma(); __builtin_amdgcn_sched_barrier(0); }
    read_frags(s);
    __builtin_amdgcn_sched_barrier(0);
    if (HF == 0) { mma(); __builtin_amdgcn_sched_barrier(0); }
    if (s == 0) {
      const int rr = 4 * q + (col >> 2);
      LDS const char* tb = smem + HF * 8192 + rr * 128 + (((2 * pq + ((col & 3) >> 1)) ^ (rr & 7)) << 4) + (col & 1) * 8;
      u32x2 t[4];
#pragma unroll
      for (int m = 0; m < 4; ++m) t[m] = __builtin_bit_cast(u32x2, tr_read(tb + m * 2048));
      u32x4 oa, ob;
#pragma unroll
      for (int m = 0; m < 4; ++m) {
        oa[m] = __builtin_amdgcn_perm(t[m][1], t[m][0], 0x05040100u);
        ob[m] = __builtin_amdgcn_perm(t[m][1], t[m][0], 0x07060302u);
      }
      LDS char* sc = smem + G_T + w * (16 * 144);
      *(LDS u32x4*)(sc + col * 144 + q * 32) = oa;
      *(LDS u32x4*)(sc + col * 144 + q * 32 + 16) = ob;
      wave_fence();
      const __amdgpu_buffer_rsrc_t rs = mk_rsrc(p.ws + WS_XT, DM * SL * 2);
#pragma unroll
      for (int u = 0; u < 2; ++u) {
        const int row = (lane >> 3) + 8 * u;
        const u32x4 v = *(LDS const u32x4*)(sc + row * 144 + (lane & 7) * 16);
        __builtin_amdgcn_raw_buffer_store_b128(v, rs, ((64 * g + 16 * pq + row) * SL + 128 * c + 64 * HF) * 2 + (lane & 7) * 16, 0, SC1);
      }
      __builtin_amdgcn_sched_barrier(0);
    }
  }
  if (HF == 1) mma();
  __builtin_amdgcn_sched_barrier(0);
  STAMP(0, 3);
  const f32x2 Lr = {lam.r, lam.r}, Li = {lam.i, lam.i};
  f32x2 er = {0.f, 0.f}, ei = {0.f, 0.f};
#pragma unroll
  for (int t = 0; t < 8; ++t) {
    const f32x2 br_ = (t & 1) ? hi2(are[t >> 1]) : lo2(are[t >> 1]);
    const f32x2 bi_ = (t & 1) ? hi2(aim[t >> 1]) : lo2(aim[t >> 1]);
    const f32x2 nr = fma2(-Li, ei, fma2(Lr, er, br_));
    ei = fma2(Li, er, fma2(Lr, ei, bi_));
    er = nr;
  }
  const cf eA = {er[0], ei[0]};
  const cf e = cfma(l8, eA, cf{er[1], ei[1]});
  const int seg = 4 * HF + q;
  *(LDS f32x2*)(smem + G_E + (seg * 64 + pl) * 8) = f32x2{e.r, e.i};
  STAMP(0, 4);
  bar();
  STAMP(0, 5);
  {
#pragma unroll
    for (int m = 0; m < 4; ++m) asm volatile("" : "+v"(are[m]), "+v"(aim[m]));
    const f32x2 Cr = {coef2.r, coef2.r}, Ci = {coef2.i, coef2.i};
#pragma unroll
    for (int m = 0; m < 4; ++m) {
      const f32x2 r0 = lo2(are[m]), r1 = hi2(are[m]), i0 = lo2(aim[m]), i1 = hi2(aim[m]);
      are[m] = cat2(fma2(-Ci, i0, Cr * r0), fma2(-Ci, i1, Cr * r1));
      aim[m] = cat2(fma2(Ci, r0, Cr * i0), fma2(Ci, r1, Cr * i1));
      asm volatile("" : "+v"(are[m]), "+v"(aim[m]));
    }
  }
  cf eAc = cmul(coef2, eA), pwc = cmul(coef2, pw);
  asm volatile("" : "+v"(eAc.r), "+v"(eAc.i), "+v"(pwc.r), "+v"(pwc.i));
  STAMP(0, 7);
  bar();
  STAMP(0, 8);
  cf cin;
  {
    f32x2 v[4];
#pragma unroll
    for (int i = 0; i < 4; ++i) v[i] = *(LDS const f32x2*)(smem + G_X + (i * 64 + pl) * 8);
    const f32x2 pf = *(LDS const f32x2*)(smem + G_PF + (seg * 64 + pl) * 8);
    const f32x2 S = (v[0] + v[1]) + (v[2] + v[3]);
    cin = cfma(pwc, cf{S[0], S[1]}, cmul(coef2, cf{pf[0], pf[1]}));
  }
  bar();
  const cf cinB = cfma(l8, cin, eAc);
  er = f32x2{cin.r, cinB.r}; ei = f32x2{cin.i, cinB.i};
  f32x2 hr[8], hi[8];
#pragma unroll
  for (int t = 0; t < 8; ++t) {
    const f32x2 br_ = (t & 1) ? hi2(are[t >> 1]) : lo2(are[t >> 1]);
    const f32x2 bi_ = (t & 1) ? hi2(aim[t >> 1]) : lo2(aim[t >> 1]);
    const f32x2 nr = fma2(-Li, ei, fma2(Lr, er, br_));
    ei = fma2(Li, er, fma2(Lr, ei, bi_));
    er = nr;
    hr[t] = er; hi[t] = ei;
  }
  u32x4 o[4];
#pragma unroll
  for (int t = 0; t < 4; ++t) {
    o[0][t] = pack_bf16(hr[2 * t][0], hr[2 * t + 1][0]);
    o[1][t] = pack_bf16(hr[2 * t][1], hr[2 * t + 1][1]);
    o[2][t] = pack_bf16(-hi[2 * t][0], -hi[2 * t + 1][0]);
    o[3][t] = pack_bf16(-hi[2 * t][1], -hi[2 * t + 1][1]);
  }
  STAMP(0, 9);
  {
    const __amdgpu_buffer_rsrc_t rs = mk_rsrc(p.ws + WS_HB, 2 * DM * SL * 2);
    const int hb = (((c * 2 + HF) * 2) * 9 + g) * 8192 + pq * 2048;
    const int p0 = (16 * q + col) * 16, p1 = 1024 + (16 * q + (col ^ 4)) * 16;
    __builtin_amdgcn_raw_buffer_store_b128(o[0], rs, hb + p0, 0, SC1);
    __builtin_amdgcn_raw_buffer_store_b128(o[1], rs, hb + p1, 0, SC1);
    __builtin_amdgcn_raw_buffer_store_b128(o[2], rs, hb + 9 * 8192 + p0, 0, SC1);
    __builtin_amdgcn_raw_buffer_store_b128(o[3], rs, hb + 9 * 8192 + p1, 0, SC1);
    wait_vm<0>();
    STAMP(0, 10);
    if (lane < 8)
      __hip_atomic_store((GLB u64*)(p.ws + WS_IN) + tile_block(2 * c + HF, lane) * 64 + IN_H + g * 4 + pq, FLAG_SET, RLX_AGENT);
  }
}

constexpr int S_CPY = 4160;
constexpr int S_X = 0;
constexpr int S_ST = 6 * 4 * S_CPY;
constexpr int S_Y = S_ST;
constexpr int S_YSZ = 2816;
constexpr int S_Z = S_Y + 12 * S_YSZ;
constexpr int S_ZSZ = (256 + SL) * 2;
constexpr int S_END = S_Z + 6 * S_ZSZ;

DEV c2 cexp_k(float zl, float th, float tl, float k) {
  const float mag = __builtin_amdgcn_exp2f(zl * k);
  const float t = fmaf(tl, k, __builtin_amdgcn_fractf(th * k));
  return c2{mag * __builtin_amdgcn_cosf(t), mag * __builtin_amdgcn_sinf(t)};
}

template <bool NEG>
DEV void octet(LDS char* dst, c2 s0, c2 m1, c2 m2) {
  c2 v[4];
  v[0] = s0; v[1] = cm2(v[0], m1); v[2] = cm2(v[1], m1); v[3] = cm2(v[2], m1);
#pragma unroll
  for (int i = 0; i < 4; ++i) {
    *(LDS u32*)(dst + i * 288) = pack_bf16(v[i][0], NEG ? -v[i][1] : v[i][1]);
    const c2 t = cm2(v[i], m2);
    *(LDS u32*)(dst + (i + 4) * 288) = pack_bf16(t[0], NEG ? -t[1] : t[1]);
  }
}

DEV void role_s(const P& p, LDS char* smem, int sg, int w, int lane) {
  const int tid = w * 64 + lane;
  const int ch = w >> 1, hh = w & 1, d = 6 * sg + ch;
  const int n = d * 128 + 64 * hh + lane;
  const u16 r_ldt = p.ldtA[d], r_la = p.la[n], r_aim = p.aim[n], r_cre = p.creA[n], r_cim = p.cimA[n];
  const u32x4 ix0 = *(const u32x4*)(g_idx.v + 1024 * hh + 16 * lane);
  const u32x4 ix1 = *(const u32x4*)(g_idx.v + 1024 * hh + 16 * lane + 8);
  const u32x2 ixn = *(const u32x2*)(g_idx.v + 1024 * hh + 1024);
  STAMP(0, 1);
  f32x4 tab[3];
  {
    const float L2E = 1.44269504088896341f;
    const float dt = rbf(__builtin_amdgcn_exp2f(bf2f(r_ldt) * L2E));
    const float ar = -rbf(__builtin_amdgcn_exp2f(bf2f(r_la) * L2E));
    const float zl = ar * dt * L2E, zi = bf2f(r_aim) * dt;
    const float th = zi * 0.15915494309189535f;
    const float tl = fmaf(zi, 0.15915494309189535f, -th) + zi * 4.6017384e-10f;
    const c2 C = {2.0f * (bf2f(r_cre) * dt), 2.0f * (bf2f(r_cim) * dt)};
    const c2 w1 = cexp_k(zl, th, tl, 1.f), w8 = cexp_k(zl, th, tl, 8.f), w128 = cexp_k(zl, th, tl, 128.f);
    const c2 w2 = cm2(w1, w1), w4 = cm2(w2, w2);
    const c2 w16 = cm2(w8, w8), w32 = cm2(w16, w16), w64 = cm2(w32, w32);
    const c2 w256 = cm2(w128, w128), w512 = cm2(w256, w256), w1024 = cm2(w512, w512), w2048 = cm2(w1024, w1024);
    STAMP(0, 2);
    LDS char* st = smem + S_ST + w * 4608;
    LDS char* wr = st + 4 * lane;
    LDS const char* rd = st + (lane & 15) * 288 + (lane >> 4) * 16;
    bf16x8 B[4];
    octet<true>(wr, c2{1.f, 0.f}, w8, w32);
    octet<true>(wr + 8 * 288, w64, w8, w32);
    wave_fence();
#pragma unroll
    for (int ks = 0; ks < 4; ++ks) B[ks] = lds_frag(rd + 64 * ks);
    wave_fence();
#pragma unroll
    for (int mt = 0; mt < 3; ++mt) {
      if (mt == 0) { octet<false>(wr, C, w1, w4); octet<false>(wr + 8 * 288, cm2(C, w128), w1, w4); }
      if (mt == 1) { octet<false>(wr, cm2(C, w256), w2, w128); octet<false>(wr + 8 * 288, cm2(C, w512), w128, w4); }
      if (mt == 2) { octet<false>(wr, cm2(C, w1024), w128, w512);
                     const c2 t = cm2(C, w2048);
                     *(LDS u32*)(wr + 8 * 288) = pack_bf16(t[0], t[1]); }
      wave_fence();
      f32x4 acc = {0, 0, 0, 0};
      bf16x8 A[4];
#pragma unroll
      for (int ks = 0; ks < 4; ++ks) A[ks] = lds_frag(rd + 64 * ks);
      __builtin_amdgcn_sched_barrier(0);
#pragma unroll
      for (int ks = 0; ks < 4; ++ks) acc = mfma16(A[ks], B[ks], acc);
      __builtin_amdgcn_sched_barrier(0);
      wave_fence();
      tab[mt] = acc;
    }
  }
  STAMP(0, 3);
  u32x4 uv[2];
  {
    GLB u64* fl = (GLB u64*)(p.ws + WS_FL) + (8 * hh + (lane & 7)) * DM + d;
    wait_flags(fl, true);
    const __amdgpu_buffer_rsrc_t rs = mk_rsrc(p.ws + WS_XT, DM * SL * 2);
#pragma unroll
    for (int v = 0; v < 2; ++v) uv[v] = __builtin_amdgcn_raw_buffer_load_b128(rs, (d * SL + 1024 * hh + 8 * (lane + 64 * v)) * 2, 0, SC1);
    if (lane < 8) __hip_atomic_store(fl, 0ull, RLX_AGENT);
  }
  STAMP(0, 4);
  bar();
  {
    LDS char* yt = smem + S_Y + w * S_YSZ + ((lane >> 4) * 4 * 16 + (lane & 15)) * 4;
#pragma unroll
    for (int mt = 0; mt < 3; ++mt)
#pragma unroll
      for (int r = 0; r < 4; ++r)
        if (mt < 2 || (lane >> 4) < 2 || ((lane >> 4) == 2 && r == 0)) *(LDS float*)(yt + (mt * 16 + r) * 64) = tab[mt][r];
    if (tid < 6 * 32) *(LDS u32x4*)(smem + S_Z + (tid >> 5) * S_ZSZ + (tid & 31) * 16) = u32x4{0, 0, 0, 0};
  }
  bar();
  STAMP(0, 5);
  {
    LDS const char* y0 = smem + S_Y + (2 * ch) * S_YSZ;
    u32 o[10];
#pragma unroll
    for (int i = 0; i < 10; ++i) {
      const u32 pr = i < 4 ? ix0[i] : (i < 8 ? ix1[i - 4] : ixn[i - 8]);
      const u32 a = pr & 0xffffu, b = pr >> 16;
      const float va = *(LDS const float*)(y0 + a) + *(LDS const float*)(y0 + S_YSZ + a);
      const float vb = *(LDS const float*)(y0 + b) + *(LDS const float*)(y0 + S_YSZ + b);
      o[i] = pack_bf16(va, vb);
    }
    o[8] = (u32)__builtin_amdgcn_update_dpp((int)o[8], (int)o[0], 0x130, 0xf, 0xf, false);
    o[9] = (u32)__builtin_amdgcn_update_dpp((int)o[9], (int)o[1], 0x130, 0xf, 0xf, false);
    u32 a[9];
#pragma unroll
    for (int i = 0; i < 9; ++i) a[i] = __builtin_amdgcn_alignbit(o[i + 1], o[i], 16);
    LDS char* xb = smem + S_X + ch * 4 * S_CPY + 32 + 2 * (1024 * hh + 16 * lane);
    *(LDS u32x4*)(xb) = u32x4{o[0], o[1], o[2], o[3]};
    *(LDS u32x4*)(xb + 16) = u32x4{o[4], o[5], o[6], o[7]};
    *(LDS u32x4*)(xb + S_CPY) = u32x4{a[0], a[1], a[2], a[3]};
    *(LDS u32x4*)(xb + S_CPY + 16) = u32x4{a[4], a[5], a[6], a[7]};
    *(LDS u32x4*)(xb + 2 * S_CPY) = u32x4{o[1], o[2], o[3], o[4]};
    *(LDS u32x4*)(xb + 2 * S_CPY + 16) = u32x4{o[5], o[6], o[7], o[8]};
    *(LDS u32x4*)(xb + 3 * S_CPY) = u32x4{a[1], a[2], a[3], a[4]};
    *(LDS u32x4*)(xb + 3 * S_CPY + 16) = u32x4{a[5], a[6], a[7], a[8]};
    if (hh == 0 && lane == 0) {
      LDS char* hb = smem + S_X + ch * 4 * S_CPY;
      const u32x4 z = {0, 0, 0, 0};
      *(LDS u32x4*)(hb) = z;
      *(LDS u32x4*)(hb + 16) = z;
      *(LDS u32x4*)(hb + S_CPY) = z;
      *(LDS u32x4*)(hb + S_CPY + 16) = u32x4{0, 0, 0, o[0] << 16};
      *(LDS u32x4*)(hb + 2 * S_CPY) = z;
      *(LDS u32x4*)(hb + 2 * S_CPY + 16) = u32x4{0, 0, 0, o[0]};
      *(LDS u32x4*)(hb + 3 * S_CPY) = z;
      *(LDS u32x4*)(hb + 3 * S_CPY + 16) = u32x4{0, 0, o[0] << 16, a[0]};
    }
    LDS char* z = smem + S_Z + ch * S_ZSZ + 512 + (1024 * hh + 8 * lane) * 2;
    *(LDS u32x4*)z = uv[0];
    *(LDS u32x4*)(z + 1024) = uv[1];
  }
  STAMP(0, 6);
  bar();
  STAMP(0, 7);
  f32x4 acc[8];
#pragma unroll
  for (int i = 0; i < 8; ++i) acc[i] = f32x4{0, 0, 0, 0};
  {
    const int b = lane & 15, kq = lane >> 4, qq = b >> 2, pp = b & 3;
    LDS const char* fb = smem + S_X + (ch * 4 + ((4 - qq) & 3)) * S_CPY +
                         2 * (16 + 16 * (kq >> 1) - 8 * (kq & 1) + 4 * pp - (qq ? 4 : 0)) + 64 * hh;
    LDS const char* gb = smem + S_Z + ch * S_ZSZ + 512 + 32 * (b - (kq >> 1)) + 16 * (kq & 1) - 64 * hh;
#pragma unroll 1
    for (int it = 0; it < 4; ++it) {
      bf16x8 F[8], G[8];
#pragma unroll
      for (int s = 0; s < 8; ++s) {
        F[s] = tr_frag(fb + 512 * s, fb + 512 * s - 8);
        G[s] = lds_frag(gb + 512 * s);
      }
      __builtin_amdgcn_sched_barrier(0);
#pragma unroll
      for (int t = 0; t < 8; ++t)
#pragma unroll
        for (int s = 0; s <= t; ++s) acc[t] = mfma16(F[s], G[t - s], acc[t]);
      fb += 128;
      gb -= 128;
    }
  }
  STAMP(0, 8);
  bar();
  {
    LDS char* ex = smem + S_X + w * 4096 + lane * 16;
#pragma unroll
    for (int i = 0; i < 4; ++i) {
      const f32x4 lo = acc[i], hi = acc[4 + i];
      *(LDS f32x4*)(ex + i * 1024) = hh ? +lo : +hi;
    }
  }
  bar();
  {
    LDS const char* ey = smem + S_X + (w ^ 1) * 4096 + lane * 16;
    const __amdgpu_buffer_rsrc_t rs = mk_rsrc(p.ws + WS_AT, DM * SL * 2);
    const int dst = (d * SL + 1024 * hh + 16 * (lane & 15) + 4 * (lane >> 4)) * 2;
#pragma unroll
    for (int i = 0; i < 4; ++i) {
      const f32x4 lo = acc[i], hi = acc[4 + i];
      const f32x4 y = (hh ? +hi : +lo) + *(LDS const f32x4*)(ey + i * 1024);
      __builtin_amdgcn_raw_buffer_store_b64(u32x2{pack_bf16(y[0], y[1]), pack_bf16(y[2], y[3])}, rs, dst + 512 * i, 0, SC1);
    }
  }
  STAMP(0, 9);
  wait_vm<0>();
  bar();
  if (w == 0) {
    const int lt = lane & 31, second = lane >> 5;
    const int dd = 6 * sg + (second ? 5 : 0);
    const int idx = dd < 480 ? dd / 96 : 5 + (dd - 480) / 32;
    const int dfirst = idx < 5 ? 96 * idx : 480 + 32 * (idx - 5);
    __hip_atomic_store((GLB u64*)(p.ws + WS_IN) + tile_block(lt, idx) * 64 + IN_A + (sg - dfirst / 6), FLAG_SET, RLX_AGENT);
  }
}

DEV void role_gate(const P& p, int rb, int w, int lane) {
  const int sub = lane & 7;
  u32x4 wv[9];
#pragma unroll
  for (int i = 0; i < 9; ++i) wv[i] = *(const u32x4*)((const char*)p.gw + (i * 8 + sub) * 16);
  for (int r = w * 8 + (lane >> 3); r < 128; r += 96) {
    const int l = rb * 128 + r;
    float s = 0.f;
    u32x4 xv[9];
#pragma unroll
    for (int i = 0; i < 9; ++i) xv[i] = *(const u32x4*)((const char*)p.x + (size_t)l * 1152 + (i * 8 + sub) * 16);
#pragma unroll
    for (int i = 0; i < 9; ++i) {
#pragma unroll
      for (int k = 0; k < 4; ++k) {
        const u32 pr = pack_bf16(bflo(xv[i][k]) * bflo(wv[i][k]), bfhi(xv[i][k]) * bfhi(wv[i][k]));
        s += bflo(pr);
        s += bfhi(pr);
      }
    }
    s += __shfl_xor(s, 1); s += __shfl_xor(s, 2); s += __shfl_xor(s, 4);
    const float gv = rbf(1.0f / (1.0f + expf(-rbf(s))));
    if (sub == 0)
      __hip_atomic_store((GLB u64*)(p.ws + WS_G) + l, ((u64)__float_as_uint(rbf(1.0f - gv)) << 32) | __float_as_uint(gv), RLX_AGENT);
  }
  wait_vm<0>();
  bar();
  if (w == 0 && lane < 16)
    __hip_atomic_store((GLB u64*)(p.ws + WS_IN) + tile_block(2 * rb + (lane >> 3), lane & 7) * 64 + IN_G, FLAG_SET, RLX_AGENT);
}

constexpr int H_C = 32768, H_CS = 12288;
DEV int h_slot(int i) { return i < 4 ? i * 8192 : 131072 + (i - 4) * 8192; }
constexpr int H_AT = H_C + 2 * H_CS;
constexpr int H_XT = H_C + 3 * H_CS;
constexpr int H_G = H_C + 4 * H_CS;
constexpr int H_OUT = H_C + 5 * H_CS;
constexpr int K_LDS = 163840;

constexpr int h_wait(int s, int NDT) {
  const int G = 2 + NDT;
  int n = 0;
  if (s <= 7) n = 2 * (7 - s) + G * (s > 1 ? s - 1 : 0);
  else n = G * ((s - 1 < 10 ? s - 1 : 10) - (s - 7));
  if (s >= 2 && s <= 8) n += 1;
  if (s >= 12) n += NDT;
  if (s >= 13) n += NDT;
  if (s >= 14) n += 1;
  return s == 17 ? 0 : n;
}
template <int S, int NDT, class F> DEV void h_steps(F&& after, u64& fv, char* wsp) {
  if constexpr (S == 9) asm volatile("s_waitcnt vmcnt(%1)" : "+v"(fv) : "n"(h_wait(S, NDT)) : "memory");
  else wait_vm<h_wait(S, NDT)>();
  LSTAMP2(1, 16 + S);
  bar();
  after(std::integral_constant<int, S>{});
  if constexpr (S < 17) h_steps<S + 1, NDT>(after, fv, wsp);
}

template <int NDT>
DEV void p2_loader(const P& p, LDS char* smem, int lt, int d0, int lw, int lane, bool scratch_barrier) {
  const char* hb = p.ws + WS_HB + (size_t)lt * 18 * 8192 + (2 * lw) * 1024 + lane * 16;
  constexpr int W = 32 * NDT;
  const int l0 = 64 * lt;
  u32 co[NDT];
#pragma unroll
  for (int i = 0; i < NDT; ++i) {
    const int row = 8 * (NDT * lw + i) + (lane >> 3), cp = lane & 7;
    co[i] = (u32)(d0 + row) * 1152u + ((cp ^ (row & 7)) << 4);
  }
  auto issue_c = [&](int s) {
    LDS char* buf = smem + H_C + (s & 7) * H_CS + NDT * lw * 1024;
    const char* cb = (const char*)(s < 9 ? p.cre : p.cim) + (s < 9 ? s : s - 9) * 128;
#pragma unroll
    for (int i = 0; i < NDT; ++i) dma16<0>(cb + co[i], buf + i * 1024);
  };
  auto issue_h = [&](int s) {
    LDS char* buf = smem + h_slot(s & 7) + 2 * lw * 1024;
#pragma unroll
    for (int i = 0; i < 2; ++i) dma16<SC1>(hb + s * 8192 + i * 1024, buf + i * 1024);
  };
  GLB u64* inb = (GLB u64*)(p.ws + WS_IN) + blockIdx.x * 64;
  const int nA = (d0 + W - 1) / 6 - d0 / 6 + 1;
  const bool need = lane == IN_G || (lane >= IN_A && lane < IN_A + nA);
#pragma unroll
  for (int s = 0; s < 8; ++s) issue_c(s);
  if (scratch_barrier) bar();
  wait_flags(inb + IN_H + (lane < 36 ? lane : 0), true);
#pragma unroll
  for (int s = 0; s < 8; ++s) issue_h(s);
  u64 fv = 0;
  h_steps<0, NDT>([&](auto S_) {
    constexpr int S = decltype(S_)::value;
    if constexpr (S >= 1 && S + 7 < 18) { issue_h(S + 7); issue_c(S + 7); }
    if constexpr (S == 1)
      asm volatile("global_load_dwordx2 %0, %1, off sc1" : "=v"(fv) : "v"(inb + lane) : "memory");
    if constexpr (S == 9) { if (!__all(!need || fv == FLAG_SET)) wait_flags(inb + lane, need); }
    if constexpr (S == 11) {
#pragma unroll
      for (int i = 0; i < NDT; ++i) {
        const int pc = NDT * lw + i;
        dma16<SC1>(p.ws + WS_AT + (size_t)(d0 + 8 * pc + (lane >> 3)) * 4096 + l0 * 2 + (lane & 7) * 16, smem + H_AT + pc * 1024);
      }
    }
    if constexpr (S == 13) { if (lane < 8) dma16<SC1>(p.ws + WS_G + (size_t)l0 * 8 + lw * 128 + lane * 16, smem + H_G + lw * 128); }
    if constexpr (S == 12) {
#pragma unroll
      for (int i = 0; i < NDT; ++i) {
        const int pc = NDT * lw + i, id = pc * 64 + lane, row = id / (4 * NDT), qq = id - row * (4 * NDT);
        dma16<0>((const char*)p.x + (size_t)(l0 + row) * 1152 + d0 * 2 + qq * 16, smem + H_XT + pc * 1024);
      }
    }
  }, fv, p.ws);
}

template <int NDT, int STAG>
DEV void p2_compute(const P& p, LDS char* smem, int lt0, int d0, int w, int lane) {
  const int lh = (w >> 1) & 1, kh = w & 1, dg = w >> 2;
  const int r = lane & 15, kq = lane >> 4;
  const int lt = 2 * lh + kh, l0 = 64 * lt0;
  constexpr int W = 32 * NDT;
  u32x2 da[NDT], db[NDT];
#pragma unroll
  for (int t = 0; t < NDT; ++t) {
    const int d = d0 + 16 * (NDT * dg + t) + 4 * kq;
    da[t] = *(const u32x2*)(p.dskA + d);
    db[t] = *(const u32x2*)(p.dskB + d);
  }
  GLB u64* inb = (GLB u64*)(p.ws + WS_IN) + blockIdx.x * 64;

  f32x4 acc[2][NDT];
#pragma unroll
  for (int i = 0; i < 2; ++i)
#pragma unroll
    for (int t = 0; t < NDT; ++t) acc[i][t] = f32x4{0, 0, 0, 0};
  int ho[2];
#pragma unroll
  for (int u = 0; u < 2; ++u) {
    const int k = 32 * kh + 8 * kq + 4 * u + (r >> 2), i = (r & 3) >> 1;
    ho[u] = (2 * (k >> 4) + i) * 1024 + (32 * lh + ((k & 15) ^ (4 * i))) * 16 + (r & 1) * 8;
  }
  const int co = H_C + (16 * NDT * dg + r) * 128 + (((4 * kh + kq) ^ (r & 7)) << 4);
  bf16x8 hb[2], ca[NDT];
#pragma unroll
  for (int i = 0; i < 2; ++i) hb[i] = bf16x8{};
#pragma unroll
  for (int t = 0; t < NDT; ++t) ca[t] = bf16x8{};
  auto mma = [&]() {
#pragma unroll
    for (int t = 0; t < NDT; ++t) {
      acc[0][t] = mfma16(ca[t], hb[0], acc[0][t]);
      acc[1][t] = mfma16(ca[t], hb[1], acc[1][t]);
    }
  };
  STAMP(1, 1);
#pragma unroll 1
  for (int s = 0; s < 18; ++s) {
    bar();
    if (s == 0) STAMP(1, 2);
    LSTAMP(1, 16 + s);
    if (STAG) { mma(); __builtin_amdgcn_sched_barrier(0); }
    LDS const char* hbuf = smem + h_slot(s & 7);
    LDS const char* cbuf = smem + (s & 7) * H_CS;
#pragma unroll
    for (int i = 0; i < 2; ++i) hb[i] = tr_frag(hbuf + ho[0] + i * 256, hbuf + ho[1] + i * 256);
#pragma unroll
    for (int t = 0; t < NDT; ++t) ca[t] = lds_frag(cbuf + co + t * 2048);
    __builtin_amdgcn_sched_barrier(0);
    if (!STAG) { mma(); __builtin_amdgcn_sched_barrier(0); }
  }
  if (STAG) mma();
  __builtin_amdgcn_sched_barrier(0);
  STAMP(1, 3);
  bar();
  if (w == 0) __hip_atomic_store(inb + lane, 0ull, RLX_AGENT);
  LDS char* ex = smem + w * (NDT * 1024) + lane * 16;
#pragma unroll
  for (int t = 0; t < NDT; ++t) {
    const f32x4 a0 = acc[0][t], a1 = acc[1][t];
    *(LDS f32x4*)(ex + t * 1024) = kh ? +a0 : +a1;
  }
  bar();
  LDS const char* ey = smem + (w ^ 1) * (NDT * 1024) + lane * 16;
  constexpr int OSTR = W * 4 + 16;
  const f32x2 gg = *(LDS const f32x2*)(smem + H_G + (16 * lt + r) * 8);
#pragma unroll
  for (int t = 0; t < NDT; ++t) {
    const int dt = NDT * dg + t;
    const f32x4 o = *(LDS const f32x4*)(ey + t * 1024);
    const f32x4 a0 = acc[0][t], a1 = acc[1][t];
    const f32x4 y = (kh ? +a1 : +a0) + o;
    const s16x4 av = tr_read(smem + H_AT + (16 * dt + 4 * kq + (r >> 2)) * 128 + lt * 32 + (r & 3) * 8);
    const u32x2 xv = *(LDS const u32x2*)(smem + H_XT + (16 * lt + r) * (W * 2) + (16 * dt + 4 * kq) * 2);
    f32x4 res;
#pragma unroll
    for (int i = 0; i < 4; ++i) {
      const u32 xw = xv[i >> 1], aw = da[t][i >> 1], bw = db[t][i >> 1];
      const float xf = (i & 1) ? bfhi(xw) : bflo(xw);
      const float sa = (i & 1) ? bfhi(aw) : bflo(aw);
      const float sb = (i & 1) ? bfhi(bw) : bflo(bw);
      const float yc = bf2f((u16)av[i]);
      const float a = rbf(yc + rbf(xf * sa));
      const float b = y[i] + rbf(xf * sb);
      res[i] = rbf(gg[0] * a) + gg[1] * b;
    }
    *(LDS f32x4*)(smem + H_OUT + (16 * lt + r) * OSTR + (16 * dt + 4 * kq) * 4) = res;
  }
  STAMP(1, 4);
  bar();
#pragma unroll
  for (int it = 0; it < NDT; ++it) {
    const int id = it * 512 + w * 64 + lane;
    const int row = id / (8 * NDT), pc = id - row * (8 * NDT);
    *(f32x4*)(p.out + (size_t)(l0 + row) * DM + d0 + pc * 4) = *(LDS const f32x4*)(smem + H_OUT + row * OSTR + pc * 16);
  }
}

template <int NDT>
DEV void phase2(const P& p, LDS char* smem, int lt, int d0, int w, int lane, bool scratch_barrier) {
  STAMP(1, 0);
  if (w >= 8) p2_loader<NDT>(p, smem, lt, d0, w - 8, lane, scratch_barrier);
  else if (w < 4) p2_compute<NDT, 0>(p, smem, lt, d0, w, lane);
  else p2_compute<NDT, 1>(p, smem, lt, d0, w, lane);
  STAMP(1, 15);
}

__global__ __launch_bounds__(768, 3) void gated_mix_s4d_s5(P p) {
  extern __shared__ __attribute__((aligned(16))) char smem_raw[];
  LDS char* smem = (LDS char*)smem_raw;
  const int w = __builtin_amdgcn_readfirstlane(threadIdx.x >> 6), lane = threadIdx.x & 63;
  const int xcd = blockIdx.x & 7, j = blockIdx.x >> 3;
  STAMP(0, 0);
  if (j >= 18 && j < 30) {
    role_s(p, smem, 12 * xcd + (j - 18), w, lane);
    STAMP(0, 15);
    phase2<1>(p, smem, 4 * xcd + (j - 18) / 3, 480 + 32 * ((j - 18) % 3), w, lane, false);
  } else {
    const bool isg = j < 18;
    if (isg) {
      const int c = 2 * xcd + j / 9, g = j % 9;
      if (w >= 8) g_loader(p, smem, c, g, w - 8, lane);
      else if (w < 4) g_compute<0>(p, smem, c, g, w, lane);
      else g_compute<1>(p, smem, c, g, w, lane);
    } else {
      role_gate(p, 2 * xcd + (j - 30), w, lane);
    }
    STAMP(0, 15);
    const int e = isg ? j : j - 12;
    phase2<3>(p, smem, 4 * xcd + e / 5, 96 * (e % 5), w, lane, isg);
  }
}

extern "C" void kernel_launch(void* const* d_in, const int* in_sizes, int n_in, void* d_out, int out_size,
                              void* d_ws, size_t ws_size, hipStream_t stream) {
  P a;
  a.x = (const u16*)d_in[0]; a.la = (const u16*)d_in[1]; a.aim = (const u16*)d_in[2];
  a.creA = (const u16*)d_in[3]; a.cimA = (const u16*)d_in[4]; a.ldtA = (const u16*)d_in[5]; a.dskA = (const u16*)d_in[6];
  a.lre = (const u16*)d_in[8]; a.lim = (const u16*)d_in[9]; a.bre = (const u16*)d_in[10]; a.bim = (const u16*)d_in[11];
  a.cre = (const u16*)d_in[12]; a.cim = (const u16*)d_in[13]; a.ldtB = (const u16*)d_in[14];
  a.dskB = (const u16*)d_in[15]; a.gw = (const u16*)d_in[16];
  a.ws = (char*)d_ws; a.out = (float*)d_out;
  (void)hipFuncSetAttribute((const void*)gated_mix_s4d_s5, hipFuncAttributeMaxDynamicSharedMemorySize, K_LDS);
  gated_mix_s4d_s5<<<256, 768, K_LDS, stream>>>(a);
}
```

```cpp
#include <hip/hip_runtime.h>
#include <hip/hip_bf16.h>
#include <stdint.h>
#include <type_traits>

typedef unsigned short u16;
typedef unsigned int u32;
typedef unsigned long long u64;
typedef __attribute__((ext_vector_type(8))) __bf16 bf16x8;
typedef __attribute__((ext_vector_type(2))) __bf16 bf16x2;
typedef __attribute__((ext_vector_type(4))) short s16x4;
typedef __attribute__((ext_vector_type(4))) float f32x4;
typedef __attribute__((ext_vector_type(2))) float f32x2;
typedef __attribute__((ext_vector_type(4))) unsigned u32x4;
typedef __attribute__((ext_vector_type(2))) unsigned u32x2;
#define LDS __attribute__((address_space(3)))
#define GLB __attribute__((address_space(1)))
#define DEV __device__ __forceinline__
#define RLX_AGENT __ATOMIC_RELAXED, __HIP_MEMORY_SCOPE_AGENT

constexpr int SL = 2048;
constexpr int DM = 576;

constexpr size_t WS_HB = 0;
constexpr size_t WS_AT = WS_HB + (size_t)2 * DM * SL * 2;
constexpr size_t WS_G = WS_AT + (size_t)DM * SL * 2;
constexpr size_t WS_GRAN = WS_G + (size_t)SL * 8;
constexpr size_t WS_EP = WS_GRAN + (size_t)9 * 16 * 2 * 64 * 8;
constexpr size_t WS_FL = WS_EP + 4096;
constexpr size_t WS_XT = WS_FL + (size_t)16 * DM * 8;
constexpr size_t WS_IN = WS_XT + (size_t)DM * SL * 2;
constexpr u64 FLAG_SET = 0x9E3779B97F4A7C15ull;
constexpr u32 SPIN_CAP = 1u << 17;
constexpr int IN_H = 0, IN_G = 36, IN_A = 40;

constexpr size_t WS_ST = (size_t)16 << 20;
#define STAMP(kern, idx) do {} while (0)
#define CSTAMP(kern, idx) do {} while (0)
#define LSTAMP(kern, idx) do {} while (0)
#define LSTAMP2(kern, idx) do {} while (0)

DEV float bf2f(u16 v) { return __uint_as_float((u32)v << 16); }
DEV float bflo(u32 v) { return __uint_as_float(v << 16); }
DEV float bfhi(u32 v) { return __uint_as_float(v & 0xffff0000u); }
DEV u32 pack_bf16(float lo, float hi) {
  f32x2 v = {lo, hi};
  bf16x2 b = __builtin_convertvector(v, bf16x2);
  return __builtin_bit_cast(u32, b);
}
DEV float rbf(float x) { return bflo(pack_bf16(x, x)); }
template <int N> DEV void wait_vm() { asm volatile("s_waitcnt vmcnt(%0)" ::"n"(N) : "memory"); }
DEV void bar() { asm volatile("s_waitcnt lgkmcnt(0)\n\ts_barrier" ::: "memory"); }
DEV void wave_fence() { __builtin_amdgcn_fence(__ATOMIC_ACQ_REL, "wavefront"); }
template <int AUX> DEV void dma16(const void* src, LDS char* dst) {
  __builtin_amdgcn_global_load_lds((const GLB u32*)src, (LDS u32*)dst, 16, 0, AUX);
}
constexpr int SC1 = 16;
DEV bf16x8 lds_frag(LDS const char* p) { return *(LDS const bf16x8*)p; }
DEV s16x4 tr_read(LDS const char* a) { return __builtin_amdgcn_ds_read_tr16_b64_v4i16((LDS s16x4*)a); }
DEV bf16x8 tr_frag(LDS const char* a, LDS const char* b) {
  const s16x4 lo = tr_read(a), hi = tr_read(b);
  return __builtin_bit_cast(bf16x8, __builtin_shufflevector(lo, hi, 0, 1, 2, 3, 4, 5, 6, 7));
}
DEV f32x4 mfma16(bf16x8 a, bf16x8 b, f32x4 c) { return __builtin_amdgcn_mfma_f32_16x16x32_bf16(a, b, c, 0, 0, 0); }
DEV __amdgpu_buffer_rsrc_t mk_rsrc(const void* base, int bytes) {
  return __builtin_amdgcn_make_buffer_rsrc(const_cast<void*>(base), (short)0, bytes, 0x00020000);
}
struct cf { float r, i; };
DEV cf cmul(cf a, cf b) { return {a.r * b.r - a.i * b.i, a.r * b.i + a.i * b.r}; }
DEV cf cfma(cf a, cf b, cf c) { return {fmaf(a.r, b.r, fmaf(-a.i, b.i, c.r)), fmaf(a.r, b.i, fmaf(a.i, b.r, c.i))}; }
typedef f32x2 c2;
DEV c2 cm2(c2 a, c2 b) {
  c2 t, r;
  asm("v_pk_mul_f32 %0, %1, %2 op_sel:[0,0] op_sel_hi:[0,1]" : "=v"(t) : "v"(a), "v"(b));
  asm("v_pk_fma_f32 %0, %1, %2, %3 op_sel:[1,1,0] op_sel_hi:[1,0,1] neg_lo:[1,0,0]" : "=v"(r) : "v"(a), "v"(b), "v"(t));
  return r;
}
DEV f32x2 fma2(f32x2 a, f32x2 b, f32x2 c) { return __builtin_elementwise_fma(a, b, c); }
DEV f32x2 lo2(f32x4 v) { return __builtin_shufflevector(v, v, 0, 1); }
DEV f32x2 hi2(f32x4 v) { return __builtin_shufflevector(v, v, 2, 3); }
DEV f32x4 cat2(f32x2 a, f32x2 b) { return __builtin_shufflevector(a, b, 0, 1, 2, 3); }
DEV void wait_flags(GLB u64* word, bool need) {
  for (u32 spin = 0;; ++spin) {
    const u64 v = __hip_atomic_load(word, RLX_AGENT);
    if (__all(!need || v == FLAG_SET) || spin > SPIN_CAP) break;
    __builtin_amdgcn_s_sleep(2);
  }
}
DEV int tile_block(int lt, int idx) {
  const int xcd = lt >> 2, a = lt & 3;
  int j;
  if (idx < 5) { const int e = 5 * a + idx; j = e < 18 ? e : e + 12; }
  else j = 18 + 3 * a + (idx - 5);
  return (j << 3) | xcd;
}

struct IdxTab { u16 v[SL + 8]; };
constexpr int round_pos(int j) {
  if (j < 256) return j;
  int sh = j < 512 ? 1 : (j < 1024 ? 2 : 3);
  int q = j >> sh, rem = j & ((1 << sh) - 1), half = 1 << (sh - 1);
  if (rem > half || (rem == half && (q & 1))) ++q;
  return q << sh;
}
constexpr IdxTab make_idx() {
  IdxTab t{};
  for (int j = 0; j < SL; ++j) {
    int pos = round_pos(j), row = 0, b = (pos & 127) >> 3;
    if (pos < 256) row = 8 * (pos >> 7) + (pos & 7);
    else if (pos < 512) row = 16 + ((pos & 7) >> 1) + 4 * ((pos - 256) >> 7);
    else if (pos < 1024) row = 24 + ((pos - 512) >> 7) + 4 * ((pos & 7) >> 2);
    else if (pos < 2048) row = 32 + ((pos - 1024) >> 7);
    else { row = 40; b = 0; }
    t.v[j] = (u16)((row * 16 + b) * 4);
  }
  return t;
}
__device__ const IdxTab g_idx = make_idx();

struct P {
  const u16 *x, *la, *aim, *creA, *cimA, *ldtA, *dskA;
  const u16 *lre, *lim, *bre, *bim, *cre, *cim, *ldtB, *dskB, *gw;
  char* ws;
  float* out;
};

constexpr int G_NB = 4, G_STAGE = 32768;
constexpr int G_E = G_NB * G_STAGE;
constexpr int G_PF = G_E + 4096;
constexpr int G_L = G_PF + 4096;
constexpr int G_X = G_L + 1024;
constexpr int G_T = G_X + 2048;
constexpr int G_END = G_T + 8 * 16 * 144;

DEV void g_loader(const P& p, LDS char* smem, int c, int g, int lw, int lane) {
  [[maybe_unused]] const int w = lw + 8;
  __builtin_amdgcn_s_setprio(3);
  GLB u32* ep = (GLB u32*)(p.ws + WS_EP + g * 256);
  GLB u64* gran = (GLB u64*)(p.ws + WS_GRAN) + (size_t)g * 16 * 128;
  const u32 tag = __hip_atomic_load(ep, RLX_AGENT) + 1u;
  u32 xo[4], bo[4];
#pragma unroll
  for (int i = 0; i < 4; ++i) {
    const int rho = 8 * (4 * lw + i) + (lane >> 3);
    const int hf = rho >> 6, m = (rho >> 4) & 3, r = rho & 15;
    const int tau = 64 * hf + 16 * (r >> 2) + 8 * (r & 1) + 2 * m + ((r >> 1) & 1);
    const int ch = (lane & 7) ^ (rho & 7);
    xo[i] = (u32)(128 * c + tau) * 1152u + ch * 16;
    const int pp = 64 * g + 16 * (rho >> 5) + r;
    bo[i] = (u32)pp * 1152u + ch * 16;
  }
  auto issue = [&](int s) {
    LDS char* buf = smem + (s & (G_NB - 1)) * G_STAGE + lw * 4096;
    int kb = g + s; kb = (kb >= 9 ? kb - 9 : kb) * 128;
#pragma unroll
    for (int i = 0; i < 4; ++i) dma16<0>((const char*)p.x + (xo[i] + kb), buf + i * 1024);
#pragma unroll
    for (int i = 0; i < 4; ++i) {
      const char* base = (const char*)((((4 * lw + i) >> 1) & 1) ? p.bim : p.bre);
      dma16<0>(base + (bo[i] + kb), buf + 16384 + i * 1024);
    }
  };
#pragma unroll
  for (int s = 0; s < G_NB - 1; ++s) issue(s);
#pragma unroll 1
  for (int s = 0; s < 6; ++s) { wait_vm<16>(); LSTAMP(0, 16 + s); bar(); issue(s + G_NB - 1); LSTAMP(0, 32 + s); }
  wait_vm<16>(); bar();
  wait_vm<8>(); bar();
  wait_vm<0>(); bar();
  STAMP(0, 1);
  bar();
  STAMP(0, 2);
  if (lw == 3) {
    const f32x2 lv = *(LDS const f32x2*)(smem + G_L + lane * 8);
    const cf l16 = {lv[0], lv[1]};
    f32x2 ev[8];
#pragma unroll
    for (int s = 0; s < 8; ++s) ev[s] = *(LDS const f32x2*)(smem + G_E + (s * 64 + lane) * 8);
    cf pre = {0.f, 0.f};
#pragma unroll
    for (int s = 0; s < 8; ++s) {
      *(LDS f32x2*)(smem + G_PF + (s * 64 + lane) * 8) = f32x2{pre.r, pre.i};
      pre = cfma(l16, pre, cf{ev[s][0], ev[s][1]});
    }
    if (c < 15) {
      __hip_atomic_store(gran + c * 128 + lane, ((u64)tag << 32) | __float_as_uint(pre.r), RLX_AGENT);
      __hip_atomic_store(gran + c * 128 + 64 + lane, ((u64)tag << 32) | __float_as_uint(pre.i), RLX_AGENT);
    }
  }
  const f32x2 lv = *(LDS const f32x2*)(smem + G_L + 512 + lane * 8);
  const cf l128 = {lv[0], lv[1]};
  const cf l256 = cmul(l128, l128), l512 = cmul(l256, l256);
  cf acc = {0.f, 0.f};
  if (lw < c) {
    const int nc = (c - lw + 3) >> 2;
    u64 a[4], b[4];
    for (u32 spin = 0;; ++spin) {
      bool ok = true;
#pragma unroll
      for (int i = 0; i < 4; ++i) {
        const int cc = lw + 4 * (i < nc ? i : 0);
        a[i] = __hip_atomic_load(gran + cc * 128 + lane, RLX_AGENT);
        b[i] = __hip_atomic_load(gran + cc * 128 + 64 + lane, RLX_AGENT);
      }
#pragma unroll
      for (int i = 0; i < 4; ++i) ok = ok && (u32)(a[i] >> 32) == tag && (u32)(b[i] >> 32) == tag;
      if (__all(ok) || spin > SPIN_CAP) break;
      __builtin_amdgcn_s_sleep(1);
    }
#pragma unroll
    for (int i = 0; i < 4; ++i)
      if (i < nc) acc = cfma(l512, acc, cf{__uint_as_float((u32)a[i]), __uint_as_float((u32)b[i])});
    const int e = c - 1 - (lw + 4 * (nc - 1));
    if (e & 1) acc = cmul(acc, l128);
    if (e & 2) acc = cmul(acc, l256);
  }
  *(LDS f32x2*)(smem + G_X + (lw * 64 + lane) * 8) = f32x2{acc.r, acc.i};
  STAMP(0, 3);
  bar();
  if (c == 15 && lw == 0 && lane == 0) __hip_atomic_store(ep, tag, RLX_AGENT);
}

template <int HF>
DEV void g_compute(const P& p, LDS char* smem, int c, int g, int w, int lane) {
  const int pq = w & 3, col = lane & 15, q = lane >> 4;
  const int pl = 16 * pq + col, pp = 64 * g + pl;
  cf lam, coef2;
  {
    const float lre = bf2f(p.lre[pp]), lim = bf2f(p.lim[pp]);
    const float dt = rbf(expf(bf2f(p.ldtB[pp])));
    const float zr = dt * lre, zi = dt * lim;
    const float er = expf(zr);
    float sn, cs;
    sincosf(zi, &sn, &cs);
    lam = {er * cs, er * sn};
    const float nr = lam.r - 1.0f, ni = lam.i;
    const float den = 1.0f / (lre * lre + lim * lim);
    coef2 = {2.0f * (nr * lre + ni * lim) * den, 2.0f * (ni * lre - nr * lim) * den};
  }
  cf l8 = cmul(lam, lam); l8 = cmul(l8, l8); l8 = cmul(l8, l8);
  const cf l16 = cmul(l8, l8);
  const cf l32 = cmul(l16, l16), l64 = cmul(l32, l32);
  if (HF == 0 && q == 0) {
    const cf l128 = cmul(l64, l64);
    *(LDS f32x2*)(smem + G_L + pl * 8) = f32x2{l16.r, l16.i};
    *(LDS f32x2*)(smem + G_L + 512 + pl * 8) = f32x2{l128.r, l128.i};
  }
  cf pw = HF ? l64 : cf{1.f, 0.f};
  { const cf t = cmul(pw, l16); if (q & 1) pw = t; }
  { const cf t = cmul(pw, l32); if (q & 2) pw = t; }
  asm volatile("" : "+v"(lam.r), "+v"(lam.i), "+v"(coef2.r), "+v"(coef2.i), "+v"(l8.r), "+v"(l8.i), "+v"(pw.r), "+v"(pw.i));
  f32x4 are[4], aim[4];
#pragma unroll
  for (int m = 0; m < 4; ++m) { are[m] = f32x4{0, 0, 0, 0}; aim[m] = f32x4{0, 0, 0, 0}; }
  const int o0 = col * 128 + (((0 + q) ^ (col & 7)) << 4);
  const int o1 = col * 128 + (((4 + q) ^ (col & 7)) << 4);
  bf16x8 br[2], bi[2], a[2][4];
#pragma unroll
  for (int ks = 0; ks < 2; ++ks) {
    br[ks] = bf16x8{}; bi[ks] = bf16x8{};
#pragma unroll
    for (int m = 0; m < 4; ++m) a[ks][m] = bf16x8{};
  }
  auto read_frags = [&](int s) {
    LDS const char* buf = smem + (s & (G_NB - 1)) * G_STAGE;
    LDS const char* ab = buf + HF * 8192;
    LDS const char* bb = buf + 16384 + pq * 4096;
#pragma unroll
    for (int ks = 0; ks < 2; ++ks) {
      const int o = ks ? o1 : o0;
      br[ks] = lds_frag(bb + o); bi[ks] = lds_frag(bb + 2048 + o);
#pragma unroll
      for (int m = 0; m < 4; ++m) a[ks][m] = lds_frag(ab + m * 2048 + o);
    }
  };
  auto mma = [&]() {
#pragma unroll
    for (int ks = 0; ks < 2; ++ks)
#pragma unroll
      for (int m = 0; m < 4; ++m) {
        are[m] = mfma16(a[ks][m], br[ks], are[m]);
        aim[m] = mfma16(a[ks][m], bi[ks], aim[m]);
      }
  };
  STAMP(0, 1);
  constexpr int ST = HF;
#pragma unroll
  for (int s = 0; s < 9; ++s) {
#define XT_WAIT 2
    if (s == XT_WAIT) wait_vm<0>();
    bar();
    if (s == 0) { STAMP(0, 2); CSTAMP(0, 60); }
    LSTAMP(0, 16 + s);
    if (HF == 0 && s == XT_WAIT && pq == 0)
      __hip_atomic_store((GLB u64*)(p.ws + WS_FL) + c * DM + 64 * g + lane, FLAG_SET, RLX_AGENT);
    if (ST == 1) { mma(); __builtin_amdgcn_sched_barrier(0); }
    read_frags(s);
    __builtin_amdgcn_sched_barrier(0);
    if (ST == 0) { mma(); __builtin_amdgcn_sched_barrier(0); }
    if (s == 0) {
      const int rr = 4 * q + (col >> 2);
      LDS const char* tb = smem + HF * 8192 + rr * 128 + (((2 * pq + ((col & 3) >> 1)) ^ (rr & 7)) << 4) + (col & 1) * 8;
      u32x2 t[4];
#pragma unroll
      for (int m = 0; m < 4; ++m) t[m] = __builtin_bit_cast(u32x2, tr_read(tb + m * 2048));
      u32x4 oa, ob;
#pragma unroll
      for (int m = 0; m < 4; ++m) {
        oa[m] = __builtin_amdgcn_perm(t[m][1], t[m][0], 0x05040100u);
        ob[m] = __builtin_amdgcn_perm(t[m][1], t[m][0], 0x07060302u);
      }
      LDS char* sc = smem + G_T + w * (16 * 144);
      *(LDS u32x4*)(sc + col * 144 + q * 32) = oa;
      *(LDS u32x4*)(sc + col * 144 + q * 32 + 16) = ob;
      wave_fence();
      const __amdgpu_buffer_rsrc_t rs = mk_rsrc(p.ws + WS_XT, DM * SL * 2);
#pragma unroll
      for (int u = 0; u < 2; ++u) {
        const int row = (lane >> 3) + 8 * u;
        const u32x4 v = *(LDS const u32x4*)(sc + row * 144 + (lane & 7) * 16);
        __builtin_amdgcn_raw_buffer_store_b128(v, rs, ((64 * g + 16 * pq + row) * SL + 128 * c + 64 * HF) * 2 + (lane & 7) * 16, 0, SC1);
      }
      __builtin_amdgcn_sched_barrier(0);
    }
  }
  if (ST == 1) mma();
  __builtin_amdgcn_sched_barrier(0);
  STAMP(0, 3); CSTAMP(0, 61);
  const f32x2 Lr = {lam.r, lam.r}, Li = {lam.i, lam.i};
  f32x2 er = {0.f, 0.f}, ei = {0.f, 0.f};
#pragma unroll
  for (int t = 0; t < 8; ++t) {
    const f32x2 br_ = (t & 1) ? hi2(are[t >> 1]) : lo2(are[t >> 1]);
    const f32x2 bi_ = (t & 1) ? hi2(aim[t >> 1]) : lo2(aim[t >> 1]);
    const f32x2 nr = fma2(-Li, ei, fma2(Lr, er, br_));
    ei = fma2(Li, er, fma2(Lr, ei, bi_));
    er = nr;
  }
  const cf eA = {er[0], ei[0]};
  const cf e = cfma(l8, eA, cf{er[1], ei[1]});
  const int seg = 4 * HF + q;
  *(LDS f32x2*)(smem + G_E + (seg * 64 + pl) * 8) = f32x2{e.r, e.i};
  STAMP(0, 4);
  bar();
  STAMP(0, 5);
  {
#pragma unroll
    for (int m = 0; m < 4; ++m) asm volatile("" : "+v"(are[m]), "+v"(aim[m]));
    const f32x2 Cr = {coef2.r, coef2.r}, Ci = {coef2.i, coef2.i};
#pragma unroll
    for (int m = 0; m < 4; ++m) {
      const f32x2 r0 = lo2(are[m]), r1 = hi2(are[m]), i0 = lo2(aim[m]), i1 = hi2(aim[m]);
      are[m] = cat2(fma2(-Ci, i0, Cr * r0), fma2(-Ci, i1, Cr * r1));
      aim[m] = cat2(fma2(Ci, r0, Cr * i0), fma2(Ci, r1, Cr * i1));
      asm volatile("" : "+v"(are[m]), "+v"(aim[m]));
    }
  }
  cf eAc = cmul(coef2, eA), pwc = cmul(coef2, pw);
  asm volatile("" : "+v"(eAc.r), "+v"(eAc.i), "+v"(pwc.r), "+v"(pwc.i));
  STAMP(0, 7);
  bar();
  STAMP(0, 8);
  cf cin;
  {
    f32x2 v[4];
#pragma unroll
    for (int i = 0; i < 4; ++i) v[i] = *(LDS const f32x2*)(smem + G_X + (i * 64 + pl) * 8);
    const f32x2 pf = *(LDS const f32x2*)(smem + G_PF + (seg * 64 + pl) * 8);
    const f32x2 S = (v[0] + v[1]) + (v[2] + v[3]);
    cin = cfma(pwc, cf{S[0], S[1]}, cmul(coef2, cf{pf[0], pf[1]}));
  }
  bar();
  const cf cinB = cfma(l8, cin, eAc);
  er = f32x2{cin.r, cinB.r}; ei = f32x2{cin.i, cinB.i};
  f32x2 hr[8], hi[8];
#pragma unroll
  for (int t = 0; t < 8; ++t) {
    const f32x2 br_ = (t & 1) ? hi2(are[t >> 1]) : lo2(are[t >> 1]);
    const f32x2 bi_ = (t & 1) ? hi2(aim[t >> 1]) : lo2(aim[t >> 1]);
    const f32x2 nr = fma2(-Li, ei, fma2(Lr, er, br_));
    ei = fma2(Li, er, fma2(Lr, ei, bi_));
    er = nr;
    hr[t] = er; hi[t] = ei;
  }
  u32x4 o[4];
#pragma unroll
  for (int t = 0; t < 4; ++t) {
    o[0][t] = pack_bf16(hr[2 * t][0], hr[2 * t + 1][0]);
    o[1][t] = pack_bf16(hr[2 * t][1], hr[2 * t + 1][1]);
    o[2][t] = pack_bf16(-hi[2 * t][0], -hi[2 * t + 1][0]);
    o[3][t] = pack_bf16(-hi[2 * t][1], -hi[2 * t + 1][1]);
  }
  STAMP(0, 9);
  {
    const __amdgpu_buffer_rsrc_t rs = mk_rsrc(p.ws + WS_HB, 2 * DM * SL * 2);
    const int hb = (((c * 2 + HF) * 2) * 9 + g) * 8192 + pq * 2048;
    const int p0 = (16 * q + col) * 16, p1 = 1024 + (16 * q + (col ^ 4)) * 16;
    __builtin_amdgcn_raw_buffer_store_b128(o[0], rs, hb + p0, 0, SC1);
    __builtin_amdgcn_raw_buffer_store_b128(o[1], rs, hb + p1, 0, SC1);
    __builtin_amdgcn_raw_buffer_store_b128(o[2], rs, hb + 9 * 8192 + p0, 0, SC1);
    __builtin_amdgcn_raw_buffer_store_b128(o[3], rs, hb + 9 * 8192 + p1, 0, SC1);
    wait_vm<0>();
    STAMP(0, 10);
    if (lane < 8)
      __hip_atomic_store((GLB u64*)(p.ws + WS_IN) + tile_block(2 * c + HF, lane) * 64 + IN_H + g * 4 + pq, FLAG_SET, RLX_AGENT);
  }
}

constexpr int S_CPY = 4160;
constexpr int S_X = 0;
constexpr int S_ST = 6 * 4 * S_CPY;
constexpr int S_Y = S_ST;
constexpr int S_YSZ = 2816;
constexpr int S_Z = S_Y + 12 * S_YSZ;
constexpr int S_ZSZ = (256 + SL) * 2;
constexpr int S_END = S_Z + 6 * S_ZSZ;

DEV c2 cexp_k(float zl, float th, float tl, float k) {
  const float mag = __builtin_amdgcn_exp2f(zl * k);
  const float t = fmaf(tl, k, __builtin_amdgcn_fractf(th * k));
  return c2{mag * __builtin_amdgcn_cosf(t), mag * __builtin_amdgcn_sinf(t)};
}

template <bool NEG>
DEV void octet(LDS char* dst, c2 s0, c2 m1, c2 m2) {
  c2 v[4];
  v[0] = s0; v[1] = cm2(v[0], m1); v[2] = cm2(v[1], m1); v[3] = cm2(v[2], m1);
#pragma unroll
  for (int i = 0; i < 4; ++i) {
    *(LDS u32*)(dst + i * 288) = pack_bf16(v[i][0], NEG ? -v[i][1] : v[i][1]);
    const c2 t = cm2(v[i], m2);
    *(LDS u32*)(dst + (i + 4) * 288) = pack_bf16(t[0], NEG ? -t[1] : t[1]);
  }
}

DEV void role_s(const P& p, LDS char* smem, int sg, int w, int lane) {
  const int tid = w * 64 + lane;
  const int ch = w >> 1, hh = w & 1, d = 6 * sg + ch;
  const int n = d * 128 + 64 * hh + lane;
  const u16 r_ldt = p.ldtA[d], r_la = p.la[n], r_aim = p.aim[n], r_cre = p.creA[n], r_cim = p.cimA[n];
  const u32x4 ix0 = *(const u32x4*)(g_idx.v + 1024 * hh + 16 * lane);
  const u32x4 ix1 = *(const u32x4*)(g_idx.v + 1024 * hh + 16 * lane + 8);
  const u32x2 ixn = *(const u32x2*)(g_idx.v + 1024 * hh + 1024);
  STAMP(0, 1);
  f32x4 tab[3];
  {
    const float L2E = 1.44269504088896341f;
    const float dt = rbf(__builtin_amdgcn_exp2f(bf2f(r_ldt) * L2E));
    const float ar = -rbf(__builtin_amdgcn_exp2f(bf2f(r_la) * L2E));
    const float zl = ar * dt * L2E, zi = bf2f(r_aim) * dt;
    const float th = zi * 0.15915494309189535f;
    const float tl = fmaf(zi, 0.15915494309189535f, -th) + zi * 4.6017384e-10f;
    const c2 C = {2.0f * (bf2f(r_cre) * dt), 2.0f * (bf2f(r_cim) * dt)};
    const c2 w1 = cexp_k(zl, th, tl, 1.f), w8 = cexp_k(zl, th, tl, 8.f), w128 = cexp_k(zl, th, tl, 128.f);
    const c2 w2 = cm2(w1, w1), w4 = cm2(w2, w2);
    const c2 w16 = cm2(w8, w8), w32 = cm2(w16, w16), w64 = cm2(w32, w32);
    const c2 w256 = cm2(w128, w128), w512 = cm2(w256, w256), w1024 = cm2(w512, w512), w2048 = cm2(w1024, w1024);
    STAMP(0, 2);
    LDS char* st = smem + S_ST + w * 4608;
    LDS char* wr = st + 4 * lane;
    LDS const char* rd = st + (lane & 15) * 288 + (lane >> 4) * 16;
    bf16x8 B[4];
    octet<true>(wr, c2{1.f, 0.f}, w8, w32);
    octet<true>(wr + 8 * 288, w64, w8, w32);
    wave_fence();
#pragma unroll
    for (int ks = 0; ks < 4; ++ks) B[ks] = lds_frag(rd + 64 * ks);
    wave_fence();
#pragma unroll
    for (int mt = 0; mt < 3; ++mt) {
      if (mt == 0) { octet<false>(wr, C, w1, w4); octet<false>(wr + 8 * 288, cm2(C, w128), w1, w4); }
      if (mt == 1) { octet<false>(wr, cm2(C, w256), w2, w128); octet<false>(wr + 8 * 288, cm2(C, w512), w128, w4); }
      if (mt == 2) { octet<false>(wr, cm2(C, w1024), w128, w512);
                     const c2 t = cm2(C, w2048);
                     *(LDS u32*)(wr + 8 * 288) = pack_bf16(t[0], t[1]); }
      wave_fence();
      f32x4 acc = {0, 0, 0, 0};
      bf16x8 A[4];
#pragma unroll
      for (int ks = 0; ks < 4; ++ks) A[ks] = lds_frag(rd + 64 * ks);
      __builtin_amdgcn_sched_barrier(0);
#pragma unroll
      for (int ks = 0; ks < 4; ++ks) acc = mfma16(A[ks], B[ks], acc);
      __builtin_amdgcn_sched_barrier(0);
      wave_fence();
      tab[mt] = acc;
    }
  }
  STAMP(0, 3);
  u32x4 uv[2];
  {
    GLB u64* fl = (GLB u64*)(p.ws + WS_FL) + (8 * hh + (lane & 7)) * DM + d;
    wait_flags(fl, true);
    const __amdgpu_buffer_rsrc_t rs = mk_rsrc(p.ws + WS_XT, DM * SL * 2);
#pragma unroll
    for (int v = 0; v < 2; ++v) uv[v] = __builtin_amdgcn_raw_buffer_load_b128(rs, (d * SL + 1024 * hh + 8 * (lane + 64 * v)) * 2, 0, SC1);
    if (lane < 8) __hip_atomic_store(fl, 0ull, RLX_AGENT);
  }
  STAMP(0, 4);
  bar();
  {
    LDS char* yt = smem + S_Y + w * S_YSZ + ((lane >> 4) * 4 * 16 + (lane & 15)) * 4;
#pragma unroll
    for (int mt = 0; mt < 3; ++mt)
#pragma unroll
      for (int r = 0; r < 4; ++r)
        if (mt < 2 || (lane >> 4) < 2 || ((lane >> 4) == 2 && r == 0)) *(LDS float*)(yt + (mt * 16 + r) * 64) = tab[mt][r];
    if (tid < 6 * 32) *(LDS u32x4*)(smem + S_Z + (tid >> 5) * S_ZSZ + (tid & 31) * 16) = u32x4{0, 0, 0, 0};
  }
  bar();
  STAMP(0, 5);
  {
    LDS const char* y0 = smem + S_Y + (2 * ch) * S_YSZ;
    u32 o[10];
#pragma unroll
    for (int i = 0; i < 10; ++i) {
      const u32 pr = i < 4 ? ix0[i] : (i < 8 ? ix1[i - 4] : ixn[i - 8]);
      const u32 a = pr & 0xffffu, b = pr >> 16;
      const float va = *(LDS const float*)(y0 + a) + *(LDS const float*)(y0 + S_YSZ + a);
      const float vb = *(LDS const float*)(y0 + b) + *(LDS const float*)(y0 + S_YSZ + b);
      o[i] = pack_bf16(va, vb);
    }
    o[8] = (u32)__builtin_amdgcn_update_dpp((int)o[8], (int)o[0], 0x130, 0xf, 0xf, false);
    o[9] = (u32)__builtin_amdgcn_update_dpp((int)o[9], (int)o[1], 0x130, 0xf, 0xf, false);
    u32 a[9];
#pragma unroll
    for (int i = 0; i < 9; ++i) a[i] = __builtin_amdgcn_alignbit(o[i + 1], o[i], 16);
    LDS char* xb = smem + S_X + ch * 4 * S_CPY + 32 + 2 * (1024 * hh + 16 * lane);
    *(LDS u32x4*)(xb) = u32x4{o[0], o[1], o[2], o[3]};
    *(LDS u32x4*)(xb + 16) = u32x4{o[4], o[5], o[6], o[7]};
    *(LDS u32x4*)(xb + S_CPY) = u32x4{a[0], a[1], a[2], a[3]};
    *(LDS u32x4*)(xb + S_CPY + 16) = u32x4{a[4], a[5], a[6], a[7]};
    *(LDS u32x4*)(xb + 2 * S_CPY) = u32x4{o[1], o[2], o[3], o[4]};
    *(LDS u32x4*)(xb + 2 * S_CPY + 16) = u32x4{o[5], o[6], o[7], o[8]};
    *(LDS u32x4*)(xb + 3 * S_CPY) = u32x4{a[1], a[2], a[3], a[4]};
    *(LDS u32x4*)(xb + 3 * S_CPY + 16) = u32x4{a[5], a[6], a[7], a[8]};
    if (hh == 0 && lane == 0) {
      LDS char* hb = smem + S_X + ch * 4 * S_CPY;
      const u32x4 z = {0, 0, 0, 0};
      *(LDS u32x4*)(hb) = z;
      *(LDS u32x4*)(hb + 16) = z;
      *(LDS u32x4*)(hb + S_CPY) = z;
      *(LDS u32x4*)(hb + S_CPY + 16) = u32x4{0, 0, 0, o[0] << 16};
      *(LDS u32x4*)(hb + 2 * S_CPY) = z;
      *(LDS u32x4*)(hb + 2 * S_CPY + 16) = u32x4{0, 0, 0, o[0]};
      *(LDS u32x4*)(hb + 3 * S_CPY) = z;
      *(LDS u32x4*)(hb + 3 * S_CPY + 16) = u32x4{0, 0, o[0] << 16, a[0]};
    }
    LDS char* z = smem + S_Z + ch * S_ZSZ + 512 + (1024 * hh + 8 * lane) * 2;
    *(LDS u32x4*)z = uv[0];
    *(LDS u32x4*)(z + 1024) = uv[1];
  }
  STAMP(0, 6);
  bar();
  STAMP(0, 7);
  f32x4 acc[8];
#pragma unroll
  for (int i = 0; i < 8; ++i) acc[i] = f32x4{0, 0, 0, 0};
  {
    const int b = lane & 15, kq = lane >> 4, qq = b >> 2, pp = b & 3;
    LDS const char* fb = smem + S_X + (ch * 4 + ((4 - qq) & 3)) * S_CPY +
                         2 * (16 + 16 * (kq >> 1) - 8 * (kq & 1) + 4 * pp - (qq ? 4 : 0)) + 64 * hh;
    LDS const char* gb = smem + S_Z + ch * S_ZSZ + 512 + 32 * (b - (kq >> 1)) + 16 * (kq & 1) - 64 * hh;
#pragma unroll 1
    for (int it = 0; it < 4; ++it) {
      bf16x8 F[8], G[8];
#pragma unroll
      for (int s = 0; s < 8; ++s) {
        F[s] = tr_frag(fb + 512 * s, fb + 512 * s - 8);
        G[s] = lds_frag(gb + 512 * s);
      }
      __builtin_amdgcn_sched_barrier(0);
#pragma unroll
      for (int t = 0; t < 8; ++t)
#pragma unroll
        for (int s = 0; s <= t; ++s) acc[t] = mfma16(F[s], G[t - s], acc[t]);
      fb += 128;
      gb -= 128;
    }
  }
  STAMP(0, 8);
  bar();
  {
    LDS char* ex = smem + S_X + w * 4096 + lane * 16;
#pragma unroll
    for (int i = 0; i < 4; ++i) {
      const f32x4 lo = acc[i], hi = acc[4 + i];
      *(LDS f32x4*)(ex + i * 1024) = hh ? +lo : +hi;
    }
  }
  bar();
  {
    LDS const char* ey = smem + S_X + (w ^ 1) * 4096 + lane * 16;
    const __amdgpu_buffer_rsrc_t rs = mk_rsrc(p.ws + WS_AT, DM * SL * 2);
    const int dst = (d * SL + 1024 * hh + 16 * (lane & 15) + 4 * (lane >> 4)) * 2;
#pragma unroll
    for (int i = 0; i < 4; ++i) {
      const f32x4 lo = acc[i], hi = acc[4 + i];
      const f32x4 y = (hh ? +hi : +lo) + *(LDS const f32x4*)(ey + i * 1024);
      __builtin_amdgcn_raw_buffer_store_b64(u32x2{pack_bf16(y[0], y[1]), pack_bf16(y[2], y[3])}, rs, dst + 512 * i, 0, SC1);
    }
  }
  STAMP(0, 9);
  wait_vm<0>();
  bar();
  if (w == 0) {
    const int lt = lane & 31, second = lane >> 5;
    const int dd = 6 * sg + (second ? 5 : 0);
    const int idx = dd < 480 ? dd / 96 : 5 + (dd - 480) / 32;
    const int dfirst = idx < 5 ? 96 * idx : 480 + 32 * (idx - 5);
    __hip_atomic_store((GLB u64*)(p.ws + WS_IN) + tile_block(lt, idx) * 64 + IN_A + (sg - dfirst / 6), FLAG_SET, RLX_AGENT);
  }
}

DEV void role_gate(const P& p, int rb, int w, int lane) {
  const int sub = lane & 7;
  u32x4 wv[9];
#pragma unroll
  for (int i = 0; i < 9; ++i) wv[i] = *(const u32x4*)((const char*)p.gw + (i * 8 + sub) * 16);
  for (int r = w * 8 + (lane >> 3); r < 128; r += 96) {
    const int l = rb * 128 + r;
    float s = 0.f;
    u32x4 xv[9];
#pragma unroll
    for (int i = 0; i < 9; ++i) xv[i] = *(const u32x4*)((const char*)p.x + (size_t)l * 1152 + (i * 8 + sub) * 16);
#pragma unroll
    for (int i = 0; i < 9; ++i) {
#pragma unroll
      for (int k = 0; k < 4; ++k) {
        const u32 pr = pack_bf16(bflo(xv[i][k]) * bflo(wv[i][k]), bfhi(xv[i][k]) * bfhi(wv[i][k]));
        s += bflo(pr);
        s += bfhi(pr);
      }
    }
    s += __shfl_xor(s, 1); s += __shfl_xor(s, 2); s += __shfl_xor(s, 4);
    const float gv = rbf(1.0f / (1.0f + expf(-rbf(s))));
    if (sub == 0)
      __hip_atomic_store((GLB u64*)(p.ws + WS_G) + l, ((u64)__float_as_uint(rbf(1.0f - gv)) << 32) | __float_as_uint(gv), RLX_AGENT);
  }
  wait_vm<0>();
  bar();
  if (w == 0 && lane < 16)
    __hip_atomic_store((GLB u64*)(p.ws + WS_IN) + tile_block(2 * rb + (lane >> 3), lane & 7) * 64 + IN_G, FLAG_SET, RLX_AGENT);
}

constexpr int H_C = 32768, H_CS = 12288;
DEV int h_slot(int i) { return i < 4 ? i * 8192 : 131072 + (i - 4) * 8192; }
constexpr int H_AT = H_C + 2 * H_CS;
constexpr int H_XT = H_C + 3 * H_CS;
constexpr int H_G = H_C + 4 * H_CS;
constexpr int H_OUT = H_C + 5 * H_CS;
constexpr int K_LDS = 163840;

constexpr int h_wait(int s, int NDT) {
  const int G = 2 + NDT;
  int n = 0;
  if (s <= 7) n = 2 * (7 - s) + G * (s > 1 ? s - 1 : 0);
  else n = G * ((s - 1 < 10 ? s - 1 : 10) - (s - 7));
  if (s >= 2 && s <= 8) n += 1;
  if (s >= 12) n += NDT;
  if (s >= 13) n += NDT;
  if (s >= 14) n += 1;
  return s == 17 ? 0 : n;
}
template <int S, int NDT, class F> DEV void h_steps(F&& after, u64& fv, char* wsp) {
  if constexpr (S == 9) asm volatile("s_waitcnt vmcnt(%1)" : "+v"(fv) : "n"(h_wait(S, NDT)) : "memory");
  else wait_vm<h_wait(S, NDT)>();
  LSTAMP2(1, 16 + S);
  bar();
  after(std::integral_constant<int, S>{});
  if constexpr (S < 17) h_steps<S + 1, NDT>(after, fv, wsp);
}

template <int NDT>
DEV void p2_loader(const P& p, LDS char* smem, int lt, int d0, int lw, int lane, bool scratch_barrier) {
  __builtin_amdgcn_s_setprio(3);
  const char* hb = p.ws + WS_HB + (size_t)lt * 18 * 8192 + (2 * lw) * 1024 + lane * 16;
  constexpr int W = 32 * NDT;
  const int l0 = 64 * lt;
  u32 co[NDT];
#pragma unroll
  for (int i = 0; i < NDT; ++i) {
    const int row = 8 * (NDT * lw + i) + (lane >> 3), cp = lane & 7;
    co[i] = (u32)(d0 + row) * 1152u + ((cp ^ (row & 7)) << 4);
  }
  auto issue_c = [&](int s) {
    LDS char* buf = smem + H_C + (s & 7) * H_CS + NDT * lw * 1024;
    const char* cb = (const char*)(s < 9 ? p.cre : p.cim) + (s < 9 ? s : s - 9) * 128;
#pragma unroll
    for (int i = 0; i < NDT; ++i) dma16<0>(cb + co[i], buf + i * 1024);
  };
  auto issue_h = [&](int s) {
    LDS char* buf = smem + h_slot(s & 7) + 2 * lw * 1024;
#pragma unroll
    for (int i = 0; i < 2; ++i) dma16<SC1>(hb + s * 8192 + i * 1024, buf + i * 1024);
  };
  GLB u64* inb = (GLB u64*)(p.ws + WS_IN) + blockIdx.x * 64;
  const int nA = (d0 + W - 1) / 6 - d0 / 6 + 1;
  const bool need = lane == IN_G || (lane >= IN_A && lane < IN_A + nA);
#pragma unroll
  for (int s = 0; s < 8; ++s) issue_c(s);
  if (scratch_barrier) bar();
  wait_flags(inb + IN_H + (lane < 36 ? lane : 0), true);
#pragma unroll
  for (int s = 0; s < 8; ++s) issue_h(s);
  u64 fv = 0;
  h_steps<0, NDT>([&](auto S_) {
    constexpr int S = decltype(S_)::value;
    if constexpr (S >= 1 && S + 7 < 18) { issue_h(S + 7); issue_c(S + 7); }
    if constexpr (S == 1)
      asm volatile("global_load_dwordx2 %0, %1, off sc1" : "=v"(fv) : "v"(inb + lane) : "memory");
    if constexpr (S == 9) { if (!__all(!need || fv == FLAG_SET)) wait_flags(inb + lane, need); }
    if constexpr (S == 11) {
#pragma unroll
      for (int i = 0; i < NDT; ++i) {
        const int pc = NDT * lw + i;
        dma16<SC1>(p.ws + WS_AT + (size_t)(d0 + 8 * pc + (lane >> 3)) * 4096 + l0 * 2 + (lane & 7) * 16, smem + H_AT + pc * 1024);
      }
    }
    if constexpr (S == 13) { if (lane < 8) dma16<SC1>(p.ws + WS_G + (size_t)l0 * 8 + lw * 128 + lane * 16, smem + H_G + lw * 128); }
    if constexpr (S == 12) {
#pragma unroll
      for (int i = 0; i < NDT; ++i) {
        const int pc = NDT * lw + i, id = pc * 64 + lane, row = id / (4 * NDT), qq = id - row * (4 * NDT);
        dma16<0>((const char*)p.x + (size_t)(l0 + row) * 1152 + d0 * 2 + qq * 16, smem + H_XT + pc * 1024);
      }
    }
  }, fv, p.ws);
}

template <int NDT, int STAG>
DEV void p2_compute(const P& p, LDS char* smem, int lt0, int d0, int w, int lane) {
  const int lh = (w >> 1) & 1, kh = w & 1, dg = w >> 2;
  const int r = lane & 15, kq = lane >> 4;
  const int lt = 2 * lh + kh, l0 = 64 * lt0;
  constexpr int W = 32 * NDT;
  u32x2 da[NDT], db[NDT];
#pragma unroll
  for (int t = 0; t < NDT; ++t) {
    const int d = d0 + 16 * (NDT * dg + t) + 4 * kq;
    da[t] = *(const u32x2*)(p.dskA + d);
    db[t] = *(const u32x2*)(p.dskB + d);
  }
  GLB u64* inb = (GLB u64*)(p.ws + WS_IN) + blockIdx.x * 64;

  f32x4 acc[2][NDT];
#pragma unroll
  for (int i = 0; i < 2; ++i)
#pragma unroll
    for (int t = 0; t < NDT; ++t) acc[i][t] = f32x4{0, 0, 0, 0};
  int ho[2];
#pragma unroll
  for (int u = 0; u < 2; ++u) {
    const int k = 32 * kh + 8 * kq + 4 * u + (r >> 2), i = (r & 3) >> 1;
    ho[u] = (2 * (k >> 4) + i) * 1024 + (32 * lh + ((k & 15) ^ (4 * i))) * 16 + (r & 1) * 8;
  }
  const int co = H_C + (16 * NDT * dg + r) * 128 + (((4 * kh + kq) ^ (r & 7)) << 4);
  bf16x8 hb[2], ca[NDT];
#pragma unroll
  for (int i = 0; i < 2; ++i) hb[i] = bf16x8{};
#pragma unroll
  for (int t = 0; t < NDT; ++t) ca[t] = bf16x8{};
  auto mma = [&]() {
#pragma unroll
    for (int t = 0; t < NDT; ++t) {
      acc[0][t] = mfma16(ca[t], hb[0], acc[0][t]);
      acc[1][t] = mfma16(ca[t], hb[1], acc[1][t]);
    }
  };
  STAMP(1, 1);
#pragma unroll 1
  for (int s = 0; s < 18; ++s) {
    bar();
    if (s == 0) STAMP(1, 2);
    LSTAMP(1, 16 + s);
    if (STAG) { mma(); __builtin_amdgcn_sched_barrier(0); }
    LDS const char* hbuf = smem + h_slot(s & 7);
    LDS const char* cbuf = smem + (s & 7) * H_CS;
#pragma unroll
    for (int i = 0; i < 2; ++i) hb[i] = tr_frag(hbuf + ho[0] + i * 256, hbuf + ho[1] + i * 256);
#pragma unroll
    for (int t = 0; t < NDT; ++t) ca[t] = lds_frag(cbuf + co + t * 2048);
    __builtin_amdgcn_sched_barrier(0);
    if (!STAG) { mma(); __builtin_amdgcn_sched_barrier(0); }
  }
  if (STAG) mma();
  __builtin_amdgcn_sched_barrier(0);
  STAMP(1, 3);
  bar();
  if (w == 0) __hip_atomic_store(inb + lane, 0ull, RLX_AGENT);
  LDS char* ex = smem + w * (NDT * 1024) + lane * 16;
#pragma unroll
  for (int t = 0; t < NDT; ++t) {
    const f32x4 a0 = acc[0][t], a1 = acc[1][t];
    *(LDS f32x4*)(ex + t * 1024) = kh ? +a0 : +a1;
  }
  bar();
  LDS const char* ey = smem + (w ^ 1) * (NDT * 1024) + lane * 16;
  constexpr int OSTR = W * 4 + 16;
  const f32x2 gg = *(LDS const f32x2*)(smem + H_G + (16 * lt + r) * 8);
#pragma unroll
  for (int t = 0; t < NDT; ++t) {
    const int dt = NDT * dg + t;
    const f32x4 o = *(LDS const f32x4*)(ey + t * 1024);
    const f32x4 a0 = acc[0][t], a1 = acc[1][t];
    const f32x4 y = (kh ? +a1 : +a0) + o;
    const s16x4 av = tr_read(smem + H_AT + (16 * dt + 4 * kq + (r >> 2)) * 128 + lt * 32 + (r & 3) * 8);
    const u32x2 xv = *(LDS const u32x2*)(smem + H_XT + (16 * lt + r) * (W * 2) + (16 * dt + 4 * kq) * 2);
    f32x4 res;
#pragma unroll
    for (int i = 0; i < 4; ++i) {
      const u32 xw = xv[i >> 1], aw = da[t][i >> 1], bw = db[t][i >> 1];
      const float xf = (i & 1) ? bfhi(xw) : bflo(xw);
      const float sa = (i & 1) ? bfhi(aw) : bflo(aw);
      const float sb = (i & 1) ? bfhi(bw) : bflo(bw);
      const float yc = bf2f((u16)av[i]);
      const float a = rbf(yc + rbf(xf * sa));
      const float b = y[i] + rbf(xf * sb);
      res[i] = rbf(gg[0] * a) + gg[1] * b;
    }
    *(LDS f32x4*)(smem + H_OUT + (16 * lt + r) * OSTR + (16 * dt + 4 * kq) * 4) = res;
  }
  STAMP(1, 4);
  bar();
#pragma unroll
  for (int it = 0; it < NDT; ++it) {
    const int id = it * 512 + w * 64 + lane;
    const int row = id / (8 * NDT), pc = id - row * (8 * NDT);
    *(f32x4*)(p.out + (size_t)(l0 + row) * DM + d0 + pc * 4) = *(LDS const f32x4*)(smem + H_OUT + row * OSTR + pc * 16);
  }
}

template <int NDT>
DEV void phase2(const P& p, LDS char* smem, int lt, int d0, int w, int lane, bool scratch_barrier) {
  STAMP(1, 0);
  if (w >= 8) p2_loader<NDT>(p, smem, lt, d0, w - 8, lane, scratch_barrier);
  else if (w < 4) p2_compute<NDT, 0>(p, smem, lt, d0, w, lane);
  else p2_compute<NDT, 1>(p, smem, lt, d0, w, lane);
  STAMP(1, 15);
}

__global__ __launch_bounds__(768, 3) void gated_mix_s4d_s5(P p) {
  extern __shared__ __attribute__((aligned(16))) char smem_raw[];
  LDS char* smem = (LDS char*)smem_raw;
  const int w = __builtin_amdgcn_readfirstlane(threadIdx.x >> 6), lane = threadIdx.x & 63;
  const int xcd = blockIdx.x & 7, j = blockIdx.x >> 3;
  STAMP(0, 0);
  if (j >= 18 && j < 30) {
    role_s(p, smem, 12 * xcd + (j - 18), w, lane);
    STAMP(0, 15);
    phase2<1>(p, smem, 4 * xcd + (j - 18) / 3, 480 + 32 * ((j - 18) % 3), w, lane, false);
  } else {
    const bool isg = j < 18;
    if (isg) {
      const int c = 2 * xcd + j / 9, g = j % 9;
      if (w >= 8) g_loader(p, smem, c, g, w - 8, lane);
      else if (w < 4) g_compute<0>(p, smem, c, g, w, lane);
      else g_compute<1>(p, smem, c, g, w, lane);
    } else {
      role_gate(p, 2 * xcd + (j - 30), w, lane);
    }
    STAMP(0, 15);
    const int e = isg ? j : j - 12;
    phase2<3>(p, smem, 4 * xcd + e / 5, 96 * (e % 5), w, lane, isg);
  }
}

extern "C" void kernel_launch(void* const* d_in, const int* in_sizes, int n_in, void* d_out, int out_size,
                              void* d_ws, size_t ws_size, hipStream_t stream) {
  P a;
  a.x = (const u16*)d_in[0]; a.la = (const u16*)d_in[1]; a.aim = (const u16*)d_in[2];
  a.creA = (const u16*)d_in[3]; a.cimA = (const u16*)d_in[4]; a.ldtA = (const u16*)d_in[5]; a.dskA = (const u16*)d_in[6];
  a.lre = (const u16*)d_in[8]; a.lim = (const u16*)d_in[9]; a.bre = (const u16*)d_in[10]; a.bim = (const u16*)d_in[11];
  a.cre = (const u16*)d_in[12]; a.cim = (const u16*)d_in[13]; a.ldtB = (const u16*)d_in[14];
  a.dskB = (const u16*)d_in[15]; a.gw = (const u16*)d_in[16];
  a.ws = (char*)d_ws; a.out = (float*)d_out;
  (void)hipFuncSetAttribute((const void*)gated_mix_s4d_s5, hipFuncAttributeMaxDynamicSharedMemorySize, K_LDS);
  gated_mix_s4d_s5<<<256, 768, K_LDS, stream>>>(a);
}
```

```cpp
#define PF_CODE 310
#include <hip/hip_runtime.h>
#include <hip/hip_bf16.h>
#include <stdint.h>
#include <type_traits>

typedef unsigned short u16;
typedef unsigned int u32;
typedef unsigned long long u64;
typedef __attribute__((ext_vector_type(8))) __bf16 bf16x8;
typedef __attribute__((ext_vector_type(2))) __bf16 bf16x2;
typedef __attribute__((ext_vector_type(4))) short s16x4;
typedef __attribute__((ext_vector_type(4))) float f32x4;
typedef __attribute__((ext_vector_type(2))) float f32x2;
typedef __attribute__((ext_vector_type(4))) unsigned u32x4;
typedef __attribute__((ext_vector_type(2))) unsigned u32x2;
#define LDS __attribute__((address_space(3)))
#define GLB __attribute__((address_space(1)))
#define DEV __device__ __forceinline__
#define RLX_AGENT __ATOMIC_RELAXED, __HIP_MEMORY_SCOPE_AGENT

constexpr int SL = 2048;
constexpr int DM = 576;

constexpr size_t WS_HB = 0;
constexpr size_t WS_AT = WS_HB + (size_t)2 * DM * SL * 2;
constexpr size_t WS_G = WS_AT + (size_t)DM * SL * 2;
constexpr size_t WS_GRAN = WS_G + (size_t)SL * 8;
constexpr size_t WS_EP = WS_GRAN + (size_t)9 * 16 * 2 * 64 * 8;
constexpr size_t WS_FL = WS_EP + 4096;
constexpr size_t WS_XT = WS_FL + (size_t)16 * DM * 8;
constexpr size_t WS_IN = WS_XT + (size_t)DM * SL * 2;
constexpr u64 FLAG_SET = 0x9E3779B97F4A7C15ull;
constexpr u32 SPIN_CAP = 1u << 17;
constexpr int IN_H = 0, IN_G = 36, IN_A = 40;

constexpr size_t WS_ST = (size_t)16 << 20;
#define STAMP(kern, idx) do {} while (0)
#define CSTAMP(kern, idx) do {} while (0)
#define LSTAMP(kern, idx) do {} while (0)
#define LSTAMP2(kern, idx) do {} while (0)

DEV float bf2f(u16 v) { return __uint_as_float((u32)v << 16); }
DEV float bflo(u32 v) { return __uint_as_float(v << 16); }
DEV float bfhi(u32 v) { return __uint_as_float(v & 0xffff0000u); }
DEV u32 pack_bf16(float lo, float hi) {
  f32x2 v = {lo, hi};
  bf16x2 b = __builtin_convertvector(v, bf16x2);
  return __builtin_bit_cast(u32, b);
}
DEV float rbf(float x) { return bflo(pack_bf16(x, x)); }
template <int N> DEV void wait_vm() { asm volatile("s_waitcnt vmcnt(%0)" ::"n"(N) : "memory"); }
DEV void bar() { asm volatile("s_waitcnt lgkmcnt(0)\n\ts_barrier" ::: "memory"); }
DEV void wave_fence() { __builtin_amdgcn_fence(__ATOMIC_ACQ_REL, "wavefront"); }
template <int AUX> DEV void dma16(const void* src, LDS char* dst) {
  __builtin_amdgcn_global_load_lds((const GLB u32*)src, (LDS u32*)dst, 16, 0, AUX);
}
constexpr int SC1 = 16;
#define XT_WAIT 2
DEV bf16x8 lds_frag(LDS const char* p) { return *(LDS const bf16x8*)p; }
DEV s16x4 tr_read(LDS const char* a) { return __builtin_amdgcn_ds_read_tr16_b64_v4i16((LDS s16x4*)a); }
DEV bf16x8 tr_frag(LDS const char* a, LDS const char* b) {
  const s16x4 lo = tr_read(a), hi = tr_read(b);
  return __builtin_bit_cast(bf16x8, __builtin_shufflevector(lo, hi, 0, 1, 2, 3, 4, 5, 6, 7));
}
DEV f32x4 mfma16(bf16x8 a, bf16x8 b, f32x4 c) { return __builtin_amdgcn_mfma_f32_16x16x32_bf16(a, b, c, 0, 0, 0); }
DEV __amdgpu_buffer_rsrc_t mk_rsrc(const void* base, int bytes) {
  return __builtin_amdgcn_make_buffer_rsrc(const_cast<void*>(base), (short)0, bytes, 0x00020000);
}
struct cf { float r, i; };
DEV cf cmul(cf a, cf b) { return {a.r * b.r - a.i * b.i, a.r * b.i + a.i * b.r}; }
DEV cf cfma(cf a, cf b, cf c) { return {fmaf(a.r, b.r, fmaf(-a.i, b.i, c.r)), fmaf(a.r, b.i, fmaf(a.i, b.r, c.i))}; }
typedef f32x2 c2;
DEV c2 cm2(c2 a, c2 b) {
  c2 t, r;
  asm("v_pk_mul_f32 %0, %1, %2 op_sel:[0,0] op_sel_hi:[0,1]" : "=v"(t) : "v"(a), "v"(b));
  asm("v_pk_fma_f32 %0, %1, %2, %3 op_sel:[1,1,0] op_sel_hi:[1,0,1] neg_lo:[1,0,0]" : "=v"(r) : "v"(a), "v"(b), "v"(t));
  return r;
}
DEV f32x2 fma2(f32x2 a, f32x2 b, f32x2 c) { return __builtin_elementwise_fma(a, b, c); }
DEV f32x2 lo2(f32x4 v) { return __builtin_shufflevector(v, v, 0, 1); }
DEV f32x2 hi2(f32x4 v) { return __builtin_shufflevector(v, v, 2, 3); }
DEV f32x4 cat2(f32x2 a, f32x2 b) { return __builtin_shufflevector(a, b, 0, 1, 2, 3); }
DEV void wait_flags(GLB u64* word, bool need) {
  for (u32 spin = 0;; ++spin) {
    const u64 v = __hip_atomic_load(word, RLX_AGENT);
    if (__all(!need || v == FLAG_SET) || spin > SPIN_CAP) break;
    __builtin_amdgcn_s_sleep(2);
  }
}
DEV int tile_block(int lt, int idx) {
  const int xcd = lt >> 2, a = lt & 3;
  int j;
  if (idx < 5) { const int e = 5 * a + idx; j = e < 18 ? e : e + 12; }
  else j = 18 + 3 * a + (idx - 5);
  return (j << 3) | xcd;
}

struct P {
  const u16 *x, *la, *aim, *creA, *cimA, *ldtA, *dskA;
  const u16 *lre, *lim, *bre, *bim, *cre, *cim, *ldtB, *dskB, *gw;
  char* ws;
  float* out;
};

constexpr int G_NB = 4, G_STAGE = 32768;
constexpr int G_E = G_NB * G_STAGE;
constexpr int G_PF = G_E + 4096;
constexpr int G_L = G_PF + 4096;
constexpr int G_X = G_L + 1024;
constexpr int G_T = G_X + 2048;
constexpr int G_END = G_T + 4 * 32 * 144;

DEV void g_loader(const P& p, LDS char* smem, int c, int g, int d0, int lw, int lane) {
  [[maybe_unused]] const int w = lw + 8;
  __builtin_amdgcn_s_setprio(3);
  GLB u32* ep = (GLB u32*)(p.ws + WS_EP + g * 256);
  GLB u64* gran = (GLB u64*)(p.ws + WS_GRAN) + (size_t)g * 16 * 128;
  const u32 tag = __hip_atomic_load(ep, RLX_AGENT) + 1u;
  u32 xo[4], bo[4];
#pragma unroll
  for (int i = 0; i < 4; ++i) {
    const int rho = 8 * (4 * lw + i) + (lane >> 3);
    const int hf = rho >> 6, m = (rho >> 4) & 3, r = rho & 15;
    const int tau = 64 * hf + 16 * (r >> 2) + 8 * (r & 1) + 2 * m + ((r >> 1) & 1);
    const int ch = (lane & 7) ^ (rho & 7);
    xo[i] = (u32)(128 * c + tau) * 1152u + ch * 16;
    const int pp = 64 * g + 16 * (rho >> 5) + r;
    bo[i] = (u32)pp * 1152u + ch * 16;
  }
  auto issue = [&](int s) {
    LDS char* buf = smem + (s & (G_NB - 1)) * G_STAGE + lw * 4096;
    int kb = g + s; kb = (kb >= 9 ? kb - 9 : kb) * 128;
#pragma unroll
    for (int i = 0; i < 4; ++i) dma16<0>((const char*)p.x + (xo[i] + kb), buf + i * 1024);
#pragma unroll
    for (int i = 0; i < 4; ++i) {
      const char* base = (const char*)((((4 * lw + i) >> 1) & 1) ? p.bim : p.bre);
      dma16<0>(base + (bo[i] + kb), buf + 16384 + i * 1024);
    }
  };
#pragma unroll
  for (int s = 0; s < G_NB - 1; ++s) issue(s);
#pragma unroll 1
  for (int s = 0; s < 6; ++s) { wait_vm<16>(); LSTAMP(0, 16 + s); bar(); issue(s + G_NB - 1); LSTAMP(0, 32 + s); }
  wait_vm<16>(); bar();
  wait_vm<8>(); bar();
  wait_vm<0>(); bar();
  STAMP(0, 1);
  if (lw < 2) {
    bar();
    u32 co[6];
#pragma unroll
    for (int i = 0; i < 6; ++i) {
      const int row = 8 * (6 * lw + i) + (lane >> 3), cp = lane & 7;
      co[i] = (u32)(d0 + row) * 1152u + ((cp ^ (row & 7)) << 4);
    }
    const int rot = ((d0 / 96) * 18) >> 3;
#pragma unroll 1
    for (int s = 0; s < 8; ++s) {
      LDS char* buf = smem + 32768 + s * 12288 + 6 * lw * 1024;
      const int ks = s + rot;
      const char* cb = ks < 9 ? (const char*)p.cre + ks * 128 : (const char*)p.cim + (ks - 9) * 128;
#pragma unroll
      for (int i = 0; i < 6; ++i) dma16<0>(cb + co[i], buf + i * 1024);
    }
    bar();
    return;
  }
  bar();
  STAMP(0, 2);
  if (lw == 3) {
    const f32x2 lv = *(LDS const f32x2*)(smem + G_L + lane * 8);
    const cf l16 = {lv[0], lv[1]};
    f32x2 ev[8];
#pragma unroll
    for (int s = 0; s < 8; ++s) ev[s] = *(LDS const f32x2*)(smem + G_E + (s * 64 + lane) * 8);
    cf tot = {0.f, 0.f};
    cf pre[8];
#pragma unroll
    for (int s = 0; s < 8; ++s) { pre[s] = tot; tot = cfma(l16, tot, cf{ev[s][0], ev[s][1]}); }
    if (c < 15) {
      __hip_atomic_store(gran + c * 128 + lane, ((u64)tag << 32) | __float_as_uint(tot.r), RLX_AGENT);
      __hip_atomic_store(gran + c * 128 + 64 + lane, ((u64)tag << 32) | __float_as_uint(tot.i), RLX_AGENT);
    }
#pragma unroll
    for (int s = 0; s < 8; ++s) *(LDS f32x2*)(smem + G_PF + (s * 64 + lane) * 8) = f32x2{pre[s].r, pre[s].i};
  }
  const int par = lw - 2;
  const f32x2 lv = *(LDS const f32x2*)(smem + G_L + 512 + lane * 8);
  const cf l128 = {lv[0], lv[1]};
  const cf l256 = cmul(l128, l128);
  cf acc = {0.f, 0.f};
  if (par < c) {
    const int nc = (c - par + 1) >> 1;
    u64 a[8], b[8];
    for (u32 spin = 0;; ++spin) {
      bool ok = true;
#pragma unroll
      for (int i = 0; i < 8; ++i) {
        const int cc = par + 2 * (i < nc ? i : 0);
        a[i] = __hip_atomic_load(gran + cc * 128 + lane, RLX_AGENT);
        b[i] = __hip_atomic_load(gran + cc * 128 + 64 + lane, RLX_AGENT);
      }
#pragma unroll
      for (int i = 0; i < 8; ++i) ok = ok && (u32)(a[i] >> 32) == tag && (u32)(b[i] >> 32) == tag;
      if (__all(ok) || spin > SPIN_CAP) break;
      __builtin_amdgcn_s_sleep(1);
    }
#pragma unroll
    for (int i = 0; i < 8; ++i)
      if (i < nc) acc = cfma(l256, acc, cf{__uint_as_float((u32)a[i]), __uint_as_float((u32)b[i])});
    if (c - 1 - (par + 2 * (nc - 1))) acc = cmul(acc, l128);
  }
  *(LDS f32x2*)(smem + G_X + (par * 64 + lane) * 8) = f32x2{acc.r, acc.i};
  STAMP(0, 3);
  bar();
  if (c == 15 && lw == 2 && lane == 0) __hip_atomic_store(ep, tag, RLX_AGENT);
}

template <int HF>
DEV void g_compute(const P& p, LDS char* smem, int c, int g, int w, int lane) {
  const int pq = w & 3, col = lane & 15, q = lane >> 4;
  const int pl = 16 * pq + col, pp = 64 * g + pl;
  cf lam, coef2;
  {
    const float lre = bf2f(p.lre[pp]), lim = bf2f(p.lim[pp]);
    const float dt = rbf(expf(bf2f(p.ldtB[pp])));
    const float zr = dt * lre, zi = dt * lim;
    const float er = expf(zr);
    float sn, cs;
    sincosf(zi, &sn, &cs);
    lam = {er * cs, er * sn};
    const float nr = lam.r - 1.0f, ni = lam.i;
    const float den = 1.0f / (lre * lre + lim * lim);
    coef2 = {2.0f * (nr * lre + ni * lim) * den, 2.0f * (ni * lre - nr * lim) * den};
  }
  cf l8 = cmul(lam, lam); l8 = cmul(l8, l8); l8 = cmul(l8, l8);
  const cf l16 = cmul(l8, l8);
  const cf l32 = cmul(l16, l16), l64 = cmul(l32, l32);
  if (HF == 0 && q == 0) {
    const cf l128 = cmul(l64, l64);
    *(LDS f32x2*)(smem + G_L + pl * 8) = f32x2{l16.r, l16.i};
    *(LDS f32x2*)(smem + G_L + 512 + pl * 8) = f32x2{l128.r, l128.i};
  }
  cf pw = HF ? l64 : cf{1.f, 0.f};
  { const cf t = cmul(pw, l16); if (q & 1) pw = t; }
  { const cf t = cmul(pw, l32); if (q & 2) pw = t; }
  asm volatile("" : "+v"(lam.r), "+v"(lam.i), "+v"(coef2.r), "+v"(coef2.i), "+v"(l8.r), "+v"(l8.i), "+v"(pw.r), "+v"(pw.i));
  f32x4 are[4], aim[4];
#pragma unroll
  for (int m = 0; m < 4; ++m) { are[m] = f32x4{0, 0, 0, 0}; aim[m] = f32x4{0, 0, 0, 0}; }
  const int o0 = col * 128 + (((0 + q) ^ (col & 7)) << 4);
  const int o1 = col * 128 + (((4 + q) ^ (col & 7)) << 4);
  bf16x8 br[2], bi[2], a[2][4];
  auto read_frags = [&](int s) {
    LDS const char* buf = smem + (s & (G_NB - 1)) * G_STAGE;
    LDS const char* ab = buf + HF * 8192;
    LDS const char* bb = buf + 16384 + pq * 4096;
#pragma unroll
    for (int ks = 0; ks < 2; ++ks) {
      const int o = ks ? o1 : o0;
      br[ks] = lds_frag(bb + o); bi[ks] = lds_frag(bb + 2048 + o);
#pragma unroll
      for (int m = 0; m < 4; ++m) a[ks][m] = lds_frag(ab + m * 2048 + o);
    }
  };
  auto mma = [&]() {
#pragma unroll
    for (int ks = 0; ks < 2; ++ks)
#pragma unroll
      for (int m = 0; m < 4; ++m) {
        are[m] = mfma16(a[ks][m], br[ks], are[m]);
        aim[m] = mfma16(a[ks][m], bi[ks], aim[m]);
      }
  };
  STAMP(0, 1);
#pragma unroll
  for (int s = 0; s < 9; ++s) {
    if (HF == 1 && s == XT_WAIT) wait_vm<0>();
    bar();
    if (s == 0) { STAMP(0, 2); CSTAMP(0, 60); }
    if (HF == 1 && s == XT_WAIT && pq == 0)
      __hip_atomic_store((GLB u64*)(p.ws + WS_FL) + c * DM + 64 * g + lane, FLAG_SET, RLX_AGENT);
    if (HF == 1 && s > 0) { mma(); __builtin_amdgcn_sched_barrier(0); }
    if (HF == 1 && s == 0) {
      const int rr = 4 * q + (col >> 2);
      LDS const char* tb = smem + rr * 128 + (((2 * pq + ((col & 3) >> 1)) ^ (rr & 7)) << 4) + (col & 1) * 8;
      LDS char* sc = smem + G_T + pq * (32 * 144);
#pragma unroll
      for (int hf = 0; hf < 2; ++hf) {
        u32x2 t[4];
#pragma unroll
        for (int m = 0; m < 4; ++m) t[m] = __builtin_bit_cast(u32x2, tr_read(tb + hf * 8192 + m * 2048));
        u32x4 oa, ob;
#pragma unroll
        for (int m = 0; m < 4; ++m) {
          oa[m] = __builtin_amdgcn_perm(t[m][1], t[m][0], 0x05040100u);
          ob[m] = __builtin_amdgcn_perm(t[m][1], t[m][0], 0x07060302u);
        }
        *(LDS u32x4*)(sc + hf * (16 * 144) + col * 144 + q * 32) = oa;
        *(LDS u32x4*)(sc + hf * (16 * 144) + col * 144 + q * 32 + 16) = ob;
      }
      wave_fence();
      const __amdgpu_buffer_rsrc_t rs = mk_rsrc(p.ws + WS_XT, DM * SL * 2);
#pragma unroll
      for (int u = 0; u < 4; ++u) {
        const int row = (lane >> 3) + 8 * (u & 1), hf = u >> 1;
        const u32x4 v = *(LDS const u32x4*)(sc + hf * (16 * 144) + row * 144 + (lane & 7) * 16);
        __builtin_amdgcn_raw_buffer_store_b128(v, rs, ((64 * g + 16 * pq + row) * SL + 128 * c + 64 * hf) * 2 + (lane & 7) * 16, 0, SC1);
      }
      __builtin_amdgcn_sched_barrier(0);
    }
    read_frags(s);
    __builtin_amdgcn_sched_barrier(0);
    if (HF == 0) { mma(); __builtin_amdgcn_sched_barrier(0); }
  }
  if (HF == 1) mma();
  __builtin_amdgcn_sched_barrier(0);
  STAMP(0, 3); CSTAMP(0, 61);
  const f32x2 Lr = {lam.r, lam.r}, Li = {lam.i, lam.i};
  f32x2 er = {0.f, 0.f}, ei = {0.f, 0.f};
#pragma unroll
  for (int t = 0; t < 8; ++t) {
    const f32x2 br_ = (t & 1) ? hi2(are[t >> 1]) : lo2(are[t >> 1]);
    const f32x2 bi_ = (t & 1) ? hi2(aim[t >> 1]) : lo2(aim[t >> 1]);
    const f32x2 nr = fma2(-Li, ei, fma2(Lr, er, br_));
    ei = fma2(Li, er, fma2(Lr, ei, bi_));
    er = nr;
  }
  const cf eA = {er[0], ei[0]};
  const cf e = cfma(l8, eA, cf{er[1], ei[1]});
  const int seg = 4 * HF + q;
  *(LDS f32x2*)(smem + G_E + (seg * 64 + pl) * 8) = f32x2{e.r, e.i};
  STAMP(0, 4);
  bar();
  STAMP(0, 5);
  {
#pragma unroll
    for (int m = 0; m < 4; ++m) asm volatile("" : "+v"(are[m]), "+v"(aim[m]));
    const f32x2 Cr = {coef2.r, coef2.r}, Ci = {coef2.i, coef2.i};
#pragma unroll
    for (int m = 0; m < 4; ++m) {
      const f32x2 r0 = lo2(are[m]), r1 = hi2(are[m]), i0 = lo2(aim[m]), i1 = hi2(aim[m]);
      are[m] = cat2(fma2(-Ci, i0, Cr * r0), fma2(-Ci, i1, Cr * r1));
      aim[m] = cat2(fma2(-Ci, r0, -Cr * i0), fma2(-Ci, r1, -Cr * i1));
      asm volatile("" : "+v"(are[m]), "+v"(aim[m]));
    }
  }
  cf eAc = cmul(coef2, eA), pwc = cmul(coef2, pw);
  asm volatile("" : "+v"(eAc.r), "+v"(eAc.i), "+v"(pwc.r), "+v"(pwc.i));
  STAMP(0, 7);
  bar();
  STAMP(0, 8);
  cf cin;
  {
    f32x2 v[2];
#pragma unroll
    for (int i = 0; i < 2; ++i) v[i] = *(LDS const f32x2*)(smem + G_X + (i * 64 + pl) * 8);
    const f32x2 pf = *(LDS const f32x2*)(smem + G_PF + (seg * 64 + pl) * 8);
    bar();
    const f32x2 S = v[0] + v[1];
    cin = cfma(pwc, cf{S[0], S[1]}, cmul(coef2, cf{pf[0], pf[1]}));
  }
  const cf cinB = cfma(l8, cin, eAc);
  er = f32x2{cin.r, cinB.r}; ei = f32x2{-cin.i, -cinB.i};
  f32x2 hr[8], hi[8];
#pragma unroll
  for (int t = 0; t < 8; ++t) {
    const f32x2 br_ = (t & 1) ? hi2(are[t >> 1]) : lo2(are[t >> 1]);
    const f32x2 bi_ = (t & 1) ? hi2(aim[t >> 1]) : lo2(aim[t >> 1]);
    const f32x2 nr = fma2(Li, ei, fma2(Lr, er, br_));
    ei = fma2(-Li, er, fma2(Lr, ei, bi_));
    er = nr;
    hr[t] = er; hi[t] = ei;
  }
  u32x4 o[4];
#pragma unroll
  for (int t = 0; t < 4; ++t) {
    o[0][t] = pack_bf16(hr[2 * t][0], hr[2 * t + 1][0]);
    o[1][t] = pack_bf16(hr[2 * t][1], hr[2 * t + 1][1]);
    o[2][t] = pack_bf16(hi[2 * t][0], hi[2 * t + 1][0]);
    o[3][t] = pack_bf16(hi[2 * t][1], hi[2 * t + 1][1]);
  }
  STAMP(0, 9);
  {
    const __amdgpu_buffer_rsrc_t rs = mk_rsrc(p.ws + WS_HB, 2 * DM * SL * 2);
    const int hb = (((c * 2 + HF) * 2) * 9 + g) * 8192 + pq * 2048;
    const int p0 = (16 * q + col) * 16, p1 = 1024 + (16 * q + (col ^ 4)) * 16;
    __builtin_amdgcn_raw_buffer_store_b128(o[0], rs, hb + p0, 0, SC1);
    __builtin_amdgcn_raw_buffer_store_b128(o[1], rs, hb + p1, 0, SC1);
    __builtin_amdgcn_raw_buffer_store_b128(o[2], rs, hb + 9 * 8192 + p0, 0, SC1);
    __builtin_amdgcn_raw_buffer_store_b128(o[3], rs, hb + 9 * 8192 + p1, 0, SC1);
    wait_vm<0>();
    STAMP(0, 10);
    if (lane < 8)
      __hip_atomic_store((GLB u64*)(p.ws + WS_IN) + tile_block(2 * c + HF, lane) * 64 + IN_H + g * 4 + pq, FLAG_SET, RLX_AGENT);
  }
}

constexpr int S_CPY = 4160;
constexpr int S_X = 0;
constexpr int S_ST = 6 * 4 * S_CPY;
constexpr int S_Z = S_ST;
constexpr int S_ZSZ = (256 + SL) * 2;
constexpr int S_T = S_ST + 12 * 4608;
constexpr int S_TSZ = 1312;
constexpr int S_END = S_T + 6 * S_TSZ;
constexpr int S_CNT = 32768 + 8192;

DEV c2 cexp_k(float zl, float th, float tl, float k) {
  const float mag = __builtin_amdgcn_exp2f(zl * k);
  const float t = fmaf(tl, k, __builtin_amdgcn_fractf(th * k));
  return c2{mag * __builtin_amdgcn_cosf(t), mag * __builtin_amdgcn_sinf(t)};
}
#define PKM(d, a, b) "v_pk_mul_f32 " d ", " a ", " b " op_sel:[0,0] op_sel_hi:[0,1]\n\t"
#define PKF(d, a, b) "v_pk_fma_f32 " d ", " a ", " b ", " d " op_sel:[1,1,0] op_sel_hi:[1,0,1] neg_lo:[1,0,0]\n\t"
DEV void octet(LDS char* dst, c2 s0, c2 m1, c2 m2) {
  c2 v1, v2, v3, t0, t1, t2, t3;
  asm(PKM("%0", "%7", "%8") PKM("%3", "%7", "%9") PKF("%0", "%7", "%8") PKF("%3", "%7", "%9")
      PKM("%1", "%0", "%8") PKM("%4", "%0", "%9") PKF("%1", "%0", "%8") PKF("%4", "%0", "%9")
      PKM("%2", "%1", "%8") PKM("%5", "%1", "%9") PKF("%2", "%1", "%8") PKF("%5", "%1", "%9")
      PKM("%6", "%2", "%9") PKF("%6", "%2", "%9")
      : "=&v"(v1), "=&v"(v2), "=&v"(v3), "=&v"(t0), "=&v"(t1), "=&v"(t2), "=&v"(t3) : "v"(s0), "v"(m1), "v"(m2));
  *(LDS u32*)(dst + 0 * 288) = pack_bf16(s0[0], s0[1]);
  *(LDS u32*)(dst + 1 * 288) = pack_bf16(v1[0], v1[1]);
  *(LDS u32*)(dst + 2 * 288) = pack_bf16(v2[0], v2[1]);
  *(LDS u32*)(dst + 3 * 288) = pack_bf16(v3[0], v3[1]);
  *(LDS u32*)(dst + 4 * 288) = pack_bf16(t0[0], t0[1]);
  *(LDS u32*)(dst + 5 * 288) = pack_bf16(t1[0], t1[1]);
  *(LDS u32*)(dst + 6 * 288) = pack_bf16(t2[0], t2[1]);
  *(LDS u32*)(dst + 7 * 288) = pack_bf16(t3[0], t3[1]);
}
DEV u32 dup_lo(u32 a) { return __builtin_amdgcn_perm(a, a, 0x01000100u); }
DEV u32 dup_hi(u32 a) { return __builtin_amdgcn_perm(a, a, 0x03020302u); }
DEV u32 hi_lo(u32 a, u32 b) { return __builtin_amdgcn_alignbit(b, a, 16); }

DEV void role_s(const P& p, LDS char* smem, int sg, int w, int lane) {
  const int tid = w * 64 + lane;
  const int ch = w >> 1, hh = w & 1, d = 6 * sg + ch;
  const int n = d * 128 + 64 * hh + lane;
  const u16 r_ldt = p.ldtA[d], r_la = p.la[n], r_aim = p.aim[n], r_cre = p.creA[n], r_cim = p.cimA[n];
  STAMP(0, 1);
  f32x4 tab[3];
  LDS char* st = smem + S_ST + w * 4608;
  {
    const float L2E = 1.44269504088896341f;
    const float dt = rbf(__builtin_amdgcn_exp2f(bf2f(r_ldt) * L2E));
    const float ar = -rbf(__builtin_amdgcn_exp2f(bf2f(r_la) * L2E));
    const float zl = ar * dt * L2E, zi = bf2f(r_aim) * dt;
    const float th = zi * 0.15915494309189535f;
    const float tl = fmaf(zi, 0.15915494309189535f, -th) + zi * 4.6017384e-10f;
    const c2 C = {2.0f * (bf2f(r_cre) * dt), 2.0f * (bf2f(r_cim) * dt)};
    const c2 w1 = cexp_k(zl, th, tl, 1.f), w8 = cexp_k(zl, th, tl, 8.f), w128 = cexp_k(zl, th, tl, 128.f);
    c2 w2, w4, w16, w32, w64, w256, w512, w1024, w2048;
    asm(PKM("%0", "%9", "%9") PKM("%2", "%10", "%10") PKM("%5", "%11", "%11") PKF("%0", "%9", "%9") PKF("%2", "%10", "%10") PKF("%5", "%11", "%11")
        PKM("%1", "%0", "%0") PKM("%3", "%2", "%2") PKM("%6", "%5", "%5") PKF("%1", "%0", "%0") PKF("%3", "%2", "%2") PKF("%6", "%5", "%5")
        PKM("%4", "%3", "%3") PKM("%7", "%6", "%6") PKF("%4", "%3", "%3") PKF("%7", "%6", "%6")
        PKM("%8", "%7", "%7") PKF("%8", "%7", "%7")
        : "=&v"(w2), "=&v"(w4), "=&v"(w16), "=&v"(w32), "=&v"(w64), "=&v"(w256), "=&v"(w512), "=&v"(w1024), "=&v"(w2048)
        : "v"(w1), "v"(w8), "v"(w128));
    c2 C128, C256, C512, C1024, C2048;
    asm(PKM("%0", "%5", "%6") PKM("%1", "%5", "%7") PKM("%2", "%5", "%8") PKM("%3", "%5", "%9") PKM("%4", "%5", "%10")
        PKF("%0", "%5", "%6") PKF("%1", "%5", "%7") PKF("%2", "%5", "%8") PKF("%3", "%5", "%9") PKF("%4", "%5", "%10")
        : "=&v"(C128), "=&v"(C256), "=&v"(C512), "=&v"(C1024), "=&v"(C2048)
        : "v"(C), "v"(w128), "v"(w256), "v"(w512), "v"(w1024), "v"(w2048));
    STAMP(0, 2);
    LDS char* wr = st + 4 * lane;
    LDS const char* rd = st + (lane & 15) * 288 + (lane >> 4) * 16;
    bf16x8 B[4];
    const c2 w8c = {w8[0], -w8[1]}, w32c = {w32[0], -w32[1]}, w64c = {w64[0], -w64[1]};
    octet(wr, c2{1.f, 0.f}, w8c, w32c);
    octet(wr + 8 * 288, w64c, w8c, w32c);
    wave_fence();
#pragma unroll
    for (int ks = 0; ks < 4; ++ks) B[ks] = lds_frag(rd + 64 * ks);
    wave_fence();
#pragma unroll
    for (int mt = 0; mt < 3; ++mt) {
      if (mt == 0) { octet(wr, C, w1, w4); octet(wr + 8 * 288, C128, w1, w4); }
      if (mt == 1) { octet(wr, C256, w2, w128); octet(wr + 8 * 288, C512, w128, w4); }
      if (mt == 2) { octet(wr, C1024, w128, w512); *(LDS u32*)(wr + 8 * 288) = pack_bf16(C2048[0], C2048[1]); }
      wave_fence();
      f32x4 acc = {0, 0, 0, 0};
      bf16x8 A[4];
#pragma unroll
      for (int ks = 0; ks < 4; ++ks) A[ks] = lds_frag(rd + 64 * ks);
      __builtin_amdgcn_sched_barrier(0);
#pragma unroll
      for (int ks = 0; ks < 4; ++ks) acc = mfma16(A[ks], B[ks], acc);
      __builtin_amdgcn_sched_barrier(0);
      wave_fence();
      tab[mt] = acc;
    }
  }
  const int b = lane & 15, q = lane >> 4;
#pragma unroll
  for (int mt = 0; mt < 3; ++mt) *(LDS f32x4*)(st + mt * 1024 + lane * 16) = tab[mt];
  STAMP(0, 3);
  bar();
  {
    LDS const char* ot = smem + S_ST + (w ^ 1) * 4608 + lane * 16;
    LDS char* T = smem + S_T + ch * S_TSZ;
    if (hh == 0) {
      const f32x4 v0 = tab[0] + *(LDS const f32x4*)(ot), v2 = tab[2] + *(LDS const f32x4*)(ot + 2048);
      *(LDS u32x2*)(T + 2 * (128 * (q >> 1) + 8 * b + 4 * (q & 1))) = u32x2{pack_bf16(v0[0], v0[1]), pack_bf16(v0[2], v0[3])};
      const u32 a0 = pack_bf16(v2[0], v2[1]), a1 = pack_bf16(v2[2], v2[3]);
      if (q < 2) {
        LDS char* t3 = T + 2 * (512 + 64 * q + b);
        *(LDS u16*)(t3) = (u16)a0; *(LDS u16*)(t3 + 32) = (u16)(a0 >> 16);
        *(LDS u16*)(t3 + 64) = (u16)a1; *(LDS u16*)(t3 + 96) = (u16)(a1 >> 16);
      } else if (lane == 32) *(LDS u16*)(T + 2 * 640) = (u16)a0;
    } else {
      const f32x4 v1 = tab[1] + *(LDS const f32x4*)(ot + 1024);
      const u32 a0 = pack_bf16(v1[0], v1[1]), a1 = pack_bf16(v1[2], v1[3]);
      if (q < 2) *(LDS u32x2*)(T + 2 * (256 + 64 * q + 4 * b)) = u32x2{a0, a1};
      else {
        LDS char* t2 = T + 2 * (384 + 2 * b + (q - 2));
        *(LDS u16*)(t2) = (u16)a0; *(LDS u16*)(t2 + 64) = (u16)(a0 >> 16);
        *(LDS u16*)(t2 + 128) = (u16)a1; *(LDS u16*)(t2 + 192) = (u16)(a1 >> 16);
      }
    }
  }
  bar();
  STAMP(0, 4);
  u32x4 uv[2];
  {
    GLB u64* fl = (GLB u64*)(p.ws + WS_FL) + (8 * hh + (lane & 7)) * DM + d;
    wait_flags(fl, true);
    const __amdgpu_buffer_rsrc_t rs = mk_rsrc(p.ws + WS_XT, DM * SL * 2);
#pragma unroll
    for (int v = 0; v < 2; ++v) uv[v] = __builtin_amdgcn_raw_buffer_load_b128(rs, (d * SL + 1024 * hh + 8 * (lane + 64 * v)) * 2, 0, SC1);
    if (lane < 8) __hip_atomic_store(fl, 0ull, RLX_AGENT);
  }
  STAMP(0, 5);
  {
    LDS const char* T = smem + S_T + ch * S_TSZ;
    u32 o[10];
    const u32 e512 = *(LDS const u16*)(T + 2 * 512);
    o[8] = o[9] = e512 | (e512 << 16);
    if (hh == 0) {
      const int off = lane < 16 ? 32 * lane : (lane < 32 ? 256 + 16 * lane : 512 + 8 * lane);
      u32x2 W[4];
#pragma unroll
      for (int i = 0; i < 4; ++i) W[i] = *(LDS const u32x2*)(T + off + 8 * i);
      if (lane < 16) {
#pragma unroll
        for (int i = 0; i < 8; ++i) o[i] = W[i >> 1][i & 1];
      } else if (lane < 32) {
#pragma unroll
        for (int i = 0; i < 4; ++i) {
          const u32 a = W[i >> 1][i & 1], nx = W[(i + 1) >> 1][(i + 1) & 1];
          o[2 * i] = dup_lo(a);
          o[2 * i + 1] = hi_lo(a, nx);
        }
      } else {
        const u32 w0 = W[0][0], w1 = W[0][1], w2 = W[1][0];
        o[0] = dup_lo(w0); o[1] = w0; o[2] = dup_hi(w0); o[3] = dup_lo(w1);
        o[4] = o[3]; o[5] = w1; o[6] = dup_hi(w1); o[7] = dup_lo(w2);
      }
    } else {
      const u32 w0 = *(LDS const u32*)(T + 1024 + 4 * lane), w1 = *(LDS const u32*)(T + 1028 + 4 * lane);
      o[0] = o[1] = dup_lo(w0); o[2] = w0; o[3] = o[4] = o[5] = dup_hi(w0); o[6] = o[7] = dup_lo(w1);
    }
    o[8] = (u32)__builtin_amdgcn_update_dpp((int)o[8], (int)o[0], 0x130, 0xf, 0xf, false);
    o[9] = (u32)__builtin_amdgcn_update_dpp((int)o[9], (int)o[1], 0x130, 0xf, 0xf, false);
    u32 a[9];
#pragma unroll
    for (int i = 0; i < 9; ++i) a[i] = hi_lo(o[i], o[i + 1]);
    LDS char* xb = smem + S_X + ch * 4 * S_CPY + 32 + 2 * (1024 * hh + 16 * lane);
    *(LDS u32x4*)(xb) = u32x4{o[0], o[1], o[2], o[3]};
    *(LDS u32x4*)(xb + 16) = u32x4{o[4], o[5], o[6], o[7]};
    *(LDS u32x4*)(xb + S_CPY) = u32x4{a[0], a[1], a[2], a[3]};
    *(LDS u32x4*)(xb + S_CPY + 16) = u32x4{a[4], a[5], a[6], a[7]};
    *(LDS u32x4*)(xb + 2 * S_CPY) = u32x4{o[1], o[2], o[3], o[4]};
    *(LDS u32x4*)(xb + 2 * S_CPY + 16) = u32x4{o[5], o[6], o[7], o[8]};
    *(LDS u32x4*)(xb + 3 * S_CPY) = u32x4{a[1], a[2], a[3], a[4]};
    *(LDS u32x4*)(xb + 3 * S_CPY + 16) = u32x4{a[5], a[6], a[7], a[8]};
    if (hh == 0 && lane == 0) {
      LDS char* hb = smem + S_X + ch * 4 * S_CPY;
      const u32x4 z = {0, 0, 0, 0};
      *(LDS u32x4*)(hb) = z;
      *(LDS u32x4*)(hb + 16) = z;
      *(LDS u32x4*)(hb + S_CPY) = z;
      *(LDS u32x4*)(hb + S_CPY + 16) = u32x4{0, 0, 0, o[0] << 16};
      *(LDS u32x4*)(hb + 2 * S_CPY) = z;
      *(LDS u32x4*)(hb + 2 * S_CPY + 16) = u32x4{0, 0, 0, o[0]};
      *(LDS u32x4*)(hb + 3 * S_CPY) = z;
      *(LDS u32x4*)(hb + 3 * S_CPY + 16) = u32x4{0, 0, o[0] << 16, a[0]};
    }
    if (tid < 6 * 32) *(LDS u32x4*)(smem + S_Z + (tid >> 5) * S_ZSZ + (tid & 31) * 16) = u32x4{0, 0, 0, 0};
    STAMP(0, 6);
    LDS char* z = smem + S_Z + ch * S_ZSZ + 512 + (1024 * hh + 8 * lane) * 2;
    *(LDS u32x4*)z = uv[0];
    *(LDS u32x4*)(z + 1024) = uv[1];
  }
  bar();
  STAMP(0, 7);
  f32x4 acc[8];
#pragma unroll
  for (int i = 0; i < 8; ++i) acc[i] = f32x4{0, 0, 0, 0};
  {
    const int kq = q, qq = b >> 2, pp = b & 3;
    LDS const char* fb = smem + S_X + (ch * 4 + ((4 - qq) & 3)) * S_CPY +
                         2 * (16 + 16 * (kq >> 1) - 8 * (kq & 1) + 4 * pp - (qq ? 4 : 0)) + 64 * hh;
    LDS const char* gb = smem + S_Z + ch * S_ZSZ + 512 + 32 * (b - (kq >> 1)) + 16 * (kq & 1) - 64 * hh;
#pragma unroll 1
    for (int it = 0; it < 4; ++it) {
      bf16x8 F[8], G[8];
#pragma unroll
      for (int s = 0; s < 8; ++s) {
        F[s] = tr_frag(fb + 512 * s, fb + 512 * s - 8);
        G[s] = lds_frag(gb + 512 * s);
      }
      __builtin_amdgcn_sched_barrier(0);
#pragma unroll
      for (int t = 0; t < 8; ++t)
#pragma unroll
        for (int s = 0; s <= t; ++s) acc[t] = mfma16(F[s], G[t - s], acc[t]);
      fb += 128;
      gb -= 128;
    }
  }
  __builtin_amdgcn_sched_barrier(0);
  STAMP(0, 8);
  bar();
  {
    LDS char* ex = smem + S_X + 49152 + w * 4096 + lane * 16;
#pragma unroll
    for (int i = 0; i < 4; ++i) {
      const f32x4 lo = acc[i], hi = acc[4 + i];
      *(LDS f32x4*)(ex + i * 1024) = hh ? +lo : +hi;
    }
    if (tid == 0) *(LDS u32*)(smem + S_CNT) = 0;
  }
  bar();
  u32x2 yo[4];
  {
    LDS const char* ey = smem + S_X + 49152 + (w ^ 1) * 4096 + lane * 16;
#pragma unroll
    for (int i = 0; i < 4; ++i) {
      const f32x4 lo = acc[i], hi = acc[4 + i];
      const f32x4 y = (hh ? +hi : +lo) + *(LDS const f32x4*)(ey + i * 1024);
      yo[i] = u32x2{pack_bf16(y[0], y[1]), pack_bf16(y[2], y[3])};
    }
  }
  bar();
  {
    const __amdgpu_buffer_rsrc_t rs = mk_rsrc(p.ws + WS_AT, DM * SL * 2);
    const int dst = (d * SL + 1024 * hh + 16 * (lane & 15) + 4 * (lane >> 4)) * 2;
#pragma unroll
    for (int i = 0; i < 4; ++i) __builtin_amdgcn_raw_buffer_store_b64(yo[i], rs, dst + 512 * i, 0, SC1);
  }
  STAMP(0, 9);
}
DEV void s_signal(const P& p, LDS char* smem, int sg, int lane) {
  u32 old = 0;
  if (lane == 0) old = __hip_atomic_fetch_add((LDS u32*)(smem + S_CNT), 1u, __ATOMIC_RELAXED, __HIP_MEMORY_SCOPE_WORKGROUP);
  if (__builtin_amdgcn_readfirstlane(old) == 11) {
    const int lt = lane & 31, second = lane >> 5;
    const int dd = 6 * sg + (second ? 5 : 0);
    const int idx = dd < 480 ? dd / 96 : 5 + (dd - 480) / 32;
    const int dfirst = idx < 5 ? 96 * idx : 480 + 32 * (idx - 5);
    __hip_atomic_store((GLB u64*)(p.ws + WS_IN) + tile_block(lt, idx) * 64 + IN_A + (sg - dfirst / 6), FLAG_SET, RLX_AGENT);
  }
}

DEV void role_gate(const P& p, int rb, int w, int lane, u64 pc) {
  u32 pf0 = 0, pf1 = 0;
  const int tid = w * 64 + lane;
  if (tid < PF_CODE) pf0 = *(const GLB u32*)((pc & ~255ull) + tid * 128);
  const int sub = lane & 7;
  u32x4 wv[9];
#pragma unroll
  for (int i = 0; i < 9; ++i) wv[i] = *(const u32x4*)((const char*)p.gw + (i * 8 + sub) * 16);
  for (int r = w * 8 + (lane >> 3); r < 128; r += 96) {
    const int l = rb * 128 + r;
    float s = 0.f;
    u32x4 xv[9];
#pragma unroll
    for (int i = 0; i < 9; ++i) xv[i] = *(const u32x4*)((const char*)p.x + (size_t)l * 1152 + (i * 8 + sub) * 16);
#pragma unroll
    for (int i = 0; i < 9; ++i) {
#pragma unroll
      for (int k = 0; k < 4; ++k) {
        const u32 pr = pack_bf16(bflo(xv[i][k]) * bflo(wv[i][k]), bfhi(xv[i][k]) * bfhi(wv[i][k]));
        s += bflo(pr);
        s += bfhi(pr);
      }
    }
    s += __shfl_xor(s, 1); s += __shfl_xor(s, 2); s += __shfl_xor(s, 4);
    const float gv = rbf(1.0f / (1.0f + expf(-rbf(s))));
    if (sub == 0)
      __hip_atomic_store((GLB u64*)(p.ws + WS_G) + l, ((u64)__float_as_uint(rbf(1.0f - gv)) << 32) | __float_as_uint(gv), RLX_AGENT);
  }
  asm volatile("" :: "v"(pf0), "v"(pf1));
  wait_vm<0>();
  bar();
  if (w == 0 && lane < 16)
    __hip_atomic_store((GLB u64*)(p.ws + WS_IN) + tile_block(2 * rb + (lane >> 3), lane & 7) * 64 + IN_G, FLAG_SET, RLX_AGENT);
}

constexpr int H_C = 32768, H_CS = 12288;
DEV int h_slot(int i) { return i < 4 ? i * 8192 : 131072 + (i - 4) * 8192; }
constexpr int H_AT = H_C + 2 * H_CS;
constexpr int H_XT = H_C + 3 * H_CS;
constexpr int H_G = H_C + 4 * H_CS;
constexpr int H_OUT = H_C + 5 * H_CS;
constexpr int K_LDS = 163840;

constexpr int h_wait(int s, int NDT) {
  int n = 0;
  int need = 0;
  for (int k = 0; k < 4; ++k) { n += 2; if (k == s) need = n; }
  for (int t = 0; t < s; ++t) {
    if (t == 0) { n += 2; if (s == 4) need = n; n += 2; if (s == 5) need = n; }
    else if (t == 1) { n += 2; if (s == 6) need = n; n += 2; if (s == 7) need = n; }
    else if (t <= 11) { n += 2 + NDT; if (s == t + 6) need = n; if (t == 2) n += 1; }
    else if (t == 12 || t == 13) n += NDT;
    else if (t == 14) n += 1;
  }
  return s == 17 ? 0 : n - need;
}
template <int S, int NDT, class F> DEV void h_steps(F&& after, u64& fv, char* wsp) {
  if constexpr (S == 9) asm volatile("s_waitcnt vmcnt(%1)" : "+v"(fv) : "n"(h_wait(S, NDT)) : "memory");
  else wait_vm<h_wait(S, NDT)>();
  LSTAMP2(1, 16 + S);
  bar();
  after(std::integral_constant<int, S>{});
  if constexpr (S < 17) h_steps<S + 1, NDT>(after, fv, wsp);
}
DEV int p2_rot(int d0) { const int idx = d0 < 480 ? d0 / 96 : 5 + (d0 - 480) / 32; return (idx * 18) >> 3; }

template <int NDT, class F>
DEV void p2_loader(const P& p, LDS char* smem, int lt, int d0, int lw, int lane, bool scratch_barrier, F&& pre_poll) {
  __builtin_amdgcn_s_setprio(3);
  const char* hb = p.ws + WS_HB + (size_t)lt * 18 * 8192 + (2 * lw) * 1024 + lane * 16;
  constexpr int W = 32 * NDT;
  const int l0 = 64 * lt, rot = p2_rot(d0);
  u32 co[NDT];
#pragma unroll
  for (int i = 0; i < NDT; ++i) {
    const int row = 8 * (NDT * lw + i) + (lane >> 3), cp = lane & 7;
    co[i] = (u32)(d0 + row) * 1152u + ((cp ^ (row & 7)) << 4);
  }
  auto issue_c = [&](int s) {
    LDS char* buf = smem + H_C + (s & 7) * H_CS + NDT * lw * 1024;
    int ks = s + rot; ks = ks >= 18 ? ks - 18 : ks;
    const char* cb = ks < 9 ? (const char*)p.cre + ks * 128 : (const char*)p.cim + (ks - 9) * 128;
#pragma unroll
    for (int i = 0; i < NDT; ++i) dma16<0>(cb + co[i], buf + i * 1024);
  };
  auto issue_h = [&](int s) {
    LDS char* buf = smem + h_slot(s & 7) + 2 * lw * 1024;
    int ks = s + rot; ks = ks >= 18 ? ks - 18 : ks;
#pragma unroll
    for (int i = 0; i < 2; ++i) dma16<SC1>(hb + ks * 8192 + i * 1024, buf + i * 1024);
  };
  GLB u64* inb = (GLB u64*)(p.ws + WS_IN) + blockIdx.x * 64;
  const int nA = (d0 + W - 1) / 6 - d0 / 6 + 1;
  const bool need = lane == IN_G || (lane >= IN_A && lane < IN_A + nA);
  if (scratch_barrier) bar();
  else {
#pragma unroll 1
    for (int s = 0; s < 8; ++s) issue_c(s);
  }
  pre_poll();
  { [[maybe_unused]] const int w = lw + 8; STAMP(1, 1); }
  wait_flags(inb + IN_H + (lane < 36 ? lane : 0), true);
  { [[maybe_unused]] const int w = lw + 8; STAMP(1, 2); }
#pragma unroll
  for (int s = 0; s < 4; ++s) issue_h(s);
  { [[maybe_unused]] const int w = lw + 8; STAMP(1, 3); }
  u64 fv = 0;
  h_steps<0, NDT>([&](auto S_) {
    constexpr int S = decltype(S_)::value;
    if constexpr (S == 0) { issue_h(4); issue_h(5); }
    if constexpr (S == 1) { issue_h(6); issue_h(7); }
    if constexpr (S >= 2 && S <= 11) { issue_h(S + 6); issue_c(S + 6); }
    if constexpr (S == 2)
      asm volatile("global_load_dwordx2 %0, %1, off sc1" : "=v"(fv) : "v"(inb + lane) : "memory");
    if constexpr (S == 9) { if (!__all(!need || fv == FLAG_SET)) wait_flags(inb + lane, need); }
    if constexpr (S == 12) {
#pragma unroll
      for (int i = 0; i < NDT; ++i) {
        const int pc = NDT * lw + i;
        dma16<SC1>(p.ws + WS_AT + (size_t)(d0 + 8 * pc + (lane >> 3)) * 4096 + l0 * 2 + (lane & 7) * 16, smem + H_AT + pc * 1024);
      }
    }
    if constexpr (S == 13) {
#pragma unroll
      for (int i = 0; i < NDT; ++i) {
        const int pc = NDT * lw + i, id = pc * 64 + lane, row = id / (4 * NDT), qq = id - row * (4 * NDT);
        dma16<0>((const char*)p.x + (size_t)(l0 + row) * 1152 + d0 * 2 + qq * 16, smem + H_XT + pc * 1024);
      }
    }
    if constexpr (S == 14) { if (lane < 8) dma16<SC1>(p.ws + WS_G + (size_t)l0 * 8 + lw * 128 + lane * 16, smem + H_G + lw * 128); }
  }, fv, p.ws);
}

template <int NDT, int STAG>
DEV void p2_compute(const P& p, LDS char* smem, int lt0, int d0, int w, int lane) {
  const int lh = (w >> 1) & 1, kh = w & 1, dg = w >> 2;
  const int r = lane & 15, kq = lane >> 4;
  const int lt = 2 * lh + kh, l0 = 64 * lt0;
  constexpr int W = 32 * NDT;
  u32x2 da[NDT], db[NDT];
#pragma unroll
  for (int t = 0; t < NDT; ++t) {
    const int d = d0 + 16 * (NDT * dg + t) + 4 * kq;
    da[t] = *(const u32x2*)(p.dskA + d);
    db[t] = *(const u32x2*)(p.dskB + d);
  }
  GLB u64* inb = (GLB u64*)(p.ws + WS_IN) + blockIdx.x * 64;

  f32x4 acc[2][NDT];
#pragma unroll
  for (int i = 0; i < 2; ++i)
#pragma unroll
    for (int t = 0; t < NDT; ++t) acc[i][t] = f32x4{0, 0, 0, 0};
  int ho[2];
#pragma unroll
  for (int u = 0; u < 2; ++u) {
    const int k = 32 * kh + 8 * kq + 4 * u + (r >> 2), i = (r & 3) >> 1;
    ho[u] = (2 * (k >> 4) + i) * 1024 + (32 * lh + ((k & 15) ^ (4 * i))) * 16 + (r & 1) * 8;
  }
  const int co = H_C + (16 * NDT * dg + r) * 128 + (((4 * kh + kq) ^ (r & 7)) << 4);
  bf16x8 hb[2], ca[NDT];
#pragma unroll
  for (int i = 0; i < 2; ++i) hb[i] = bf16x8{};
#pragma unroll
  for (int t = 0; t < NDT; ++t) ca[t] = bf16x8{};
  auto mma = [&]() {
#pragma unroll
    for (int t = 0; t < NDT; ++t) {
      acc[0][t] = mfma16(ca[t], hb[0], acc[0][t]);
      acc[1][t] = mfma16(ca[t], hb[1], acc[1][t]);
    }
  };
  STAMP(1, 1);
#pragma unroll 1
  for (int s = 0; s < 18; ++s) {
    bar();
    if (s == 0) STAMP(1, 2);
    LSTAMP(1, 16 + s);
    if (STAG) { mma(); __builtin_amdgcn_sched_barrier(0); }
    LDS const char* hbuf = smem + h_slot(s & 7);
    LDS const char* cbuf = smem + (s & 7) * H_CS;
#pragma unroll
    for (int i = 0; i < 2; ++i) hb[i] = tr_frag(hbuf + ho[0] + i * 256, hbuf + ho[1] + i * 256);
#pragma unroll
    for (int t = 0; t < NDT; ++t) ca[t] = lds_frag(cbuf + co + t * 2048);
    __builtin_amdgcn_sched_barrier(0);
    if (!STAG) { mma(); __builtin_amdgcn_sched_barrier(0); }
  }
  if (STAG) mma();
  __builtin_amdgcn_sched_barrier(0);
  STAMP(1, 3);
  bar();
  if (w == 0) __hip_atomic_store(inb + lane, 0ull, RLX_AGENT);
  LDS char* ex = smem + w * (NDT * 1024) + lane * 16;
#pragma unroll
  for (int t = 0; t < NDT; ++t) {
    const f32x4 a0 = acc[0][t], a1 = acc[1][t];
    *(LDS f32x4*)(ex + t * 1024) = kh ? +a0 : +a1;
  }
  bar();
  LDS const char* ey = smem + (w ^ 1) * (NDT * 1024) + lane * 16;
  constexpr int OSTR = W * 4 + 16;
  const f32x2 gg = *(LDS const f32x2*)(smem + H_G + (16 * lt + r) * 8);
#pragma unroll
  for (int t = 0; t < NDT; ++t) {
    const int dt = NDT * dg + t;
    const f32x4 o = *(LDS const f32x4*)(ey + t * 1024);
    const f32x4 a0 = acc[0][t], a1 = acc[1][t];
    const f32x4 y = (kh ? +a1 : +a0) + o;
    const s16x4 av = tr_read(smem + H_AT + (16 * dt + 4 * kq + (r >> 2)) * 128 + lt * 32 + (r & 3) * 8);
    const u32x2 xv = *(LDS const u32x2*)(smem + H_XT + (16 * lt + r) * (W * 2) + (16 * dt + 4 * kq) * 2);
    f32x4 res;
#pragma unroll
    for (int i = 0; i < 4; ++i) {
      const u32 xw = xv[i >> 1], aw = da[t][i >> 1], bw = db[t][i >> 1];
      const float xf = (i & 1) ? bfhi(xw) : bflo(xw);
      const float sa = (i & 1) ? bfhi(aw) : bflo(aw);
      const float sb = (i & 1) ? bfhi(bw) : bflo(bw);
      const float yc = bf2f((u16)av[i]);
      const float a = rbf(yc + rbf(xf * sa));
      const float b = y[i] + rbf(xf * sb);
      res[i] = rbf(gg[0] * a) + gg[1] * b;
    }
    *(LDS f32x4*)(smem + H_OUT + (16 * lt + r) * OSTR + (16 * dt + 4 * kq) * 4) = res;
  }
  STAMP(1, 4);
  bar();
#pragma unroll
  for (int it = 0; it < NDT; ++it) {
    const int id = it * 512 + w * 64 + lane;
    const int row = id / (8 * NDT), pc = id - row * (8 * NDT);
    *(f32x4*)(p.out + (size_t)(l0 + row) * DM + d0 + pc * 4) = *(LDS const f32x4*)(smem + H_OUT + row * OSTR + pc * 16);
  }
}

template <int NDT>
DEV void phase2(const P& p, LDS char* smem, int lt, int d0, int w, int lane, bool scratch_barrier, int sg) {
  STAMP(1, 0);
  if (w >= 8) {
    p2_loader<NDT>(p, smem, lt, d0, w - 8, lane, scratch_barrier, [&]() { if (NDT == 1) { wait_vm<8>(); s_signal(p, smem, sg, lane); } });
  } else {
    if (NDT == 1) { wait_vm<0>(); s_signal(p, smem, sg, lane); }
    if (w < 4) p2_compute<NDT, 0>(p, smem, lt, d0, w, lane);
    else p2_compute<NDT, 1>(p, smem, lt, d0, w, lane);
  }
  STAMP(1, 15);
}

__global__ __launch_bounds__(768, 3) void gated_mix_s4d_s5(P p) {
  extern __shared__ __attribute__((aligned(16))) char smem_raw[];
  LDS char* smem = (LDS char*)smem_raw;
  u64 pc;
  asm volatile("s_getpc_b64 %0" : "=s"(pc));
  const int w = __builtin_amdgcn_readfirstlane(threadIdx.x >> 6), lane = threadIdx.x & 63;
  const int xcd = blockIdx.x & 7, j = blockIdx.x >> 3;
  STAMP(0, 0);
  if (j >= 18 && j < 30) {
    const int sg = 12 * xcd + (j - 18);
    role_s(p, smem, sg, w, lane);
    STAMP(0, 15);
    phase2<1>(p, smem, 4 * xcd + (j - 18) / 3, 480 + 32 * ((j - 18) % 3), w, lane, false, sg);
  } else {
    const bool isg = j < 18;
    if (isg) {
      const int c = 2 * xcd + j / 9, g = j % 9;
      if (w >= 8) g_loader(p, smem, c, g, 96 * (j % 5), w - 8, lane);
      else if (w < 4) g_compute<0>(p, smem, c, g, w, lane);
      else g_compute<1>(p, smem, c, g, w, lane);
    } else {
      role_gate(p, 2 * xcd + (j - 30), w, lane, pc);
    }
    STAMP(0, 15);
    const int e = isg ? j : j - 12;
    phase2<3>(p, smem, 4 * xcd + e / 5, 96 * (e % 5), w, lane, isg, 0);
  }
}

extern "C" void kernel_launch(void* const* d_in, const int* in_sizes, int n_in, void* d_out, int out_size,
                              void* d_ws, size_t ws_size, hipStream_t stream) {
  P a;
  a.x = (const u16*)d_in[0]; a.la = (const u16*)d_in[1]; a.aim = (const u16*)d_in[2];
  a.creA = (const u16*)d_in[3]; a.cimA = (const u16*)d_in[4]; a.ldtA = (const u16*)d_in[5]; a.dskA = (const u16*)d_in[6];
  a.lre = (const u16*)d_in[8]; a.lim = (const u16*)d_in[9]; a.bre = (const u16*)d_in[10]; a.bim = (const u16*)d_in[11];
  a.cre = (const u16*)d_in[12]; a.cim = (const u16*)d_in[13]; a.ldtB = (const u16*)d_in[14];
  a.dskB = (const u16*)d_in[15]; a.gw = (const u16*)d_in[16];
  a.ws = (char*)d_ws; a.out = (float*)d_out;
  (void)hipFuncSetAttribute((const void*)gated_mix_s4d_s5, hipFuncAttributeMaxDynamicSharedMemorySize, K_LDS);
  gated_mix_s4d_s5<<<256, 768, K_LDS, stream>>>(a);
}
```

```cpp
#define WARM_B 1
#define WARM_C 1
#include <hip/hip_runtime.h>
#include <hip/hip_bf16.h>
#include <stdint.h>
#include <type_traits>

typedef unsigned short u16;
typedef unsigned int u32;
typedef unsigned long long u64;
typedef __attribute__((ext_vector_type(8))) __bf16 bf16x8;
typedef __attribute__((ext_vector_type(2))) __bf16 bf16x2;
typedef __attribute__((ext_vector_type(4))) short s16x4;
typedef __attribute__((ext_vector_type(4))) float f32x4;
typedef __attribute__((ext_vector_type(2))) float f32x2;
typedef __attribute__((ext_vector_type(4))) unsigned u32x4;
typedef __attribute__((ext_vector_type(2))) unsigned u32x2;
#define LDS __attribute__((address_space(3)))
#define GLB __attribute__((address_space(1)))
#define DEV __device__ __forceinline__
#define RLX_AGENT __ATOMIC_RELAXED, __HIP_MEMORY_SCOPE_AGENT

constexpr int SL = 2048;
constexpr int DM = 576;

constexpr size_t WS_HB = 0;
constexpr size_t WS_AT = WS_HB + (size_t)2 * DM * SL * 2;
constexpr size_t WS_G = WS_AT + (size_t)DM * SL * 2;
constexpr size_t WS_GRAN = WS_G + (size_t)SL * 8;
constexpr size_t WS_EP = WS_GRAN + (size_t)9 * 16 * 2 * 64 * 8;
constexpr size_t WS_FL = WS_EP + 4096;
constexpr size_t WS_XT = WS_FL + (size_t)16 * DM * 8;
constexpr size_t WS_IN = WS_XT + (size_t)DM * SL * 2;
constexpr u64 FLAG_SET = 0x9E3779B97F4A7C15ull;
constexpr u32 SPIN_CAP = 1u << 17;
constexpr int IN_H = 0, IN_G = 36, IN_A = 40;

constexpr size_t WS_ST = (size_t)16 << 20;
#define STAMP(kern, idx) do {} while (0)
#define CSTAMP(kern, idx) do {} while (0)
#define LSTAMP(kern, idx) do {} while (0)
#define LSTAMP2(kern, idx) do {} while (0)

DEV float bf2f(u16 v) { return __uint_as_float((u32)v << 16); }
DEV float bflo(u32 v) { return __uint_as_float(v << 16); }
DEV float bfhi(u32 v) { return __uint_as_float(v & 0xffff0000u); }
DEV u32 pack_bf16(float lo, float hi) {
  f32x2 v = {lo, hi};
  bf16x2 b = __builtin_convertvector(v, bf16x2);
  return __builtin_bit_cast(u32, b);
}
DEV float rbf(float x) { return bflo(pack_bf16(x, x)); }
template <int N> DEV void wait_vm() { asm volatile("s_waitcnt vmcnt(%0)" ::"n"(N) : "memory"); }
DEV void bar() { asm volatile("s_waitcnt lgkmcnt(0)\n\ts_barrier" ::: "memory"); }
DEV void wave_fence() { __builtin_amdgcn_fence(__ATOMIC_ACQ_REL, "wavefront"); }
template <int AUX> DEV void dma16(const void* src, LDS char* dst) {
  __builtin_amdgcn_global_load_lds((const GLB u32*)src, (LDS u32*)dst, 16, 0, AUX);
}
constexpr int SC1 = 16;
#define XT_WAIT 2
DEV bf16x8 lds_frag(LDS const char* p) { return *(LDS const bf16x8*)p; }
DEV s16x4 tr_read(LDS const char* a) { return __builtin_amdgcn_ds_read_tr16_b64_v4i16((LDS s16x4*)a); }
DEV bf16x8 tr_frag(LDS const char* a, LDS const char* b) {
  const s16x4 lo = tr_read(a), hi = tr_read(b);
  return __builtin_bit_cast(bf16x8, __builtin_shufflevector(lo, hi, 0, 1, 2, 3, 4, 5, 6, 7));
}
DEV f32x4 mfma16(bf16x8 a, bf16x8 b, f32x4 c) { return __builtin_amdgcn_mfma_f32_16x16x32_bf16(a, b, c, 0, 0, 0); }
DEV __amdgpu_buffer_rsrc_t mk_rsrc(const void* base, int bytes) {
  return __builtin_amdgcn_make_buffer_rsrc(const_cast<void*>(base), (short)0, bytes, 0x00020000);
}
struct cf { float r, i; };
DEV cf cmul(cf a, cf b) { return {a.r * b.r - a.i * b.i, a.r * b.i + a.i * b.r}; }
DEV cf cfma(cf a, cf b, cf c) { return {fmaf(a.r, b.r, fmaf(-a.i, b.i, c.r)), fmaf(a.r, b.i, fmaf(a.i, b.r, c.i))}; }
typedef f32x2 c2;
DEV c2 cm2(c2 a, c2 b) {
  c2 t, r;
  asm("v_pk_mul_f32 %0, %1, %2 op_sel:[0,0] op_sel_hi:[0,1]" : "=v"(t) : "v"(a), "v"(b));
  asm("v_pk_fma_f32 %0, %1, %2, %3 op_sel:[1,1,0] op_sel_hi:[1,0,1] neg_lo:[1,0,0]" : "=v"(r) : "v"(a), "v"(b), "v"(t));
  return r;
}
DEV f32x2 fma2(f32x2 a, f32x2 b, f32x2 c) { return __builtin_elementwise_fma(a, b, c); }
DEV f32x2 lo2(f32x4 v) { return __builtin_shufflevector(v, v, 0, 1); }
DEV f32x2 hi2(f32x4 v) { return __builtin_shufflevector(v, v, 2, 3); }
DEV f32x4 cat2(f32x2 a, f32x2 b) { return __builtin_shufflevector(a, b, 0, 1, 2, 3); }
DEV void wait_flags(GLB u64* word, bool need) {
  for (u32 spin = 0;; ++spin) {
    const u64 v = __hip_atomic_load(word, RLX_AGENT);
    if (__all(!need || v == FLAG_SET) || spin > SPIN_CAP) break;
    __builtin_amdgcn_s_sleep(2);
  }
}
DEV int tile_block(int lt, int idx) {
  const int xcd = lt >> 2, a = lt & 3;
  int j;
  if (idx < 5) { const int e = 5 * a + idx; j = e < 18 ? e : e + 12; }
  else j = 18 + 3 * a + (idx - 5);
  return (j << 3) | xcd;
}

struct P {
  const u16 *x, *la, *aim, *creA, *cimA, *ldtA, *dskA;
  const u16 *lre, *lim, *bre, *bim, *cre, *cim, *ldtB, *dskB, *gw;
  char* ws;
  float* out;
};

constexpr int G_NB = 4, G_STAGE = 32768;
constexpr int G_E = G_NB * G_STAGE;
constexpr int G_PF = G_E + 4096;
constexpr int G_L = G_PF + 4096;
constexpr int G_X = G_L + 1024;
constexpr int G_T = G_X + 2048;
constexpr int G_END = G_T + 4 * 32 * 144;

DEV void g_loader(const P& p, LDS char* smem, int c, int g, int d0, int lw, int lane) {
  [[maybe_unused]] const int w = lw + 8;
  __builtin_amdgcn_s_setprio(3);
  GLB u32* ep = (GLB u32*)(p.ws + WS_EP + g * 256);
  GLB u64* gran = (GLB u64*)(p.ws + WS_GRAN) + (size_t)g * 16 * 128;
  const u32 tag = __hip_atomic_load(ep, RLX_AGENT) + 1u;
  u32 xo[4], bo[4];
#pragma unroll
  for (int i = 0; i < 4; ++i) {
    const int rho = 8 * (4 * lw + i) + (lane >> 3);
    const int hf = rho >> 6, m = (rho >> 4) & 3, r = rho & 15;
    const int tau = 64 * hf + 16 * (r >> 2) + 8 * (r & 1) + 2 * m + ((r >> 1) & 1);
    const int ch = (lane & 7) ^ (rho & 7);
    xo[i] = (u32)(128 * c + tau) * 1152u + ch * 16;
    const int pp = 64 * g + 16 * (rho >> 5) + r;
    bo[i] = (u32)pp * 1152u + ch * 16;
  }
  auto issue = [&](int s) {
    LDS char* buf = smem + (s & (G_NB - 1)) * G_STAGE + lw * 4096;
    int kb = g + s; kb = (kb >= 9 ? kb - 9 : kb) * 128;
#pragma unroll
    for (int i = 0; i < 4; ++i) dma16<0>((const char*)p.x + (xo[i] + kb), buf + i * 1024);
#pragma unroll
    for (int i = 0; i < 4; ++i) {
      const char* base = (const char*)((((4 * lw + i) >> 1) & 1) ? p.bim : p.bre);
      dma16<0>(base + (bo[i] + kb), buf + 16384 + i * 1024);
    }
  };
#pragma unroll
  for (int s = 0; s < G_NB - 1; ++s) issue(s);
#pragma unroll 1
  for (int s = 0; s < 6; ++s) { wait_vm<16>(); LSTAMP(0, 16 + s); bar(); issue(s + G_NB - 1); LSTAMP(0, 32 + s); }
  wait_vm<16>(); bar();
  wait_vm<8>(); bar();
  wait_vm<0>(); bar();
  STAMP(0, 1);
  if (lw < 2) {
    bar();
    u32 co[6];
#pragma unroll
    for (int i = 0; i < 6; ++i) {
      const int row = 8 * (6 * lw + i) + (lane >> 3), cp = lane & 7;
      co[i] = (u32)(d0 + row) * 1152u + ((cp ^ (row & 7)) << 4);
    }
    const int rot = ((d0 / 96) * 18) >> 3;
#pragma unroll 1
    for (int s = 0; s < 8; ++s) {
      LDS char* buf = smem + 32768 + s * 12288 + 6 * lw * 1024;
      const int ks = s + rot;
      const char* cb = ks < 9 ? (const char*)p.cre + ks * 128 : (const char*)p.cim + (ks - 9) * 128;
#pragma unroll
      for (int i = 0; i < 6; ++i) dma16<0>(cb + co[i], buf + i * 1024);
    }
    bar();
    return;
  }
  bar();
  STAMP(0, 2);
  if (lw == 3) {
    const f32x2 lv = *(LDS const f32x2*)(smem + G_L + lane * 8);
    const cf l16 = {lv[0], lv[1]};
    f32x2 ev[8];
#pragma unroll
    for (int s = 0; s < 8; ++s) ev[s] = *(LDS const f32x2*)(smem + G_E + (s * 64 + lane) * 8);
    cf tot = {0.f, 0.f};
    cf pre[8];
#pragma unroll
    for (int s = 0; s < 8; ++s) { pre[s] = tot; tot = cfma(l16, tot, cf{ev[s][0], ev[s][1]}); }
    if (c < 15) {
      __hip_atomic_store(gran + c * 128 + lane, ((u64)tag << 32) | __float_as_uint(tot.r), RLX_AGENT);
      __hip_atomic_store(gran + c * 128 + 64 + lane, ((u64)tag << 32) | __float_as_uint(tot.i), RLX_AGENT);
    }
#pragma unroll
    for (int s = 0; s < 8; ++s) *(LDS f32x2*)(smem + G_PF + (s * 64 + lane) * 8) = f32x2{pre[s].r, pre[s].i};
  }
  const int par = lw - 2;
  const f32x2 lv = *(LDS const f32x2*)(smem + G_L + 512 + lane * 8);
  const cf l128 = {lv[0], lv[1]};
  const cf l256 = cmul(l128, l128);
  cf acc = {0.f, 0.f};
  if (par < c) {
    const int nc = (c - par + 1) >> 1;
    u64 a[8], b[8];
    for (u32 spin = 0;; ++spin) {
      bool ok = true;
#pragma unroll
      for (int i = 0; i < 8; ++i) {
        const int cc = par + 2 * (i < nc ? i : 0);
        a[i] = __hip_atomic_load(gran + cc * 128 + lane, RLX_AGENT);
        b[i] = __hip_atomic_load(gran + cc * 128 + 64 + lane, RLX_AGENT);
      }
#pragma unroll
      for (int i = 0; i < 8; ++i) ok = ok && (u32)(a[i] >> 32) == tag && (u32)(b[i] >> 32) == tag;
      if (__all(ok) || spin > SPIN_CAP) break;
      __builtin_amdgcn_s_sleep(1);
    }
#pragma unroll
    for (int i = 0; i < 8; ++i)
      if (i < nc) acc = cfma(l256, acc, cf{__uint_as_float((u32)a[i]), __uint_as_float((u32)b[i])});
    if (c - 1 - (par + 2 * (nc - 1))) acc = cmul(acc, l128);
  }
  *(LDS f32x2*)(smem + G_X + (par * 64 + lane) * 8) = f32x2{acc.r, acc.i};
  STAMP(0, 3);
  bar();
  if (c == 15 && lw == 2 && lane == 0) __hip_atomic_store(ep, tag, RLX_AGENT);
}

template <int HF>
DEV void g_compute(const P& p, LDS char* smem, int c, int g, int w, int lane) {
  const int pq = w & 3, col = lane & 15, q = lane >> 4;
  const int pl = 16 * pq + col, pp = 64 * g + pl;
  cf lam, coef2;
  {
    const float lre = bf2f(p.lre[pp]), lim = bf2f(p.lim[pp]);
    const float dt = rbf(expf(bf2f(p.ldtB[pp])));
    const float zr = dt * lre, zi = dt * lim;
    const float er = expf(zr);
    float sn, cs;
    sincosf(zi, &sn, &cs);
    lam = {er * cs, er * sn};
    const float nr = lam.r - 1.0f, ni = lam.i;
    const float den = 1.0f / (lre * lre + lim * lim);
    coef2 = {2.0f * (nr * lre + ni * lim) * den, 2.0f * (ni * lre - nr * lim) * den};
  }
  cf l8 = cmul(lam, lam); l8 = cmul(l8, l8); l8 = cmul(l8, l8);
  const cf l16 = cmul(l8, l8);
  const cf l32 = cmul(l16, l16), l64 = cmul(l32, l32);
  if (HF == 0 && q == 0) {
    const cf l128 = cmul(l64, l64);
    *(LDS f32x2*)(smem + G_L + pl * 8) = f32x2{l16.r, l16.i};
    *(LDS f32x2*)(smem + G_L + 512 + pl * 8) = f32x2{l128.r, l128.i};
  }
  cf pw = HF ? l64 : cf{1.f, 0.f};
  { const cf t = cmul(pw, l16); if (q & 1) pw = t; }
  { const cf t = cmul(pw, l32); if (q & 2) pw = t; }
  u32 wb[3] = {0, 0, 0};
  if (HF == 0) {
#pragma unroll
    for (int i = 0; i < 3; ++i) {
      const int ln = i * 256 + pq * 64 + lane;
      if (ln < 576) {
        const int row = ln / 9, kb = ln - row * 9;
        wb[i] = *(const GLB u32*)((const char*)((c & 1) ? p.bim : p.bre) + (size_t)(64 * g + row) * 1152 + kb * 128);
      }
    }
  }
  asm volatile("" : "+v"(lam.r), "+v"(lam.i), "+v"(coef2.r), "+v"(coef2.i), "+v"(l8.r), "+v"(l8.i), "+v"(pw.r), "+v"(pw.i));
  f32x4 are[4], aim[4];
#pragma unroll
  for (int m = 0; m < 4; ++m) { are[m] = f32x4{0, 0, 0, 0}; aim[m] = f32x4{0, 0, 0, 0}; }
  const int o0 = col * 128 + (((0 + q) ^ (col & 7)) << 4);
  const int o1 = col * 128 + (((4 + q) ^ (col & 7)) << 4);
  bf16x8 br[2], bi[2], a[2][4];
  auto read_frags = [&](int s) {
    LDS const char* buf = smem + (s & (G_NB - 1)) * G_STAGE;
    LDS const char* ab = buf + HF * 8192;
    LDS const char* bb = buf + 16384 + pq * 4096;
#pragma unroll
    for (int ks = 0; ks < 2; ++ks) {
      const int o = ks ? o1 : o0;
      br[ks] = lds_frag(bb + o); bi[ks] = lds_frag(bb + 2048 + o);
#pragma unroll
      for (int m = 0; m < 4; ++m) a[ks][m] = lds_frag(ab + m * 2048 + o);
    }
  };
  auto mma = [&]() {
#pragma unroll
    for (int ks = 0; ks < 2; ++ks)
#pragma unroll
      for (int m = 0; m < 4; ++m) {
        are[m] = mfma16(a[ks][m], br[ks], are[m]);
        aim[m] = mfma16(a[ks][m], bi[ks], aim[m]);
      }
  };
  STAMP(0, 1);
#pragma unroll
  for (int s = 0; s < 9; ++s) {
    if (HF == 1 && s == XT_WAIT) wait_vm<0>();
    bar();
    if (s == 0) { STAMP(0, 2); CSTAMP(0, 60); }
    if (HF == 1 && s == XT_WAIT && pq == 0)
      __hip_atomic_store((GLB u64*)(p.ws + WS_FL) + c * DM + 64 * g + lane, FLAG_SET, RLX_AGENT);
    if (HF == 1 && s > 0) { mma(); __builtin_amdgcn_sched_barrier(0); }
    if (HF == 1 && s == 0) {
      const int rr = 4 * q + (col >> 2);
      LDS const char* tb = smem + rr * 128 + (((2 * pq + ((col & 3) >> 1)) ^ (rr & 7)) << 4) + (col & 1) * 8;
      LDS char* sc = smem + G_T + pq * (32 * 144);
#pragma unroll
      for (int hf = 0; hf < 2; ++hf) {
        u32x2 t[4];
#pragma unroll
        for (int m = 0; m < 4; ++m) t[m] = __builtin_bit_cast(u32x2, tr_read(tb + hf * 8192 + m * 2048));
        u32x4 oa, ob;
#pragma unroll
        for (int m = 0; m < 4; ++m) {
          oa[m] = __builtin_amdgcn_perm(t[m][1], t[m][0], 0x05040100u);
          ob[m] = __builtin_amdgcn_perm(t[m][1], t[m][0], 0x07060302u);
        }
        *(LDS u32x4*)(sc + hf * (16 * 144) + col * 144 + q * 32) = oa;
        *(LDS u32x4*)(sc + hf * (16 * 144) + col * 144 + q * 32 + 16) = ob;
      }
      wave_fence();
      const __amdgpu_buffer_rsrc_t rs = mk_rsrc(p.ws + WS_XT, DM * SL * 2);
#pragma unroll
      for (int u = 0; u < 4; ++u) {
        const int row = (lane >> 3) + 8 * (u & 1), hf = u >> 1;
        const u32x4 v = *(LDS const u32x4*)(sc + hf * (16 * 144) + row * 144 + (lane & 7) * 16);
        __builtin_amdgcn_raw_buffer_store_b128(v, rs, ((64 * g + 16 * pq + row) * SL + 128 * c + 64 * hf) * 2 + (lane & 7) * 16, 0, SC1);
      }
      __builtin_amdgcn_sched_barrier(0);
    }
    read_frags(s);
    __builtin_amdgcn_sched_barrier(0);
    if (HF == 0) { mma(); __builtin_amdgcn_sched_barrier(0); }
  }
  if (HF == 1) mma();
  __builtin_amdgcn_sched_barrier(0);
  STAMP(0, 3); CSTAMP(0, 61);
  const f32x2 Lr = {lam.r, lam.r}, Li = {lam.i, lam.i};
  f32x2 er = {0.f, 0.f}, ei = {0.f, 0.f};
#pragma unroll
  for (int t = 0; t < 8; ++t) {
    const f32x2 br_ = (t & 1) ? hi2(are[t >> 1]) : lo2(are[t >> 1]);
    const f32x2 bi_ = (t & 1) ? hi2(aim[t >> 1]) : lo2(aim[t >> 1]);
    const f32x2 nr = fma2(-Li, ei, fma2(Lr, er, br_));
    ei = fma2(Li, er, fma2(Lr, ei, bi_));
    er = nr;
  }
  const cf eA = {er[0], ei[0]};
  const cf e = cfma(l8, eA, cf{er[1], ei[1]});
  const int seg = 4 * HF + q;
  *(LDS f32x2*)(smem + G_E + (seg * 64 + pl) * 8) = f32x2{e.r, e.i};
  asm volatile("" :: "v"(wb[0]), "v"(wb[1]), "v"(wb[2]));
  STAMP(0, 4);
  bar();
  STAMP(0, 5);
  {
#pragma unroll
    for (int m = 0; m < 4; ++m) asm volatile("" : "+v"(are[m]), "+v"(aim[m]));
    const f32x2 Cr = {coef2.r, coef2.r}, Ci = {coef2.i, coef2.i};
#pragma unroll
    for (int m = 0; m < 4; ++m) {
      const f32x2 r0 = lo2(are[m]), r1 = hi2(are[m]), i0 = lo2(aim[m]), i1 = hi2(aim[m]);
      are[m] = cat2(fma2(-Ci, i0, Cr * r0), fma2(-Ci, i1, Cr * r1));
      aim[m] = cat2(fma2(-Ci, r0, -Cr * i0), fma2(-Ci, r1, -Cr * i1));
      asm volatile("" : "+v"(are[m]), "+v"(aim[m]));
    }
  }
  cf eAc = cmul(coef2, eA), pwc = cmul(coef2, pw);
  asm volatile("" : "+v"(eAc.r), "+v"(eAc.i), "+v"(pwc.r), "+v"(pwc.i));
  STAMP(0, 7);
  bar();
  STAMP(0, 8);
  cf cin;
  {
    f32x2 v[2];
#pragma unroll
    for (int i = 0; i < 2; ++i) v[i] = *(LDS const f32x2*)(smem + G_X + (i * 64 + pl) * 8);
    const f32x2 pf = *(LDS const f32x2*)(smem + G_PF + (seg * 64 + pl) * 8);
    bar();
    const f32x2 S = v[0] + v[1];
    cin = cfma(pwc, cf{S[0], S[1]}, cmul(coef2, cf{pf[0], pf[1]}));
  }
  const cf cinB = cfma(l8, cin, eAc);
  er = f32x2{cin.r, cinB.r}; ei = f32x2{-cin.i, -cinB.i};
  f32x2 hr[8], hi[8];
#pragma unroll
  for (int t = 0; t < 8; ++t) {
    const f32x2 br_ = (t & 1) ? hi2(are[t >> 1]) : lo2(are[t >> 1]);
    const f32x2 bi_ = (t & 1) ? hi2(aim[t >> 1]) : lo2(aim[t >> 1]);
    const f32x2 nr = fma2(Li, ei, fma2(Lr, er, br_));
    ei = fma2(-Li, er, fma2(Lr, ei, bi_));
    er = nr;
    hr[t] = er; hi[t] = ei;
  }
  u32x4 o[4];
#pragma unroll
  for (int t = 0; t < 4; ++t) {
    o[0][t] = pack_bf16(hr[2 * t][0], hr[2 * t + 1][0]);
    o[1][t] = pack_bf16(hr[2 * t][1], hr[2 * t + 1][1]);
    o[2][t] = pack_bf16(hi[2 * t][0], hi[2 * t + 1][0]);
    o[3][t] = pack_bf16(hi[2 * t][1], hi[2 * t + 1][1]);
  }
  STAMP(0, 9);
  {
    const __amdgpu_buffer_rsrc_t rs = mk_rsrc(p.ws + WS_HB, 2 * DM * SL * 2);
    const int hb = (((c * 2 + HF) * 2) * 9 + g) * 8192 + pq * 2048;
    const int p0 = (16 * q + col) * 16, p1 = 1024 + (16 * q + (col ^ 4)) * 16;
    __builtin_amdgcn_raw_buffer_store_b128(o[0], rs, hb + p0, 0, SC1);
    __builtin_amdgcn_raw_buffer_store_b128(o[1], rs, hb + p1, 0, SC1);
    __builtin_amdgcn_raw_buffer_store_b128(o[2], rs, hb + 9 * 8192 + p0, 0, SC1);
    __builtin_amdgcn_raw_buffer_store_b128(o[3], rs, hb + 9 * 8192 + p1, 0, SC1);
    wait_vm<0>();
    STAMP(0, 10);
    if (lane < 8)
      __hip_atomic_store((GLB u64*)(p.ws + WS_IN) + tile_block(2 * c + HF, lane) * 64 + IN_H + g * 4 + pq, FLAG_SET, RLX_AGENT);
  }
}

constexpr int S_CPY = 4160;
constexpr int S_X = 0;
constexpr int S_ST = 6 * 4 * S_CPY;
constexpr int S_Z = S_ST;
constexpr int S_ZSZ = (256 + SL) * 2;
constexpr int S_T = S_ST + 12 * 4608;
constexpr int S_TSZ = 1312;
constexpr int S_END = S_T + 6 * S_TSZ;
constexpr int S_CNT = 32768 + 8192;

DEV c2 cexp_k(float zl, float th, float tl, float k) {
  const float mag = __builtin_amdgcn_exp2f(zl * k);
  const float t = fmaf(tl, k, __builtin_amdgcn_fractf(th * k));
  return c2{mag * __builtin_amdgcn_cosf(t), mag * __builtin_amdgcn_sinf(t)};
}
#define PKM(d, a, b) "v_pk_mul_f32 " d ", " a ", " b " op_sel:[0,0] op_sel_hi:[0,1]\n\t"
#define PKF(d, a, b) "v_pk_fma_f32 " d ", " a ", " b ", " d " op_sel:[1,1,0] op_sel_hi:[1,0,1] neg_lo:[1,0,0]\n\t"
DEV void octet(LDS char* dst, c2 s0, c2 m1, c2 m2) {
  c2 v1, v2, v3, t0, t1, t2, t3;
  asm(PKM("%0", "%7", "%8") PKM("%3", "%7", "%9") PKF("%0", "%7", "%8") PKF("%3", "%7", "%9")
      PKM("%1", "%0", "%8") PKM("%4", "%0", "%9") PKF("%1", "%0", "%8") PKF("%4", "%0", "%9")
      PKM("%2", "%1", "%8") PKM("%5", "%1", "%9") PKF("%2", "%1", "%8") PKF("%5", "%1", "%9")
      PKM("%6", "%2", "%9") PKF("%6", "%2", "%9")
      : "=&v"(v1), "=&v"(v2), "=&v"(v3), "=&v"(t0), "=&v"(t1), "=&v"(t2), "=&v"(t3) : "v"(s0), "v"(m1), "v"(m2));
  *(LDS u32*)(dst + 0 * 288) = pack_bf16(s0[0], s0[1]);
  *(LDS u32*)(dst + 1 * 288) = pack_bf16(v1[0], v1[1]);
  *(LDS u32*)(dst + 2 * 288) = pack_bf16(v2[0], v2[1]);
  *(LDS u32*)(dst + 3 * 288) = pack_bf16(v3[0], v3[1]);
  *(LDS u32*)(dst + 4 * 288) = pack_bf16(t0[0], t0[1]);
  *(LDS u32*)(dst + 5 * 288) = pack_bf16(t1[0], t1[1]);
  *(LDS u32*)(dst + 6 * 288) = pack_bf16(t2[0], t2[1]);
  *(LDS u32*)(dst + 7 * 288) = pack_bf16(t3[0], t3[1]);
}
DEV u32 dup_lo(u32 a) { return __builtin_amdgcn_perm(a, a, 0x01000100u); }
DEV u32 dup_hi(u32 a) { return __builtin_amdgcn_perm(a, a, 0x03020302u); }
DEV u32 hi_lo(u32 a, u32 b) { return __builtin_amdgcn_alignbit(b, a, 16); }

DEV void role_s(const P& p, LDS char* smem, int sg, int w, int lane) {
  const int tid = w * 64 + lane;
  const int ch = w >> 1, hh = w & 1, d = 6 * sg + ch;
  const int n = d * 128 + 64 * hh + lane;
  const u16 r_ldt = p.ldtA[d], r_la = p.la[n], r_aim = p.aim[n], r_cre = p.creA[n], r_cim = p.cimA[n];
  STAMP(0, 1);
  f32x4 tab[3];
  [[maybe_unused]] u32 wc[2] = {0, 0};
  LDS char* st = smem + S_ST + w * 4608;
  {
    const float L2E = 1.44269504088896341f;
    const float dt = rbf(__builtin_amdgcn_exp2f(bf2f(r_ldt) * L2E));
    const float ar = -rbf(__builtin_amdgcn_exp2f(bf2f(r_la) * L2E));
    const float zl = ar * dt * L2E, zi = bf2f(r_aim) * dt;
    const float th = zi * 0.15915494309189535f;
    const float tl = fmaf(zi, 0.15915494309189535f, -th) + zi * 4.6017384e-10f;
    const c2 C = {2.0f * (bf2f(r_cre) * dt), 2.0f * (bf2f(r_cim) * dt)};
    const c2 w1 = cexp_k(zl, th, tl, 1.f), w8 = cexp_k(zl, th, tl, 8.f), w128 = cexp_k(zl, th, tl, 128.f);
    c2 w2, w4, w16, w32, w64, w256, w512, w1024, w2048;
    asm(PKM("%0", "%9", "%9") PKM("%2", "%10", "%10") PKM("%5", "%11", "%11") PKF("%0", "%9", "%9") PKF("%2", "%10", "%10") PKF("%5", "%11", "%11")
        PKM("%1", "%0", "%0") PKM("%3", "%2", "%2") PKM("%6", "%5", "%5") PKF("%1", "%0", "%0") PKF("%3", "%2", "%2") PKF("%6", "%5", "%5")
        PKM("%4", "%3", "%3") PKM("%7", "%6", "%6") PKF("%4", "%3", "%3") PKF("%7", "%6", "%6")
        PKM("%8", "%7", "%7") PKF("%8", "%7", "%7")
        : "=&v"(w2), "=&v"(w4), "=&v"(w16), "=&v"(w32), "=&v"(w64), "=&v"(w256), "=&v"(w512), "=&v"(w1024), "=&v"(w2048)
        : "v"(w1), "v"(w8), "v"(w128));
    c2 C128, C256, C512, C1024, C2048;
    asm(PKM("%0", "%5", "%6") PKM("%1", "%5", "%7") PKM("%2", "%5", "%8") PKM("%3", "%5", "%9") PKM("%4", "%5", "%10")
        PKF("%0", "%5", "%6") PKF("%1", "%5", "%7") PKF("%2", "%5", "%8") PKF("%3", "%5", "%9") PKF("%4", "%5", "%10")
        : "=&v"(C128), "=&v"(C256), "=&v"(C512), "=&v"(C1024), "=&v"(C2048)
        : "v"(C), "v"(w128), "v"(w256), "v"(w512), "v"(w1024), "v"(w2048));
#pragma unroll
    for (int i = 0; i < 2; ++i) {
      const int ln = i * 768 + tid, all = (sg % 12) * 864 + ln;
      if (ln < 864) wc[i] = *(const GLB u32*)((const char*)(all < 5184 ? p.cre : p.cim) + (size_t)(all < 5184 ? all : all - 5184) * 128);
    }
    STAMP(0, 2);
    LDS char* wr = st + 4 * lane;
    LDS const char* rd = st + (lane & 15) * 288 + (lane >> 4) * 16;
    bf16x8 B[4];
    const c2 w8c = {w8[0], -w8[1]}, w32c = {w32[0], -w32[1]}, w64c = {w64[0], -w64[1]};
    octet(wr, c2{1.f, 0.f}, w8c, w32c);
    octet(wr + 8 * 288, w64c, w8c, w32c);
    wave_fence();
#pragma unroll
    for (int ks = 0; ks < 4; ++ks) B[ks] = lds_frag(rd + 64 * ks);
    wave_fence();
#pragma unroll
    for (int mt = 0; mt < 3; ++mt) {
      if (mt == 0) { octet(wr, C, w1, w4); octet(wr + 8 * 288, C128, w1, w4); }
      if (mt == 1) { octet(wr, C256, w2, w128); octet(wr + 8 * 288, C512, w128, w4); }
      if (mt == 2) { octet(wr, C1024, w128, w512); *(LDS u32*)(wr + 8 * 288) = pack_bf16(C2048[0], C2048[1]); }
      wave_fence();
      f32x4 acc = {0, 0, 0, 0};
      bf16x8 A[4];
#pragma unroll
      for (int ks = 0; ks < 4; ++ks) A[ks] = lds_frag(rd + 64 * ks);
      __builtin_amdgcn_sched_barrier(0);
#pragma unroll
      for (int ks = 0; ks < 4; ++ks) acc = mfma16(A[ks], B[ks], acc);
      __builtin_amdgcn_sched_barrier(0);
      wave_fence();
      tab[mt] = acc;
    }
  }
  const int b = lane & 15, q = lane >> 4;
#pragma unroll
  for (int mt = 0; mt < 3; ++mt) *(LDS f32x4*)(st + mt * 1024 + lane * 16) = tab[mt];
  STAMP(0, 3);
  bar();
  {
    LDS const char* ot = smem + S_ST + (w ^ 1) * 4608 + lane * 16;
    LDS char* T = smem + S_T + ch * S_TSZ;
    if (hh == 0) {
      const f32x4 v0 = tab[0] + *(LDS const f32x4*)(ot), v2 = tab[2] + *(LDS const f32x4*)(ot + 2048);
      *(LDS u32x2*)(T + 2 * (128 * (q >> 1) + 8 * b + 4 * (q & 1))) = u32x2{pack_bf16(v0[0], v0[1]), pack_bf16(v0[2], v0[3])};
      const u32 a0 = pack_bf16(v2[0], v2[1]), a1 = pack_bf16(v2[2], v2[3]);
      if (q < 2) {
        LDS char* t3 = T + 2 * (512 + 64 * q + b);
        *(LDS u16*)(t3) = (u16)a0; *(LDS u16*)(t3 + 32) = (u16)(a0 >> 16);
        *(LDS u16*)(t3 + 64) = (u16)a1; *(LDS u16*)(t3 + 96) = (u16)(a1 >> 16);
      } else if (lane == 32) *(LDS u16*)(T + 2 * 640) = (u16)a0;
    } else {
      const f32x4 v1 = tab[1] + *(LDS const f32x4*)(ot + 1024);
      const u32 a0 = pack_bf16(v1[0], v1[1]), a1 = pack_bf16(v1[2], v1[3]);
      if (q < 2) *(LDS u32x2*)(T + 2 * (256 + 64 * q + 4 * b)) = u32x2{a0, a1};
      else {
        LDS char* t2 = T + 2 * (384 + 2 * b + (q - 2));
        *(LDS u16*)(t2) = (u16)a0; *(LDS u16*)(t2 + 64) = (u16)(a0 >> 16);
        *(LDS u16*)(t2 + 128) = (u16)a1; *(LDS u16*)(t2 + 192) = (u16)(a1 >> 16);
      }
    }
  }
  bar();
  STAMP(0, 4);
  u32x4 uv[2];
  {
    GLB u64* fl = (GLB u64*)(p.ws + WS_FL) + (8 * hh + (lane & 7)) * DM + d;
    asm volatile("" :: "v"(wc[0]), "v"(wc[1]));
    wait_flags(fl, true);
    const __amdgpu_buffer_rsrc_t rs = mk_rsrc(p.ws + WS_XT, DM * SL * 2);
#pragma unroll
    for (int v = 0; v < 2; ++v) uv[v] = __builtin_amdgcn_raw_buffer_load_b128(rs, (d * SL + 1024 * hh + 8 * (lane + 64 * v)) * 2, 0, SC1);
    if (lane < 8) __hip_atomic_store(fl, 0ull, RLX_AGENT);
  }
  STAMP(0, 5);
  {
    LDS const char* T = smem + S_T + ch * S_TSZ;
    u32 o[10];
    const u32 e512 = *(LDS const u16*)(T + 2 * 512);
    o[8] = o[9] = e512 | (e512 << 16);
    if (hh == 0) {
      const int off = lane < 16 ? 32 * lane : (lane < 32 ? 256 + 16 * lane : 512 + 8 * lane);
      u32x2 W[4];
#pragma unroll
      for (int i = 0; i < 4; ++i) W[i] = *(LDS const u32x2*)(T + off + 8 * i);
      if (lane < 16) {
#pragma unroll
        for (int i = 0; i < 8; ++i) o[i] = W[i >> 1][i & 1];
      } else if (lane < 32) {
#pragma unroll
        for (int i = 0; i < 4; ++i) {
          const u32 a = W[i >> 1][i & 1], nx = W[(i + 1) >> 1][(i + 1) & 1];
          o[2 * i] = dup_lo(a);
          o[2 * i + 1] = hi_lo(a, nx);
        }
      } else {
        const u32 w0 = W[0][0], w1 = W[0][1], w2 = W[1][0];
        o[0] = dup_lo(w0); o[1] = w0; o[2] = dup_hi(w0); o[3] = dup_lo(w1);
        o[4] = o[3]; o[5] = w1; o[6] = dup_hi(w1); o[7] = dup_lo(w2);
      }
    } else {
      const u32 w0 = *(LDS const u32*)(T + 1024 + 4 * lane), w1 = *(LDS const u32*)(T + 1028 + 4 * lane);
      o[0] = o[1] = dup_lo(w0); o[2] = w0; o[3] = o[4] = o[5] = dup_hi(w0); o[6] = o[7] = dup_lo(w1);
    }
    o[8] = (u32)__builtin_amdgcn_update_dpp((int)o[8], (int)o[0], 0x130, 0xf, 0xf, false);
    o[9] = (u32)__builtin_amdgcn_update_dpp((int)o[9], (int)o[1], 0x130, 0xf, 0xf, false);
    u32 a[9];
#pragma unroll
    for (int i = 0; i < 9; ++i) a[i] = hi_lo(o[i], o[i + 1]);
    LDS char* xb = smem + S_X + ch * 4 * S_CPY + 32 + 2 * (1024 * hh + 16 * lane);
    *(LDS u32x4*)(xb) = u32x4{o[0], o[1], o[2], o[3]};
    *(LDS u32x4*)(xb + 16) = u32x4{o[4], o[5], o[6], o[7]};
    *(LDS u32x4*)(xb + S_CPY) = u32x4{a[0], a[1], a[2], a[3]};
    *(LDS u32x4*)(xb + S_CPY + 16) = u32x4{a[4], a[5], a[6], a[7]};
    *(LDS u32x4*)(xb + 2 * S_CPY) = u32x4{o[1], o[2], o[3], o[4]};
    *(LDS u32x4*)(xb + 2 * S_CPY + 16) = u32x4{o[5], o[6], o[7], o[8]};
    *(LDS u32x4*)(xb + 3 * S_CPY) = u32x4{a[1], a[2], a[3], a[4]};
    *(LDS u32x4*)(xb + 3 * S_CPY + 16) = u32x4{a[5], a[6], a[7], a[8]};
    if (hh == 0 && lane == 0) {
      LDS char* hb = smem + S_X + ch * 4 * S_CPY;
      const u32x4 z = {0, 0, 0, 0};
      *(LDS u32x4*)(hb) = z;
      *(LDS u32x4*)(hb + 16) = z;
      *(LDS u32x4*)(hb + S_CPY) = z;
      *(LDS u32x4*)(hb + S_CPY + 16) = u32x4{0, 0, 0, o[0] << 16};
      *(LDS u32x4*)(hb + 2 * S_CPY) = z;
      *(LDS u32x4*)(hb + 2 * S_CPY + 16) = u32x4{0, 0, 0, o[0]};
      *(LDS u32x4*)(hb + 3 * S_CPY) = z;
      *(LDS u32x4*)(hb + 3 * S_CPY + 16) = u32x4{0, 0, o[0] << 16, a[0]};
    }
    if (tid < 6 * 32) *(LDS u32x4*)(smem + S_Z + (tid >> 5) * S_ZSZ + (tid & 31) * 16) = u32x4{0, 0, 0, 0};
    STAMP(0, 6);
    LDS char* z = smem + S_Z + ch * S_ZSZ + 512 + (1024 * hh + 8 * lane) * 2;
    *(LDS u32x4*)z = uv[0];
    *(LDS u32x4*)(z + 1024) = uv[1];
  }
  bar();
  STAMP(0, 7);
  f32x4 acc[8];
#pragma unroll
  for (int i = 0; i < 8; ++i) acc[i] = f32x4{0, 0, 0, 0};
  {
    const int kq = q, qq = b >> 2, pp = b & 3;
    LDS const char* fb = smem + S_X + (ch * 4 + ((4 - qq) & 3)) * S_CPY +
                         2 * (16 + 16 * (kq >> 1) - 8 * (kq & 1) + 4 * pp - (qq ? 4 : 0)) + 64 * hh;
    LDS const char* gb = smem + S_Z + ch * S_ZSZ + 512 + 32 * (b - (kq >> 1)) + 16 * (kq & 1) - 64 * hh;
#pragma unroll 1
    for (int it = 0; it < 4; ++it) {
      bf16x8 F[8], G[8];
#pragma unroll
      for (int s = 0; s < 8; ++s) {
        F[s] = tr_frag(fb + 512 * s, fb + 512 * s - 8);
        G[s] = lds_frag(gb + 512 * s);
      }
      __builtin_amdgcn_sched_barrier(0);
#pragma unroll
      for (int t = 0; t < 8; ++t)
#pragma unroll
        for (int s = 0; s <= t; ++s) acc[t] = mfma16(F[s], G[t - s], acc[t]);
      fb += 128;
      gb -= 128;
    }
  }
  __builtin_amdgcn_sched_barrier(0);
  STAMP(0, 8);
  bar();
  {
    LDS char* ex = smem + S_X + 49152 + w * 4096 + lane * 16;
#pragma unroll
    for (int i = 0; i < 4; ++i) {
      const f32x4 lo = acc[i], hi = acc[4 + i];
      *(LDS f32x4*)(ex + i * 1024) = hh ? +lo : +hi;
    }
    if (tid == 0) *(LDS u32*)(smem + S_CNT) = 0;
  }
  bar();
  u32x2 yo[4];
  {
    LDS const char* ey = smem + S_X + 49152 + (w ^ 1) * 4096 + lane * 16;
#pragma unroll
    for (int i = 0; i < 4; ++i) {
      const f32x4 lo = acc[i], hi = acc[4 + i];
      const f32x4 y = (hh ? +hi : +lo) + *(LDS const f32x4*)(ey + i * 1024);
      yo[i] = u32x2{pack_bf16(y[0], y[1]), pack_bf16(y[2], y[3])};
    }
  }
  bar();
  {
    const __amdgpu_buffer_rsrc_t rs = mk_rsrc(p.ws + WS_AT, DM * SL * 2);
    const int dst = (d * SL + 1024 * hh + 16 * (lane & 15) + 4 * (lane >> 4)) * 2;
#pragma unroll
    for (int i = 0; i < 4; ++i) __builtin_amdgcn_raw_buffer_store_b64(yo[i], rs, dst + 512 * i, 0, SC1);
  }
  STAMP(0, 9);
}
DEV void s_signal(const P& p, LDS char* smem, int sg, int lane) {
  u32 old = 0;
  if (lane == 0) old = __hip_atomic_fetch_add((LDS u32*)(smem + S_CNT), 1u, __ATOMIC_RELAXED, __HIP_MEMORY_SCOPE_WORKGROUP);
  if (__builtin_amdgcn_readfirstlane(old) == 11) {
    const int lt = lane & 31, second = lane >> 5;
    const int dd = 6 * sg + (second ? 5 : 0);
    const int idx = dd < 480 ? dd / 96 : 5 + (dd - 480) / 32;
    const int dfirst = idx < 5 ? 96 * idx : 480 + 32 * (idx - 5);
    __hip_atomic_store((GLB u64*)(p.ws + WS_IN) + tile_block(lt, idx) * 64 + IN_A + (sg - dfirst / 6), FLAG_SET, RLX_AGENT);
  }
}

DEV void role_gate(const P& p, int rb, int w, int lane, u64 pc) {
  u32 pf0 = 0, pf1 = 0;
  const int tid = w * 64 + lane;
  const int sub = lane & 7;
  u32x4 wv[9];
#pragma unroll
  for (int i = 0; i < 9; ++i) wv[i] = *(const u32x4*)((const char*)p.gw + (i * 8 + sub) * 16);
  for (int r = w * 8 + (lane >> 3); r < 128; r += 96) {
    const int l = rb * 128 + r;
    float s = 0.f;
    u32x4 xv[9];
#pragma unroll
    for (int i = 0; i < 9; ++i) xv[i] = *(const u32x4*)((const char*)p.x + (size_t)l * 1152 + (i * 8 + sub) * 16);
#pragma unroll
    for (int i = 0; i < 9; ++i) {
#pragma unroll
      for (int k = 0; k < 4; ++k) {
        const u32 pr = pack_bf16(bflo(xv[i][k]) * bflo(wv[i][k]), bfhi(xv[i][k]) * bfhi(wv[i][k]));
        s += bflo(pr);
        s += bfhi(pr);
      }
    }
    s += __shfl_xor(s, 1); s += __shfl_xor(s, 2); s += __shfl_xor(s, 4);
    const float gv = rbf(1.0f / (1.0f + expf(-rbf(s))));
    if (sub == 0)
      __hip_atomic_store((GLB u64*)(p.ws + WS_G) + l, ((u64)__float_as_uint(rbf(1.0f - gv)) << 32) | __float_as_uint(gv), RLX_AGENT);
  }
  asm volatile("" :: "v"(pf0), "v"(pf1));
  wait_vm<0>();
  bar();
  if (w == 0 && lane < 16)
    __hip_atomic_store((GLB u64*)(p.ws + WS_IN) + tile_block(2 * rb + (lane >> 3), lane & 7) * 64 + IN_G, FLAG_SET, RLX_AGENT);
}

constexpr int H_C = 32768, H_CS = 12288;
DEV int h_slot(int i) { return i < 4 ? i * 8192 : 131072 + (i - 4) * 8192; }
constexpr int H_AT = H_C + 2 * H_CS;
constexpr int H_XT = H_C + 3 * H_CS;
constexpr int H_G = H_C + 4 * H_CS;
constexpr int H_OUT = H_C + 5 * H_CS;
constexpr int K_LDS = 163840;

constexpr int h_wait(int s, int NDT) {
  int n = 0;
  int need = 0;
  for (int k = 0; k < 4; ++k) { n += 2; if (k == s) need = n; }
  for (int t = 0; t < s; ++t) {
    if (t == 0) { n += 2; if (s == 4) need = n; n += 2; if (s == 5) need = n; }
    else if (t == 1) { n += 2; if (s == 6) need = n; n += 2; if (s == 7) need = n; }
    else if (t <= 11) { n += 2 + NDT; if (s == t + 6) need = n; if (t == 2) n += 1; }
    else if (t == 12 || t == 13) n += NDT;
    else if (t == 14) n += 1;
  }
  return s == 17 ? 0 : n - need;
}
template <int S, int NDT, class F> DEV void h_steps(F&& after, u64& fv, char* wsp) {
  if constexpr (S == 9) asm volatile("s_waitcnt vmcnt(%1)" : "+v"(fv) : "n"(h_wait(S, NDT)) : "memory");
  else wait_vm<h_wait(S, NDT)>();
  LSTAMP2(1, 16 + S);
  bar();
  after(std::integral_constant<int, S>{});
  if constexpr (S < 17) h_steps<S + 1, NDT>(after, fv, wsp);
}
DEV int p2_rot(int d0) { const int idx = d0 < 480 ? d0 / 96 : 5 + (d0 - 480) / 32; return (idx * 18) >> 3; }

template <int NDT, class F>
DEV void p2_loader(const P& p, LDS char* smem, int lt, int d0, int lw, int lane, bool scratch_barrier, F&& pre_poll) {
  __builtin_amdgcn_s_setprio(3);
  const char* hb = p.ws + WS_HB + (size_t)lt * 18 * 8192 + (2 * lw) * 1024 + lane * 16;
  constexpr int W = 32 * NDT;
  const int l0 = 64 * lt, rot = p2_rot(d0);
  u32 co[NDT];
#pragma unroll
  for (int i = 0; i < NDT; ++i) {
    const int row = 8 * (NDT * lw + i) + (lane >> 3), cp = lane & 7;
    co[i] = (u32)(d0 + row) * 1152u + ((cp ^ (row & 7)) << 4);
  }
  auto issue_c = [&](int s) {
    LDS char* buf = smem + H_C + (s & 7) * H_CS + NDT * lw * 1024;
    int ks = s + rot; ks = ks >= 18 ? ks - 18 : ks;
    const char* cb = ks < 9 ? (const char*)p.cre + ks * 128 : (const char*)p.cim + (ks - 9) * 128;
#pragma unroll
    for (int i = 0; i < NDT; ++i) dma16<0>(cb + co[i], buf + i * 1024);
  };
  auto issue_h = [&](int s) {
    LDS char* buf = smem + h_slot(s & 7) + 2 * lw * 1024;
    int ks = s + rot; ks = ks >= 18 ? ks - 18 : ks;
#pragma unroll
    for (int i = 0; i < 2; ++i) dma16<SC1>(hb + ks * 8192 + i * 1024, buf + i * 1024);
  };
  GLB u64* inb = (GLB u64*)(p.ws + WS_IN) + blockIdx.x * 64;
  const int nA = (d0 + W - 1) / 6 - d0 / 6 + 1;
  const bool need = lane == IN_G || (lane >= IN_A && lane < IN_A + nA);
  if (scratch_barrier) bar();
  else {
#pragma unroll 1
    for (int s = 0; s < 8; ++s) issue_c(s);
  }
  pre_poll();
  { [[maybe_unused]] const int w = lw + 8; STAMP(1, 1); }
  wait_flags(inb + IN_H + (lane < 36 ? lane : 0), true);
  { [[maybe_unused]] const int w = lw + 8; STAMP(1, 2); }
#pragma unroll
  for (int s = 0; s < 4; ++s) issue_h(s);
  { [[maybe_unused]] const int w = lw + 8; STAMP(1, 3); }
  u64 fv = 0;
  h_steps<0, NDT>([&](auto S_) {
    constexpr int S = decltype(S_)::value;
    if constexpr (S == 0) { issue_h(4); issue_h(5); }
    if constexpr (S == 1) { issue_h(6); issue_h(7); }
    if constexpr (S >= 2 && S <= 11) { issue_h(S + 6); issue_c(S + 6); }
    if constexpr (S == 2)
      asm volatile("global_load_dwordx2 %0, %1, off sc1" : "=v"(fv) : "v"(inb + lane) : "memory");
    if constexpr (S == 9) { if (!__all(!need || fv == FLAG_SET)) wait_flags(inb + lane, need); }
    if constexpr (S == 12) {
#pragma unroll
      for (int i = 0; i < NDT; ++i) {
        const int pc = NDT * lw + i;
        dma16<SC1>(p.ws + WS_AT + (size_t)(d0 + 8 * pc + (lane >> 3)) * 4096 + l0 * 2 + (lane & 7) * 16, smem + H_AT + pc * 1024);
      }
    }
    if constexpr (S == 13) {
#pragma unroll
      for (int i = 0; i < NDT; ++i) {
        const int pc = NDT * lw + i, id = pc * 64 + lane, row = id / (4 * NDT), qq = id - row * (4 * NDT);
        dma16<0>((const char*)p.x + (size_t)(l0 + row) * 1152 + d0 * 2 + qq * 16, smem + H_XT + pc * 1024);
      }
    }
    if constexpr (S == 14) { if (lane < 8) dma16<SC1>(p.ws + WS_G + (size_t)l0 * 8 + lw * 128 + lane * 16, smem + H_G + lw * 128); }
  }, fv, p.ws);
}

template <int NDT, int STAG>
DEV void p2_compute(const P& p, LDS char* smem, int lt0, int d0, int w, int lane) {
  const int lh = (w >> 1) & 1, kh = w & 1, dg = w >> 2;
  const int r = lane & 15, kq = lane >> 4;
  const int lt = 2 * lh + kh, l0 = 64 * lt0;
  constexpr int W = 32 * NDT;
  u32x2 da[NDT], db[NDT];
#pragma unroll
  for (int t = 0; t < NDT; ++t) {
    const int d = d0 + 16 * (NDT * dg + t) + 4 * kq;
    da[t] = *(const u32x2*)(p.dskA + d);
    db[t] = *(const u32x2*)(p.dskB + d);
  }
  GLB u64* inb = (GLB u64*)(p.ws + WS_IN) + blockIdx.x * 64;

  f32x4 acc[2][NDT];
#pragma unroll
  for (int i = 0; i < 2; ++i)
#pragma unroll
    for (int t = 0; t < NDT; ++t) acc[i][t] = f32x4{0, 0, 0, 0};
  int ho[2];
#pragma unroll
  for (int u = 0; u < 2; ++u) {
    const int k = 32 * kh + 8 * kq + 4 * u + (r >> 2), i = (r & 3) >> 1;
    ho[u] = (2 * (k >> 4) + i) * 1024 + (32 * lh + ((k & 15) ^ (4 * i))) * 16 + (r & 1) * 8;
  }
  const int co = H_C + (16 * NDT * dg + r) * 128 + (((4 * kh + kq) ^ (r & 7)) << 4);
  bf16x8 hb[2], ca[NDT];
#pragma unroll
  for (int i = 0; i < 2; ++i) hb[i] = bf16x8{};
#pragma unroll
  for (int t = 0; t < NDT; ++t) ca[t] = bf16x8{};
  auto mma = [&]() {
#pragma unroll
    for (int t = 0; t < NDT; ++t) {
      acc[0][t] = mfma16(ca[t], hb[0], acc[0][t]);
      acc[1][t] = mfma16(ca[t], hb[1], acc[1][t]);
    }
  };
  STAMP(1, 1);
#pragma unroll 1
  for (int s = 0; s < 18; ++s) {
    bar();
    if (s == 0) STAMP(1, 2);
    LSTAMP(1, 16 + s);
    if (STAG) { mma(); __builtin_amdgcn_sched_barrier(0); }
    LDS const char* hbuf = smem + h_slot(s & 7);
    LDS const char* cbuf = smem + (s & 7) * H_CS;
#pragma unroll
    for (int i = 0; i < 2; ++i) hb[i] = tr_frag(hbuf + ho[0] + i * 256, hbuf + ho[1] + i * 256);
#pragma unroll
    for (int t = 0; t < NDT; ++t) ca[t] = lds_frag(cbuf + co + t * 2048);
    __builtin_amdgcn_sched_barrier(0);
    if (!STAG) { mma(); __builtin_amdgcn_sched_barrier(0); }
  }
  if (STAG) mma();
  __builtin_amdgcn_sched_barrier(0);
  STAMP(1, 3);
  bar();
  if (w == 0) __hip_atomic_store(inb + lane, 0ull, RLX_AGENT);
  LDS char* ex = smem + w * (NDT * 1024) + lane * 16;
#pragma unroll
  for (int t = 0; t < NDT; ++t) {
    const f32x4 a0 = acc[0][t], a1 = acc[1][t];
    *(LDS f32x4*)(ex + t * 1024) = kh ? +a0 : +a1;
  }
  bar();
  LDS const char* ey = smem + (w ^ 1) * (NDT * 1024) + lane * 16;
  constexpr int OSTR = W * 4 + 16;
  const f32x2 gg = *(LDS const f32x2*)(smem + H_G + (16 * lt + r) * 8);
#pragma unroll
  for (int t = 0; t < NDT; ++t) {
    const int dt = NDT * dg + t;
    const f32x4 o = *(LDS const f32x4*)(ey + t * 1024);
    const f32x4 a0 = acc[0][t], a1 = acc[1][t];
    const f32x4 y = (kh ? +a1 : +a0) + o;
    const s16x4 av = tr_read(smem + H_AT + (16 * dt + 4 * kq + (r >> 2)) * 128 + lt * 32 + (r & 3) * 8);
    const u32x2 xv = *(LDS const u32x2*)(smem + H_XT + (16 * lt + r) * (W * 2) + (16 * dt + 4 * kq) * 2);
    f32x4 res;
#pragma unroll
    for (int i = 0; i < 4; ++i) {
      const u32 xw = xv[i >> 1], aw = da[t][i >> 1], bw = db[t][i >> 1];
      const float xf = (i & 1) ? bfhi(xw) : bflo(xw);
      const float sa = (i & 1) ? bfhi(aw) : bflo(aw);
      const float sb = (i & 1) ? bfhi(bw) : bflo(bw);
      const float yc = bf2f((u16)av[i]);
      const float a = rbf(yc + rbf(xf * sa));
      const float b = y[i] + rbf(xf * sb);
      res[i] = rbf(gg[0] * a) + gg[1] * b;
    }
    *(LDS f32x4*)(smem + H_OUT + (16 * lt + r) * OSTR + (16 * dt + 4 * kq) * 4) = res;
  }
  STAMP(1, 4);
  bar();
#pragma unroll
  for (int it = 0; it < NDT; ++it) {
    const int id = it * 512 + w * 64 + lane;
    const int row = id / (8 * NDT), pc = id - row * (8 * NDT);
    *(f32x4*)(p.out + (size_t)(l0 + row) * DM + d0 + pc * 4) = *(LDS const f32x4*)(smem + H_OUT + row * OSTR + pc * 16);
  }
}

template <int NDT>
DEV void phase2(const P& p, LDS char* smem, int lt, int d0, int w, int lane, bool scratch_barrier, int sg) {
  STAMP(1, 0);
  if (w >= 8) {
    p2_loader<NDT>(p, smem, lt, d0, w - 8, lane, scratch_barrier, [&]() { if (NDT == 1) { wait_vm<8>(); s_signal(p, smem, sg, lane); } });
  } else {
    if (NDT == 1) { wait_vm<0>(); s_signal(p, smem, sg, lane); }
    if (w < 4) p2_compute<NDT, 0>(p, smem, lt, d0, w, lane);
    else p2_compute<NDT, 1>(p, smem, lt, d0, w, lane);
  }
  STAMP(1, 15);
}

__global__ __launch_bounds__(768, 3) void gated_mix_s4d_s5(P p) {
  extern __shared__ __attribute__((aligned(16))) char smem_raw[];
  LDS char* smem = (LDS char*)smem_raw;
  u64 pc;
  asm volatile("s_getpc_b64 %0" : "=s"(pc));
  const int w = __builtin_amdgcn_readfirstlane(threadIdx.x >> 6), lane = threadIdx.x & 63;
  const int xcd = blockIdx.x & 7, j = blockIdx.x >> 3;
  STAMP(0, 0);
  if (j >= 18 && j < 30) {
    const int sg = 12 * xcd + (j - 18);
    role_s(p, smem, sg, w, lane);
    STAMP(0, 15);
    phase2<1>(p, smem, 4 * xcd + (j - 18) / 3, 480 + 32 * ((j - 18) % 3), w, lane, false, sg);
  } else {
    const bool isg = j < 18;
    if (isg) {
      const int c = 2 * xcd + j / 9, g = j % 9;
      if (w >= 8) g_loader(p, smem, c, g, 96 * (j % 5), w - 8, lane);
      else if (w < 4) g_compute<0>(p, smem, c, g, w, lane);
      else g_compute<1>(p, smem, c, g, w, lane);
    } else {
      role_gate(p, 2 * xcd + (j - 30), w, lane, pc);
    }
    STAMP(0, 15);
    const int e = isg ? j : j - 12;
    phase2<3>(p, smem, 4 * xcd + e / 5, 96 * (e % 5), w, lane, isg, 0);
  }
}

extern "C" void kernel_launch(void* const* d_in, const int* in_sizes, int n_in, void* d_out, int out_size,
                              void* d_ws, size_t ws_size, hipStream_t stream) {
  P a;
  a.x = (const u16*)d_in[0]; a.la = (const u16*)d_in[1]; a.aim = (const u16*)d_in[2];
  a.creA = (const u16*)d_in[3]; a.cimA = (const u16*)d_in[4]; a.ldtA = (const u16*)d_in[5]; a.dskA = (const u16*)d_in[6];
  a.lre = (const u16*)d_in[8]; a.lim = (const u16*)d_in[9]; a.bre = (const u16*)d_in[10]; a.bim = (const u16*)d_in[11];
  a.cre = (const u16*)d_in[12]; a.cim = (const u16*)d_in[13]; a.ldtB = (const u16*)d_in[14];
  a.dskB = (const u16*)d_in[15]; a.gw = (const u16*)d_in[16];
  a.ws = (char*)d_ws; a.out = (float*)d_out;
  (void)hipFuncSetAttribute((const void*)gated_mix_s4d_s5, hipFuncAttributeMaxDynamicSharedMemorySize, K_LDS);
  gated_mix_s4d_s5<<<256, 768, K_LDS, stream>>>(a);
}
```

```cpp
#define OUT_AUX 16
#define DELAY_S 1
#include <hip/hip_runtime.h>
#include <hip/hip_bf16.h>
#include <stdint.h>
#include <type_traits>

typedef unsigned short u16;
typedef unsigned int u32;
typedef unsigned long long u64;
typedef __attribute__((ext_vector_type(8))) __bf16 bf16x8;
typedef __attribute__((ext_vector_type(2))) __bf16 bf16x2;
typedef __attribute__((ext_vector_type(4))) short s16x4;
typedef __attribute__((ext_vector_type(4))) float f32x4;
typedef __attribute__((ext_vector_type(2))) float f32x2;
typedef __attribute__((ext_vector_type(4))) unsigned u32x4;
typedef __attribute__((ext_vector_type(2))) unsigned u32x2;
#define LDS __attribute__((address_space(3)))
#define GLB __attribute__((address_space(1)))
#define DEV __device__ __forceinline__
#define RLX_AGENT __ATOMIC_RELAXED, __HIP_MEMORY_SCOPE_AGENT

constexpr int SL = 2048;
constexpr int DM = 576;

constexpr size_t WS_HB = 0;
constexpr size_t WS_AT = WS_HB + (size_t)2 * DM * SL * 2;
constexpr size_t WS_G = WS_AT + (size_t)DM * SL * 2;
constexpr size_t WS_GRAN = WS_G + (size_t)SL * 8;
constexpr size_t WS_EP = WS_GRAN + (size_t)9 * 16 * 2 * 64 * 8;
constexpr size_t WS_FL = WS_EP + 4096;
constexpr size_t WS_XT = WS_FL + (size_t)16 * DM * 8;
constexpr size_t WS_IN = WS_XT + (size_t)DM * SL * 2;
constexpr u64 FLAG_SET = 0x9E3779B97F4A7C15ull;
constexpr u32 SPIN_CAP = 1u << 17;
constexpr int IN_H = 0, IN_G = 36, IN_A = 40;

constexpr size_t WS_ST = (size_t)16 << 20;
#define STAMP(kern, idx) do {} while (0)
#define CSTAMP(kern, idx) do {} while (0)
#define LSTAMP(kern, idx) do {} while (0)
#define LSTAMP2(kern, idx) do {} while (0)

DEV float bf2f(u16 v) { return __uint_as_float((u32)v << 16); }
DEV float bflo(u32 v) { return __uint_as_float(v << 16); }
DEV float bfhi(u32 v) { return __uint_as_float(v & 0xffff0000u); }
DEV u32 pack_bf16(float lo, float hi) {
  f32x2 v = {lo, hi};
  bf16x2 b = __builtin_convertvector(v, bf16x2);
  return __builtin_bit_cast(u32, b);
}
DEV float rbf(float x) { return bflo(pack_bf16(x, x)); }
template <int N> DEV void wait_vm() { asm volatile("s_waitcnt vmcnt(%0)" ::"n"(N) : "memory"); }
DEV void bar() { asm volatile("s_waitcnt lgkmcnt(0)\n\ts_barrier" ::: "memory"); }
DEV void wave_fence() { __builtin_amdgcn_fence(__ATOMIC_ACQ_REL, "wavefront"); }
template <int AUX> DEV void dma16(const void* src, LDS char* dst) {
  __builtin_amdgcn_global_load_lds((const GLB u32*)src, (LDS u32*)dst, 16, 0, AUX);
}
constexpr int SC1 = 16;
#define XT_WAIT 3
DEV bf16x8 lds_frag(LDS const char* p) { return *(LDS const bf16x8*)p; }
DEV s16x4 tr_read(LDS const char* a) { return __builtin_amdgcn_ds_read_tr16_b64_v4i16((LDS s16x4*)a); }
DEV bf16x8 tr_frag(LDS const char* a, LDS const char* b) {
  const s16x4 lo = tr_read(a), hi = tr_read(b);
  return __builtin_bit_cast(bf16x8, __builtin_shufflevector(lo, hi, 0, 1, 2, 3, 4, 5, 6, 7));
}
DEV f32x4 mfma16(bf16x8 a, bf16x8 b, f32x4 c) { return __builtin_amdgcn_mfma_f32_16x16x32_bf16(a, b, c, 0, 0, 0); }
DEV __amdgpu_buffer_rsrc_t mk_rsrc(const void* base, int bytes) {
  return __builtin_amdgcn_make_buffer_rsrc(const_cast<void*>(base), (short)0, bytes, 0x00020000);
}
struct cf { float r, i; };
DEV cf cmul(cf a, cf b) { return {a.r * b.r - a.i * b.i, a.r * b.i + a.i * b.r}; }
DEV cf cfma(cf a, cf b, cf c) { return {fmaf(a.r, b.r, fmaf(-a.i, b.i, c.r)), fmaf(a.r, b.i, fmaf(a.i, b.r, c.i))}; }
typedef f32x2 c2;
DEV c2 cm2(c2 a, c2 b) {
  c2 t, r;
  asm("v_pk_mul_f32 %0, %1, %2 op_sel:[0,0] op_sel_hi:[0,1]" : "=v"(t) : "v"(a), "v"(b));
  asm("v_pk_fma_f32 %0, %1, %2, %3 op_sel:[1,1,0] op_sel_hi:[1,0,1] neg_lo:[1,0,0]" : "=v"(r) : "v"(a), "v"(b), "v"(t));
  return r;
}
DEV f32x2 fma2(f32x2 a, f32x2 b, f32x2 c) { return __builtin_elementwise_fma(a, b, c); }
DEV f32x2 lo2(f32x4 v) { return __builtin_shufflevector(v, v, 0, 1); }
DEV f32x2 hi2(f32x4 v) { return __builtin_shufflevector(v, v, 2, 3); }
DEV f32x4 cat2(f32x2 a, f32x2 b) { return __builtin_shufflevector(a, b, 0, 1, 2, 3); }
DEV void wait_flags(GLB u64* word, bool need) {
  for (u32 spin = 0;; ++spin) {
    const u64 v = __hip_atomic_load(word, RLX_AGENT);
    if (__all(!need || v == FLAG_SET) || spin > SPIN_CAP) break;
    __builtin_amdgcn_s_sleep(2);
  }
}
DEV int tile_block(int lt, int idx) {
  const int xcd = lt >> 2, a = lt & 3;
  int j;
  if (idx < 5) { const int e = 5 * a + idx; j = e < 18 ? e : e + 12; }
  else j = 18 + 3 * a + (idx - 5);
  return (j << 3) | xcd;
}

struct P {
  const u16 *x, *la, *aim, *creA, *cimA, *ldtA, *dskA;
  const u16 *lre, *lim, *bre, *bim, *cre, *cim, *ldtB, *dskB, *gw;
  char* ws;
  float* out;
};

constexpr int G_NB = 4, G_STAGE = 32768;
constexpr int G_E = G_NB * G_STAGE;
constexpr int G_PF = G_E + 4096;
constexpr int G_L = G_PF + 4096;
constexpr int G_X = G_L + 1024;
constexpr int G_T = G_X + 2048;
constexpr int G_END = G_T + 4 * 32 * 144;

DEV void g_loader(const P& p, LDS char* smem, int c, int g, int d0, int lw, int lane) {
  [[maybe_unused]] const int w = lw + 8;
  __builtin_amdgcn_s_setprio(3);
  GLB u32* ep = (GLB u32*)(p.ws + WS_EP + g * 256);
  GLB u64* gran = (GLB u64*)(p.ws + WS_GRAN) + (size_t)g * 16 * 128;
  const u32 tag = __hip_atomic_load(ep, RLX_AGENT) + 1u;
  u32 xo[4], bo[4];
#pragma unroll
  for (int i = 0; i < 4; ++i) {
    const int rho = 8 * (4 * lw + i) + (lane >> 3);
    const int hf = rho >> 6, m = (rho >> 4) & 3, r = rho & 15;
    const int tau = 64 * hf + 16 * (r >> 2) + 8 * (r & 1) + 2 * m + ((r >> 1) & 1);
    const int ch = (lane & 7) ^ (rho & 7);
    xo[i] = (u32)(128 * c + tau) * 1152u + ch * 16;
    const int pp = 64 * g + 16 * (rho >> 5) + r;
    bo[i] = (u32)pp * 1152u + ch * 16;
  }
  auto issue = [&](int s) {
    LDS char* buf = smem + (s & (G_NB - 1)) * G_STAGE + lw * 4096;
    int kb = g + s; kb = (kb >= 9 ? kb - 9 : kb) * 128;
#pragma unroll
    for (int i = 0; i < 4; ++i) dma16<0>((const char*)p.x + (xo[i] + kb), buf + i * 1024);
#pragma unroll
    for (int i = 0; i < 4; ++i) {
      const char* base = (const char*)((((4 * lw + i) >> 1) & 1) ? p.bim : p.bre);
      dma16<0>(base + (bo[i] + kb), buf + 16384 + i * 1024);
    }
  };
#pragma unroll
  for (int s = 0; s < G_NB - 1; ++s) issue(s);
#pragma unroll 1
  for (int s = 0; s < 6; ++s) { wait_vm<16>(); LSTAMP(0, 16 + s); bar(); issue(s + G_NB - 1); LSTAMP(0, 32 + s); }
  wait_vm<16>(); bar();
  wait_vm<8>(); bar();
  wait_vm<0>(); bar();
  STAMP(0, 1);
  if (lw < 2) {
    bar();
    u32 co[6];
#pragma unroll
    for (int i = 0; i < 6; ++i) {
      const int row = 8 * (6 * lw + i) + (lane >> 3), cp = lane & 7;
      co[i] = (u32)(d0 + row) * 1152u + ((cp ^ (row & 7)) << 4);
    }
    const int rot = ((d0 / 96) * 18) >> 3;
#pragma unroll 1
    for (int s = 0; s < 8; ++s) {
      LDS char* buf = smem + 32768 + s * 12288 + 6 * lw * 1024;
      const int ks = s + rot;
      const char* cb = ks < 9 ? (const char*)p.cre + ks * 128 : (const char*)p.cim + (ks - 9) * 128;
#pragma unroll
      for (int i = 0; i < 6; ++i) dma16<0>(cb + co[i], buf + i * 1024);
    }
    bar();
    return;
  }
  bar();
  STAMP(0, 2);
  if (lw == 3) {
    const f32x2 lv = *(LDS const f32x2*)(smem + G_L + lane * 8);
    const cf l16 = {lv[0], lv[1]};
    f32x2 ev[8];
#pragma unroll
    for (int s = 0; s < 8; ++s) ev[s] = *(LDS const f32x2*)(smem + G_E + (s * 64 + lane) * 8);
    cf tot = {0.f, 0.f};
    cf pre[8];
#pragma unroll
    for (int s = 0; s < 8; ++s) { pre[s] = tot; tot = cfma(l16, tot, cf{ev[s][0], ev[s][1]}); }
    if (c < 15) {
      __hip_atomic_store(gran + c * 128 + lane, ((u64)tag << 32) | __float_as_uint(tot.r), RLX_AGENT);
      __hip_atomic_store(gran + c * 128 + 64 + lane, ((u64)tag << 32) | __float_as_uint(tot.i), RLX_AGENT);
    }
#pragma unroll
    for (int s = 0; s < 8; ++s) *(LDS f32x2*)(smem + G_PF + (s * 64 + lane) * 8) = f32x2{pre[s].r, pre[s].i};
  }
  const int par = lw - 2;
  const f32x2 lv = *(LDS const f32x2*)(smem + G_L + 512 + lane * 8);
  const cf l128 = {lv[0], lv[1]};
  const cf l256 = cmul(l128, l128);
  cf acc = {0.f, 0.f};
  if (par < c) {
    const int nc = (c - par + 1) >> 1;
    u64 a[8], b[8];
    for (u32 spin = 0;; ++spin) {
      bool ok = true;
#pragma unroll
      for (int i = 0; i < 8; ++i) {
        const int cc = par + 2 * (i < nc ? i : 0);
        a[i] = __hip_atomic_load(gran + cc * 128 + lane, RLX_AGENT);
        b[i] = __hip_atomic_load(gran + cc * 128 + 64 + lane, RLX_AGENT);
      }
#pragma unroll
      for (int i = 0; i < 8; ++i) ok = ok && (u32)(a[i] >> 32) == tag && (u32)(b[i] >> 32) == tag;
      if (__all(ok) || spin > SPIN_CAP) break;
      __builtin_amdgcn_s_sleep(1);
    }
#pragma unroll
    for (int i = 0; i < 8; ++i)
      if (i < nc) acc = cfma(l256, acc, cf{__uint_as_float((u32)a[i]), __uint_as_float((u32)b[i])});
    if (c - 1 - (par + 2 * (nc - 1))) acc = cmul(acc, l128);
  }
  *(LDS f32x2*)(smem + G_X + (par * 64 + lane) * 8) = f32x2{acc.r, acc.i};
  STAMP(0, 3);
  bar();
  if (c == 15 && lw == 2 && lane == 0) __hip_atomic_store(ep, tag, RLX_AGENT);
}

template <int HF>
DEV void g_compute(const P& p, LDS char* smem, int c, int g, int w, int lane) {
  const int pq = w & 3, col = lane & 15, q = lane >> 4;
  const int pl = 16 * pq + col, pp = 64 * g + pl;
  cf lam, coef2;
  {
    const float lre = bf2f(p.lre[pp]), lim = bf2f(p.lim[pp]);
    const float dt = rbf(expf(bf2f(p.ldtB[pp])));
    const float zr = dt * lre, zi = dt * lim;
    const float er = expf(zr);
    float sn, cs;
    sincosf(zi, &sn, &cs);
    lam = {er * cs, er * sn};
    const float nr = lam.r - 1.0f, ni = lam.i;
    const float den = 1.0f / (lre * lre + lim * lim);
    coef2 = {2.0f * (nr * lre + ni * lim) * den, 2.0f * (ni * lre - nr * lim) * den};
  }
  cf l8 = cmul(lam, lam); l8 = cmul(l8, l8); l8 = cmul(l8, l8);
  const cf l16 = cmul(l8, l8);
  const cf l32 = cmul(l16, l16), l64 = cmul(l32, l32);
  if (HF == 0 && q == 0) {
    const cf l128 = cmul(l64, l64);
    *(LDS f32x2*)(smem + G_L + pl * 8) = f32x2{l16.r, l16.i};
    *(LDS f32x2*)(smem + G_L + 512 + pl * 8) = f32x2{l128.r, l128.i};
  }
  cf pw = HF ? l64 : cf{1.f, 0.f};
  { const cf t = cmul(pw, l16); if (q & 1) pw = t; }
  { const cf t = cmul(pw, l32); if (q & 2) pw = t; }
  u32 wb[3] = {0, 0, 0};
  if (HF == 0) {
#pragma unroll
    for (int i = 0; i < 3; ++i) {
      const int ln = i * 256 + pq * 64 + lane;
      if (ln < 576) {
        const int row = ln / 9, kb = ln - row * 9;
        wb[i] = *(const GLB u32*)((const char*)((c & 1) ? p.bim : p.bre) + (size_t)(64 * g + row) * 1152 + kb * 128);
      }
    }
  }
  asm volatile("" : "+v"(lam.r), "+v"(lam.i), "+v"(coef2.r), "+v"(coef2.i), "+v"(l8.r), "+v"(l8.i), "+v"(pw.r), "+v"(pw.i));
  f32x4 are[4], aim[4];
#pragma unroll
  for (int m = 0; m < 4; ++m) { are[m] = f32x4{0, 0, 0, 0}; aim[m] = f32x4{0, 0, 0, 0}; }
  const int o0 = col * 128 + (((0 + q) ^ (col & 7)) << 4);
  const int o1 = col * 128 + (((4 + q) ^ (col & 7)) << 4);
  bf16x8 br[2], bi[2], a[2][4];
  auto read_frags = [&](int s) {
    LDS const char* buf = smem + (s & (G_NB - 1)) * G_STAGE;
    LDS const char* ab = buf + HF * 8192;
    LDS const char* bb = buf + 16384 + pq * 4096;
#pragma unroll
    for (int ks = 0; ks < 2; ++ks) {
      const int o = ks ? o1 : o0;
      br[ks] = lds_frag(bb + o); bi[ks] = lds_frag(bb + 2048 + o);
#pragma unroll
      for (int m = 0; m < 4; ++m) a[ks][m] = lds_frag(ab + m * 2048 + o);
    }
  };
  auto mma = [&]() {
#pragma unroll
    for (int ks = 0; ks < 2; ++ks)
#pragma unroll
      for (int m = 0; m < 4; ++m) {
        are[m] = mfma16(a[ks][m], br[ks], are[m]);
        aim[m] = mfma16(a[ks][m], bi[ks], aim[m]);
      }
  };
  STAMP(0, 1);
#pragma unroll
  for (int s = 0; s < 9; ++s) {
    if (HF == 1 && s == XT_WAIT) {
      wait_vm<0>();
      if (lane < 16) __hip_atomic_store((GLB u64*)(p.ws + WS_FL) + c * DM + 64 * g + 16 * pq + lane, FLAG_SET, RLX_AGENT);
    }
    bar();
    if (s == 0) { STAMP(0, 2); CSTAMP(0, 60); }
    if (HF == 1 && s > 0) { mma(); __builtin_amdgcn_sched_barrier(0); }
    if (HF == 1 && s == 0) {
      const int rr = 4 * q + (col >> 2);
      LDS const char* tb = smem + rr * 128 + (((2 * pq + ((col & 3) >> 1)) ^ (rr & 7)) << 4) + (col & 1) * 8;
      LDS char* sc = smem + G_T + pq * (32 * 144);
#pragma unroll
      for (int hf = 0; hf < 2; ++hf) {
        u32x2 t[4];
#pragma unroll
        for (int m = 0; m < 4; ++m) t[m] = __builtin_bit_cast(u32x2, tr_read(tb + hf * 8192 + m * 2048));
        u32x4 oa, ob;
#pragma unroll
        for (int m = 0; m < 4; ++m) {
          oa[m] = __builtin_amdgcn_perm(t[m][1], t[m][0], 0x05040100u);
          ob[m] = __builtin_amdgcn_perm(t[m][1], t[m][0], 0x07060302u);
        }
        *(LDS u32x4*)(sc + hf * (16 * 144) + col * 144 + q * 32) = oa;
        *(LDS u32x4*)(sc + hf * (16 * 144) + col * 144 + q * 32 + 16) = ob;
      }
      wave_fence();
      const __amdgpu_buffer_rsrc_t rs = mk_rsrc(p.ws + WS_XT, DM * SL * 2);
#pragma unroll
      for (int u = 0; u < 4; ++u) {
        const int row = (lane >> 3) + 8 * (u & 1), hf = u >> 1;
        const u32x4 v = *(LDS const u32x4*)(sc + hf * (16 * 144) + row * 144 + (lane & 7) * 16);
        __builtin_amdgcn_raw_buffer_store_b128(v, rs, ((64 * g + 16 * pq + row) * SL + 128 * c + 64 * hf) * 2 + (lane & 7) * 16, 0, SC1);
      }
      __builtin_amdgcn_sched_barrier(0);
    }
    read_frags(s);
    __builtin_amdgcn_sched_barrier(0);
    if (HF == 0) { mma(); __builtin_amdgcn_sched_barrier(0); }
  }
  if (HF == 1) mma();
  __builtin_amdgcn_sched_barrier(0);
  STAMP(0, 3); CSTAMP(0, 61);
  const f32x2 Lr = {lam.r, lam.r}, Li = {lam.i, lam.i};
  f32x2 er = {0.f, 0.f}, ei = {0.f, 0.f};
#pragma unroll
  for (int t = 0; t < 8; ++t) {
    const f32x2 br_ = (t & 1) ? hi2(are[t >> 1]) : lo2(are[t >> 1]);
    const f32x2 bi_ = (t & 1) ? hi2(aim[t >> 1]) : lo2(aim[t >> 1]);
    const f32x2 nr = fma2(-Li, ei, fma2(Lr, er, br_));
    ei = fma2(Li, er, fma2(Lr, ei, bi_));
    er = nr;
  }
  const cf eA = {er[0], ei[0]};
  const cf e = cfma(l8, eA, cf{er[1], ei[1]});
  const int seg = 4 * HF + q;
  *(LDS f32x2*)(smem + G_E + (seg * 64 + pl) * 8) = f32x2{e.r, e.i};
  asm volatile("" :: "v"(wb[0]), "v"(wb[1]), "v"(wb[2]));
  STAMP(0, 4);
  bar();
  STAMP(0, 5);
  {
#pragma unroll
    for (int m = 0; m < 4; ++m) asm volatile("" : "+v"(are[m]), "+v"(aim[m]));
    const f32x2 Cr = {coef2.r, coef2.r}, Ci = {coef2.i, coef2.i};
#pragma unroll
    for (int m = 0; m < 4; ++m) {
      const f32x2 r0 = lo2(are[m]), r1 = hi2(are[m]), i0 = lo2(aim[m]), i1 = hi2(aim[m]);
      are[m] = cat2(fma2(-Ci, i0, Cr * r0), fma2(-Ci, i1, Cr * r1));
      aim[m] = cat2(fma2(-Ci, r0, -Cr * i0), fma2(-Ci, r1, -Cr * i1));
      asm volatile("" : "+v"(are[m]), "+v"(aim[m]));
    }
  }
  cf eAc = cmul(coef2, eA), pwc = cmul(coef2, pw);
  asm volatile("" : "+v"(eAc.r), "+v"(eAc.i), "+v"(pwc.r), "+v"(pwc.i));
  STAMP(0, 7);
  bar();
  STAMP(0, 8);
  cf cin;
  {
    f32x2 v[2];
#pragma unroll
    for (int i = 0; i < 2; ++i) v[i] = *(LDS const f32x2*)(smem + G_X + (i * 64 + pl) * 8);
    const f32x2 pf = *(LDS const f32x2*)(smem + G_PF + (seg * 64 + pl) * 8);
    const f32x2 S = v[0] + v[1];
    cin = cfma(pwc, cf{S[0], S[1]}, cmul(coef2, cf{pf[0], pf[1]}));
  }
  const cf cinB = cfma(l8, cin, eAc);
  er = f32x2{cin.r, cinB.r}; ei = f32x2{-cin.i, -cinB.i};
  f32x2 hr[8], hi[8];
#pragma unroll
  for (int t = 0; t < 8; ++t) {
    const f32x2 br_ = (t & 1) ? hi2(are[t >> 1]) : lo2(are[t >> 1]);
    const f32x2 bi_ = (t & 1) ? hi2(aim[t >> 1]) : lo2(aim[t >> 1]);
    const f32x2 nr = fma2(Li, ei, fma2(Lr, er, br_));
    ei = fma2(-Li, er, fma2(Lr, ei, bi_));
    er = nr;
    hr[t] = er; hi[t] = ei;
  }
  u32x4 o[4];
#pragma unroll
  for (int t = 0; t < 4; ++t) {
    o[0][t] = pack_bf16(hr[2 * t][0], hr[2 * t + 1][0]);
    o[1][t] = pack_bf16(hr[2 * t][1], hr[2 * t + 1][1]);
    o[2][t] = pack_bf16(hi[2 * t][0], hi[2 * t + 1][0]);
    o[3][t] = pack_bf16(hi[2 * t][1], hi[2 * t + 1][1]);
  }
  STAMP(0, 9);
  {
    const __amdgpu_buffer_rsrc_t rs = mk_rsrc(p.ws + WS_HB, 2 * DM * SL * 2);
    const int hb = (((c * 2 + HF) * 2) * 9 + g) * 8192 + pq * 2048;
    const int p0 = (16 * q + col) * 16, p1 = 1024 + (16 * q + (col ^ 4)) * 16;
    __builtin_amdgcn_raw_buffer_store_b128(o[0], rs, hb + p0, 0, SC1);
    __builtin_amdgcn_raw_buffer_store_b128(o[1], rs, hb + p1, 0, SC1);
    __builtin_amdgcn_raw_buffer_store_b128(o[2], rs, hb + 9 * 8192 + p0, 0, SC1);
    __builtin_amdgcn_raw_buffer_store_b128(o[3], rs, hb + 9 * 8192 + p1, 0, SC1);
    bar();
    wait_vm<0>();
    STAMP(0, 10);
    if (lane < 8)
      __hip_atomic_store((GLB u64*)(p.ws + WS_IN) + tile_block(2 * c + HF, lane) * 64 + IN_H + g * 4 + pq, FLAG_SET, RLX_AGENT);
  }
}

constexpr int S_CPY = 4160;
constexpr int S_X = 0;
constexpr int S_ST = 6 * 4 * S_CPY;
constexpr int S_Z = S_ST;
constexpr int S_ZSZ = (256 + SL) * 2;
constexpr int S_T = S_ST + 12 * 4608;
constexpr int S_TSZ = 1312;
constexpr int S_END = S_T + 6 * S_TSZ;
constexpr int S_CNT = 32768 + 8192;

DEV c2 cexp_k(float zl, float th, float tl, float k) {
  const float mag = __builtin_amdgcn_exp2f(zl * k);
  const float t = fmaf(tl, k, __builtin_amdgcn_fractf(th * k));
  return c2{mag * __builtin_amdgcn_cosf(t), mag * __builtin_amdgcn_sinf(t)};
}
#define PKM(d, a, b) "v_pk_mul_f32 " d ", " a ", " b " op_sel:[0,0] op_sel_hi:[0,1]\n\t"
#define PKF(d, a, b) "v_pk_fma_f32 " d ", " a ", " b ", " d " op_sel:[1,1,0] op_sel_hi:[1,0,1] neg_lo:[1,0,0]\n\t"
DEV void octet(LDS char* dst, c2 s0, c2 m1, c2 m2) {
  c2 v1, v2, v3, t0, t1, t2, t3;
  asm(PKM("%0", "%7", "%8") PKM("%3", "%7", "%9") PKF("%0", "%7", "%8") PKF("%3", "%7", "%9")
      PKM("%1", "%0", "%8") PKM("%4", "%0", "%9") PKF("%1", "%0", "%8") PKF("%4", "%0", "%9")
      PKM("%2", "%1", "%8") PKM("%5", "%1", "%9") PKF("%2", "%1", "%8") PKF("%5", "%1", "%9")
      PKM("%6", "%2", "%9") PKF("%6", "%2", "%9")
      : "=&v"(v1), "=&v"(v2), "=&v"(v3), "=&v"(t0), "=&v"(t1), "=&v"(t2), "=&v"(t3) : "v"(s0), "v"(m1), "v"(m2));
  *(LDS u32*)(dst + 0 * 288) = pack_bf16(s0[0], s0[1]);
  *(LDS u32*)(dst + 1 * 288) = pack_bf16(v1[0], v1[1]);
  *(LDS u32*)(dst + 2 * 288) = pack_bf16(v2[0], v2[1]);
  *(LDS u32*)(dst + 3 * 288) = pack_bf16(v3[0], v3[1]);
  *(LDS u32*)(dst + 4 * 288) = pack_bf16(t0[0], t0[1]);
  *(LDS u32*)(dst + 5 * 288) = pack_bf16(t1[0], t1[1]);
  *(LDS u32*)(dst + 6 * 288) = pack_bf16(t2[0], t2[1]);
  *(LDS u32*)(dst + 7 * 288) = pack_bf16(t3[0], t3[1]);
}
DEV u32 dup_lo(u32 a) { return __builtin_amdgcn_perm(a, a, 0x01000100u); }
DEV u32 dup_hi(u32 a) { return __builtin_amdgcn_perm(a, a, 0x03020302u); }
DEV u32 hi_lo(u32 a, u32 b) { return __builtin_amdgcn_alignbit(b, a, 16); }

DEV void role_s(const P& p, LDS char* smem, int sg, int w, int lane) {
  const int tid = w * 64 + lane;
  const int ch = w >> 1, hh = w & 1, d = 6 * sg + ch;
  const int n = d * 128 + 64 * hh + lane;
  const u16 r_ldt = p.ldtA[d], r_la = p.la[n], r_aim = p.aim[n], r_cre = p.creA[n], r_cim = p.cimA[n];
  STAMP(0, 1);
  f32x4 tab[3];
  [[maybe_unused]] u32 wc[2] = {0, 0};
  LDS char* st = smem + S_ST + w * 4608;
  {
    const float L2E = 1.44269504088896341f;
    const float dt = rbf(__builtin_amdgcn_exp2f(bf2f(r_ldt) * L2E));
    const float ar = -rbf(__builtin_amdgcn_exp2f(bf2f(r_la) * L2E));
    const float zl = ar * dt * L2E, zi = bf2f(r_aim) * dt;
    const float th = zi * 0.15915494309189535f;
    const float tl = fmaf(zi, 0.15915494309189535f, -th) + zi * 4.6017384e-10f;
    const c2 C = {2.0f * (bf2f(r_cre) * dt), 2.0f * (bf2f(r_cim) * dt)};
    const c2 w1 = cexp_k(zl, th, tl, 1.f), w8 = cexp_k(zl, th, tl, 8.f), w128 = cexp_k(zl, th, tl, 128.f);
    c2 w2, w4, w16, w32, w64, w256, w512, w1024, w2048;
    asm(PKM("%0", "%9", "%9") PKM("%2", "%10", "%10") PKM("%5", "%11", "%11") PKF("%0", "%9", "%9") PKF("%2", "%10", "%10") PKF("%5", "%11", "%11")
        PKM("%1", "%0", "%0") PKM("%3", "%2", "%2") PKM("%6", "%5", "%5") PKF("%1", "%0", "%0") PKF("%3", "%2", "%2") PKF("%6", "%5", "%5")
        PKM("%4", "%3", "%3") PKM("%7", "%6", "%6") PKF("%4", "%3", "%3") PKF("%7", "%6", "%6")
        PKM("%8", "%7", "%7") PKF("%8", "%7", "%7")
        : "=&v"(w2), "=&v"(w4), "=&v"(w16), "=&v"(w32), "=&v"(w64), "=&v"(w256), "=&v"(w512), "=&v"(w1024), "=&v"(w2048)
        : "v"(w1), "v"(w8), "v"(w128));
    c2 C128, C256, C512, C1024, C2048;
    asm(PKM("%0", "%5", "%6") PKM("%1", "%5", "%7") PKM("%2", "%5", "%8") PKM("%3", "%5", "%9") PKM("%4", "%5", "%10")
        PKF("%0", "%5", "%6") PKF("%1", "%5", "%7") PKF("%2", "%5", "%8") PKF("%3", "%5", "%9") PKF("%4", "%5", "%10")
        : "=&v"(C128), "=&v"(C256), "=&v"(C512), "=&v"(C1024), "=&v"(C2048)
        : "v"(C), "v"(w128), "v"(w256), "v"(w512), "v"(w1024), "v"(w2048));
#pragma unroll
    for (int i = 0; i < 2; ++i) {
      const int ln = i * 768 + tid, all = (sg % 12) * 864 + ln;
      if (ln < 864) wc[i] = *(const GLB u32*)((const char*)(all < 5184 ? p.cre : p.cim) + (size_t)(all < 5184 ? all : all - 5184) * 128);
    }
    STAMP(0, 2);
    LDS char* wr = st + 4 * lane;
    LDS const char* rd = st + (lane & 15) * 288 + (lane >> 4) * 16;
    bf16x8 B[4];
    const c2 w8c = {w8[0], -w8[1]}, w32c = {w32[0], -w32[1]}, w64c = {w64[0], -w64[1]};
    octet(wr, c2{1.f, 0.f}, w8c, w32c);
    octet(wr + 8 * 288, w64c, w8c, w32c);
    wave_fence();
#pragma unroll
    for (int ks = 0; ks < 4; ++ks) B[ks] = lds_frag(rd + 64 * ks);
    wave_fence();
#pragma unroll
    for (int mt = 0; mt < 3; ++mt) {
      if (mt == 0) { octet(wr, C, w1, w4); octet(wr + 8 * 288, C128, w1, w4); }
      if (mt == 1) { octet(wr, C256, w2, w128); octet(wr + 8 * 288, C512, w128, w4); }
      if (mt == 2) { octet(wr, C1024, w128, w512); *(LDS u32*)(wr + 8 * 288) = pack_bf16(C2048[0], C2048[1]); }
      wave_fence();
      f32x4 acc = {0, 0, 0, 0};
      bf16x8 A[4];
#pragma unroll
      for (int ks = 0; ks < 4; ++ks) A[ks] = lds_frag(rd + 64 * ks);
      __builtin_amdgcn_sched_barrier(0);
#pragma unroll
      for (int ks = 0; ks < 4; ++ks) acc = mfma16(A[ks], B[ks], acc);
      __builtin_amdgcn_sched_barrier(0);
      wave_fence();
      tab[mt] = acc;
    }
  }
  const int b = lane & 15, q = lane >> 4;
#pragma unroll
  for (int mt = 0; mt < 3; ++mt) *(LDS f32x4*)(st + mt * 1024 + lane * 16) = tab[mt];
  STAMP(0, 3);
  bar();
  {
    LDS const char* ot = smem + S_ST + (w ^ 1) * 4608 + lane * 16;
    LDS char* T = smem + S_T + ch * S_TSZ;
    if (hh == 0) {
      const f32x4 v0 = tab[0] + *(LDS const f32x4*)(ot), v2 = tab[2] + *(LDS const f32x4*)(ot + 2048);
      *(LDS u32x2*)(T + 2 * (128 * (q >> 1) + 8 * b + 4 * (q & 1))) = u32x2{pack_bf16(v0[0], v0[1]), pack_bf16(v0[2], v0[3])};
      const u32 a0 = pack_bf16(v2[0], v2[1]), a1 = pack_bf16(v2[2], v2[3]);
      if (q < 2) {
        LDS char* t3 = T + 2 * (512 + 64 * q + b);
        *(LDS u16*)(t3) = (u16)a0; *(LDS u16*)(t3 + 32) = (u16)(a0 >> 16);
        *(LDS u16*)(t3 + 64) = (u16)a1; *(LDS u16*)(t3 + 96) = (u16)(a1 >> 16);
      } else if (lane == 32) *(LDS u16*)(T + 2 * 640) = (u16)a0;
    } else {
      const f32x4 v1 = tab[1] + *(LDS const f32x4*)(ot + 1024);
      const u32 a0 = pack_bf16(v1[0], v1[1]), a1 = pack_bf16(v1[2], v1[3]);
      if (q < 2) *(LDS u32x2*)(T + 2 * (256 + 64 * q + 4 * b)) = u32x2{a0, a1};
      else {
        LDS char* t2 = T + 2 * (384 + 2 * b + (q - 2));
        *(LDS u16*)(t2) = (u16)a0; *(LDS u16*)(t2 + 64) = (u16)(a0 >> 16);
        *(LDS u16*)(t2 + 128) = (u16)a1; *(LDS u16*)(t2 + 192) = (u16)(a1 >> 16);
      }
    }
  }
  bar();
  STAMP(0, 4);
  u32x4 uv[2];
  {
    GLB u64* fl = (GLB u64*)(p.ws + WS_FL) + (8 * hh + (lane & 7)) * DM + d;
    asm volatile("" :: "v"(wc[0]), "v"(wc[1]));
    wait_flags(fl, true);
    const __amdgpu_buffer_rsrc_t rs = mk_rsrc(p.ws + WS_XT, DM * SL * 2);
#pragma unroll
    for (int v = 0; v < 2; ++v) uv[v] = __builtin_amdgcn_raw_buffer_load_b128(rs, (d * SL + 1024 * hh + 8 * (lane + 64 * v)) * 2, 0, SC1);
    if (lane < 8) __hip_atomic_store(fl, 0ull, RLX_AGENT);
  }
  STAMP(0, 5);
  {
    LDS const char* T = smem + S_T + ch * S_TSZ;
    u32 o[10];
    const u32 e512 = *(LDS const u16*)(T + 2 * 512);
    o[8] = o[9] = e512 | (e512 << 16);
    if (hh == 0) {
      const int off = lane < 16 ? 32 * lane : (lane < 32 ? 256 + 16 * lane : 512 + 8 * lane);
      u32x2 W[4];
#pragma unroll
      for (int i = 0; i < 4; ++i) W[i] = *(LDS const u32x2*)(T + off + 8 * i);
      if (lane < 16) {
#pragma unroll
        for (int i = 0; i < 8; ++i) o[i] = W[i >> 1][i & 1];
      } else if (lane < 32) {
#pragma unroll
        for (int i = 0; i < 4; ++i) {
          const u32 a = W[i >> 1][i & 1], nx = W[(i + 1) >> 1][(i + 1) & 1];
          o[2 * i] = dup_lo(a);
          o[2 * i + 1] = hi_lo(a, nx);
        }
      } else {
        const u32 w0 = W[0][0], w1 = W[0][1], w2 = W[1][0];
        o[0] = dup_lo(w0); o[1] = w0; o[2] = dup_hi(w0); o[3] = dup_lo(w1);
        o[4] = o[3]; o[5] = w1; o[6] = dup_hi(w1); o[7] = dup_lo(w2);
      }
    } else {
      const u32 w0 = *(LDS const u32*)(T + 1024 + 4 * lane), w1 = *(LDS const u32*)(T + 1028 + 4 * lane);
      o[0] = o[1] = dup_lo(w0); o[2] = w0; o[3] = o[4] = o[5] = dup_hi(w0); o[6] = o[7] = dup_lo(w1);
    }
    o[8] = (u32)__builtin_amdgcn_update_dpp((int)o[8], (int)o[0], 0x130, 0xf, 0xf, false);
    o[9] = (u32)__builtin_amdgcn_update_dpp((int)o[9], (int)o[1], 0x130, 0xf, 0xf, false);
    u32 a[9];
#pragma unroll
    for (int i = 0; i < 9; ++i) a[i] = hi_lo(o[i], o[i + 1]);
    LDS char* xb = smem + S_X + ch * 4 * S_CPY + 32 + 2 * (1024 * hh + 16 * lane);
    *(LDS u32x4*)(xb) = u32x4{o[0], o[1], o[2], o[3]};
    *(LDS u32x4*)(xb + 16) = u32x4{o[4], o[5], o[6], o[7]};
    *(LDS u32x4*)(xb + S_CPY) = u32x4{a[0], a[1], a[2], a[3]};
    *(LDS u32x4*)(xb + S_CPY + 16) = u32x4{a[4], a[5], a[6], a[7]};
    *(LDS u32x4*)(xb + 2 * S_CPY) = u32x4{o[1], o[2], o[3], o[4]};
    *(LDS u32x4*)(xb + 2 * S_CPY + 16) = u32x4{o[5], o[6], o[7], o[8]};
    *(LDS u32x4*)(xb + 3 * S_CPY) = u32x4{a[1], a[2], a[3], a[4]};
    *(LDS u32x4*)(xb + 3 * S_CPY + 16) = u32x4{a[5], a[6], a[7], a[8]};
    if (hh == 0 && lane == 0) {
      LDS char* hb = smem + S_X + ch * 4 * S_CPY;
      const u32x4 z = {0, 0, 0, 0};
      *(LDS u32x4*)(hb) = z;
      *(LDS u32x4*)(hb + 16) = z;
      *(LDS u32x4*)(hb + S_CPY) = z;
      *(LDS u32x4*)(hb + S_CPY + 16) = u32x4{0, 0, 0, o[0] << 16};
      *(LDS u32x4*)(hb + 2 * S_CPY) = z;
      *(LDS u32x4*)(hb + 2 * S_CPY + 16) = u32x4{0, 0, 0, o[0]};
      *(LDS u32x4*)(hb + 3 * S_CPY) = z;
      *(LDS u32x4*)(hb + 3 * S_CPY + 16) = u32x4{0, 0, o[0] << 16, a[0]};
    }
    if (tid < 6 * 32) *(LDS u32x4*)(smem + S_Z + (tid >> 5) * S_ZSZ + (tid & 31) * 16) = u32x4{0, 0, 0, 0};
    STAMP(0, 6);
    LDS char* z = smem + S_Z + ch * S_ZSZ + 512 + (1024 * hh + 8 * lane) * 2;
    *(LDS u32x4*)z = uv[0];
    *(LDS u32x4*)(z + 1024) = uv[1];
  }
  bar();
  STAMP(0, 7);
  __builtin_amdgcn_s_sleep(64);
  f32x4 acc[8];
#pragma unroll
  for (int i = 0; i < 8; ++i) acc[i] = f32x4{0, 0, 0, 0};
  {
    const int kq = q, qq = b >> 2, pp = b & 3;
    LDS const char* fb = smem + S_X + (ch * 4 + ((4 - qq) & 3)) * S_CPY +
                         2 * (16 + 16 * (kq >> 1) - 8 * (kq & 1) + 4 * pp - (qq ? 4 : 0)) + 64 * hh;
    LDS const char* gb = smem + S_Z + ch * S_ZSZ + 512 + 32 * (b - (kq >> 1)) + 16 * (kq & 1) - 64 * hh;
#pragma unroll 1
    for (int it = 0; it < 4; ++it) {
      bf16x8 F[8], G[8];
#pragma unroll
      for (int s = 0; s < 8; ++s) {
        F[s] = tr_frag(fb + 512 * s, fb + 512 * s - 8);
        G[s] = lds_frag(gb + 512 * s);
      }
      __builtin_amdgcn_sched_barrier(0);
#pragma unroll
      for (int t = 0; t < 8; ++t)
#pragma unroll
        for (int s = 0; s <= t; ++s) acc[t] = mfma16(F[s], G[t - s], acc[t]);
      fb += 128;
      gb -= 128;
    }
  }
  __builtin_amdgcn_sched_barrier(0);
  STAMP(0, 8);
  bar();
  {
    LDS char* ex = smem + S_X + 49152 + w * 4096 + lane * 16;
#pragma unroll
    for (int i = 0; i < 4; ++i) {
      const f32x4 lo = acc[i], hi = acc[4 + i];
      *(LDS f32x4*)(ex + i * 1024) = hh ? +lo : +hi;
    }
    if (tid == 0) *(LDS u32*)(smem + S_CNT) = 0;
  }
  bar();
  u32x2 yo[4];
  {
    LDS const char* ey = smem + S_X + 49152 + (w ^ 1) * 4096 + lane * 16;
#pragma unroll
    for (int i = 0; i < 4; ++i) {
      const f32x4 lo = acc[i], hi = acc[4 + i];
      const f32x4 y = (hh ? +hi : +lo) + *(LDS const f32x4*)(ey + i * 1024);
      yo[i] = u32x2{pack_bf16(y[0], y[1]), pack_bf16(y[2], y[3])};
    }
  }
  bar();
  {
    const __amdgpu_buffer_rsrc_t rs = mk_rsrc(p.ws + WS_AT, DM * SL * 2);
    const int dst = (d * SL + 1024 * hh + 16 * (lane & 15) + 4 * (lane >> 4)) * 2;
#pragma unroll
    for (int i = 0; i < 4; ++i) __builtin_amdgcn_raw_buffer_store_b64(yo[i], rs, dst + 512 * i, 0, SC1);
  }
  STAMP(0, 9);
}
DEV void s_signal(const P& p, LDS char* smem, int sg, int lane) {
  u32 old = 0;
  if (lane == 0) old = __hip_atomic_fetch_add((LDS u32*)(smem + S_CNT), 1u, __ATOMIC_RELAXED, __HIP_MEMORY_SCOPE_WORKGROUP);
  if (__builtin_amdgcn_readfirstlane(old) == 11) {
    const int lt = lane & 31, second = lane >> 5;
    const int dd = 6 * sg + (second ? 5 : 0);
    const int idx = dd < 480 ? dd / 96 : 5 + (dd - 480) / 32;
    const int dfirst = idx < 5 ? 96 * idx : 480 + 32 * (idx - 5);
    __hip_atomic_store((GLB u64*)(p.ws + WS_IN) + tile_block(lt, idx) * 64 + IN_A + (sg - dfirst / 6), FLAG_SET, RLX_AGENT);
  }
}

DEV void role_gate(const P& p, int rb, int w, int lane, u64 pc) {
  u32 pf0 = 0, pf1 = 0;
  const int tid = w * 64 + lane;
  const int sub = lane & 7;
  u32x4 wv[9];
#pragma unroll
  for (int i = 0; i < 9; ++i) wv[i] = *(const u32x4*)((const char*)p.gw + (i * 8 + sub) * 16);
  for (int r = w * 8 + (lane >> 3); r < 128; r += 96) {
    const int l = rb * 128 + r;
    float s = 0.f;
    u32x4 xv[9];
#pragma unroll
    for (int i = 0; i < 9; ++i) xv[i] = *(const u32x4*)((const char*)p.x + (size_t)l * 1152 + (i * 8 + sub) * 16);
#pragma unroll
    for (int i = 0; i < 9; ++i) {
#pragma unroll
      for (int k = 0; k < 4; ++k) {
        const u32 pr = pack_bf16(bflo(xv[i][k]) * bflo(wv[i][k]), bfhi(xv[i][k]) * bfhi(wv[i][k]));
        s += bflo(pr);
        s += bfhi(pr);
      }
    }
    s += __shfl_xor(s, 1); s += __shfl_xor(s, 2); s += __shfl_xor(s, 4);
    const float gv = rbf(1.0f / (1.0f + expf(-rbf(s))));
    if (sub == 0)
      __hip_atomic_store((GLB u64*)(p.ws + WS_G) + l, ((u64)__float_as_uint(rbf(1.0f - gv)) << 32) | __float_as_uint(gv), RLX_AGENT);
  }
  asm volatile("" :: "v"(pf0), "v"(pf1));
  wait_vm<0>();
  bar();
  if (w == 0 && lane < 16)
    __hip_atomic_store((GLB u64*)(p.ws + WS_IN) + tile_block(2 * rb + (lane >> 3), lane & 7) * 64 + IN_G, FLAG_SET, RLX_AGENT);
}

constexpr int H_C = 32768, H_CS = 12288;
DEV int h_slot(int i) { return i < 4 ? i * 8192 : 131072 + (i - 4) * 8192; }
constexpr int H_AT = H_C + 2 * H_CS;
constexpr int H_XT = H_C + 3 * H_CS;
constexpr int H_G = H_C + 4 * H_CS;
constexpr int H_OUT = H_C + 5 * H_CS;
constexpr int K_LDS = 163840;

constexpr int h_wait(int s, int NDT) {
  int n = 0;
  int need = 0;
  for (int k = 0; k < 4; ++k) { n += 2; if (k == s) need = n; }
  for (int t = 0; t < s; ++t) {
    if (t == 0) { n += 2; if (s == 4) need = n; n += 2; if (s == 5) need = n; }
    else if (t == 1) { n += 2; if (s == 6) need = n; n += 2; if (s == 7) need = n; }
    else if (t <= 11) { n += 2 + NDT; if (s == t + 6) need = n; if (t == 2) n += 1; }
    else if (t == 12 || t == 13) n += NDT;
    else if (t == 14) n += 1;
  }
  return s == 17 ? 0 : n - need;
}
template <int S, int NDT, class F> DEV void h_steps(F&& after, u64& fv, char* wsp) {
  if constexpr (S == 9) asm volatile("s_waitcnt vmcnt(%1)" : "+v"(fv) : "n"(h_wait(S, NDT)) : "memory");
  else wait_vm<h_wait(S, NDT)>();
  LSTAMP2(1, 16 + S);
  bar();
  after(std::integral_constant<int, S>{});
  if constexpr (S < 17) h_steps<S + 1, NDT>(after, fv, wsp);
}
DEV int p2_rot(int d0) { const int idx = d0 < 480 ? d0 / 96 : 5 + (d0 - 480) / 32; return (idx * 18) >> 3; }

template <int NDT, class F>
DEV void p2_loader(const P& p, LDS char* smem, int lt, int d0, int lw, int lane, bool scratch_barrier, F&& pre_poll) {
  __builtin_amdgcn_s_setprio(3);
  const char* hb = p.ws + WS_HB + (size_t)lt * 18 * 8192 + (2 * lw) * 1024 + lane * 16;
  constexpr int W = 32 * NDT;
  const int l0 = 64 * lt, rot = p2_rot(d0);
  u32 co[NDT];
#pragma unroll
  for (int i = 0; i < NDT; ++i) {
    const int row = 8 * (NDT * lw + i) + (lane >> 3), cp = lane & 7;
    co[i] = (u32)(d0 + row) * 1152u + ((cp ^ (row & 7)) << 4);
  }
  auto issue_c = [&](int s) {
    LDS char* buf = smem + H_C + (s & 7) * H_CS + NDT * lw * 1024;
    int ks = s + rot; ks = ks >= 18 ? ks - 18 : ks;
    const char* cb = ks < 9 ? (const char*)p.cre + ks * 128 : (const char*)p.cim + (ks - 9) * 128;
#pragma unroll
    for (int i = 0; i < NDT; ++i) dma16<0>(cb + co[i], buf + i * 1024);
  };
  auto issue_h = [&](int s) {
    LDS char* buf = smem + h_slot(s & 7) + 2 * lw * 1024;
    int ks = s + rot; ks = ks >= 18 ? ks - 18 : ks;
#pragma unroll
    for (int i = 0; i < 2; ++i) dma16<SC1>(hb + ks * 8192 + i * 1024, buf + i * 1024);
  };
  GLB u64* inb = (GLB u64*)(p.ws + WS_IN) + blockIdx.x * 64;
  const int nA = (d0 + W - 1) / 6 - d0 / 6 + 1;
  const bool need = lane == IN_G || (lane >= IN_A && lane < IN_A + nA);
  if (scratch_barrier) bar();
  else {
#pragma unroll 1
    for (int s = 0; s < 8; ++s) issue_c(s);
  }
  pre_poll();
  { [[maybe_unused]] const int w = lw + 8; STAMP(1, 1); }
  wait_flags(inb + IN_H + (lane < 36 ? lane : 0), true);
  { [[maybe_unused]] const int w = lw + 8; STAMP(1, 2); }
#pragma unroll
  for (int s = 0; s < 4; ++s) issue_h(s);
  { [[maybe_unused]] const int w = lw + 8; STAMP(1, 3); }
  u64 fv = 0;
  h_steps<0, NDT>([&](auto S_) {
    constexpr int S = decltype(S_)::value;
    if constexpr (S == 0) { issue_h(4); issue_h(5); }
    if constexpr (S == 1) { issue_h(6); issue_h(7); }
    if constexpr (S >= 2 && S <= 11) { issue_h(S + 6); issue_c(S + 6); }
    if constexpr (S == 2)
      asm volatile("global_load_dwordx2 %0, %1, off sc1" : "=v"(fv) : "v"(inb + lane) : "memory");
    if constexpr (S == 9) { if (!__all(!need || fv == FLAG_SET)) wait_flags(inb + lane, need); }
    if constexpr (S == 12) {
#pragma unroll
      for (int i = 0; i < NDT; ++i) {
        const int pc = NDT * lw + i;
        dma16<SC1>(p.ws + WS_AT + (size_t)(d0 + 8 * pc + (lane >> 3)) * 4096 + l0 * 2 + (lane & 7) * 16, smem + H_AT + pc * 1024);
      }
    }
    if constexpr (S == 13) {
#pragma unroll
      for (int i = 0; i < NDT; ++i) {
        const int pc = NDT * lw + i, id = pc * 64 + lane, row = id / (4 * NDT), qq = id - row * (4 * NDT);
        dma16<0>((const char*)p.x + (size_t)(l0 + row) * 1152 + d0 * 2 + qq * 16, smem + H_XT + pc * 1024);
      }
    }
    if constexpr (S == 14) { if (lane < 8) dma16<SC1>(p.ws + WS_G + (size_t)l0 * 8 + lw * 128 + lane * 16, smem + H_G + lw * 128); }
  }, fv, p.ws);
}

template <int NDT, int STAG>
DEV void p2_compute(const P& p, LDS char* smem, int lt0, int d0, int w, int lane) {
  const int lh = (w >> 1) & 1, kh = w & 1, dg = w >> 2;
  const int r = lane & 15, kq = lane >> 4;
  const int lt = 2 * lh + kh, l0 = 64 * lt0;
  constexpr int W = 32 * NDT;
  u32x2 da[NDT], db[NDT];
#pragma unroll
  for (int t = 0; t < NDT; ++t) {
    const int d = d0 + 16 * (NDT * dg + t) + 4 * kq;
    da[t] = *(const u32x2*)(p.dskA + d);
    db[t] = *(const u32x2*)(p.dskB + d);
  }
  GLB u64* inb = (GLB u64*)(p.ws + WS_IN) + blockIdx.x * 64;

  f32x4 acc[2][NDT];
#pragma unroll
  for (int i = 0; i < 2; ++i)
#pragma unroll
    for (int t = 0; t < NDT; ++t) acc[i][t] = f32x4{0, 0, 0, 0};
  int ho[2];
#pragma unroll
  for (int u = 0; u < 2; ++u) {
    const int k = 32 * kh + 8 * kq + 4 * u + (r >> 2), i = (r & 3) >> 1;
    ho[u] = (2 * (k >> 4) + i) * 1024 + (32 * lh + ((k & 15) ^ (4 * i))) * 16 + (r & 1) * 8;
  }
  const int co = H_C + (16 * NDT * dg + r) * 128 + (((4 * kh + kq) ^ (r & 7)) << 4);
  constexpr int NREG = 0, NRING = 18 - NREG;
  bf16x8 cr[NREG ? NREG : 1][NDT];
  if constexpr (NREG > 0) {
    const int rot = p2_rot(d0);
#pragma unroll
    for (int i = 0; i < NREG; ++i) {
      int ks = NRING + i + rot; ks = ks >= 18 ? ks - 18 : ks;
      const char* cb = (ks < 9 ? (const char*)p.cre + ks * 128 : (const char*)p.cim + (ks - 9) * 128) +
                       (size_t)(d0 + 16 * NDT * dg + r) * 1152 + (4 * kh + kq) * 16;
#pragma unroll
      for (int t = 0; t < NDT; ++t) cr[i][t] = *(const GLB bf16x8*)(cb + t * 16 * 1152);
    }
  }
  bf16x8 hb[2], ca[NDT];
#pragma unroll
  for (int i = 0; i < 2; ++i) hb[i] = bf16x8{};
#pragma unroll
  for (int t = 0; t < NDT; ++t) ca[t] = bf16x8{};
  auto mma_with = [&](const bf16x8 (&cc)[NDT]) {
#pragma unroll
    for (int t = 0; t < NDT; ++t) {
      acc[0][t] = mfma16(cc[t], hb[0], acc[0][t]);
      acc[1][t] = mfma16(cc[t], hb[1], acc[1][t]);
    }
    __builtin_amdgcn_sched_barrier(0);
  };
  auto read_h = [&](int s) {
    LDS const char* hbuf = smem + h_slot(s & 7);
#pragma unroll
    for (int i = 0; i < 2; ++i) hb[i] = tr_frag(hbuf + ho[0] + i * 256, hbuf + ho[1] + i * 256);
  };
  STAMP(1, 1);
#pragma unroll 1
  for (int s = 0; s < NRING; ++s) {
    bar();
    if (s == 0) STAMP(1, 2);
    LSTAMP(1, 16 + s);
    if (STAG) mma_with(ca);
    read_h(s);
    LDS const char* cbuf = smem + (s & 7) * H_CS;
#pragma unroll
    for (int t = 0; t < NDT; ++t) ca[t] = lds_frag(cbuf + co + t * 2048);
    __builtin_amdgcn_sched_barrier(0);
    if (!STAG) mma_with(ca);
  }
#pragma unroll
  for (int i = 0; i < NREG; ++i) {
    bar();
    if (STAG) { if (i == 0) mma_with(ca); else mma_with(cr[i ? i - 1 : 0]); }
    read_h(NRING + i);
    __builtin_amdgcn_sched_barrier(0);
    if (!STAG) mma_with(cr[i]);
  }
  if (STAG) { if (NREG) mma_with(cr[NREG ? NREG - 1 : 0]); else mma_with(ca); }
  __builtin_amdgcn_sched_barrier(0);
  STAMP(1, 3);
  bar();
  if (w == 0) __hip_atomic_store(inb + lane, 0ull, RLX_AGENT);
  LDS char* ex = smem + w * (NDT * 1024) + lane * 16;
#pragma unroll
  for (int t = 0; t < NDT; ++t) {
    const f32x4 a0 = acc[0][t], a1 = acc[1][t];
    *(LDS f32x4*)(ex + t * 1024) = kh ? +a0 : +a1;
  }
  bar();
  LDS const char* ey = smem + (w ^ 1) * (NDT * 1024) + lane * 16;
  constexpr int OSTR = W * 4 + 16;
  const f32x2 gg = *(LDS const f32x2*)(smem + H_G + (16 * lt + r) * 8);
#pragma unroll
  for (int t = 0; t < NDT; ++t) {
    const int dt = NDT * dg + t;
    const f32x4 o = *(LDS const f32x4*)(ey + t * 1024);
    const f32x4 a0 = acc[0][t], a1 = acc[1][t];
    const f32x4 y = (kh ? +a1 : +a0) + o;
    const s16x4 av = tr_read(smem + H_AT + (16 * dt + 4 * kq + (r >> 2)) * 128 + lt * 32 + (r & 3) * 8);
    const u32x2 xv = *(LDS const u32x2*)(smem + H_XT + (16 * lt + r) * (W * 2) + (16 * dt + 4 * kq) * 2);
    f32x4 res;
#pragma unroll
    for (int i = 0; i < 4; ++i) {
      const u32 xw = xv[i >> 1], aw = da[t][i >> 1], bw = db[t][i >> 1];
      const float xf = (i & 1) ? bfhi(xw) : bflo(xw);
      const float sa = (i & 1) ? bfhi(aw) : bflo(aw);
      const float sb = (i & 1) ? bfhi(bw) : bflo(bw);
      const float yc = bf2f((u16)av[i]);
      const float a = rbf(yc + rbf(xf * sa));
      const float b = y[i] + rbf(xf * sb);
      res[i] = rbf(gg[0] * a) + gg[1] * b;
    }
    *(LDS f32x4*)(smem + H_OUT + (16 * lt + r) * OSTR + (16 * dt + 4 * kq) * 4) = res;
  }
  STAMP(1, 4);
  bar();
#pragma unroll
  for (int it = 0; it < NDT; ++it) {
    const int id = it * 512 + w * 64 + lane;
    const int row = id / (8 * NDT), pc = id - row * (8 * NDT);
    const f32x4 v = *(LDS const f32x4*)(smem + H_OUT + row * OSTR + pc * 16);
    __builtin_amdgcn_raw_buffer_store_b128(__builtin_bit_cast(u32x4, v), mk_rsrc(p.out, SL * DM * 4), ((l0 + row) * DM + d0 + pc * 4) * 4, 0, OUT_AUX);
  }
}

template <int NDT>
DEV void phase2(const P& p, LDS char* smem, int lt, int d0, int w, int lane, bool scratch_barrier, int sg) {
  STAMP(1, 0);
  if (w >= 8) {
    p2_loader<NDT>(p, smem, lt, d0, w - 8, lane, scratch_barrier, [&]() { if (NDT == 1) { wait_vm<8>(); s_signal(p, smem, sg, lane); } });
  } else {
    if (NDT == 1) { wait_vm<0>(); s_signal(p, smem, sg, lane); }
    if (w < 4) p2_compute<NDT, 0>(p, smem, lt, d0, w, lane);
    else p2_compute<NDT, 1>(p, smem, lt, d0, w, lane);
  }
  STAMP(1, 15);
}

__global__ __launch_bounds__(768, 3) void gated_mix_s4d_s5(P p) {
  extern __shared__ __attribute__((aligned(16))) char smem_raw[];
  LDS char* smem = (LDS char*)smem_raw;
  u64 pc;
  asm volatile("s_getpc_b64 %0" : "=s"(pc));
  const int w = __builtin_amdgcn_readfirstlane(threadIdx.x >> 6), lane = threadIdx.x & 63;
  const int xcd = blockIdx.x & 7, j = blockIdx.x >> 3;
  STAMP(0, 0);
  if (j >= 18 && j < 30) {
    const int sg = 12 * xcd + (j - 18);
    role_s(p, smem, sg, w, lane);
    STAMP(0, 15);
    phase2<1>(p, smem, 4 * xcd + (j - 18) / 3, 480 + 32 * ((j - 18) % 3), w, lane, false, sg);
  } else {
    const bool isg = j < 18;
    if (isg) {
      const int c = 2 * xcd + j / 9, g = j % 9;
      if (w >= 8) g_loader(p, smem, c, g, 96 * (j % 5), w - 8, lane);
      else if (w < 4) g_compute<0>(p, smem, c, g, w, lane);
      else g_compute<1>(p, smem, c, g, w, lane);
    } else {
      role_gate(p, 2 * xcd + (j - 30), w, lane, pc);
    }
    STAMP(0, 15);
    const int e = isg ? j : j - 12;
    phase2<3>(p, smem, 4 * xcd + e / 5, 96 * (e % 5), w, lane, isg, 0);
  }
}

extern "C" void kernel_launch(void* const* d_in, const int* in_sizes, int n_in, void* d_out, int out_size,
                              void* d_ws, size_t ws_size, hipStream_t stream) {
  P a;
  a.x = (const u16*)d_in[0]; a.la = (const u16*)d_in[1]; a.aim = (const u16*)d_in[2];
  a.creA = (const u16*)d_in[3]; a.cimA = (const u16*)d_in[4]; a.ldtA = (const u16*)d_in[5]; a.dskA = (const u16*)d_in[6];
  a.lre = (const u16*)d_in[8]; a.lim = (const u16*)d_in[9]; a.bre = (const u16*)d_in[10]; a.bim = (const u16*)d_in[11];
  a.cre = (const u16*)d_in[12]; a.cim = (const u16*)d_in[13]; a.ldtB = (const u16*)d_in[14];
  a.dskB = (const u16*)d_in[15]; a.gw = (const u16*)d_in[16];
  a.ws = (char*)d_ws; a.out = (float*)d_out;
  (void)hipFuncSetAttribute((const void*)gated_mix_s4d_s5, hipFuncAttributeMaxDynamicSharedMemorySize, K_LDS);
  gated_mix_s4d_s5<<<256, 768, K_LDS, stream>>>(a);
}
```

```cpp
#define OUT_AUX 16
#define PF_TOP 312
#include <hip/hip_runtime.h>
#include <hip/hip_bf16.h>
#include <stdint.h>
#include <type_traits>

typedef unsigned short u16;
typedef unsigned int u32;
typedef unsigned long long u64;
typedef __attribute__((ext_vector_type(8))) __bf16 bf16x8;
typedef __attribute__((ext_vector_type(2))) __bf16 bf16x2;
typedef __attribute__((ext_vector_type(4))) short s16x4;
typedef __attribute__((ext_vector_type(4))) float f32x4;
typedef __attribute__((ext_vector_type(2))) float f32x2;
typedef __attribute__((ext_vector_type(4))) unsigned u32x4;
typedef __attribute__((ext_vector_type(2))) unsigned u32x2;
#define LDS __attribute__((address_space(3)))
#define GLB __attribute__((address_space(1)))
#define DEV __device__ __forceinline__
#define RLX_AGENT __ATOMIC_RELAXED, __HIP_MEMORY_SCOPE_AGENT

constexpr int SL = 2048;
constexpr int DM = 576;

constexpr size_t WS_HB = 0;
constexpr size_t WS_AT = WS_HB + (size_t)2 * DM * SL * 2;
constexpr size_t WS_G = WS_AT + (size_t)DM * SL * 2;
constexpr size_t WS_GRAN = WS_G + (size_t)SL * 8;
constexpr size_t WS_EP = WS_GRAN + (size_t)9 * 16 * 2 * 64 * 8;
constexpr size_t WS_FL = WS_EP + 4096;
constexpr size_t WS_XT = WS_FL + (size_t)16 * DM * 8;
constexpr size_t WS_IN = WS_XT + (size_t)DM * SL * 2;
constexpr u64 FLAG_SET = 0x9E3779B97F4A7C15ull;
constexpr u32 SPIN_CAP = 1u << 17;
constexpr int IN_H = 0, IN_G = 36, IN_A = 40;

constexpr size_t WS_ST = (size_t)16 << 20;
#define STAMP(kern, idx) do {} while (0)
#define CSTAMP(kern, idx) do {} while (0)
#define LSTAMP(kern, idx) do {} while (0)
#define LSTAMP2(kern, idx) do {} while (0)

DEV float bf2f(u16 v) { return __uint_as_float((u32)v << 16); }
DEV float bflo(u32 v) { return __uint_as_float(v << 16); }
DEV float bfhi(u32 v) { return __uint_as_float(v & 0xffff0000u); }
DEV u32 pack_bf16(float lo, float hi) {
  f32x2 v = {lo, hi};
  bf16x2 b = __builtin_convertvector(v, bf16x2);
  return __builtin_bit_cast(u32, b);
}
DEV float rbf(float x) { return bflo(pack_bf16(x, x)); }
template <int N> DEV void wait_vm() { asm volatile("s_waitcnt vmcnt(%0)" ::"n"(N) : "memory"); }
DEV void bar() { asm volatile("s_waitcnt lgkmcnt(0)\n\ts_barrier" ::: "memory"); }
DEV void wave_fence() { __builtin_amdgcn_fence(__ATOMIC_ACQ_REL, "wavefront"); }
template <int AUX> DEV void dma16(const void* src, LDS char* dst) {
  __builtin_amdgcn_global_load_lds((const GLB u32*)src, (LDS u32*)dst, 16, 0, AUX);
}
constexpr int SC1 = 16;
#define XT_WAIT 3
DEV bf16x8 lds_frag(LDS const char* p) { return *(LDS const bf16x8*)p; }
DEV s16x4 tr_read(LDS const char* a) { return __builtin_amdgcn_ds_read_tr16_b64_v4i16((LDS s16x4*)a); }
DEV bf16x8 tr_frag(LDS const char* a, LDS const char* b) {
  const s16x4 lo = tr_read(a), hi = tr_read(b);
  return __builtin_bit_cast(bf16x8, __builtin_shufflevector(lo, hi, 0, 1, 2, 3, 4, 5, 6, 7));
}
DEV f32x4 mfma16(bf16x8 a, bf16x8 b, f32x4 c) { return __builtin_amdgcn_mfma_f32_16x16x32_bf16(a, b, c, 0, 0, 0); }
DEV __amdgpu_buffer_rsrc_t mk_rsrc(const void* base, int bytes) {
  return __builtin_amdgcn_make_buffer_rsrc(const_cast<void*>(base), (short)0, bytes, 0x00020000);
}
struct cf { float r, i; };
DEV cf cmul(cf a, cf b) { return {a.r * b.r - a.i * b.i, a.r * b.i + a.i * b.r}; }
DEV cf cfma(cf a, cf b, cf c) { return {fmaf(a.r, b.r, fmaf(-a.i, b.i, c.r)), fmaf(a.r, b.i, fmaf(a.i, b.r, c.i))}; }
typedef f32x2 c2;
DEV c2 cm2(c2 a, c2 b) {
  c2 t, r;
  asm("v_pk_mul_f32 %0, %1, %2 op_sel:[0,0] op_sel_hi:[0,1]" : "=v"(t) : "v"(a), "v"(b));
  asm("v_pk_fma_f32 %0, %1, %2, %3 op_sel:[1,1,0] op_sel_hi:[1,0,1] neg_lo:[1,0,0]" : "=v"(r) : "v"(a), "v"(b), "v"(t));
  return r;
}
DEV f32x2 fma2(f32x2 a, f32x2 b, f32x2 c) { return __builtin_elementwise_fma(a, b, c); }
DEV f32x2 lo2(f32x4 v) { return __builtin_shufflevector(v, v, 0, 1); }
DEV f32x2 hi2(f32x4 v) { return __builtin_shufflevector(v, v, 2, 3); }
DEV f32x4 cat2(f32x2 a, f32x2 b) { return __builtin_shufflevector(a, b, 0, 1, 2, 3); }
DEV void wait_flags(GLB u64* word, bool need) {
  for (u32 spin = 0;; ++spin) {
    const u64 v = __hip_atomic_load(word, RLX_AGENT);
    if (__all(!need || v == FLAG_SET) || spin > SPIN_CAP) break;
    __builtin_amdgcn_s_sleep(2);
  }
}
DEV int tile_block(int lt, int idx) {
  const int xcd = lt >> 2, a = lt & 3;
  int j;
  if (idx < 5) { const int e = 5 * a + idx; j = e < 18 ? e : e + 12; }
  else j = 18 + 3 * a + (idx - 5);
  return (j << 3) | xcd;
}

struct P {
  const u16 *x, *la, *aim, *creA, *cimA, *ldtA, *dskA;
  const u16 *lre, *lim, *bre, *bim, *cre, *cim, *ldtB, *dskB, *gw;
  char* ws;
  float* out;
};

constexpr int G_NB = 4, G_STAGE = 32768;
constexpr int G_E = G_NB * G_STAGE;
constexpr int G_PF = G_E + 4096;
constexpr int G_L = G_PF + 4096;
constexpr int G_X = G_L + 1024;
constexpr int G_T = G_X + 2048;
constexpr int G_END = G_T + 4 * 32 * 144;

DEV void g_loader(const P& p, LDS char* smem, int c, int g, int d0, int lw, int lane) {
  [[maybe_unused]] const int w = lw + 8;
  __builtin_amdgcn_s_setprio(3);
  GLB u32* ep = (GLB u32*)(p.ws + WS_EP + g * 256);
  GLB u64* gran = (GLB u64*)(p.ws + WS_GRAN) + (size_t)g * 16 * 128;
  const u32 tag = __hip_atomic_load(ep, RLX_AGENT) + 1u;
  u32 xo[4], bo[4];
#pragma unroll
  for (int i = 0; i < 4; ++i) {
    const int rho = 8 * (4 * lw + i) + (lane >> 3);
    const int hf = rho >> 6, m = (rho >> 4) & 3, r = rho & 15;
    const int tau = 64 * hf + 16 * (r >> 2) + 8 * (r & 1) + 2 * m + ((r >> 1) & 1);
    const int ch = (lane & 7) ^ (rho & 7);
    xo[i] = (u32)(128 * c + tau) * 1152u + ch * 16;
    const int pp = 64 * g + 16 * (rho >> 5) + r;
    bo[i] = (u32)pp * 1152u + ch * 16;
  }
  auto issue = [&](int s) {
    LDS char* buf = smem + (s & (G_NB - 1)) * G_STAGE + lw * 4096;
    int kb = g + s; kb = (kb >= 9 ? kb - 9 : kb) * 128;
#pragma unroll
    for (int i = 0; i < 4; ++i) dma16<0>((const char*)p.x + (xo[i] + kb), buf + i * 1024);
#pragma unroll
    for (int i = 0; i < 4; ++i) {
      const char* base = (const char*)((((4 * lw + i) >> 1) & 1) ? p.bim : p.bre);
      dma16<0>(base + (bo[i] + kb), buf + 16384 + i * 1024);
    }
  };
#pragma unroll
  for (int s = 0; s < G_NB - 1; ++s) issue(s);
#pragma unroll 1
  for (int s = 0; s < 6; ++s) { wait_vm<16>(); LSTAMP(0, 16 + s); bar(); issue(s + G_NB - 1); LSTAMP(0, 32 + s); }
  wait_vm<16>(); bar();
  wait_vm<8>(); bar();
  wait_vm<0>(); bar();
  STAMP(0, 1);
  if (lw < 2) {
    bar();
    u32 co[6];
#pragma unroll
    for (int i = 0; i < 6; ++i) {
      const int row = 8 * (6 * lw + i) + (lane >> 3), cp = lane & 7;
      co[i] = (u32)(d0 + row) * 1152u + ((cp ^ (row & 7)) << 4);
    }
    const int rot = ((d0 / 96) * 18) >> 3;
#pragma unroll 1
    for (int s = 0; s < 8; ++s) {
      LDS char* buf = smem + 32768 + s * 12288 + 6 * lw * 1024;
      const int ks = s + rot;
      const char* cb = ks < 9 ? (const char*)p.cre + ks * 128 : (const char*)p.cim + (ks - 9) * 128;
#pragma unroll
      for (int i = 0; i < 6; ++i) dma16<0>(cb + co[i], buf + i * 1024);
    }
    bar();
    return;
  }
  bar();
  STAMP(0, 2);
  if (lw == 3) {
    const f32x2 lv = *(LDS const f32x2*)(smem + G_L + lane * 8);
    const cf l16 = {lv[0], lv[1]};
    f32x2 ev[8];
#pragma unroll
    for (int s = 0; s < 8; ++s) ev[s] = *(LDS const f32x2*)(smem + G_E + (s * 64 + lane) * 8);
    cf tot = {0.f, 0.f};
    cf pre[8];
#pragma unroll
    for (int s = 0; s < 8; ++s) { pre[s] = tot; tot = cfma(l16, tot, cf{ev[s][0], ev[s][1]}); }
    if (c < 15) {
      __hip_atomic_store(gran + c * 128 + lane, ((u64)tag << 32) | __float_as_uint(tot.r), RLX_AGENT);
      __hip_atomic_store(gran + c * 128 + 64 + lane, ((u64)tag << 32) | __float_as_uint(tot.i), RLX_AGENT);
    }
#pragma unroll
    for (int s = 0; s < 8; ++s) *(LDS f32x2*)(smem + G_PF + (s * 64 + lane) * 8) = f32x2{pre[s].r, pre[s].i};
  }
  const int par = lw - 2;
  const f32x2 lv = *(LDS const f32x2*)(smem + G_L + 512 + lane * 8);
  const cf l128 = {lv[0], lv[1]};
  const cf l256 = cmul(l128, l128);
  cf acc = {0.f, 0.f};
  if (par < c) {
    const int nc = (c - par + 1) >> 1;
    u64 a[8], b[8];
    for (u32 spin = 0;; ++spin) {
      bool ok = true;
#pragma unroll
      for (int i = 0; i < 8; ++i) {
        const int cc = par + 2 * (i < nc ? i : 0);
        a[i] = __hip_atomic_load(gran + cc * 128 + lane, RLX_AGENT);
        b[i] = __hip_atomic_load(gran + cc * 128 + 64 + lane, RLX_AGENT);
      }
#pragma unroll
      for (int i = 0; i < 8; ++i) ok = ok && (u32)(a[i] >> 32) == tag && (u32)(b[i] >> 32) == tag;
      if (__all(ok) || spin > SPIN_CAP) break;
      __builtin_amdgcn_s_sleep(1);
    }
#pragma unroll
    for (int i = 0; i < 8; ++i)
      if (i < nc) acc = cfma(l256, acc, cf{__uint_as_float((u32)a[i]), __uint_as_float((u32)b[i])});
    if (c - 1 - (par + 2 * (nc - 1))) acc = cmul(acc, l128);
  }
  *(LDS f32x2*)(smem + G_X + (par * 64 + lane) * 8) = f32x2{acc.r, acc.i};
  STAMP(0, 3);
  bar();
  if (c == 15 && lw == 2 && lane == 0) __hip_atomic_store(ep, tag, RLX_AGENT);
}

template <int HF>
DEV void g_compute(const P& p, LDS char* smem, int c, int g, int w, int lane) {
  const int pq = w & 3, col = lane & 15, q = lane >> 4;
  const int pl = 16 * pq + col, pp = 64 * g + pl;
  cf lam, coef2;
  {
    const float lre = bf2f(p.lre[pp]), lim = bf2f(p.lim[pp]);
    const float dt = rbf(expf(bf2f(p.ldtB[pp])));
    const float zr = dt * lre, zi = dt * lim;
    const float er = expf(zr);
    float sn, cs;
    sincosf(zi, &sn, &cs);
    lam = {er * cs, er * sn};
    const float nr = lam.r - 1.0f, ni = lam.i;
    const float den = 1.0f / (lre * lre + lim * lim);
    coef2 = {2.0f * (nr * lre + ni * lim) * den, 2.0f * (ni * lre - nr * lim) * den};
  }
  cf l8 = cmul(lam, lam); l8 = cmul(l8, l8); l8 = cmul(l8, l8);
  const cf l16 = cmul(l8, l8);
  const cf l32 = cmul(l16, l16), l64 = cmul(l32, l32);
  if (HF == 0 && q == 0) {
    const cf l128 = cmul(l64, l64);
    *(LDS f32x2*)(smem + G_L + pl * 8) = f32x2{l16.r, l16.i};
    *(LDS f32x2*)(smem + G_L + 512 + pl * 8) = f32x2{l128.r, l128.i};
  }
  cf pw = HF ? l64 : cf{1.f, 0.f};
  { const cf t = cmul(pw, l16); if (q & 1) pw = t; }
  { const cf t = cmul(pw, l32); if (q & 2) pw = t; }
  u32 wb[3] = {0, 0, 0};
  if (HF == 0) {
#pragma unroll
    for (int i = 0; i < 3; ++i) {
      const int ln = i * 256 + pq * 64 + lane;
      if (ln < 576) {
        const int row = ln / 9, kb = ln - row * 9;
        wb[i] = *(const GLB u32*)((const char*)((c & 1) ? p.bim : p.bre) + (size_t)(64 * g + row) * 1152 + kb * 128);
      }
    }
  }
  asm volatile("" : "+v"(lam.r), "+v"(lam.i), "+v"(coef2.r), "+v"(coef2.i), "+v"(l8.r), "+v"(l8.i), "+v"(pw.r), "+v"(pw.i));
  f32x4 are[4], aim[4];
#pragma unroll
  for (int m = 0; m < 4; ++m) { are[m] = f32x4{0, 0, 0, 0}; aim[m] = f32x4{0, 0, 0, 0}; }
  const int o0 = col * 128 + (((0 + q) ^ (col & 7)) << 4);
  const int o1 = col * 128 + (((4 + q) ^ (col & 7)) << 4);
  bf16x8 br[2], bi[2], a[2][4];
  auto read_frags = [&](int s) {
    LDS const char* buf = smem + (s & (G_NB - 1)) * G_STAGE;
    LDS const char* ab = buf + HF * 8192;
    LDS const char* bb = buf + 16384 + pq * 4096;
#pragma unroll
    for (int ks = 0; ks < 2; ++ks) {
      const int o = ks ? o1 : o0;
      br[ks] = lds_frag(bb + o); bi[ks] = lds_frag(bb + 2048 + o);
#pragma unroll
      for (int m = 0; m < 4; ++m) a[ks][m] = lds_frag(ab + m * 2048 + o);
    }
  };
  auto mma = [&]() {
#pragma unroll
    for (int ks = 0; ks < 2; ++ks)
#pragma unroll
      for (int m = 0; m < 4; ++m) {
        are[m] = mfma16(a[ks][m], br[ks], are[m]);
        aim[m] = mfma16(a[ks][m], bi[ks], aim[m]);
      }
  };
  STAMP(0, 1);
#pragma unroll
  for (int s = 0; s < 9; ++s) {
    if (HF == 1 && s == XT_WAIT) {
      wait_vm<0>();
      if (lane < 16) __hip_atomic_store((GLB u64*)(p.ws + WS_FL) + c * DM + 64 * g + 16 * pq + lane, FLAG_SET, RLX_AGENT);
    }
    bar();
    if (s == 0) { STAMP(0, 2); CSTAMP(0, 60); }
    if (HF == 1 && s > 0) { mma(); __builtin_amdgcn_sched_barrier(0); }
    if (HF == 1 && s == 0) {
      const int rr = 4 * q + (col >> 2);
      LDS const char* tb = smem + rr * 128 + (((2 * pq + ((col & 3) >> 1)) ^ (rr & 7)) << 4) + (col & 1) * 8;
      LDS char* sc = smem + G_T + pq * (32 * 144);
#pragma unroll
      for (int hf = 0; hf < 2; ++hf) {
        u32x2 t[4];
#pragma unroll
        for (int m = 0; m < 4; ++m) t[m] = __builtin_bit_cast(u32x2, tr_read(tb + hf * 8192 + m * 2048));
        u32x4 oa, ob;
#pragma unroll
        for (int m = 0; m < 4; ++m) {
          oa[m] = __builtin_amdgcn_perm(t[m][1], t[m][0], 0x05040100u);
          ob[m] = __builtin_amdgcn_perm(t[m][1], t[m][0], 0x07060302u);
        }
        *(LDS u32x4*)(sc + hf * (16 * 144) + col * 144 + q * 32) = oa;
        *(LDS u32x4*)(sc + hf * (16 * 144) + col * 144 + q * 32 + 16) = ob;
      }
      wave_fence();
      const __amdgpu_buffer_rsrc_t rs = mk_rsrc(p.ws + WS_XT, DM * SL * 2);
#pragma unroll
      for (int u = 0; u < 4; ++u) {
        const int row = (lane >> 3) + 8 * (u & 1), hf = u >> 1;
        const u32x4 v = *(LDS const u32x4*)(sc + hf * (16 * 144) + row * 144 + (lane & 7) * 16);
        __builtin_amdgcn_raw_buffer_store_b128(v, rs, ((64 * g + 16 * pq + row) * SL + 128 * c + 64 * hf) * 2 + (lane & 7) * 16, 0, SC1);
      }
      __builtin_amdgcn_sched_barrier(0);
    }
    read_frags(s);
    __builtin_amdgcn_sched_barrier(0);
    if (HF == 0) { mma(); __builtin_amdgcn_sched_barrier(0); }
  }
  if (HF == 1) mma();
  __builtin_amdgcn_sched_barrier(0);
  STAMP(0, 3); CSTAMP(0, 61);
  const f32x2 Lr = {lam.r, lam.r}, Li = {lam.i, lam.i};
  f32x2 er = {0.f, 0.f}, ei = {0.f, 0.f};
#pragma unroll
  for (int t = 0; t < 8; ++t) {
    const f32x2 br_ = (t & 1) ? hi2(are[t >> 1]) : lo2(are[t >> 1]);
    const f32x2 bi_ = (t & 1) ? hi2(aim[t >> 1]) : lo2(aim[t >> 1]);
    const f32x2 nr = fma2(-Li, ei, fma2(Lr, er, br_));
    ei = fma2(Li, er, fma2(Lr, ei, bi_));
    er = nr;
  }
  const cf eA = {er[0], ei[0]};
  const cf e = cfma(l8, eA, cf{er[1], ei[1]});
  const int seg = 4 * HF + q;
  *(LDS f32x2*)(smem + G_E + (seg * 64 + pl) * 8) = f32x2{e.r, e.i};
  asm volatile("" :: "v"(wb[0]), "v"(wb[1]), "v"(wb[2]));
  STAMP(0, 4);
  bar();
  STAMP(0, 5);
  {
#pragma unroll
    for (int m = 0; m < 4; ++m) asm volatile("" : "+v"(are[m]), "+v"(aim[m]));
    const f32x2 Cr = {coef2.r, coef2.r}, Ci = {coef2.i, coef2.i};
#pragma unroll
    for (int m = 0; m < 4; ++m) {
      const f32x2 r0 = lo2(are[m]), r1 = hi2(are[m]), i0 = lo2(aim[m]), i1 = hi2(aim[m]);
      are[m] = cat2(fma2(-Ci, i0, Cr * r0), fma2(-Ci, i1, Cr * r1));
      aim[m] = cat2(fma2(-Ci, r0, -Cr * i0), fma2(-Ci, r1, -Cr * i1));
      asm volatile("" : "+v"(are[m]), "+v"(aim[m]));
    }
  }
  cf eAc = cmul(coef2, eA), pwc = cmul(coef2, pw);
  asm volatile("" : "+v"(eAc.r), "+v"(eAc.i), "+v"(pwc.r), "+v"(pwc.i));
  STAMP(0, 7);
  bar();
  STAMP(0, 8);
  cf cin;
  {
    f32x2 v[2];
#pragma unroll
    for (int i = 0; i < 2; ++i) v[i] = *(LDS const f32x2*)(smem + G_X + (i * 64 + pl) * 8);
    const f32x2 pf = *(LDS const f32x2*)(smem + G_PF + (seg * 64 + pl) * 8);
    const f32x2 S = v[0] + v[1];
    cin = cfma(pwc, cf{S[0], S[1]}, cmul(coef2, cf{pf[0], pf[1]}));
  }
  const cf cinB = cfma(l8, cin, eAc);
  er = f32x2{cin.r, cinB.r}; ei = f32x2{-cin.i, -cinB.i};
  f32x2 hr[8], hi[8];
#pragma unroll
  for (int t = 0; t < 8; ++t) {
    const f32x2 br_ = (t & 1) ? hi2(are[t >> 1]) : lo2(are[t >> 1]);
    const f32x2 bi_ = (t & 1) ? hi2(aim[t >> 1]) : lo2(aim[t >> 1]);
    const f32x2 nr = fma2(Li, ei, fma2(Lr, er, br_));
    ei = fma2(-Li, er, fma2(Lr, ei, bi_));
    er = nr;
    hr[t] = er; hi[t] = ei;
  }
  u32x4 o[4];
#pragma unroll
  for (int t = 0; t < 4; ++t) {
    o[0][t] = pack_bf16(hr[2 * t][0], hr[2 * t + 1][0]);
    o[1][t] = pack_bf16(hr[2 * t][1], hr[2 * t + 1][1]);
    o[2][t] = pack_bf16(hi[2 * t][0], hi[2 * t + 1][0]);
    o[3][t] = pack_bf16(hi[2 * t][1], hi[2 * t + 1][1]);
  }
  STAMP(0, 9);
  {
    const __amdgpu_buffer_rsrc_t rs = mk_rsrc(p.ws + WS_HB, 2 * DM * SL * 2);
    const int hb = (((c * 2 + HF) * 2) * 9 + g) * 8192 + pq * 2048;
    const int p0 = (16 * q + col) * 16, p1 = 1024 + (16 * q + (col ^ 4)) * 16;
    __builtin_amdgcn_raw_buffer_store_b128(o[0], rs, hb + p0, 0, SC1);
    __builtin_amdgcn_raw_buffer_store_b128(o[1], rs, hb + p1, 0, SC1);
    __builtin_amdgcn_raw_buffer_store_b128(o[2], rs, hb + 9 * 8192 + p0, 0, SC1);
    __builtin_amdgcn_raw_buffer_store_b128(o[3], rs, hb + 9 * 8192 + p1, 0, SC1);
    bar();
    wait_vm<0>();
    STAMP(0, 10);
    if (lane < 8)
      __hip_atomic_store((GLB u64*)(p.ws + WS_IN) + tile_block(2 * c + HF, lane) * 64 + IN_H + g * 4 + pq, FLAG_SET, RLX_AGENT);
  }
}

constexpr int S_CPY = 4160;
constexpr int S_X = 0;
constexpr int S_ST = 6 * 4 * S_CPY;
constexpr int S_Z = S_ST;
constexpr int S_ZSZ = (256 + SL) * 2;
constexpr int S_T = S_ST + 12 * 4608;
constexpr int S_TSZ = 1312;
constexpr int S_END = S_T + 6 * S_TSZ;
constexpr int S_CNT = 32768 + 8192;

DEV c2 cexp_k(float zl, float th, float tl, float k) {
  const float mag = __builtin_amdgcn_exp2f(zl * k);
  const float t = fmaf(tl, k, __builtin_amdgcn_fractf(th * k));
  return c2{mag * __builtin_amdgcn_cosf(t), mag * __builtin_amdgcn_sinf(t)};
}
#define PKM(d, a, b) "v_pk_mul_f32 " d ", " a ", " b " op_sel:[0,0] op_sel_hi:[0,1]\n\t"
#define PKF(d, a, b) "v_pk_fma_f32 " d ", " a ", " b ", " d " op_sel:[1,1,0] op_sel_hi:[1,0,1] neg_lo:[1,0,0]\n\t"
DEV void octet(LDS char* dst, c2 s0, c2 m1, c2 m2) {
  c2 v1, v2, v3, t0, t1, t2, t3;
  asm(PKM("%0", "%7", "%8") PKM("%3", "%7", "%9") PKF("%0", "%7", "%8") PKF("%3", "%7", "%9")
      PKM("%1", "%0", "%8") PKM("%4", "%0", "%9") PKF("%1", "%0", "%8") PKF("%4", "%0", "%9")
      PKM("%2", "%1", "%8") PKM("%5", "%1", "%9") PKF("%2", "%1", "%8") PKF("%5", "%1", "%9")
      PKM("%6", "%2", "%9") PKF("%6", "%2", "%9")
      : "=&v"(v1), "=&v"(v2), "=&v"(v3), "=&v"(t0), "=&v"(t1), "=&v"(t2), "=&v"(t3) : "v"(s0), "v"(m1), "v"(m2));
  *(LDS u32*)(dst + 0 * 288) = pack_bf16(s0[0], s0[1]);
  *(LDS u32*)(dst + 1 * 288) = pack_bf16(v1[0], v1[1]);
  *(LDS u32*)(dst + 2 * 288) = pack_bf16(v2[0], v2[1]);
  *(LDS u32*)(dst + 3 * 288) = pack_bf16(v3[0], v3[1]);
  *(LDS u32*)(dst + 4 * 288) = pack_bf16(t0[0], t0[1]);
  *(LDS u32*)(dst + 5 * 288) = pack_bf16(t1[0], t1[1]);
  *(LDS u32*)(dst + 6 * 288) = pack_bf16(t2[0], t2[1]);
  *(LDS u32*)(dst + 7 * 288) = pack_bf16(t3[0], t3[1]);
}
DEV u32 dup_lo(u32 a) { return __builtin_amdgcn_perm(a, a, 0x01000100u); }
DEV u32 dup_hi(u32 a) { return __builtin_amdgcn_perm(a, a, 0x03020302u); }
DEV u32 hi_lo(u32 a, u32 b) { return __builtin_amdgcn_alignbit(b, a, 16); }

DEV void role_s(const P& p, LDS char* smem, int sg, int w, int lane) {
  const int tid = w * 64 + lane;
  const int ch = w >> 1, hh = w & 1, d = 6 * sg + ch;
  const int n = d * 128 + 64 * hh + lane;
  const u16 r_ldt = p.ldtA[d], r_la = p.la[n], r_aim = p.aim[n], r_cre = p.creA[n], r_cim = p.cimA[n];
  STAMP(0, 1);
  f32x4 tab[3];
  [[maybe_unused]] u32 wc[2] = {0, 0};
  LDS char* st = smem + S_ST + w * 4608;
  {
    const float L2E = 1.44269504088896341f;
    const float dt = rbf(__builtin_amdgcn_exp2f(bf2f(r_ldt) * L2E));
    const float ar = -rbf(__builtin_amdgcn_exp2f(bf2f(r_la) * L2E));
    const float zl = ar * dt * L2E, zi = bf2f(r_aim) * dt;
    const float th = zi * 0.15915494309189535f;
    const float tl = fmaf(zi, 0.15915494309189535f, -th) + zi * 4.6017384e-10f;
    const c2 C = {2.0f * (bf2f(r_cre) * dt), 2.0f * (bf2f(r_cim) * dt)};
    const c2 w1 = cexp_k(zl, th, tl, 1.f), w8 = cexp_k(zl, th, tl, 8.f), w128 = cexp_k(zl, th, tl, 128.f);
    c2 w2, w4, w16, w32, w64, w256, w512, w1024, w2048;
    asm(PKM("%0", "%9", "%9") PKM("%2", "%10", "%10") PKM("%5", "%11", "%11") PKF("%0", "%9", "%9") PKF("%2", "%10", "%10") PKF("%5", "%11", "%11")
        PKM("%1", "%0", "%0") PKM("%3", "%2", "%2") PKM("%6", "%5", "%5") PKF("%1", "%0", "%0") PKF("%3", "%2", "%2") PKF("%6", "%5", "%5")
        PKM("%4", "%3", "%3") PKM("%7", "%6", "%6") PKF("%4", "%3", "%3") PKF("%7", "%6", "%6")
        PKM("%8", "%7", "%7") PKF("%8", "%7", "%7")
        : "=&v"(w2), "=&v"(w4), "=&v"(w16), "=&v"(w32), "=&v"(w64), "=&v"(w256), "=&v"(w512), "=&v"(w1024), "=&v"(w2048)
        : "v"(w1), "v"(w8), "v"(w128));
    c2 C128, C256, C512, C1024, C2048;
    asm(PKM("%0", "%5", "%6") PKM("%1", "%5", "%7") PKM("%2", "%5", "%8") PKM("%3", "%5", "%9") PKM("%4", "%5", "%10")
        PKF("%0", "%5", "%6") PKF("%1", "%5", "%7") PKF("%2", "%5", "%8") PKF("%3", "%5", "%9") PKF("%4", "%5", "%10")
        : "=&v"(C128), "=&v"(C256), "=&v"(C512), "=&v"(C1024), "=&v"(C2048)
        : "v"(C), "v"(w128), "v"(w256), "v"(w512), "v"(w1024), "v"(w2048));
#pragma unroll
    for (int i = 0; i < 2; ++i) {
      const int ln = i * 768 + tid, all = (sg % 12) * 864 + ln;
      if (ln < 864) wc[i] = *(const GLB u32*)((const char*)(all < 5184 ? p.cre : p.cim) + (size_t)(all < 5184 ? all : all - 5184) * 128);
    }
    STAMP(0, 2);
    LDS char* wr = st + 4 * lane;
    LDS const char* rd = st + (lane & 15) * 288 + (lane >> 4) * 16;
    bf16x8 B[4];
    const c2 w8c = {w8[0], -w8[1]}, w32c = {w32[0], -w32[1]}, w64c = {w64[0], -w64[1]};
    octet(wr, c2{1.f, 0.f}, w8c, w32c);
    octet(wr + 8 * 288, w64c, w8c, w32c);
    wave_fence();
#pragma unroll
    for (int ks = 0; ks < 4; ++ks) B[ks] = lds_frag(rd + 64 * ks);
    wave_fence();
#pragma unroll
    for (int mt = 0; mt < 3; ++mt) {
      if (mt == 0) { octet(wr, C, w1, w4); octet(wr + 8 * 288, C128, w1, w4); }
      if (mt == 1) { octet(wr, C256, w2, w128); octet(wr + 8 * 288, C512, w128, w4); }
      if (mt == 2) { octet(wr, C1024, w128, w512); *(LDS u32*)(wr + 8 * 288) = pack_bf16(C2048[0], C2048[1]); }
      wave_fence();
      f32x4 acc = {0, 0, 0, 0};
      bf16x8 A[4];
#pragma unroll
      for (int ks = 0; ks < 4; ++ks) A[ks] = lds_frag(rd + 64 * ks);
      __builtin_amdgcn_sched_barrier(0);
#pragma unroll
      for (int ks = 0; ks < 4; ++ks) acc = mfma16(A[ks], B[ks], acc);
      __builtin_amdgcn_sched_barrier(0);
      wave_fence();
      tab[mt] = acc;
    }
  }
  const int b = lane & 15, q = lane >> 4;
#pragma unroll
  for (int mt = 0; mt < 3; ++mt) *(LDS f32x4*)(st + mt * 1024 + lane * 16) = tab[mt];
  STAMP(0, 3);
  bar();
  {
    LDS const char* ot = smem + S_ST + (w ^ 1) * 4608 + lane * 16;
    LDS char* T = smem + S_T + ch * S_TSZ;
    if (hh == 0) {
      const f32x4 v0 = tab[0] + *(LDS const f32x4*)(ot), v2 = tab[2] + *(LDS const f32x4*)(ot + 2048);
      *(LDS u32x2*)(T + 2 * (128 * (q >> 1) + 8 * b + 4 * (q & 1))) = u32x2{pack_bf16(v0[0], v0[1]), pack_bf16(v0[2], v0[3])};
      const u32 a0 = pack_bf16(v2[0], v2[1]), a1 = pack_bf16(v2[2], v2[3]);
      if (q < 2) {
        LDS char* t3 = T + 2 * (512 + 64 * q + b);
        *(LDS u16*)(t3) = (u16)a0; *(LDS u16*)(t3 + 32) = (u16)(a0 >> 16);
        *(LDS u16*)(t3 + 64) = (u16)a1; *(LDS u16*)(t3 + 96) = (u16)(a1 >> 16);
      } else if (lane == 32) *(LDS u16*)(T + 2 * 640) = (u16)a0;
    } else {
      const f32x4 v1 = tab[1] + *(LDS const f32x4*)(ot + 1024);
      const u32 a0 = pack_bf16(v1[0], v1[1]), a1 = pack_bf16(v1[2], v1[3]);
      if (q < 2) *(LDS u32x2*)(T + 2 * (256 + 64 * q + 4 * b)) = u32x2{a0, a1};
      else {
        LDS char* t2 = T + 2 * (384 + 2 * b + (q - 2));
        *(LDS u16*)(t2) = (u16)a0; *(LDS u16*)(t2 + 64) = (u16)(a0 >> 16);
        *(LDS u16*)(t2 + 128) = (u16)a1; *(LDS u16*)(t2 + 192) = (u16)(a1 >> 16);
      }
    }
  }
  bar();
  STAMP(0, 4);
  u32x4 uv[2];
  {
    GLB u64* fl = (GLB u64*)(p.ws + WS_FL) + (8 * hh + (lane & 7)) * DM + d;
    asm volatile("" :: "v"(wc[0]), "v"(wc[1]));
    wait_flags(fl, true);
    const __amdgpu_buffer_rsrc_t rs = mk_rsrc(p.ws + WS_XT, DM * SL * 2);
#pragma unroll
    for (int v = 0; v < 2; ++v) uv[v] = __builtin_amdgcn_raw_buffer_load_b128(rs, (d * SL + 1024 * hh + 8 * (lane + 64 * v)) * 2, 0, SC1);
    if (lane < 8) __hip_atomic_store(fl, 0ull, RLX_AGENT);
  }
  STAMP(0, 5);
  {
    LDS const char* T = smem + S_T + ch * S_TSZ;
    u32 o[10];
    const u32 e512 = *(LDS const u16*)(T + 2 * 512);
    o[8] = o[9] = e512 | (e512 << 16);
    if (hh == 0) {
      const int off = lane < 16 ? 32 * lane : (lane < 32 ? 256 + 16 * lane : 512 + 8 * lane);
      u32x2 W[4];
#pragma unroll
      for (int i = 0; i < 4; ++i) W[i] = *(LDS const u32x2*)(T + off + 8 * i);
      if (lane < 16) {
#pragma unroll
        for (int i = 0; i < 8; ++i) o[i] = W[i >> 1][i & 1];
      } else if (lane < 32) {
#pragma unroll
        for (int i = 0; i < 4; ++i) {
          const u32 a = W[i >> 1][i & 1], nx = W[(i + 1) >> 1][(i + 1) & 1];
          o[2 * i] = dup_lo(a);
          o[2 * i + 1] = hi_lo(a, nx);
        }
      } else {
        const u32 w0 = W[0][0], w1 = W[0][1], w2 = W[1][0];
        o[0] = dup_lo(w0); o[1] = w0; o[2] = dup_hi(w0); o[3] = dup_lo(w1);
        o[4] = o[3]; o[5] = w1; o[6] = dup_hi(w1); o[7] = dup_lo(w2);
      }
    } else {
      const u32 w0 = *(LDS const u32*)(T + 1024 + 4 * lane), w1 = *(LDS const u32*)(T + 1028 + 4 * lane);
      o[0] = o[1] = dup_lo(w0); o[2] = w0; o[3] = o[4] = o[5] = dup_hi(w0); o[6] = o[7] = dup_lo(w1);
    }
    o[8] = (u32)__builtin_amdgcn_update_dpp((int)o[8], (int)o[0], 0x130, 0xf, 0xf, false);
    o[9] = (u32)__builtin_amdgcn_update_dpp((int)o[9], (int)o[1], 0x130, 0xf, 0xf, false);
    u32 a[9];
#pragma unroll
    for (int i = 0; i < 9; ++i) a[i] = hi_lo(o[i], o[i + 1]);
    LDS char* xb = smem + S_X + ch * 4 * S_CPY + 32 + 2 * (1024 * hh + 16 * lane);
    *(LDS u32x4*)(xb) = u32x4{o[0], o[1], o[2], o[3]};
    *(LDS u32x4*)(xb + 16) = u32x4{o[4], o[5], o[6], o[7]};
    *(LDS u32x4*)(xb + S_CPY) = u32x4{a[0], a[1], a[2], a[3]};
    *(LDS u32x4*)(xb + S_CPY + 16) = u32x4{a[4], a[5], a[6], a[7]};
    *(LDS u32x4*)(xb + 2 * S_CPY) = u32x4{o[1], o[2], o[3], o[4]};
    *(LDS u32x4*)(xb + 2 * S_CPY + 16) = u32x4{o[5], o[6], o[7], o[8]};
    *(LDS u32x4*)(xb + 3 * S_CPY) = u32x4{a[1], a[2], a[3], a[4]};
    *(LDS u32x4*)(xb + 3 * S_CPY + 16) = u32x4{a[5], a[6], a[7], a[8]};
    if (hh == 0 && lane == 0) {
      LDS char* hb = smem + S_X + ch * 4 * S_CPY;
      const u32x4 z = {0, 0, 0, 0};
      *(LDS u32x4*)(hb) = z;
      *(LDS u32x4*)(hb + 16) = z;
      *(LDS u32x4*)(hb + S_CPY) = z;
      *(LDS u32x4*)(hb + S_CPY + 16) = u32x4{0, 0, 0, o[0] << 16};
      *(LDS u32x4*)(hb + 2 * S_CPY) = z;
      *(LDS u32x4*)(hb + 2 * S_CPY + 16) = u32x4{0, 0, 0, o[0]};
      *(LDS u32x4*)(hb + 3 * S_CPY) = z;
      *(LDS u32x4*)(hb + 3 * S_CPY + 16) = u32x4{0, 0, o[0] << 16, a[0]};
    }
    if (tid < 6 * 32) *(LDS u32x4*)(smem + S_Z + (tid >> 5) * S_ZSZ + (tid & 31) * 16) = u32x4{0, 0, 0, 0};
    STAMP(0, 6);
    LDS char* z = smem + S_Z + ch * S_ZSZ + 512 + (1024 * hh + 8 * lane) * 2;
    *(LDS u32x4*)z = uv[0];
    *(LDS u32x4*)(z + 1024) = uv[1];
  }
  bar();
  STAMP(0, 7);
  f32x4 acc[8];
#pragma unroll
  for (int i = 0; i < 8; ++i) acc[i] = f32x4{0, 0, 0, 0};
  {
    const int kq = q, qq = b >> 2, pp = b & 3;
    LDS const char* fb = smem + S_X + (ch * 4 + ((4 - qq) & 3)) * S_CPY +
                         2 * (16 + 16 * (kq >> 1) - 8 * (kq & 1) + 4 * pp - (qq ? 4 : 0)) + 64 * hh;
    LDS const char* gb = smem + S_Z + ch * S_ZSZ + 512 + 32 * (b - (kq >> 1)) + 16 * (kq & 1) - 64 * hh;
#pragma unroll 1
    for (int it = 0; it < 4; ++it) {
      bf16x8 F[8], G[8];
#pragma unroll
      for (int s = 0; s < 8; ++s) {
        F[s] = tr_frag(fb + 512 * s, fb + 512 * s - 8);
        G[s] = lds_frag(gb + 512 * s);
      }
      __builtin_amdgcn_sched_barrier(0);
#pragma unroll
      for (int t = 0; t < 8; ++t)
#pragma unroll
        for (int s = 0; s <= t; ++s) acc[t] = mfma16(F[s], G[t - s], acc[t]);
      fb += 128;
      gb -= 128;
    }
  }
  __builtin_amdgcn_sched_barrier(0);
  STAMP(0, 8);
  bar();
  {
    LDS char* ex = smem + S_X + 49152 + w * 4096 + lane * 16;
#pragma unroll
    for (int i = 0; i < 4; ++i) {
      const f32x4 lo = acc[i], hi = acc[4 + i];
      *(LDS f32x4*)(ex + i * 1024) = hh ? +lo : +hi;
    }
    if (tid == 0) *(LDS u32*)(smem + S_CNT) = 0;
  }
  bar();
  u32x2 yo[4];
  {
    LDS const char* ey = smem + S_X + 49152 + (w ^ 1) * 4096 + lane * 16;
#pragma unroll
    for (int i = 0; i < 4; ++i) {
      const f32x4 lo = acc[i], hi = acc[4 + i];
      const f32x4 y = (hh ? +hi : +lo) + *(LDS const f32x4*)(ey + i * 1024);
      yo[i] = u32x2{pack_bf16(y[0], y[1]), pack_bf16(y[2], y[3])};
    }
  }
  bar();
  {
    const __amdgpu_buffer_rsrc_t rs = mk_rsrc(p.ws + WS_AT, DM * SL * 2);
    const int dst = (d * SL + 1024 * hh + 16 * (lane & 15) + 4 * (lane >> 4)) * 2;
#pragma unroll
    for (int i = 0; i < 4; ++i) __builtin_amdgcn_raw_buffer_store_b64(yo[i], rs, dst + 512 * i, 0, SC1);
  }
  STAMP(0, 9);
}
DEV void s_signal(const P& p, LDS char* smem, int sg, int lane) {
  u32 old = 0;
  if (lane == 0) old = __hip_atomic_fetch_add((LDS u32*)(smem + S_CNT), 1u, __ATOMIC_RELAXED, __HIP_MEMORY_SCOPE_WORKGROUP);
  if (__builtin_amdgcn_readfirstlane(old) == 11) {
    const int lt = lane & 31, second = lane >> 5;
    const int dd = 6 * sg + (second ? 5 : 0);
    const int idx = dd < 480 ? dd / 96 : 5 + (dd - 480) / 32;
    const int dfirst = idx < 5 ? 96 * idx : 480 + 32 * (idx - 5);
    __hip_atomic_store((GLB u64*)(p.ws + WS_IN) + tile_block(lt, idx) * 64 + IN_A + (sg - dfirst / 6), FLAG_SET, RLX_AGENT);
  }
}

DEV void role_gate(const P& p, int rb, int w, int lane, u64 pc) {
  u32 pf0 = 0, pf1 = 0;
  const int tid = w * 64 + lane;
  const int sub = lane & 7;
  u32x4 wv[9];
#pragma unroll
  for (int i = 0; i < 9; ++i) wv[i] = *(const u32x4*)((const char*)p.gw + (i * 8 + sub) * 16);
  for (int r = w * 8 + (lane >> 3); r < 128; r += 96) {
    const int l = rb * 128 + r;
    float s = 0.f;
    u32x4 xv[9];
#pragma unroll
    for (int i = 0; i < 9; ++i) xv[i] = *(const u32x4*)((const char*)p.x + (size_t)l * 1152 + (i * 8 + sub) * 16);
#pragma unroll
    for (int i = 0; i < 9; ++i) {
#pragma unroll
      for (int k = 0; k < 4; ++k) {
        const u32 pr = pack_bf16(bflo(xv[i][k]) * bflo(wv[i][k]), bfhi(xv[i][k]) * bfhi(wv[i][k]));
        s += bflo(pr);
        s += bfhi(pr);
      }
    }
    s += __shfl_xor(s, 1); s += __shfl_xor(s, 2); s += __shfl_xor(s, 4);
    const float gv = rbf(1.0f / (1.0f + expf(-rbf(s))));
    if (sub == 0)
      __hip_atomic_store((GLB u64*)(p.ws + WS_G) + l, ((u64)__float_as_uint(rbf(1.0f - gv)) << 32) | __float_as_uint(gv), RLX_AGENT);
  }
  asm volatile("" :: "v"(pf0), "v"(pf1));
  wait_vm<0>();
  bar();
  if (w == 0 && lane < 16)
    __hip_atomic_store((GLB u64*)(p.ws + WS_IN) + tile_block(2 * rb + (lane >> 3), lane & 7) * 64 + IN_G, FLAG_SET, RLX_AGENT);
}

constexpr int H_C = 32768, H_CS = 12288;
DEV int h_slot(int i) { return i < 4 ? i * 8192 : 131072 + (i - 4) * 8192; }
constexpr int H_AT = H_C + 2 * H_CS;
constexpr int H_XT = H_C + 3 * H_CS;
constexpr int H_G = H_C + 4 * H_CS;
constexpr int H_OUT = H_C + 5 * H_CS;
constexpr int K_LDS = 163840;

constexpr int h_wait(int s, int NDT) {
  int n = 0;
  int need = 0;
  for (int k = 0; k < 4; ++k) { n += 2; if (k == s) need = n; }
  for (int t = 0; t < s; ++t) {
    if (t == 0) { n += 2; if (s == 4) need = n; n += 2; if (s == 5) need = n; }
    else if (t == 1) { n += 2; if (s == 6) need = n; n += 2; if (s == 7) need = n; }
    else if (t <= 11) { n += 2 + NDT; if (s == t + 6) need = n; if (t == 2) n += 1; }
    else if (t == 12 || t == 13) n += NDT;
    else if (t == 14) n += 1;
  }
  return s == 17 ? 0 : n - need;
}
template <int S, int NDT, class F> DEV void h_steps(F&& after, u64& fv, char* wsp) {
  if constexpr (S == 9) asm volatile("s_waitcnt vmcnt(%1)" : "+v"(fv) : "n"(h_wait(S, NDT)) : "memory");
  else wait_vm<h_wait(S, NDT)>();
  LSTAMP2(1, 16 + S);
  bar();
  after(std::integral_constant<int, S>{});
  if constexpr (S < 17) h_steps<S + 1, NDT>(after, fv, wsp);
}
DEV int p2_rot(int d0) { const int idx = d0 < 480 ? d0 / 96 : 5 + (d0 - 480) / 32; return (idx * 18) >> 3; }

template <int NDT, class F>
DEV void p2_loader(const P& p, LDS char* smem, int lt, int d0, int lw, int lane, bool scratch_barrier, F&& pre_poll) {
  __builtin_amdgcn_s_setprio(3);
  const char* hb = p.ws + WS_HB + (size_t)lt * 18 * 8192 + (2 * lw) * 1024 + lane * 16;
  constexpr int W = 32 * NDT;
  const int l0 = 64 * lt, rot = p2_rot(d0);
  u32 co[NDT];
#pragma unroll
  for (int i = 0; i < NDT; ++i) {
    const int row = 8 * (NDT * lw + i) + (lane >> 3), cp = lane & 7;
    co[i] = (u32)(d0 + row) * 1152u + ((cp ^ (row & 7)) << 4);
  }
  auto issue_c = [&](int s) {
    LDS char* buf = smem + H_C + (s & 7) * H_CS + NDT * lw * 1024;
    int ks = s + rot; ks = ks >= 18 ? ks - 18 : ks;
    const char* cb = ks < 9 ? (const char*)p.cre + ks * 128 : (const char*)p.cim + (ks - 9) * 128;
#pragma unroll
    for (int i = 0; i < NDT; ++i) dma16<0>(cb + co[i], buf + i * 1024);
  };
  auto issue_h = [&](int s) {
    LDS char* buf = smem + h_slot(s & 7) + 2 * lw * 1024;
    int ks = s + rot; ks = ks >= 18 ? ks - 18 : ks;
#pragma unroll
    for (int i = 0; i < 2; ++i) dma16<SC1>(hb + ks * 8192 + i * 1024, buf + i * 1024);
  };
  GLB u64* inb = (GLB u64*)(p.ws + WS_IN) + blockIdx.x * 64;
  const int nA = (d0 + W - 1) / 6 - d0 / 6 + 1;
  const bool need = lane == IN_G || (lane >= IN_A && lane < IN_A + nA);
  if (scratch_barrier) bar();
  else {
#pragma unroll 1
    for (int s = 0; s < 8; ++s) issue_c(s);
  }
  pre_poll();
  { [[maybe_unused]] const int w = lw + 8; STAMP(1, 1); }
  wait_flags(inb + IN_H + (lane < 36 ? lane : 0), true);
  { [[maybe_unused]] const int w = lw + 8; STAMP(1, 2); }
#pragma unroll
  for (int s = 0; s < 4; ++s) issue_h(s);
  { [[maybe_unused]] const int w = lw + 8; STAMP(1, 3); }
  u64 fv = 0;
  h_steps<0, NDT>([&](auto S_) {
    constexpr int S = decltype(S_)::value;
    if constexpr (S == 0) { issue_h(4); issue_h(5); }
    if constexpr (S == 1) { issue_h(6); issue_h(7); }
    if constexpr (S >= 2 && S <= 11) { issue_h(S + 6); issue_c(S + 6); }
    if constexpr (S == 2)
      asm volatile("global_load_dwordx2 %0, %1, off sc1" : "=v"(fv) : "v"(inb + lane) : "memory");
    if constexpr (S == 9) { if (!__all(!need || fv == FLAG_SET)) wait_flags(inb + lane, need); }
    if constexpr (S == 12) {
#pragma unroll
      for (int i = 0; i < NDT; ++i) {
        const int pc = NDT * lw + i;
        dma16<SC1>(p.ws + WS_AT + (size_t)(d0 + 8 * pc + (lane >> 3)) * 4096 + l0 * 2 + (lane & 7) * 16, smem + H_AT + pc * 1024);
      }
    }
    if constexpr (S == 13) {
#pragma unroll
      for (int i = 0; i < NDT; ++i) {
        const int pc = NDT * lw + i, id = pc * 64 + lane, row = id / (4 * NDT), qq = id - row * (4 * NDT);
        dma16<0>((const char*)p.x + (size_t)(l0 + row) * 1152 + d0 * 2 + qq * 16, smem + H_XT + pc * 1024);
      }
    }
    if constexpr (S == 14) { if (lane < 8) dma16<SC1>(p.ws + WS_G + (size_t)l0 * 8 + lw * 128 + lane * 16, smem + H_G + lw * 128); }
  }, fv, p.ws);
}

template <int NDT, int STAG>
DEV void p2_compute(const P& p, LDS char* smem, int lt0, int d0, int w, int lane) {
  const int lh = (w >> 1) & 1, kh = w & 1, dg = w >> 2;
  const int r = lane & 15, kq = lane >> 4;
  const int lt = 2 * lh + kh, l0 = 64 * lt0;
  constexpr int W = 32 * NDT;
  u32x2 da[NDT], db[NDT];
#pragma unroll
  for (int t = 0; t < NDT; ++t) {
    const int d = d0 + 16 * (NDT * dg + t) + 4 * kq;
    da[t] = *(const u32x2*)(p.dskA + d);
    db[t] = *(const u32x2*)(p.dskB + d);
  }
  GLB u64* inb = (GLB u64*)(p.ws + WS_IN) + blockIdx.x * 64;

  f32x4 acc[2][NDT];
#pragma unroll
  for (int i = 0; i < 2; ++i)
#pragma unroll
    for (int t = 0; t < NDT; ++t) acc[i][t] = f32x4{0, 0, 0, 0};
  int ho[2];
#pragma unroll
  for (int u = 0; u < 2; ++u) {
    const int k = 32 * kh + 8 * kq + 4 * u + (r >> 2), i = (r & 3) >> 1;
    ho[u] = (2 * (k >> 4) + i) * 1024 + (32 * lh + ((k & 15) ^ (4 * i))) * 16 + (r & 1) * 8;
  }
  const int co = H_C + (16 * NDT * dg + r) * 128 + (((4 * kh + kq) ^ (r & 7)) << 4);
  constexpr int NREG = 0, NRING = 18 - NREG;
  bf16x8 cr[NREG ? NREG : 1][NDT];
  if constexpr (NREG > 0) {
    const int rot = p2_rot(d0);
#pragma unroll
    for (int i = 0; i < NREG; ++i) {
      int ks = NRING + i + rot; ks = ks >= 18 ? ks - 18 : ks;
      const char* cb = (ks < 9 ? (const char*)p.cre + ks * 128 : (const char*)p.cim + (ks - 9) * 128) +
                       (size_t)(d0 + 16 * NDT * dg + r) * 1152 + (4 * kh + kq) * 16;
#pragma unroll
      for (int t = 0; t < NDT; ++t) cr[i][t] = *(const GLB bf16x8*)(cb + t * 16 * 1152);
    }
  }
  bf16x8 hb[2], ca[NDT];
#pragma unroll
  for (int i = 0; i < 2; ++i) hb[i] = bf16x8{};
#pragma unroll
  for (int t = 0; t < NDT; ++t) ca[t] = bf16x8{};
  auto mma_with = [&](const bf16x8 (&cc)[NDT]) {
#pragma unroll
    for (int t = 0; t < NDT; ++t) {
      acc[0][t] = mfma16(cc[t], hb[0], acc[0][t]);
      acc[1][t] = mfma16(cc[t], hb[1], acc[1][t]);
    }
    __builtin_amdgcn_sched_barrier(0);
  };
  auto read_h = [&](int s) {
    LDS const char* hbuf = smem + h_slot(s & 7);
#pragma unroll
    for (int i = 0; i < 2; ++i) hb[i] = tr_frag(hbuf + ho[0] + i * 256, hbuf + ho[1] + i * 256);
  };
  STAMP(1, 1);
#pragma unroll 1
  for (int s = 0; s < NRING; ++s) {
    bar();
    if (s == 0) STAMP(1, 2);
    LSTAMP(1, 16 + s);
    if (STAG) mma_with(ca);
    read_h(s);
    LDS const char* cbuf = smem + (s & 7) * H_CS;
#pragma unroll
    for (int t = 0; t < NDT; ++t) ca[t] = lds_frag(cbuf + co + t * 2048);
    __builtin_amdgcn_sched_barrier(0);
    if (!STAG) mma_with(ca);
  }
#pragma unroll
  for (int i = 0; i < NREG; ++i) {
    bar();
    if (STAG) { if (i == 0) mma_with(ca); else mma_with(cr[i ? i - 1 : 0]); }
    read_h(NRING + i);
    __builtin_amdgcn_sched_barrier(0);
    if (!STAG) mma_with(cr[i]);
  }
  if (STAG) { if (NREG) mma_with(cr[NREG ? NREG - 1 : 0]); else mma_with(ca); }
  __builtin_amdgcn_sched_barrier(0);
  STAMP(1, 3);
  bar();
  if (w == 0) __hip_atomic_store(inb + lane, 0ull, RLX_AGENT);
  LDS char* ex = smem + w * (NDT * 1024) + lane * 16;
#pragma unroll
  for (int t = 0; t < NDT; ++t) {
    const f32x4 a0 = acc[0][t], a1 = acc[1][t];
    *(LDS f32x4*)(ex + t * 1024) = kh ? +a0 : +a1;
  }
  bar();
  LDS const char* ey = smem + (w ^ 1) * (NDT * 1024) + lane * 16;
  constexpr int OSTR = W * 4 + 16;
  const f32x2 gg = *(LDS const f32x2*)(smem + H_G + (16 * lt + r) * 8);
#pragma unroll
  for (int t = 0; t < NDT; ++t) {
    const int dt = NDT * dg + t;
    const f32x4 o = *(LDS const f32x4*)(ey + t * 1024);
    const f32x4 a0 = acc[0][t], a1 = acc[1][t];
    const f32x4 y = (kh ? +a1 : +a0) + o;
    const s16x4 av = tr_read(smem + H_AT + (16 * dt + 4 * kq + (r >> 2)) * 128 + lt * 32 + (r & 3) * 8);
    const u32x2 xv = *(LDS const u32x2*)(smem + H_XT + (16 * lt + r) * (W * 2) + (16 * dt + 4 * kq) * 2);
    f32x4 res;
#pragma unroll
    for (int i = 0; i < 4; ++i) {
      const u32 xw = xv[i >> 1], aw = da[t][i >> 1], bw = db[t][i >> 1];
      const float xf = (i & 1) ? bfhi(xw) : bflo(xw);
      const float sa = (i & 1) ? bfhi(aw) : bflo(aw);
      const float sb = (i & 1) ? bfhi(bw) : bflo(bw);
      const float yc = bf2f((u16)av[i]);
      const float a = rbf(yc + rbf(xf * sa));
      const float b = y[i] + rbf(xf * sb);
      res[i] = rbf(gg[0] * a) + gg[1] * b;
    }
    *(LDS f32x4*)(smem + H_OUT + (16 * lt + r) * OSTR + (16 * dt + 4 * kq) * 4) = res;
  }
  STAMP(1, 4);
  bar();
#pragma unroll
  for (int it = 0; it < NDT; ++it) {
    const int id = it * 512 + w * 64 + lane;
    const int row = id / (8 * NDT), pc = id - row * (8 * NDT);
    const f32x4 v = *(LDS const f32x4*)(smem + H_OUT + row * OSTR + pc * 16);
    __builtin_amdgcn_raw_buffer_store_b128(__builtin_bit_cast(u32x4, v), mk_rsrc(p.out, SL * DM * 4), ((l0 + row) * DM + d0 + pc * 4) * 4, 0, OUT_AUX);
  }
}

template <int NDT>
DEV void phase2(const P& p, LDS char* smem, int lt, int d0, int w, int lane, bool scratch_barrier, int sg) {
  STAMP(1, 0);
  if (w >= 8) {
    p2_loader<NDT>(p, smem, lt, d0, w - 8, lane, scratch_barrier, [&]() { if (NDT == 1) { wait_vm<8>(); s_signal(p, smem, sg, lane); } });
  } else {
    if (NDT == 1) { wait_vm<0>(); s_signal(p, smem, sg, lane); }
    if (w < 4) p2_compute<NDT, 0>(p, smem, lt, d0, w, lane);
    else p2_compute<NDT, 1>(p, smem, lt, d0, w, lane);
  }
  STAMP(1, 15);
}

__global__ __launch_bounds__(768, 3) void gated_mix_s4d_s5(P p) {
  extern __shared__ __attribute__((aligned(16))) char smem_raw[];
  LDS char* smem = (LDS char*)smem_raw;
  u64 pc;
  asm volatile("s_getpc_b64 %0" : "=s"(pc));
  u32 pf = 0;
  if (threadIdx.x < PF_TOP)
    asm volatile("global_load_dword %0, %1, off" : "=v"(pf) : "v"((pc & ~255ull) + threadIdx.x * 128));
  const int w = __builtin_amdgcn_readfirstlane(threadIdx.x >> 6), lane = threadIdx.x & 63;
  const int xcd = blockIdx.x & 7, j = blockIdx.x >> 3;
  STAMP(0, 0);
  if (j >= 18 && j < 30) {
    const int sg = 12 * xcd + (j - 18);
    role_s(p, smem, sg, w, lane);
    STAMP(0, 15);
    phase2<1>(p, smem, 4 * xcd + (j - 18) / 3, 480 + 32 * ((j - 18) % 3), w, lane, false, sg);
  } else {
    const bool isg = j < 18;
    if (isg) {
      const int c = 2 * xcd + j / 9, g = j % 9;
      if (w >= 8) g_loader(p, smem, c, g, 96 * (j % 5), w - 8, lane);
      else if (w < 4) g_compute<0>(p, smem, c, g, w, lane);
      else g_compute<1>(p, smem, c, g, w, lane);
    } else {
      role_gate(p, 2 * xcd + (j - 30), w, lane, pc);
    }
    STAMP(0, 15);
    const int e = isg ? j : j - 12;
    phase2<3>(p, smem, 4 * xcd + e / 5, 96 * (e % 5), w, lane, isg, 0);
  }
  asm volatile("s_waitcnt vmcnt(0)" :: "v"(pf));
}

extern "C" void kernel_launch(void* const* d_in, const int* in_sizes, int n_in, void* d_out, int out_size,
                              void* d_ws, size_t ws_size, hipStream_t stream) {
  P a;
  a.x = (const u16*)d_in[0]; a.la = (const u16*)d_in[1]; a.aim = (const u16*)d_in[2];
  a.creA = (const u16*)d_in[3]; a.cimA = (const u16*)d_in[4]; a.ldtA = (const u16*)d_in[5]; a.dskA = (const u16*)d_in[6];
  a.lre = (const u16*)d_in[8]; a.lim = (const u16*)d_in[9]; a.bre = (const u16*)d_in[10]; a.bim = (const u16*)d_in[11];
  a.cre = (const u16*)d_in[12]; a.cim = (const u16*)d_in[13]; a.ldtB = (const u16*)d_in[14];
  a.dskB = (const u16*)d_in[15]; a.gw = (const u16*)d_in[16];
  a.ws = (char*)d_ws; a.out = (float*)d_out;
  (void)hipFuncSetAttribute((const void*)gated_mix_s4d_s5, hipFuncAttributeMaxDynamicSharedMemorySize, K_LDS);
  gated_mix_s4d_s5<<<256, 768, K_LDS, stream>>>(a);
}
```

```cpp
#define OUT_AUX 16
#define FIXED_T 4000
#define FIXED_WG blockIdx.x == 0
#include <hip/hip_runtime.h>
#include <hip/hip_bf16.h>
#include <stdint.h>
#include <type_traits>

typedef unsigned short u16;
typedef unsigned int u32;
typedef unsigned long long u64;
typedef __attribute__((ext_vector_type(8))) __bf16 bf16x8;
typedef __attribute__((ext_vector_type(2))) __bf16 bf16x2;
typedef __attribute__((ext_vector_type(4))) short s16x4;
typedef __attribute__((ext_vector_type(4))) float f32x4;
typedef __attribute__((ext_vector_type(2))) float f32x2;
typedef __attribute__((ext_vector_type(4))) unsigned u32x4;
typedef __attribute__((ext_vector_type(2))) unsigned u32x2;
#define LDS __attribute__((address_space(3)))
#define GLB __attribute__((address_space(1)))
#define DEV __device__ __forceinline__
#define RLX_AGENT __ATOMIC_RELAXED, __HIP_MEMORY_SCOPE_AGENT

constexpr int SL = 2048;
constexpr int DM = 576;

constexpr size_t WS_HB = 0;
constexpr size_t WS_AT = WS_HB + (size_t)2 * DM * SL * 2;
constexpr size_t WS_G = WS_AT + (size_t)DM * SL * 2;
constexpr size_t WS_GRAN = WS_G + (size_t)SL * 8;
constexpr size_t WS_EP = WS_GRAN + (size_t)9 * 16 * 2 * 64 * 8;
constexpr size_t WS_FL = WS_EP + 4096;
constexpr size_t WS_XT = WS_FL + (size_t)16 * DM * 8;
constexpr size_t WS_IN = WS_XT + (size_t)DM * SL * 2;
constexpr u64 FLAG_SET = 0x9E3779B97F4A7C15ull;
constexpr u32 SPIN_CAP = 1u << 17;
constexpr int IN_H = 0, IN_G = 36, IN_A = 40;

constexpr size_t WS_ST = (size_t)16 << 20;
#define STAMP(kern, idx) do {} while (0)
#define CSTAMP(kern, idx) do {} while (0)
#define LSTAMP(kern, idx) do {} while (0)
#define LSTAMP2(kern, idx) do {} while (0)

DEV float bf2f(u16 v) { return __uint_as_float((u32)v << 16); }
DEV float bflo(u32 v) { return __uint_as_float(v << 16); }
DEV float bfhi(u32 v) { return __uint_as_float(v & 0xffff0000u); }
DEV u32 pack_bf16(float lo, float hi) {
  f32x2 v = {lo, hi};
  bf16x2 b = __builtin_convertvector(v, bf16x2);
  return __builtin_bit_cast(u32, b);
}
DEV float rbf(float x) { return bflo(pack_bf16(x, x)); }
template <int N> DEV void wait_vm() { asm volatile("s_waitcnt vmcnt(%0)" ::"n"(N) : "memory"); }
DEV void bar() { asm volatile("s_waitcnt lgkmcnt(0)\n\ts_barrier" ::: "memory"); }
DEV void wave_fence() { __builtin_amdgcn_fence(__ATOMIC_ACQ_REL, "wavefront"); }
template <int AUX> DEV void dma16(const void* src, LDS char* dst) {
  __builtin_amdgcn_global_load_lds((const GLB u32*)src, (LDS u32*)dst, 16, 0, AUX);
}
constexpr int SC1 = 16;
#define WS_OFF 0
#define XT_WAIT 3
DEV bf16x8 lds_frag(LDS const char* p) { return *(LDS const bf16x8*)p; }
DEV s16x4 tr_read(LDS const char* a) { return __builtin_amdgcn_ds_read_tr16_b64_v4i16((LDS s16x4*)a); }
DEV bf16x8 tr_frag(LDS const char* a, LDS const char* b) {
  const s16x4 lo = tr_read(a), hi = tr_read(b);
  return __builtin_bit_cast(bf16x8, __builtin_shufflevector(lo, hi, 0, 1, 2, 3, 4, 5, 6, 7));
}
DEV f32x4 mfma16(bf16x8 a, bf16x8 b, f32x4 c) { return __builtin_amdgcn_mfma_f32_16x16x32_bf16(a, b, c, 0, 0, 0); }
DEV __amdgpu_buffer_rsrc_t mk_rsrc(const void* base, int bytes) {
  return __builtin_amdgcn_make_buffer_rsrc(const_cast<void*>(base), (short)0, bytes, 0x00020000);
}
struct cf { float r, i; };
DEV cf cmul(cf a, cf b) { return {a.r * b.r - a.i * b.i, a.r * b.i + a.i * b.r}; }
DEV cf cfma(cf a, cf b, cf c) { return {fmaf(a.r, b.r, fmaf(-a.i, b.i, c.r)), fmaf(a.r, b.i, fmaf(a.i, b.r, c.i))}; }
typedef f32x2 c2;
DEV c2 cm2(c2 a, c2 b) {
  c2 t, r;
  asm("v_pk_mul_f32 %0, %1, %2 op_sel:[0,0] op_sel_hi:[0,1]" : "=v"(t) : "v"(a), "v"(b));
  asm("v_pk_fma_f32 %0, %1, %2, %3 op_sel:[1,1,0] op_sel_hi:[1,0,1] neg_lo:[1,0,0]" : "=v"(r) : "v"(a), "v"(b), "v"(t));
  return r;
}
DEV f32x2 fma2(f32x2 a, f32x2 b, f32x2 c) { return __builtin_elementwise_fma(a, b, c); }
DEV f32x2 lo2(f32x4 v) { return __builtin_shufflevector(v, v, 0, 1); }
DEV f32x2 hi2(f32x4 v) { return __builtin_shufflevector(v, v, 2, 3); }
DEV f32x4 cat2(f32x2 a, f32x2 b) { return __builtin_shufflevector(a, b, 0, 1, 2, 3); }
DEV void wait_flags(GLB u64* word, bool need) {
  for (u32 spin = 0;; ++spin) {
    const u64 v = __hip_atomic_load(word, RLX_AGENT);
    if (__all(!need || v == FLAG_SET) || spin > SPIN_CAP) break;
    __builtin_amdgcn_s_sleep(2);
  }
}
DEV int tile_block(int lt, int idx) {
  const int xcd = lt >> 2, a = lt & 3;
  int j;
  if (idx < 5) { const int e = 5 * a + idx; j = e < 18 ? e : e + 12; }
  else j = 18 + 3 * a + (idx - 5);
  return (j << 3) | xcd;
}

struct P {
  const u16 *x, *la, *aim, *creA, *cimA, *ldtA, *dskA;
  const u16 *lre, *lim, *bre, *bim, *cre, *cim, *ldtB, *dskB, *gw;
  char* ws;
  float* out;
};

constexpr int G_NB = 4, G_STAGE = 32768;
constexpr int G_E = G_NB * G_STAGE;
constexpr int G_PF = G_E + 4096;
constexpr int G_L = G_PF + 4096;
constexpr int G_X = G_L + 1024;
constexpr int G_T = G_X + 2048;
constexpr int G_END = G_T + 4 * 32 * 144;

DEV void g_loader(const P& p, LDS char* smem, int c, int g, int d0, int lw, int lane) {
  [[maybe_unused]] const int w = lw + 8;
  __builtin_amdgcn_s_setprio(3);
  GLB u32* ep = (GLB u32*)(p.ws + WS_EP + g * 256);
  GLB u64* gran = (GLB u64*)(p.ws + WS_GRAN) + (size_t)g * 16 * 128;
  const u32 tag = __hip_atomic_load(ep, RLX_AGENT) + 1u;
  u32 xo[4], bo[4];
#pragma unroll
  for (int i = 0; i < 4; ++i) {
    const int rho = 8 * (4 * lw + i) + (lane >> 3);
    const int hf = rho >> 6, m = (rho >> 4) & 3, r = rho & 15;
    const int tau = 64 * hf + 16 * (r >> 2) + 8 * (r & 1) + 2 * m + ((r >> 1) & 1);
    const int ch = (lane & 7) ^ (rho & 7);
    xo[i] = (u32)(128 * c + tau) * 1152u + ch * 16;
    const int pp = 64 * g + 16 * (rho >> 5) + r;
    bo[i] = (u32)pp * 1152u + ch * 16;
  }
  auto issue = [&](int s) {
    LDS char* buf = smem + (s & (G_NB - 1)) * G_STAGE + lw * 4096;
    int kb = g + s; kb = (kb >= 9 ? kb - 9 : kb) * 128;
#pragma unroll
    for (int i = 0; i < 4; ++i) dma16<0>((const char*)p.x + (xo[i] + kb), buf + i * 1024);
#pragma unroll
    for (int i = 0; i < 4; ++i) {
      const char* base = (const char*)((((4 * lw + i) >> 1) & 1) ? p.bim : p.bre);
      dma16<0>(base + (bo[i] + kb), buf + 16384 + i * 1024);
    }
  };
#pragma unroll
  for (int s = 0; s < G_NB - 1; ++s) issue(s);
#pragma unroll 1
  for (int s = 0; s < 6; ++s) { wait_vm<16>(); LSTAMP(0, 16 + s); bar(); issue(s + G_NB - 1); LSTAMP(0, 32 + s); }
  wait_vm<16>(); bar();
  wait_vm<8>(); bar();
  wait_vm<0>(); bar();
  STAMP(0, 1);
  if (lw < 2) {
    bar();
    u32 co[6];
#pragma unroll
    for (int i = 0; i < 6; ++i) {
      const int row = 8 * (6 * lw + i) + (lane >> 3), cp = lane & 7;
      co[i] = (u32)(d0 + row) * 1152u + ((cp ^ (row & 7)) << 4);
    }
    const int rot = ((d0 / 96) * 18) >> 3;
#pragma unroll 1
    for (int s = 0; s < 8; ++s) {
      LDS char* buf = smem + 32768 + s * 12288 + 6 * lw * 1024;
      const int ks = s + rot;
      const char* cb = ks < 9 ? (const char*)p.cre + ks * 128 : (const char*)p.cim + (ks - 9) * 128;
#pragma unroll
      for (int i = 0; i < 6; ++i) dma16<0>(cb + co[i], buf + i * 1024);
    }
    bar();
    return;
  }
  bar();
  STAMP(0, 2);
  if (lw == 3) {
    const f32x2 lv = *(LDS const f32x2*)(smem + G_L + lane * 8);
    const cf l16 = {lv[0], lv[1]};
    f32x2 ev[8];
#pragma unroll
    for (int s = 0; s < 8; ++s) ev[s] = *(LDS const f32x2*)(smem + G_E + (s * 64 + lane) * 8);
    cf tot = {0.f, 0.f};
    cf pre[8];
#pragma unroll
    for (int s = 0; s < 8; ++s) { pre[s] = tot; tot = cfma(l16, tot, cf{ev[s][0], ev[s][1]}); }
    if (c < 15) {
      __hip_atomic_store(gran + c * 128 + lane, ((u64)tag << 32) | __float_as_uint(tot.r), RLX_AGENT);
      __hip_atomic_store(gran + c * 128 + 64 + lane, ((u64)tag << 32) | __float_as_uint(tot.i), RLX_AGENT);
    }
#pragma unroll
    for (int s = 0; s < 8; ++s) *(LDS f32x2*)(smem + G_PF + (s * 64 + lane) * 8) = f32x2{pre[s].r, pre[s].i};
  }
  const int par = lw - 2;
  const f32x2 lv = *(LDS const f32x2*)(smem + G_L + 512 + lane * 8);
  const cf l128 = {lv[0], lv[1]};
  const cf l256 = cmul(l128, l128);
  cf acc = {0.f, 0.f};
  if (par < c) {
    const int nc = (c - par + 1) >> 1;
    u64 a[8], b[8];
    for (u32 spin = 0;; ++spin) {
      bool ok = true;
#pragma unroll
      for (int i = 0; i < 8; ++i) {
        const int cc = par + 2 * (i < nc ? i : 0);
        a[i] = __hip_atomic_load(gran + cc * 128 + lane, RLX_AGENT);
        b[i] = __hip_atomic_load(gran + cc * 128 + 64 + lane, RLX_AGENT);
      }
#pragma unroll
      for (int i = 0; i < 8; ++i) ok = ok && (u32)(a[i] >> 32) == tag && (u32)(b[i] >> 32) == tag;
      if (__all(ok) || spin > SPIN_CAP) break;
      __builtin_amdgcn_s_sleep(1);
    }
#pragma unroll
    for (int i = 0; i < 8; ++i)
      if (i < nc) acc = cfma(l256, acc, cf{__uint_as_float((u32)a[i]), __uint_as_float((u32)b[i])});
    if (c - 1 - (par + 2 * (nc - 1))) acc = cmul(acc, l128);
  }
  *(LDS f32x2*)(smem + G_X + (par * 64 + lane) * 8) = f32x2{acc.r, acc.i};
  STAMP(0, 3);
  bar();
  if (c == 15 && lw == 2 && lane == 0) __hip_atomic_store(ep, tag, RLX_AGENT);
}

template <int HF>
DEV void g_compute(const P& p, LDS char* smem, int c, int g, int w, int lane) {
  const int pq = w & 3, col = lane & 15, q = lane >> 4;
  const int pl = 16 * pq + col, pp = 64 * g + pl;
  cf lam, coef2;
  {
    const float lre = bf2f(p.lre[pp]), lim = bf2f(p.lim[pp]);
    const float dt = rbf(expf(bf2f(p.ldtB[pp])));
    const float zr = dt * lre, zi = dt * lim;
    const float er = expf(zr);
    float sn, cs;
    sincosf(zi, &sn, &cs);
    lam = {er * cs, er * sn};
    const float nr = lam.r - 1.0f, ni = lam.i;
    const float den = 1.0f / (lre * lre + lim * lim);
    coef2 = {2.0f * (nr * lre + ni * lim) * den, 2.0f * (ni * lre - nr * lim) * den};
  }
  cf l8 = cmul(lam, lam); l8 = cmul(l8, l8); l8 = cmul(l8, l8);
  const cf l16 = cmul(l8, l8);
  const cf l32 = cmul(l16, l16), l64 = cmul(l32, l32);
  if (HF == 0 && q == 0) {
    const cf l128 = cmul(l64, l64);
    *(LDS f32x2*)(smem + G_L + pl * 8) = f32x2{l16.r, l16.i};
    *(LDS f32x2*)(smem + G_L + 512 + pl * 8) = f32x2{l128.r, l128.i};
  }
  cf pw = HF ? l64 : cf{1.f, 0.f};
  { const cf t = cmul(pw, l16); if (q & 1) pw = t; }
  { const cf t = cmul(pw, l32); if (q & 2) pw = t; }
  u32 wb[3] = {0, 0, 0};
  if (HF == 0) {
#pragma unroll
    for (int i = 0; i < 3; ++i) {
      const int ln = i * 256 + pq * 64 + lane;
      if (ln < 576) {
        const int row = ln / 9, kb = ln - row * 9;
        wb[i] = *(const GLB u32*)((const char*)((c & 1) ? p.bim : p.bre) + (size_t)(64 * g + row) * 1152 + kb * 128);
      }
    }
  }
  asm volatile("" : "+v"(lam.r), "+v"(lam.i), "+v"(coef2.r), "+v"(coef2.i), "+v"(l8.r), "+v"(l8.i), "+v"(pw.r), "+v"(pw.i));
  f32x4 are[4], aim[4];
#pragma unroll
  for (int m = 0; m < 4; ++m) { are[m] = f32x4{0, 0, 0, 0}; aim[m] = f32x4{0, 0, 0, 0}; }
  const int o0 = col * 128 + (((0 + q) ^ (col & 7)) << 4);
  const int o1 = col * 128 + (((4 + q) ^ (col & 7)) << 4);
  bf16x8 br[2], bi[2], a[2][4];
  auto read_frags = [&](int s) {
    LDS const char* buf = smem + (s & (G_NB - 1)) * G_STAGE;
    LDS const char* ab = buf + HF * 8192;
    LDS const char* bb = buf + 16384 + pq * 4096;
#pragma unroll
    for (int ks = 0; ks < 2; ++ks) {
      const int o = ks ? o1 : o0;
      br[ks] = lds_frag(bb + o); bi[ks] = lds_frag(bb + 2048 + o);
#pragma unroll
      for (int m = 0; m < 4; ++m) a[ks][m] = lds_frag(ab + m * 2048 + o);
    }
  };
  auto mma = [&]() {
#pragma unroll
    for (int ks = 0; ks < 2; ++ks)
#pragma unroll
      for (int m = 0; m < 4; ++m) {
        are[m] = mfma16(a[ks][m], br[ks], are[m]);
        aim[m] = mfma16(a[ks][m], bi[ks], aim[m]);
      }
  };
  STAMP(0, 1);
#pragma unroll
  for (int s = 0; s < 9; ++s) {
    if (HF == 1 && s == XT_WAIT) {
      wait_vm<0>();
      if (lane < 16) __hip_atomic_store((GLB u64*)(p.ws + WS_FL) + c * DM + 64 * g + 16 * pq + lane, FLAG_SET, RLX_AGENT);
    }
    bar();
    if (s == 0) { STAMP(0, 2); CSTAMP(0, 60); }
    if (HF == 1 && s > 0) { mma(); __builtin_amdgcn_sched_barrier(0); }
    if (HF == 1 && s == 0) {
      const int rr = 4 * q + (col >> 2);
      LDS const char* tb = smem + rr * 128 + (((2 * pq + ((col & 3) >> 1)) ^ (rr & 7)) << 4) + (col & 1) * 8;
      LDS char* sc = smem + G_T + pq * (32 * 144);
#pragma unroll
      for (int hf = 0; hf < 2; ++hf) {
        u32x2 t[4];
#pragma unroll
        for (int m = 0; m < 4; ++m) t[m] = __builtin_bit_cast(u32x2, tr_read(tb + hf * 8192 + m * 2048));
        u32x4 oa, ob;
#pragma unroll
        for (int m = 0; m < 4; ++m) {
          oa[m] = __builtin_amdgcn_perm(t[m][1], t[m][0], 0x05040100u);
          ob[m] = __builtin_amdgcn_perm(t[m][1], t[m][0], 0x07060302u);
        }
        *(LDS u32x4*)(sc + hf * (16 * 144) + col * 144 + q * 32) = oa;
        *(LDS u32x4*)(sc + hf * (16 * 144) + col * 144 + q * 32 + 16) = ob;
      }
      wave_fence();
      const __amdgpu_buffer_rsrc_t rs = mk_rsrc(p.ws + WS_XT, DM * SL * 2);
#pragma unroll
      for (int u = 0; u < 4; ++u) {
        const int row = (lane >> 3) + 8 * (u & 1), hf = u >> 1;
        const u32x4 v = *(LDS const u32x4*)(sc + hf * (16 * 144) + row * 144 + (lane & 7) * 16);
        __builtin_amdgcn_raw_buffer_store_b128(v, rs, ((64 * g + 16 * pq + row) * SL + 128 * c + 64 * hf) * 2 + (lane & 7) * 16, 0, SC1);
      }
      __builtin_amdgcn_sched_barrier(0);
    }
    read_frags(s);
    __builtin_amdgcn_sched_barrier(0);
    if (HF == 0) { mma(); __builtin_amdgcn_sched_barrier(0); }
  }
  if (HF == 1) mma();
  __builtin_amdgcn_sched_barrier(0);
  STAMP(0, 3); CSTAMP(0, 61);
  const f32x2 Lr = {lam.r, lam.r}, Li = {lam.i, lam.i};
  f32x2 er = {0.f, 0.f}, ei = {0.f, 0.f};
#pragma unroll
  for (int t = 0; t < 8; ++t) {
    const f32x2 br_ = (t & 1) ? hi2(are[t >> 1]) : lo2(are[t >> 1]);
    const f32x2 bi_ = (t & 1) ? hi2(aim[t >> 1]) : lo2(aim[t >> 1]);
    const f32x2 nr = fma2(-Li, ei, fma2(Lr, er, br_));
    ei = fma2(Li, er, fma2(Lr, ei, bi_));
    er = nr;
  }
  const cf eA = {er[0], ei[0]};
  const cf e = cfma(l8, eA, cf{er[1], ei[1]});
  const int seg = 4 * HF + q;
  *(LDS f32x2*)(smem + G_E + (seg * 64 + pl) * 8) = f32x2{e.r, e.i};
  asm volatile("" :: "v"(wb[0]), "v"(wb[1]), "v"(wb[2]));
  STAMP(0, 4);
  bar();
  STAMP(0, 5);
  {
#pragma unroll
    for (int m = 0; m < 4; ++m) asm volatile("" : "+v"(are[m]), "+v"(aim[m]));
    const f32x2 Cr = {coef2.r, coef2.r}, Ci = {coef2.i, coef2.i};
#pragma unroll
    for (int m = 0; m < 4; ++m) {
      const f32x2 r0 = lo2(are[m]), r1 = hi2(are[m]), i0 = lo2(aim[m]), i1 = hi2(aim[m]);
      are[m] = cat2(fma2(-Ci, i0, Cr * r0), fma2(-Ci, i1, Cr * r1));
      aim[m] = cat2(fma2(-Ci, r0, -Cr * i0), fma2(-Ci, r1, -Cr * i1));
      asm volatile("" : "+v"(are[m]), "+v"(aim[m]));
    }
  }
  cf eAc = cmul(coef2, eA), pwc = cmul(coef2, pw);
  asm volatile("" : "+v"(eAc.r), "+v"(eAc.i), "+v"(pwc.r), "+v"(pwc.i));
  STAMP(0, 7);
  bar();
  STAMP(0, 8);
  cf cin;
  {
    f32x2 v[2];
#pragma unroll
    for (int i = 0; i < 2; ++i) v[i] = *(LDS const f32x2*)(smem + G_X + (i * 64 + pl) * 8);
    const f32x2 pf = *(LDS const f32x2*)(smem + G_PF + (seg * 64 + pl) * 8);
    const f32x2 S = v[0] + v[1];
    cin = cfma(pwc, cf{S[0], S[1]}, cmul(coef2, cf{pf[0], pf[1]}));
  }
  const cf cinB = cfma(l8, cin, eAc);
  er = f32x2{cin.r, cinB.r}; ei = f32x2{-cin.i, -cinB.i};
  f32x2 hr[8], hi[8];
#pragma unroll
  for (int t = 0; t < 8; ++t) {
    const f32x2 br_ = (t & 1) ? hi2(are[t >> 1]) : lo2(are[t >> 1]);
    const f32x2 bi_ = (t & 1) ? hi2(aim[t >> 1]) : lo2(aim[t >> 1]);
    const f32x2 nr = fma2(Li, ei, fma2(Lr, er, br_));
    ei = fma2(-Li, er, fma2(Lr, ei, bi_));
    er = nr;
    hr[t] = er; hi[t] = ei;
  }
  u32x4 o[4];
#pragma unroll
  for (int t = 0; t < 4; ++t) {
    o[0][t] = pack_bf16(hr[2 * t][0], hr[2 * t + 1][0]);
    o[1][t] = pack_bf16(hr[2 * t][1], hr[2 * t + 1][1]);
    o[2][t] = pack_bf16(hi[2 * t][0], hi[2 * t + 1][0]);
    o[3][t] = pack_bf16(hi[2 * t][1], hi[2 * t + 1][1]);
  }
  STAMP(0, 9);
  {
    const __amdgpu_buffer_rsrc_t rs = mk_rsrc(p.ws + WS_HB, 2 * DM * SL * 2);
    const int hb = (((c * 2 + HF) * 2) * 9 + g) * 8192 + pq * 2048;
    const int p0 = (16 * q + col) * 16, p1 = 1024 + (16 * q + (col ^ 4)) * 16;
    __builtin_amdgcn_raw_buffer_store_b128(o[0], rs, hb + p0, 0, SC1);
    __builtin_amdgcn_raw_buffer_store_b128(o[1], rs, hb + p1, 0, SC1);
    __builtin_amdgcn_raw_buffer_store_b128(o[2], rs, hb + 9 * 8192 + p0, 0, SC1);
    __builtin_amdgcn_raw_buffer_store_b128(o[3], rs, hb + 9 * 8192 + p1, 0, SC1);
    bar();
    wait_vm<0>();
    STAMP(0, 10);
    if (lane < 8)
      __hip_atomic_store((GLB u64*)(p.ws + WS_IN) + tile_block(2 * c + HF, lane) * 64 + IN_H + g * 4 + pq, FLAG_SET, RLX_AGENT);
  }
}

constexpr int S_CPY = 4160;
constexpr int S_X = 0;
constexpr int S_ST = 6 * 4 * S_CPY;
constexpr int S_Z = S_ST;
constexpr int S_ZSZ = (256 + SL) * 2;
constexpr int S_T = S_ST + 12 * 4608;
constexpr int S_TSZ = 1312;
constexpr int S_END = S_T + 6 * S_TSZ;
constexpr int S_CNT = 32768 + 8192;

DEV c2 cexp_k(float zl, float th, float tl, float k) {
  const float mag = __builtin_amdgcn_exp2f(zl * k);
  const float t = fmaf(tl, k, __builtin_amdgcn_fractf(th * k));
  return c2{mag * __builtin_amdgcn_cosf(t), mag * __builtin_amdgcn_sinf(t)};
}
#define PKM(d, a, b) "v_pk_mul_f32 " d ", " a ", " b " op_sel:[0,0] op_sel_hi:[0,1]\n\t"
#define PKF(d, a, b) "v_pk_fma_f32 " d ", " a ", " b ", " d " op_sel:[1,1,0] op_sel_hi:[1,0,1] neg_lo:[1,0,0]\n\t"
DEV void octet(LDS char* dst, c2 s0, c2 m1, c2 m2) {
  c2 v1, v2, v3, t0, t1, t2, t3;
  asm(PKM("%0", "%7", "%8") PKM("%3", "%7", "%9") PKF("%0", "%7", "%8") PKF("%3", "%7", "%9")
      PKM("%1", "%0", "%8") PKM("%4", "%0", "%9") PKF("%1", "%0", "%8") PKF("%4", "%0", "%9")
      PKM("%2", "%1", "%8") PKM("%5", "%1", "%9") PKF("%2", "%1", "%8") PKF("%5", "%1", "%9")
      PKM("%6", "%2", "%9") PKF("%6", "%2", "%9")
      : "=&v"(v1), "=&v"(v2), "=&v"(v3), "=&v"(t0), "=&v"(t1), "=&v"(t2), "=&v"(t3) : "v"(s0), "v"(m1), "v"(m2));
  *(LDS u32*)(dst + 0 * 288) = pack_bf16(s0[0], s0[1]);
  *(LDS u32*)(dst + 1 * 288) = pack_bf16(v1[0], v1[1]);
  *(LDS u32*)(dst + 2 * 288) = pack_bf16(v2[0], v2[1]);
  *(LDS u32*)(dst + 3 * 288) = pack_bf16(v3[0], v3[1]);
  *(LDS u32*)(dst + 4 * 288) = pack_bf16(t0[0], t0[1]);
  *(LDS u32*)(dst + 5 * 288) = pack_bf16(t1[0], t1[1]);
  *(LDS u32*)(dst + 6 * 288) = pack_bf16(t2[0], t2[1]);
  *(LDS u32*)(dst + 7 * 288) = pack_bf16(t3[0], t3[1]);
}
DEV u32 dup_lo(u32 a) { return __builtin_amdgcn_perm(a, a, 0x01000100u); }
DEV u32 dup_hi(u32 a) { return __builtin_amdgcn_perm(a, a, 0x03020302u); }
DEV u32 hi_lo(u32 a, u32 b) { return __builtin_amdgcn_alignbit(b, a, 16); }

DEV void role_s(const P& p, LDS char* smem, int sg, int w, int lane) {
  const int tid = w * 64 + lane;
  const int ch = w >> 1, hh = w & 1, d = 6 * sg + ch;
  const int n = d * 128 + 64 * hh + lane;
  const u16 r_ldt = p.ldtA[d], r_la = p.la[n], r_aim = p.aim[n], r_cre = p.creA[n], r_cim = p.cimA[n];
  STAMP(0, 1);
  f32x4 tab[3];
  [[maybe_unused]] u32 wc[2] = {0, 0};
  LDS char* st = smem + S_ST + w * 4608;
  {
    const float L2E = 1.44269504088896341f;
    const float dt = rbf(__builtin_amdgcn_exp2f(bf2f(r_ldt) * L2E));
    const float ar = -rbf(__builtin_amdgcn_exp2f(bf2f(r_la) * L2E));
    const float zl = ar * dt * L2E, zi = bf2f(r_aim) * dt;
    const float th = zi * 0.15915494309189535f;
    const float tl = fmaf(zi, 0.15915494309189535f, -th) + zi * 4.6017384e-10f;
    const c2 C = {2.0f * (bf2f(r_cre) * dt), 2.0f * (bf2f(r_cim) * dt)};
    const c2 w1 = cexp_k(zl, th, tl, 1.f), w8 = cexp_k(zl, th, tl, 8.f), w128 = cexp_k(zl, th, tl, 128.f);
    c2 w2, w4, w16, w32, w64, w256, w512, w1024, w2048;
    asm(PKM("%0", "%9", "%9") PKM("%2", "%10", "%10") PKM("%5", "%11", "%11") PKF("%0", "%9", "%9") PKF("%2", "%10", "%10") PKF("%5", "%11", "%11")
        PKM("%1", "%0", "%0") PKM("%3", "%2", "%2") PKM("%6", "%5", "%5") PKF("%1", "%0", "%0") PKF("%3", "%2", "%2") PKF("%6", "%5", "%5")
        PKM("%4", "%3", "%3") PKM("%7", "%6", "%6") PKF("%4", "%3", "%3") PKF("%7", "%6", "%6")
        PKM("%8", "%7", "%7") PKF("%8", "%7", "%7")
        : "=&v"(w2), "=&v"(w4), "=&v"(w16), "=&v"(w32), "=&v"(w64), "=&v"(w256), "=&v"(w512), "=&v"(w1024), "=&v"(w2048)
        : "v"(w1), "v"(w8), "v"(w128));
    c2 C128, C256, C512, C1024, C2048;
    asm(PKM("%0", "%5", "%6") PKM("%1", "%5", "%7") PKM("%2", "%5", "%8") PKM("%3", "%5", "%9") PKM("%4", "%5", "%10")
        PKF("%0", "%5", "%6") PKF("%1", "%5", "%7") PKF("%2", "%5", "%8") PKF("%3", "%5", "%9") PKF("%4", "%5", "%10")
        : "=&v"(C128), "=&v"(C256), "=&v"(C512), "=&v"(C1024), "=&v"(C2048)
        : "v"(C), "v"(w128), "v"(w256), "v"(w512), "v"(w1024), "v"(w2048));
#pragma unroll
    for (int i = 0; i < 2; ++i) {
      const int ln = i * 768 + tid, all = (sg % 12) * 864 + ln;
      if (ln < 864) wc[i] = *(const GLB u32*)((const char*)(all < 5184 ? p.cre : p.cim) + (size_t)(all < 5184 ? all : all - 5184) * 128);
    }
    STAMP(0, 2);
    LDS char* wr = st + 4 * lane;
    LDS const char* rd = st + (lane & 15) * 288 + (lane >> 4) * 16;
    bf16x8 B[4];
    const c2 w8c = {w8[0], -w8[1]}, w32c = {w32[0], -w32[1]}, w64c = {w64[0], -w64[1]};
    octet(wr, c2{1.f, 0.f}, w8c, w32c);
    octet(wr + 8 * 288, w64c, w8c, w32c);
    wave_fence();
#pragma unroll
    for (int ks = 0; ks < 4; ++ks) B[ks] = lds_frag(rd + 64 * ks);
    wave_fence();
#pragma unroll
    for (int mt = 0; mt < 3; ++mt) {
      if (mt == 0) { octet(wr, C, w1, w4); octet(wr + 8 * 288, C128, w1, w4); }
      if (mt == 1) { octet(wr, C256, w2, w128); octet(wr + 8 * 288, C512, w128, w4); }
      if (mt == 2) { octet(wr, C1024, w128, w512); *(LDS u32*)(wr + 8 * 288) = pack_bf16(C2048[0], C2048[1]); }
      wave_fence();
      f32x4 acc = {0, 0, 0, 0};
      bf16x8 A[4];
#pragma unroll
      for (int ks = 0; ks < 4; ++ks) A[ks] = lds_frag(rd + 64 * ks);
      __builtin_amdgcn_sched_barrier(0);
#pragma unroll
      for (int ks = 0; ks < 4; ++ks) acc = mfma16(A[ks], B[ks], acc);
      __builtin_amdgcn_sched_barrier(0);
      wave_fence();
      tab[mt] = acc;
    }
  }
  const int b = lane & 15, q = lane >> 4;
#pragma unroll
  for (int mt = 0; mt < 3; ++mt) *(LDS f32x4*)(st + mt * 1024 + lane * 16) = tab[mt];
  STAMP(0, 3);
  bar();
  {
    LDS const char* ot = smem + S_ST + (w ^ 1) * 4608 + lane * 16;
    LDS char* T = smem + S_T + ch * S_TSZ;
    if (hh == 0) {
      const f32x4 v0 = tab[0] + *(LDS const f32x4*)(ot), v2 = tab[2] + *(LDS const f32x4*)(ot + 2048);
      *(LDS u32x2*)(T + 2 * (128 * (q >> 1) + 8 * b + 4 * (q & 1))) = u32x2{pack_bf16(v0[0], v0[1]), pack_bf16(v0[2], v0[3])};
      const u32 a0 = pack_bf16(v2[0], v2[1]), a1 = pack_bf16(v2[2], v2[3]);
      if (q < 2) {
        LDS char* t3 = T + 2 * (512 + 64 * q + b);
        *(LDS u16*)(t3) = (u16)a0; *(LDS u16*)(t3 + 32) = (u16)(a0 >> 16);
        *(LDS u16*)(t3 + 64) = (u16)a1; *(LDS u16*)(t3 + 96) = (u16)(a1 >> 16);
      } else if (lane == 32) *(LDS u16*)(T + 2 * 640) = (u16)a0;
    } else {
      const f32x4 v1 = tab[1] + *(LDS const f32x4*)(ot + 1024);
      const u32 a0 = pack_bf16(v1[0], v1[1]), a1 = pack_bf16(v1[2], v1[3]);
      if (q < 2) *(LDS u32x2*)(T + 2 * (256 + 64 * q + 4 * b)) = u32x2{a0, a1};
      else {
        LDS char* t2 = T + 2 * (384 + 2 * b + (q - 2));
        *(LDS u16*)(t2) = (u16)a0; *(LDS u16*)(t2 + 64) = (u16)(a0 >> 16);
        *(LDS u16*)(t2 + 128) = (u16)a1; *(LDS u16*)(t2 + 192) = (u16)(a1 >> 16);
      }
    }
  }
  bar();
  STAMP(0, 4);
  u32x4 uv[2];
  {
    GLB u64* fl = (GLB u64*)(p.ws + WS_FL) + (8 * hh + (lane & 7)) * DM + d;
    asm volatile("" :: "v"(wc[0]), "v"(wc[1]));
    wait_flags(fl, true);
    const __amdgpu_buffer_rsrc_t rs = mk_rsrc(p.ws + WS_XT, DM * SL * 2);
#pragma unroll
    for (int v = 0; v < 2; ++v) uv[v] = __builtin_amdgcn_raw_buffer_load_b128(rs, (d * SL + 1024 * hh + 8 * (lane + 64 * v)) * 2, 0, SC1);
    if (lane < 8) __hip_atomic_store(fl, 0ull, RLX_AGENT);
  }
  STAMP(0, 5);
  {
    LDS const char* T = smem + S_T + ch * S_TSZ;
    u32 o[10];
    const u32 e512 = *(LDS const u16*)(T + 2 * 512);
    o[8] = o[9] = e512 | (e512 << 16);
    if (hh == 0) {
      const int off = lane < 16 ? 32 * lane : (lane < 32 ? 256 + 16 * lane : 512 + 8 * lane);
      u32x2 W[4];
#pragma unroll
      for (int i = 0; i < 4; ++i) W[i] = *(LDS const u32x2*)(T + off + 8 * i);
      if (lane < 16) {
#pragma unroll
        for (int i = 0; i < 8; ++i) o[i] = W[i >> 1][i & 1];
      } else if (lane < 32) {
#pragma unroll
        for (int i = 0; i < 4; ++i) {
          const u32 a = W[i >> 1][i & 1], nx = W[(i + 1) >> 1][(i + 1) & 1];
          o[2 * i] = dup_lo(a);
          o[2 * i + 1] = hi_lo(a, nx);
        }
      } else {
        const u32 w0 = W[0][0], w1 = W[0][1], w2 = W[1][0];
        o[0] = dup_lo(w0); o[1] = w0; o[2] = dup_hi(w0); o[3] = dup_lo(w1);
        o[4] = o[3]; o[5] = w1; o[6] = dup_hi(w1); o[7] = dup_lo(w2);
      }
    } else {
      const u32 w0 = *(LDS const u32*)(T + 1024 + 4 * lane), w1 = *(LDS const u32*)(T + 1028 + 4 * lane);
      o[0] = o[1] = dup_lo(w0); o[2] = w0; o[3] = o[4] = o[5] = dup_hi(w0); o[6] = o[7] = dup_lo(w1);
    }
    o[8] = (u32)__builtin_amdgcn_update_dpp((int)o[8], (int)o[0], 0x130, 0xf, 0xf, false);
    o[9] = (u32)__builtin_amdgcn_update_dpp((int)o[9], (int)o[1], 0x130, 0xf, 0xf, false);
    u32 a[9];
#pragma unroll
    for (int i = 0; i < 9; ++i) a[i] = hi_lo(o[i], o[i + 1]);
    LDS char* xb = smem + S_X + ch * 4 * S_CPY + 32 + 2 * (1024 * hh + 16 * lane);
    *(LDS u32x4*)(xb) = u32x4{o[0], o[1], o[2], o[3]};
    *(LDS u32x4*)(xb + 16) = u32x4{o[4], o[5], o[6], o[7]};
    *(LDS u32x4*)(xb + S_CPY) = u32x4{a[0], a[1], a[2], a[3]};
    *(LDS u32x4*)(xb + S_CPY + 16) = u32x4{a[4], a[5], a[6], a[7]};
    *(LDS u32x4*)(xb + 2 * S_CPY) = u32x4{o[1], o[2], o[3], o[4]};
    *(LDS u32x4*)(xb + 2 * S_CPY + 16) = u32x4{o[5], o[6], o[7], o[8]};
    *(LDS u32x4*)(xb + 3 * S_CPY) = u32x4{a[1], a[2], a[3], a[4]};
    *(LDS u32x4*)(xb + 3 * S_CPY + 16) = u32x4{a[5], a[6], a[7], a[8]};
    if (hh == 0 && lane == 0) {
      LDS char* hb = smem + S_X + ch * 4 * S_CPY;
      const u32x4 z = {0, 0, 0, 0};
      *(LDS u32x4*)(hb) = z;
      *(LDS u32x4*)(hb + 16) = z;
      *(LDS u32x4*)(hb + S_CPY) = z;
      *(LDS u32x4*)(hb + S_CPY + 16) = u32x4{0, 0, 0, o[0] << 16};
      *(LDS u32x4*)(hb + 2 * S_CPY) = z;
      *(LDS u32x4*)(hb + 2 * S_CPY + 16) = u32x4{0, 0, 0, o[0]};
      *(LDS u32x4*)(hb + 3 * S_CPY) = z;
      *(LDS u32x4*)(hb + 3 * S_CPY + 16) = u32x4{0, 0, o[0] << 16, a[0]};
    }
    if (tid < 6 * 32) *(LDS u32x4*)(smem + S_Z + (tid >> 5) * S_ZSZ + (tid & 31) * 16) = u32x4{0, 0, 0, 0};
    STAMP(0, 6);
    LDS char* z = smem + S_Z + ch * S_ZSZ + 512 + (1024 * hh + 8 * lane) * 2;
    *(LDS u32x4*)z = uv[0];
    *(LDS u32x4*)(z + 1024) = uv[1];
  }
  bar();
  STAMP(0, 7);
  f32x4 acc[8];
#pragma unroll
  for (int i = 0; i < 8; ++i) acc[i] = f32x4{0, 0, 0, 0};
  {
    const int kq = q, qq = b >> 2, pp = b & 3;
    LDS const char* fb = smem + S_X + (ch * 4 + ((4 - qq) & 3)) * S_CPY +
                         2 * (16 + 16 * (kq >> 1) - 8 * (kq & 1) + 4 * pp - (qq ? 4 : 0)) + 64 * hh;
    LDS const char* gb = smem + S_Z + ch * S_ZSZ + 512 + 32 * (b - (kq >> 1)) + 16 * (kq & 1) - 64 * hh;
#pragma unroll 1
    for (int it = 0; it < 4; ++it) {
      bf16x8 F[8], G[8];
#pragma unroll
      for (int s = 0; s < 8; ++s) {
        F[s] = tr_frag(fb + 512 * s, fb + 512 * s - 8);
        G[s] = lds_frag(gb + 512 * s);
      }
      __builtin_amdgcn_sched_barrier(0);
#pragma unroll
      for (int t = 0; t < 8; ++t)
#pragma unroll
        for (int s = 0; s <= t; ++s) acc[t] = mfma16(F[s], G[t - s], acc[t]);
      fb += 128;
      gb -= 128;
    }
  }
  __builtin_amdgcn_sched_barrier(0);
  STAMP(0, 8);
  bar();
  {
    LDS char* ex = smem + S_X + 49152 + w * 4096 + lane * 16;
#pragma unroll
    for (int i = 0; i < 4; ++i) {
      const f32x4 lo = acc[i], hi = acc[4 + i];
      *(LDS f32x4*)(ex + i * 1024) = hh ? +lo : +hi;
    }
    if (tid == 0) *(LDS u32*)(smem + S_CNT) = 0;
  }
  bar();
  u32x2 yo[4];
  {
    LDS const char* ey = smem + S_X + 49152 + (w ^ 1) * 4096 + lane * 16;
#pragma unroll
    for (int i = 0; i < 4; ++i) {
      const f32x4 lo = acc[i], hi = acc[4 + i];
      const f32x4 y = (hh ? +hi : +lo) + *(LDS const f32x4*)(ey + i * 1024);
      yo[i] = u32x2{pack_bf16(y[0], y[1]), pack_bf16(y[2], y[3])};
    }
  }
  bar();
  {
    const __amdgpu_buffer_rsrc_t rs = mk_rsrc(p.ws + WS_AT, DM * SL * 2);
    const int dst = (d * SL + 1024 * hh + 16 * (lane & 15) + 4 * (lane >> 4)) * 2;
#pragma unroll
    for (int i = 0; i < 4; ++i) __builtin_amdgcn_raw_buffer_store_b64(yo[i], rs, dst + 512 * i, 0, SC1);
  }
  STAMP(0, 9);
}
DEV void s_signal(const P& p, LDS char* smem, int sg, int lane) {
  u32 old = 0;
  if (lane == 0) old = __hip_atomic_fetch_add((LDS u32*)(smem + S_CNT), 1u, __ATOMIC_RELAXED, __HIP_MEMORY_SCOPE_WORKGROUP);
  if (__builtin_amdgcn_readfirstlane(old) == 11) {
    const int lt = lane & 31, second = lane >> 5;
    const int dd = 6 * sg + (second ? 5 : 0);
    const int idx = dd < 480 ? dd / 96 : 5 + (dd - 480) / 32;
    const int dfirst = idx < 5 ? 96 * idx : 480 + 32 * (idx - 5);
    __hip_atomic_store((GLB u64*)(p.ws + WS_IN) + tile_block(lt, idx) * 64 + IN_A + (sg - dfirst / 6), FLAG_SET, RLX_AGENT);
  }
}

DEV void role_gate(const P& p, int rb, int w, int lane, u64 pc) {
  u32 pf0 = 0, pf1 = 0;
  const int tid = w * 64 + lane;
  const int sub = lane & 7;
  u32x4 wv[9];
#pragma unroll
  for (int i = 0; i < 9; ++i) wv[i] = *(const u32x4*)((const char*)p.gw + (i * 8 + sub) * 16);
  for (int r = w * 8 + (lane >> 3); r < 128; r += 96) {
    const int l = rb * 128 + r;
    float s = 0.f;
    u32x4 xv[9];
#pragma unroll
    for (int i = 0; i < 9; ++i) xv[i] = *(const u32x4*)((const char*)p.x + (size_t)l * 1152 + (i * 8 + sub) * 16);
#pragma unroll
    for (int i = 0; i < 9; ++i) {
#pragma unroll
      for (int k = 0; k < 4; ++k) {
        const u32 pr = pack_bf16(bflo(xv[i][k]) * bflo(wv[i][k]), bfhi(xv[i][k]) * bfhi(wv[i][k]));
        s += bflo(pr);
        s += bfhi(pr);
      }
    }
    s += __shfl_xor(s, 1); s += __shfl_xor(s, 2); s += __shfl_xor(s, 4);
    const float gv = rbf(1.0f / (1.0f + expf(-rbf(s))));
    if (sub == 0)
      __hip_atomic_store((GLB u64*)(p.ws + WS_G) + l, ((u64)__float_as_uint(rbf(1.0f - gv)) << 32) | __float_as_uint(gv), RLX_AGENT);
  }
  asm volatile("" :: "v"(pf0), "v"(pf1));
  wait_vm<0>();
  bar();
  if (w == 0 && lane < 16)
    __hip_atomic_store((GLB u64*)(p.ws + WS_IN) + tile_block(2 * rb + (lane >> 3), lane & 7) * 64 + IN_G, FLAG_SET, RLX_AGENT);
}

constexpr int H_C = 32768, H_CS = 12288;
DEV int h_slot(int i) { return i < 4 ? i * 8192 : 131072 + (i - 4) * 8192; }
constexpr int H_AT = H_C + 2 * H_CS;
constexpr int H_XT = H_C + 3 * H_CS;
constexpr int H_G = H_C + 4 * H_CS;
constexpr int H_OUT = H_C + 5 * H_CS;
constexpr int K_LDS = 163840;

constexpr int h_wait(int s, int NDT) {
  int n = 0;
  int need = 0;
  for (int k = 0; k < 4; ++k) { n += 2; if (k == s) need = n; }
  for (int t = 0; t < s; ++t) {
    if (t == 0) { n += 2; if (s == 4) need = n; n += 2; if (s == 5) need = n; }
    else if (t == 1) { n += 2; if (s == 6) need = n; n += 2; if (s == 7) need = n; }
    else if (t <= 11) { n += 2 + NDT; if (s == t + 6) need = n; if (t == 2) n += 1; }
    else if (t == 12 || t == 13) n += NDT;
    else if (t == 14) n += 1;
  }
  return s == 17 ? 0 : n - need;
}
template <int S, int NDT, class F> DEV void h_steps(F&& after, u64& fv, char* wsp) {
  if constexpr (S == 9) asm volatile("s_waitcnt vmcnt(%1)" : "+v"(fv) : "n"(h_wait(S, NDT)) : "memory");
  else wait_vm<h_wait(S, NDT)>();
  LSTAMP2(1, 16 + S);
  bar();
  after(std::integral_constant<int, S>{});
  if constexpr (S < 17) h_steps<S + 1, NDT>(after, fv, wsp);
}
DEV int p2_rot(int d0) { const int idx = d0 < 480 ? d0 / 96 : 5 + (d0 - 480) / 32; return (idx * 18) >> 3; }

template <int NDT, class F>
DEV void p2_loader(const P& p, LDS char* smem, int lt, int d0, int lw, int lane, bool scratch_barrier, F&& pre_poll) {
  __builtin_amdgcn_s_setprio(3);
  const char* hb = p.ws + WS_HB + (size_t)lt * 18 * 8192 + (2 * lw) * 1024 + lane * 16;
  constexpr int W = 32 * NDT;
  const int l0 = 64 * lt, rot = p2_rot(d0);
  u32 co[NDT];
#pragma unroll
  for (int i = 0; i < NDT; ++i) {
    const int row = 8 * (NDT * lw + i) + (lane >> 3), cp = lane & 7;
    co[i] = (u32)(d0 + row) * 1152u + ((cp ^ (row & 7)) << 4);
  }
  auto issue_c = [&](int s) {
    LDS char* buf = smem + H_C + (s & 7) * H_CS + NDT * lw * 1024;
    int ks = s + rot; ks = ks >= 18 ? ks - 18 : ks;
    const char* cb = ks < 9 ? (const char*)p.cre + ks * 128 : (const char*)p.cim + (ks - 9) * 128;
#pragma unroll
    for (int i = 0; i < NDT; ++i) dma16<0>(cb + co[i], buf + i * 1024);
  };
  auto issue_h = [&](int s) {
    LDS char* buf = smem + h_slot(s & 7) + 2 * lw * 1024;
    int ks = s + rot; ks = ks >= 18 ? ks - 18 : ks;
#pragma unroll
    for (int i = 0; i < 2; ++i) dma16<SC1>(hb + ks * 8192 + i * 1024, buf + i * 1024);
  };
  GLB u64* inb = (GLB u64*)(p.ws + WS_IN) + blockIdx.x * 64;
  const int nA = (d0 + W - 1) / 6 - d0 / 6 + 1;
  const bool need = lane == IN_G || (lane >= IN_A && lane < IN_A + nA);
  if (scratch_barrier) bar();
  else {
#pragma unroll 1
    for (int s = 0; s < 8; ++s) issue_c(s);
  }
  pre_poll();
  { [[maybe_unused]] const int w = lw + 8; STAMP(1, 1); }
  wait_flags(inb + IN_H + (lane < 36 ? lane : 0), true);
  { [[maybe_unused]] const int w = lw + 8; STAMP(1, 2); }
#pragma unroll
  for (int s = 0; s < 4; ++s) issue_h(s);
  { [[maybe_unused]] const int w = lw + 8; STAMP(1, 3); }
  u64 fv = 0;
  h_steps<0, NDT>([&](auto S_) {
    constexpr int S = decltype(S_)::value;
    if constexpr (S == 0) { issue_h(4); issue_h(5); }
    if constexpr (S == 1) { issue_h(6); issue_h(7); }
    if constexpr (S >= 2 && S <= 11) { issue_h(S + 6); issue_c(S + 6); }
    if constexpr (S == 2)
      asm volatile("global_load_dwordx2 %0, %1, off sc1" : "=v"(fv) : "v"(inb + lane) : "memory");
    if constexpr (S == 9) { if (!__all(!need || fv == FLAG_SET)) wait_flags(inb + lane, need); }
    if constexpr (S == 12) {
#pragma unroll
      for (int i = 0; i < NDT; ++i) {
        const int pc = NDT * lw + i;
        dma16<SC1>(p.ws + WS_AT + (size_t)(d0 + 8 * pc + (lane >> 3)) * 4096 + l0 * 2 + (lane & 7) * 16, smem + H_AT + pc * 1024);
      }
    }
    if constexpr (S == 13) {
#pragma unroll
      for (int i = 0; i < NDT; ++i) {
        const int pc = NDT * lw + i, id = pc * 64 + lane, row = id / (4 * NDT), qq = id - row * (4 * NDT);
        dma16<0>((const char*)p.x + (size_t)(l0 + row) * 1152 + d0 * 2 + qq * 16, smem + H_XT + pc * 1024);
      }
    }
    if constexpr (S == 14) { if (lane < 8) dma16<SC1>(p.ws + WS_G + (size_t)l0 * 8 + lw * 128 + lane * 16, smem + H_G + lw * 128); }
  }, fv, p.ws);
}

template <int NDT, int STAG>
DEV void p2_compute(const P& p, LDS char* smem, int lt0, int d0, int w, int lane) {
  const int lh = (w >> 1) & 1, kh = w & 1, dg = w >> 2;
  const int r = lane & 15, kq = lane >> 4;
  const int lt = 2 * lh + kh, l0 = 64 * lt0;
  constexpr int W = 32 * NDT;
  u32x2 da[NDT], db[NDT];
#pragma unroll
  for (int t = 0; t < NDT; ++t) {
    const int d = d0 + 16 * (NDT * dg + t) + 4 * kq;
    da[t] = *(const u32x2*)(p.dskA + d);
    db[t] = *(const u32x2*)(p.dskB + d);
  }
  GLB u64* inb = (GLB u64*)(p.ws + WS_IN) + blockIdx.x * 64;

  f32x4 acc[2][NDT];
#pragma unroll
  for (int i = 0; i < 2; ++i)
#pragma unroll
    for (int t = 0; t < NDT; ++t) acc[i][t] = f32x4{0, 0, 0, 0};
  int ho[2];
#pragma unroll
  for (int u = 0; u < 2; ++u) {
    const int k = 32 * kh + 8 * kq + 4 * u + (r >> 2), i = (r & 3) >> 1;
    ho[u] = (2 * (k >> 4) + i) * 1024 + (32 * lh + ((k & 15) ^ (4 * i))) * 16 + (r & 1) * 8;
  }
  const int co = H_C + (16 * NDT * dg + r) * 128 + (((4 * kh + kq) ^ (r & 7)) << 4);
  constexpr int NREG = 0, NRING = 18 - NREG;
  bf16x8 cr[NREG ? NREG : 1][NDT];
  if constexpr (NREG > 0) {
    const int rot = p2_rot(d0);
#pragma unroll
    for (int i = 0; i < NREG; ++i) {
      int ks = NRING + i + rot; ks = ks >= 18 ? ks - 18 : ks;
      const char* cb = (ks < 9 ? (const char*)p.cre + ks * 128 : (const char*)p.cim + (ks - 9) * 128) +
                       (size_t)(d0 + 16 * NDT * dg + r) * 1152 + (4 * kh + kq) * 16;
#pragma unroll
      for (int t = 0; t < NDT; ++t) cr[i][t] = *(const GLB bf16x8*)(cb + t * 16 * 1152);
    }
  }
  bf16x8 hb[2], ca[NDT];
#pragma unroll
  for (int i = 0; i < 2; ++i) hb[i] = bf16x8{};
#pragma unroll
  for (int t = 0; t < NDT; ++t) ca[t] = bf16x8{};
  auto mma_with = [&](const bf16x8 (&cc)[NDT]) {
#pragma unroll
    for (int t = 0; t < NDT; ++t) {
      acc[0][t] = mfma16(cc[t], hb[0], acc[0][t]);
      acc[1][t] = mfma16(cc[t], hb[1], acc[1][t]);
    }
    __builtin_amdgcn_sched_barrier(0);
  };
  auto read_h = [&](int s) {
    LDS const char* hbuf = smem + h_slot(s & 7);
#pragma unroll
    for (int i = 0; i < 2; ++i) hb[i] = tr_frag(hbuf + ho[0] + i * 256, hbuf + ho[1] + i * 256);
  };
  STAMP(1, 1);
#pragma unroll 1
  for (int s = 0; s < NRING; ++s) {
    bar();
    if (s == 0) STAMP(1, 2);
    LSTAMP(1, 16 + s);
    if (STAG) mma_with(ca);
    read_h(s);
    LDS const char* cbuf = smem + (s & 7) * H_CS;
#pragma unroll
    for (int t = 0; t < NDT; ++t) ca[t] = lds_frag(cbuf + co + t * 2048);
    __builtin_amdgcn_sched_barrier(0);
    if (!STAG) mma_with(ca);
  }
#pragma unroll
  for (int i = 0; i < NREG; ++i) {
    bar();
    if (STAG) { if (i == 0) mma_with(ca); else mma_with(cr[i ? i - 1 : 0]); }
    read_h(NRING + i);
    __builtin_amdgcn_sched_barrier(0);
    if (!STAG) mma_with(cr[i]);
  }
  if (STAG) { if (NREG) mma_with(cr[NREG ? NREG - 1 : 0]); else mma_with(ca); }
  __builtin_amdgcn_sched_barrier(0);
  STAMP(1, 3);
  bar();
  if (w == 0) __hip_atomic_store(inb + lane, 0ull, RLX_AGENT);
  LDS char* ex = smem + w * (NDT * 1024) + lane * 16;
#pragma unroll
  for (int t = 0; t < NDT; ++t) {
    const f32x4 a0 = acc[0][t], a1 = acc[1][t];
    *(LDS f32x4*)(ex + t * 1024) = kh ? +a0 : +a1;
  }
  bar();
  LDS const char* ey = smem + (w ^ 1) * (NDT * 1024) + lane * 16;
  constexpr int OSTR = W * 4 + 16;
  const f32x2 gg = *(LDS const f32x2*)(smem + H_G + (16 * lt + r) * 8);
#pragma unroll
  for (int t = 0; t < NDT; ++t) {
    const int dt = NDT * dg + t;
    const f32x4 o = *(LDS const f32x4*)(ey + t * 1024);
    const f32x4 a0 = acc[0][t], a1 = acc[1][t];
    const f32x4 y = (kh ? +a1 : +a0) + o;
    const s16x4 av = tr_read(smem + H_AT + (16 * dt + 4 * kq + (r >> 2)) * 128 + lt * 32 + (r & 3) * 8);
    const u32x2 xv = *(LDS const u32x2*)(smem + H_XT + (16 * lt + r) * (W * 2) + (16 * dt + 4 * kq) * 2);
    f32x4 res;
#pragma unroll
    for (int i = 0; i < 4; ++i) {
      const u32 xw = xv[i >> 1], aw = da[t][i >> 1], bw = db[t][i >> 1];
      const float xf = (i & 1) ? bfhi(xw) : bflo(xw);
      const float sa = (i & 1) ? bfhi(aw) : bflo(aw);
      const float sb = (i & 1) ? bfhi(bw) : bflo(bw);
      const float yc = bf2f((u16)av[i]);
      const float a = rbf(yc + rbf(xf * sa));
      const float b = y[i] + rbf(xf * sb);
      res[i] = rbf(gg[0] * a) + gg[1] * b;
    }
    *(LDS f32x4*)(smem + H_OUT + (16 * lt + r) * OSTR + (16 * dt + 4 * kq) * 4) = res;
  }
  STAMP(1, 4);
  bar();
#pragma unroll
  for (int it = 0; it < NDT; ++it) {
    const int id = it * 512 + w * 64 + lane;
    const int row = id / (8 * NDT), pc = id - row * (8 * NDT);
    const f32x4 v = *(LDS const f32x4*)(smem + H_OUT + row * OSTR + pc * 16);
    __builtin_amdgcn_raw_buffer_store_b128(__builtin_bit_cast(u32x4, v), mk_rsrc(p.out, SL * DM * 4), ((l0 + row) * DM + d0 + pc * 4) * 4, 0, OUT_AUX);
  }
}

template <int NDT>
DEV void phase2(const P& p, LDS char* smem, int lt, int d0, int w, int lane, bool scratch_barrier, int sg) {
  STAMP(1, 0);
  if (w >= 8) {
    p2_loader<NDT>(p, smem, lt, d0, w - 8, lane, scratch_barrier, [&]() { if (NDT == 1) { wait_vm<8>(); s_signal(p, smem, sg, lane); } });
  } else {
    if (NDT == 1) { wait_vm<0>(); s_signal(p, smem, sg, lane); }
    if (w < 4) p2_compute<NDT, 0>(p, smem, lt, d0, w, lane);
    else p2_compute<NDT, 1>(p, smem, lt, d0, w, lane);
  }
  STAMP(1, 15);
}

__global__ __launch_bounds__(768, 3) void gated_mix_s4d_s5(P p) {
  extern __shared__ __attribute__((aligned(16))) char smem_raw[];
  LDS char* smem = (LDS char*)smem_raw;
  u64 pc;
  asm volatile("s_getpc_b64 %0" : "=s"(pc));
  u32 pf = 0;
  const u64 t_begin = __builtin_amdgcn_s_memrealtime();
#define PF_TOP 312
  if (threadIdx.x < PF_TOP)
    asm volatile("global_load_dword %0, %1, off" : "=v"(pf) : "v"((pc & ~255ull) + threadIdx.x * 128));
  const int w = __builtin_amdgcn_readfirstlane(threadIdx.x >> 6), lane = threadIdx.x & 63;
  const int xcd = blockIdx.x & 7, j = blockIdx.x >> 3;
  STAMP(0, 0);
  if (j >= 18 && j < 30) {
    const int sg = 12 * xcd + (j - 18);
    role_s(p, smem, sg, w, lane);
    STAMP(0, 15);
    phase2<1>(p, smem, 4 * xcd + (j - 18) / 3, 480 + 32 * ((j - 18) % 3), w, lane, false, sg);
  } else {
    const bool isg = j < 18;
    if (isg) {
      const int c = 2 * xcd + j / 9, g = j % 9;
      if (w >= 8) g_loader(p, smem, c, g, 96 * (j % 5), w - 8, lane);
      else if (w < 4) g_compute<0>(p, smem, c, g, w, lane);
      else g_compute<1>(p, smem, c, g, w, lane);
    } else {
      role_gate(p, 2 * xcd + (j - 30), w, lane, pc);
    }
    STAMP(0, 15);
    const int e = isg ? j : j - 12;
    phase2<3>(p, smem, 4 * xcd + e / 5, 96 * (e % 5), w, lane, isg, 0);
  }
  asm volatile("s_waitcnt vmcnt(0)" :: "v"(pf));
  if (w == 0 && (FIXED_WG)) { while (__builtin_amdgcn_s_memrealtime() - t_begin < FIXED_T) __builtin_amdgcn_s_sleep(4); }
}

extern "C" void kernel_launch(void* const* d_in, const int* in_sizes, int n_in, void* d_out, int out_size,
                              void* d_ws, size_t ws_size, hipStream_t stream) {
  P a;
  a.x = (const u16*)d_in[0]; a.la = (const u16*)d_in[1]; a.aim = (const u16*)d_in[2];
  a.creA = (const u16*)d_in[3]; a.cimA = (const u16*)d_in[4]; a.ldtA = (const u16*)d_in[5]; a.dskA = (const u16*)d_in[6];
  a.lre = (const u16*)d_in[8]; a.lim = (const u16*)d_in[9]; a.bre = (const u16*)d_in[10]; a.bim = (const u16*)d_in[11];
  a.cre = (const u16*)d_in[12]; a.cim = (const u16*)d_in[13]; a.ldtB = (const u16*)d_in[14];
  a.dskB = (const u16*)d_in[15]; a.gw = (const u16*)d_in[16];
  a.ws = (char*)d_ws + WS_OFF; a.out = (float*)d_out;
  (void)hipFuncSetAttribute((const void*)gated_mix_s4d_s5, hipFuncAttributeMaxDynamicSharedMemorySize, K_LDS);
  gated_mix_s4d_s5<<<256, 768, K_LDS, stream>>>(a);
}
```

```cpp
#define OUT_AUX 16
#define BURN_S 2
#include <hip/hip_runtime.h>
#include <hip/hip_bf16.h>
#include <stdint.h>
#include <type_traits>

typedef unsigned short u16;
typedef unsigned int u32;
typedef unsigned long long u64;
typedef __attribute__((ext_vector_type(8))) __bf16 bf16x8;
typedef __attribute__((ext_vector_type(2))) __bf16 bf16x2;
typedef __attribute__((ext_vector_type(4))) short s16x4;
typedef __attribute__((ext_vector_type(4))) float f32x4;
typedef __attribute__((ext_vector_type(2))) float f32x2;
typedef __attribute__((ext_vector_type(4))) unsigned u32x4;
typedef __attribute__((ext_vector_type(2))) unsigned u32x2;
#define LDS __attribute__((address_space(3)))
#define GLB __attribute__((address_space(1)))
#define DEV __device__ __forceinline__
#define RLX_AGENT __ATOMIC_RELAXED, __HIP_MEMORY_SCOPE_AGENT

constexpr int SL = 2048;
constexpr int DM = 576;

constexpr size_t WS_HB = 0;
constexpr size_t WS_AT = WS_HB + (size_t)2 * DM * SL * 2;
constexpr size_t WS_G = WS_AT + (size_t)DM * SL * 2;
constexpr size_t WS_GRAN = WS_G + (size_t)SL * 8;
constexpr size_t WS_EP = WS_GRAN + (size_t)9 * 16 * 2 * 64 * 8;
constexpr size_t WS_FL = WS_EP + 4096;
constexpr size_t WS_XT = WS_FL + (size_t)16 * DM * 8;
constexpr size_t WS_IN = WS_XT + (size_t)DM * SL * 2;
constexpr u64 FLAG_SET = 0x9E3779B97F4A7C15ull;
constexpr u32 SPIN_CAP = 1u << 17;
constexpr int IN_H = 0, IN_G = 36, IN_A = 40;

constexpr size_t WS_ST = (size_t)16 << 20;
#define STAMP(kern, idx) do {} while (0)
#define CSTAMP(kern, idx) do {} while (0)
#define LSTAMP(kern, idx) do {} while (0)
#define LSTAMP2(kern, idx) do {} while (0)

DEV float bf2f(u16 v) { return __uint_as_float((u32)v << 16); }
DEV float bflo(u32 v) { return __uint_as_float(v << 16); }
DEV float bfhi(u32 v) { return __uint_as_float(v & 0xffff0000u); }
DEV u32 pack_bf16(float lo, float hi) {
  f32x2 v = {lo, hi};
  bf16x2 b = __builtin_convertvector(v, bf16x2);
  return __builtin_bit_cast(u32, b);
}
DEV float rbf(float x) { return bflo(pack_bf16(x, x)); }
template <int N> DEV void wait_vm() { asm volatile("s_waitcnt vmcnt(%0)" ::"n"(N) : "memory"); }
DEV void bar() { asm volatile("s_waitcnt lgkmcnt(0)\n\ts_barrier" ::: "memory"); }
DEV void wave_fence() { __builtin_amdgcn_fence(__ATOMIC_ACQ_REL, "wavefront"); }
template <int AUX> DEV void dma16(const void* src, LDS char* dst) {
  __builtin_amdgcn_global_load_lds((const GLB u32*)src, (LDS u32*)dst, 16, 0, AUX);
}
constexpr int SC1 = 16;
#define WS_OFF 0
#define XT_WAIT 4
DEV bf16x8 lds_frag(LDS const char* p) { return *(LDS const bf16x8*)p; }
DEV s16x4 tr_read(LDS const char* a) { return __builtin_amdgcn_ds_read_tr16_b64_v4i16((LDS s16x4*)a); }
DEV bf16x8 tr_frag(LDS const char* a, LDS const char* b) {
  const s16x4 lo = tr_read(a), hi = tr_read(b);
  return __builtin_bit_cast(bf16x8, __builtin_shufflevector(lo, hi, 0, 1, 2, 3, 4, 5, 6, 7));
}
DEV f32x4 mfma16(bf16x8 a, bf16x8 b, f32x4 c) { return __builtin_amdgcn_mfma_f32_16x16x32_bf16(a, b, c, 0, 0, 0); }
DEV __amdgpu_buffer_rsrc_t mk_rsrc(const void* base, int bytes) {
  return __builtin_amdgcn_make_buffer_rsrc(const_cast<void*>(base), (short)0, bytes, 0x00020000);
}
struct cf { float r, i; };
DEV cf cmul(cf a, cf b) { return {a.r * b.r - a.i * b.i, a.r * b.i + a.i * b.r}; }
DEV cf cfma(cf a, cf b, cf c) { return {fmaf(a.r, b.r, fmaf(-a.i, b.i, c.r)), fmaf(a.r, b.i, fmaf(a.i, b.r, c.i))}; }
typedef f32x2 c2;
DEV c2 cm2(c2 a, c2 b) {
  c2 t, r;
  asm("v_pk_mul_f32 %0, %1, %2 op_sel:[0,0] op_sel_hi:[0,1]" : "=v"(t) : "v"(a), "v"(b));
  asm("v_pk_fma_f32 %0, %1, %2, %3 op_sel:[1,1,0] op_sel_hi:[1,0,1] neg_lo:[1,0,0]" : "=v"(r) : "v"(a), "v"(b), "v"(t));
  return r;
}
DEV f32x2 fma2(f32x2 a, f32x2 b, f32x2 c) { return __builtin_elementwise_fma(a, b, c); }
DEV f32x2 lo2(f32x4 v) { return __builtin_shufflevector(v, v, 0, 1); }
DEV f32x2 hi2(f32x4 v) { return __builtin_shufflevector(v, v, 2, 3); }
DEV f32x4 cat2(f32x2 a, f32x2 b) { return __builtin_shufflevector(a, b, 0, 1, 2, 3); }
DEV void wait_flags(GLB u64* word, bool need) {
  for (u32 spin = 0;; ++spin) {
    const u64 v = __hip_atomic_load(word, RLX_AGENT);
    if (__all(!need || v == FLAG_SET) || spin > SPIN_CAP) break;
    __builtin_amdgcn_s_sleep(2);
  }
}
DEV int tile_block(int lt, int idx) {
  const int xcd = lt >> 2, a = lt & 3;
  int j;
  if (idx < 5) { const int e = 5 * a + idx; j = e < 18 ? e : e + 12; }
  else j = 18 + 3 * a + (idx - 5);
  return (j << 3) | xcd;
}

struct P {
  const u16 *x, *la, *aim, *creA, *cimA, *ldtA, *dskA;
  const u16 *lre, *lim, *bre, *bim, *cre, *cim, *ldtB, *dskB, *gw;
  char* ws;
  float* out;
};

constexpr int G_NB = 4, G_STAGE = 32768;
constexpr int G_E = G_NB * G_STAGE;
constexpr int G_PF = G_E + 4096;
constexpr int G_L = G_PF + 4096;
constexpr int G_X = G_L + 1024;
constexpr int G_T = G_X + 2048;
constexpr int G_END = G_T + 4 * 32 * 144;

DEV void g_loader(const P& p, LDS char* smem, int c, int g, int d0, int lw, int lane) {
  [[maybe_unused]] const int w = lw + 8;
  __builtin_amdgcn_s_setprio(3);
  GLB u32* ep = (GLB u32*)(p.ws + WS_EP + g * 256);
  GLB u64* gran = (GLB u64*)(p.ws + WS_GRAN) + (size_t)g * 16 * 128;
  const u32 tag = __hip_atomic_load(ep, RLX_AGENT) + 1u;
  u32 xo[4], bo[4];
#pragma unroll
  for (int i = 0; i < 4; ++i) {
    const int rho = 8 * (4 * lw + i) + (lane >> 3);
    const int hf = rho >> 6, m = (rho >> 4) & 3, r = rho & 15;
    const int tau = 64 * hf + 16 * (r >> 2) + 8 * (r & 1) + 2 * m + ((r >> 1) & 1);
    const int ch = (lane & 7) ^ (rho & 7);
    xo[i] = (u32)(128 * c + tau) * 1152u + ch * 16;
    const int pp = 64 * g + 16 * (rho >> 5) + r;
    bo[i] = (u32)pp * 1152u + ch * 16;
  }
  auto issue = [&](int s) {
    LDS char* buf = smem + (s & (G_NB - 1)) * G_STAGE + lw * 4096;
    int kb = g + s; kb = (kb >= 9 ? kb - 9 : kb) * 128;
#pragma unroll
    for (int i = 0; i < 4; ++i) dma16<0>((const char*)p.x + (xo[i] + kb), buf + i * 1024);
#pragma unroll
    for (int i = 0; i < 4; ++i) {
      const char* base = (const char*)((((4 * lw + i) >> 1) & 1) ? p.bim : p.bre);
      dma16<0>(base + (bo[i] + kb), buf + 16384 + i * 1024);
    }
  };
#pragma unroll
  for (int s = 0; s < G_NB - 1; ++s) issue(s);
#pragma unroll 1
  for (int s = 0; s < 6; ++s) { wait_vm<16>(); LSTAMP(0, 16 + s); bar(); issue(s + G_NB - 1); LSTAMP(0, 32 + s); }
  wait_vm<16>(); bar();
  wait_vm<8>(); bar();
  wait_vm<0>(); bar();
  STAMP(0, 1);
  if (lw < 2) {
    bar();
    u32 co[6];
#pragma unroll
    for (int i = 0; i < 6; ++i) {
      const int row = 8 * (6 * lw + i) + (lane >> 3), cp = lane & 7;
      co[i] = (u32)(d0 + row) * 1152u + ((cp ^ (row & 7)) << 4);
    }
    const int rot = ((d0 / 96) * 18) >> 3;
#pragma unroll 1
    for (int s = 0; s < 8; ++s) {
      LDS char* buf = smem + 32768 + s * 12288 + 6 * lw * 1024;
      const int ks = s + rot;
      const char* cb = ks < 9 ? (const char*)p.cre + ks * 128 : (const char*)p.cim + (ks - 9) * 128;
#pragma unroll
      for (int i = 0; i < 6; ++i) dma16<0>(cb + co[i], buf + i * 1024);
    }
    bar();
    return;
  }
  bar();
  STAMP(0, 2);
  if (lw == 3) {
    const f32x2 lv = *(LDS const f32x2*)(smem + G_L + lane * 8);
    const cf l16 = {lv[0], lv[1]};
    f32x2 ev[8];
#pragma unroll
    for (int s = 0; s < 8; ++s) ev[s] = *(LDS const f32x2*)(smem + G_E + (s * 64 + lane) * 8);
    cf tot = {0.f, 0.f};
    cf pre[8];
#pragma unroll
    for (int s = 0; s < 8; ++s) { pre[s] = tot; tot = cfma(l16, tot, cf{ev[s][0], ev[s][1]}); }
    if (c < 15) {
      __hip_atomic_store(gran + c * 128 + lane, ((u64)tag << 32) | __float_as_uint(tot.r), RLX_AGENT);
      __hip_atomic_store(gran + c * 128 + 64 + lane, ((u64)tag << 32) | __float_as_uint(tot.i), RLX_AGENT);
    }
#pragma unroll
    for (int s = 0; s < 8; ++s) *(LDS f32x2*)(smem + G_PF + (s * 64 + lane) * 8) = f32x2{pre[s].r, pre[s].i};
  }
  const int par = lw - 2;
  const f32x2 lv = *(LDS const f32x2*)(smem + G_L + 512 + lane * 8);
  const cf l128 = {lv[0], lv[1]};
  const cf l256 = cmul(l128, l128);
  cf acc = {0.f, 0.f};
  if (par < c) {
    const int nc = (c - par + 1) >> 1;
    u64 a[8], b[8];
    for (u32 spin = 0;; ++spin) {
      bool ok = true;
#pragma unroll
      for (int i = 0; i < 8; ++i) {
        const int cc = par + 2 * (i < nc ? i : 0);
        a[i] = __hip_atomic_load(gran + cc * 128 + lane, RLX_AGENT);
        b[i] = __hip_atomic_load(gran + cc * 128 + 64 + lane, RLX_AGENT);
      }
#pragma unroll
      for (int i = 0; i < 8; ++i) ok = ok && (u32)(a[i] >> 32) == tag && (u32)(b[i] >> 32) == tag;
      if (__all(ok) || spin > SPIN_CAP) break;
      __builtin_amdgcn_s_sleep(1);
    }
#pragma unroll
    for (int i = 0; i < 8; ++i)
      if (i < nc) acc = cfma(l256, acc, cf{__uint_as_float((u32)a[i]), __uint_as_float((u32)b[i])});
    if (c - 1 - (par + 2 * (nc - 1))) acc = cmul(acc, l128);
  }
  *(LDS f32x2*)(smem + G_X + (par * 64 + lane) * 8) = f32x2{acc.r, acc.i};
  STAMP(0, 3);
  bar();
  if (c == 15 && lw == 2 && lane == 0) __hip_atomic_store(ep, tag, RLX_AGENT);
}

template <int HF>
DEV void g_compute(const P& p, LDS char* smem, int c, int g, int w, int lane) {
  const int pq = w & 3, col = lane & 15, q = lane >> 4;
  const int pl = 16 * pq + col, pp = 64 * g + pl;
  cf lam, coef2;
  {
    const float lre = bf2f(p.lre[pp]), lim = bf2f(p.lim[pp]);
    const float dt = rbf(expf(bf2f(p.ldtB[pp])));
    const float zr = dt * lre, zi = dt * lim;
    const float er = expf(zr);
    float sn, cs;
    sincosf(zi, &sn, &cs);
    lam = {er * cs, er * sn};
    const float nr = lam.r - 1.0f, ni = lam.i;
    const float den = 1.0f / (lre * lre + lim * lim);
    coef2 = {2.0f * (nr * lre + ni * lim) * den, 2.0f * (ni * lre - nr * lim) * den};
  }
  cf l8 = cmul(lam, lam); l8 = cmul(l8, l8); l8 = cmul(l8, l8);
  const cf l16 = cmul(l8, l8);
  const cf l32 = cmul(l16, l16), l64 = cmul(l32, l32);
  if (HF == 0 && q == 0) {
    const cf l128 = cmul(l64, l64);
    *(LDS f32x2*)(smem + G_L + pl * 8) = f32x2{l16.r, l16.i};
    *(LDS f32x2*)(smem + G_L + 512 + pl * 8) = f32x2{l128.r, l128.i};
  }
  cf pw = HF ? l64 : cf{1.f, 0.f};
  { const cf t = cmul(pw, l16); if (q & 1) pw = t; }
  { const cf t = cmul(pw, l32); if (q & 2) pw = t; }
  u32 wb[3] = {0, 0, 0};
  if (HF == 0) {
#pragma unroll
    for (int i = 0; i < 3; ++i) {
      const int ln = i * 256 + pq * 64 + lane;
      if (ln < 576) {
        const int row = ln / 9, kb = ln - row * 9;
        wb[i] = *(const GLB u32*)((const char*)((c & 1) ? p.bim : p.bre) + (size_t)(64 * g + row) * 1152 + kb * 128);
      }
    }
  }
  asm volatile("" : "+v"(lam.r), "+v"(lam.i), "+v"(coef2.r), "+v"(coef2.i), "+v"(l8.r), "+v"(l8.i), "+v"(pw.r), "+v"(pw.i));
  f32x4 are[4], aim[4];
#pragma unroll
  for (int m = 0; m < 4; ++m) { are[m] = f32x4{0, 0, 0, 0}; aim[m] = f32x4{0, 0, 0, 0}; }
  const int o0 = col * 128 + (((0 + q) ^ (col & 7)) << 4);
  const int o1 = col * 128 + (((4 + q) ^ (col & 7)) << 4);
  bf16x8 br[2], bi[2], a[2][4];
  auto read_frags = [&](int s) {
    LDS const char* buf = smem + (s & (G_NB - 1)) * G_STAGE;
    LDS const char* ab = buf + HF * 8192;
    LDS const char* bb = buf + 16384 + pq * 4096;
#pragma unroll
    for (int ks = 0; ks < 2; ++ks) {
      const int o = ks ? o1 : o0;
      br[ks] = lds_frag(bb + o); bi[ks] = lds_frag(bb + 2048 + o);
#pragma unroll
      for (int m = 0; m < 4; ++m) a[ks][m] = lds_frag(ab + m * 2048 + o);
    }
  };
  auto mma = [&]() {
#pragma unroll
    for (int ks = 0; ks < 2; ++ks)
#pragma unroll
      for (int m = 0; m < 4; ++m) {
        are[m] = mfma16(a[ks][m], br[ks], are[m]);
        aim[m] = mfma16(a[ks][m], bi[ks], aim[m]);
      }
  };
  STAMP(0, 1);
#pragma unroll
  for (int s = 0; s < 9; ++s) {
    if (HF == 1 && s == XT_WAIT) {
      wait_vm<0>();
      if (lane < 16) __hip_atomic_store((GLB u64*)(p.ws + WS_FL) + c * DM + 64 * g + 16 * pq + lane, FLAG_SET, RLX_AGENT);
    }
    bar();
    if (s == 0) { STAMP(0, 2); CSTAMP(0, 60); }
    if (HF == 1 && s > 0) { mma(); __builtin_amdgcn_sched_barrier(0); }
    if (HF == 1 && s == 0) {
      const int rr = 4 * q + (col >> 2);
      LDS const char* tb = smem + rr * 128 + (((2 * pq + ((col & 3) >> 1)) ^ (rr & 7)) << 4) + (col & 1) * 8;
      LDS char* sc = smem + G_T + pq * (32 * 144);
#pragma unroll
      for (int hf = 0; hf < 2; ++hf) {
        u32x2 t[4];
#pragma unroll
        for (int m = 0; m < 4; ++m) t[m] = __builtin_bit_cast(u32x2, tr_read(tb + hf * 8192 + m * 2048));
        u32x4 oa, ob;
#pragma unroll
        for (int m = 0; m < 4; ++m) {
          oa[m] = __builtin_amdgcn_perm(t[m][1], t[m][0], 0x05040100u);
          ob[m] = __builtin_amdgcn_perm(t[m][1], t[m][0], 0x07060302u);
        }
        *(LDS u32x4*)(sc + hf * (16 * 144) + col * 144 + q * 32) = oa;
        *(LDS u32x4*)(sc + hf * (16 * 144) + col * 144 + q * 32 + 16) = ob;
      }
      wave_fence();
      const __amdgpu_buffer_rsrc_t rs = mk_rsrc(p.ws + WS_XT, DM * SL * 2);
#pragma unroll
      for (int u = 0; u < 4; ++u) {
        const int row = (lane >> 3) + 8 * (u & 1), hf = u >> 1;
        const u32x4 v = *(LDS const u32x4*)(sc + hf * (16 * 144) + row * 144 + (lane & 7) * 16);
        __builtin_amdgcn_raw_buffer_store_b128(v, rs, ((64 * g + 16 * pq + row) * SL + 128 * c + 64 * hf) * 2 + (lane & 7) * 16, 0, SC1);
      }
      __builtin_amdgcn_sched_barrier(0);
    }
    read_frags(s);
    __builtin_amdgcn_sched_barrier(0);
    if (HF == 0) { mma(); __builtin_amdgcn_sched_barrier(0); }
  }
  if (HF == 1) mma();
  __builtin_amdgcn_sched_barrier(0);
  STAMP(0, 3); CSTAMP(0, 61);
  const f32x2 Lr = {lam.r, lam.r}, Li = {lam.i, lam.i};
  f32x2 er = {0.f, 0.f}, ei = {0.f, 0.f};
#pragma unroll
  for (int t = 0; t < 8; ++t) {
    const f32x2 br_ = (t & 1) ? hi2(are[t >> 1]) : lo2(are[t >> 1]);
    const f32x2 bi_ = (t & 1) ? hi2(aim[t >> 1]) : lo2(aim[t >> 1]);
    const f32x2 nr = fma2(-Li, ei, fma2(Lr, er, br_));
    ei = fma2(Li, er, fma2(Lr, ei, bi_));
    er = nr;
  }
  const cf eA = {er[0], ei[0]};
  const cf e = cfma(l8, eA, cf{er[1], ei[1]});
  const int seg = 4 * HF + q;
  *(LDS f32x2*)(smem + G_E + (seg * 64 + pl) * 8) = f32x2{e.r, e.i};
  asm volatile("" :: "v"(wb[0]), "v"(wb[1]), "v"(wb[2]));
  STAMP(0, 4);
  bar();
  STAMP(0, 5);
  {
#pragma unroll
    for (int m = 0; m < 4; ++m) asm volatile("" : "+v"(are[m]), "+v"(aim[m]));
    const f32x2 Cr = {coef2.r, coef2.r}, Ci = {coef2.i, coef2.i};
#pragma unroll
    for (int m = 0; m < 4; ++m) {
      const f32x2 r0 = lo2(are[m]), r1 = hi2(are[m]), i0 = lo2(aim[m]), i1 = hi2(aim[m]);
      are[m] = cat2(fma2(-Ci, i0, Cr * r0), fma2(-Ci, i1, Cr * r1));
      aim[m] = cat2(fma2(-Ci, r0, -Cr * i0), fma2(-Ci, r1, -Cr * i1));
      asm volatile("" : "+v"(are[m]), "+v"(aim[m]));
    }
  }
  cf eAc = cmul(coef2, eA), pwc = cmul(coef2, pw);
  asm volatile("" : "+v"(eAc.r), "+v"(eAc.i), "+v"(pwc.r), "+v"(pwc.i));
  STAMP(0, 7);
  bar();
  STAMP(0, 8);
  cf cin;
  {
    f32x2 v[2];
#pragma unroll
    for (int i = 0; i < 2; ++i) v[i] = *(LDS const f32x2*)(smem + G_X + (i * 64 + pl) * 8);
    const f32x2 pf = *(LDS const f32x2*)(smem + G_PF + (seg * 64 + pl) * 8);
    const f32x2 S = v[0] + v[1];
    cin = cfma(pwc, cf{S[0], S[1]}, cmul(coef2, cf{pf[0], pf[1]}));
  }
  const cf cinB = cfma(l8, cin, eAc);
  er = f32x2{cin.r, cinB.r}; ei = f32x2{-cin.i, -cinB.i};
  f32x2 hr[8], hi[8];
#pragma unroll
  for (int t = 0; t < 8; ++t) {
    const f32x2 br_ = (t & 1) ? hi2(are[t >> 1]) : lo2(are[t >> 1]);
    const f32x2 bi_ = (t & 1) ? hi2(aim[t >> 1]) : lo2(aim[t >> 1]);
    const f32x2 nr = fma2(Li, ei, fma2(Lr, er, br_));
    ei = fma2(-Li, er, fma2(Lr, ei, bi_));
    er = nr;
    hr[t] = er; hi[t] = ei;
  }
  u32x4 o[4];
#pragma unroll
  for (int t = 0; t < 4; ++t) {
    o[0][t] = pack_bf16(hr[2 * t][0], hr[2 * t + 1][0]);
    o[1][t] = pack_bf16(hr[2 * t][1], hr[2 * t + 1][1]);
    o[2][t] = pack_bf16(hi[2 * t][0], hi[2 * t + 1][0]);
    o[3][t] = pack_bf16(hi[2 * t][1], hi[2 * t + 1][1]);
  }
  STAMP(0, 9);
  {
    const __amdgpu_buffer_rsrc_t rs = mk_rsrc(p.ws + WS_HB, 2 * DM * SL * 2);
    const int hb = (((c * 2 + HF) * 2) * 9 + g) * 8192 + pq * 2048;
    const int p0 = (16 * q + col) * 16, p1 = 1024 + (16 * q + (col ^ 4)) * 16;
    __builtin_amdgcn_raw_buffer_store_b128(o[0], rs, hb + p0, 0, SC1);
    __builtin_amdgcn_raw_buffer_store_b128(o[1], rs, hb + p1, 0, SC1);
    __builtin_amdgcn_raw_buffer_store_b128(o[2], rs, hb + 9 * 8192 + p0, 0, SC1);
    __builtin_amdgcn_raw_buffer_store_b128(o[3], rs, hb + 9 * 8192 + p1, 0, SC1);
    bar();
    wait_vm<0>();
    STAMP(0, 10);
    if (lane < 8)
      __hip_atomic_store((GLB u64*)(p.ws + WS_IN) + tile_block(2 * c + HF, lane) * 64 + IN_H + g * 4 + pq, FLAG_SET, RLX_AGENT);
  }
}

constexpr int S_CPY = 4160;
constexpr int S_X = 0;
constexpr int S_ST = 6 * 4 * S_CPY;
constexpr int S_Z = S_ST;
constexpr int S_ZSZ = (256 + SL) * 2;
constexpr int S_T = S_ST + 12 * 4608;
constexpr int S_TSZ = 1312;
constexpr int S_END = S_T + 6 * S_TSZ;
constexpr int S_CNT = 32768 + 8192;

DEV c2 cexp_k(float zl, float th, float tl, float k) {
  const float mag = __builtin_amdgcn_exp2f(zl * k);
  const float t = fmaf(tl, k, __builtin_amdgcn_fractf(th * k));
  return c2{mag * __builtin_amdgcn_cosf(t), mag * __builtin_amdgcn_sinf(t)};
}
#define PKM(d, a, b) "v_pk_mul_f32 " d ", " a ", " b " op_sel:[0,0] op_sel_hi:[0,1]\n\t"
#define PKF(d, a, b) "v_pk_fma_f32 " d ", " a ", " b ", " d " op_sel:[1,1,0] op_sel_hi:[1,0,1] neg_lo:[1,0,0]\n\t"
DEV void octet(LDS char* dst, c2 s0, c2 m1, c2 m2) {
  c2 v1, v2, v3, t0, t1, t2, t3;
  asm(PKM("%0", "%7", "%8") PKM("%3", "%7", "%9") PKF("%0", "%7", "%8") PKF("%3", "%7", "%9")
      PKM("%1", "%0", "%8") PKM("%4", "%0", "%9") PKF("%1", "%0", "%8") PKF("%4", "%0", "%9")
      PKM("%2", "%1", "%8") PKM("%5", "%1", "%9") PKF("%2", "%1", "%8") PKF("%5", "%1", "%9")
      PKM("%6", "%2", "%9") PKF("%6", "%2", "%9")
      : "=&v"(v1), "=&v"(v2), "=&v"(v3), "=&v"(t0), "=&v"(t1), "=&v"(t2), "=&v"(t3) : "v"(s0), "v"(m1), "v"(m2));
  *(LDS u32*)(dst + 0 * 288) = pack_bf16(s0[0], s0[1]);
  *(LDS u32*)(dst + 1 * 288) = pack_bf16(v1[0], v1[1]);
  *(LDS u32*)(dst + 2 * 288) = pack_bf16(v2[0], v2[1]);
  *(LDS u32*)(dst + 3 * 288) = pack_bf16(v3[0], v3[1]);
  *(LDS u32*)(dst + 4 * 288) = pack_bf16(t0[0], t0[1]);
  *(LDS u32*)(dst + 5 * 288) = pack_bf16(t1[0], t1[1]);
  *(LDS u32*)(dst + 6 * 288) = pack_bf16(t2[0], t2[1]);
  *(LDS u32*)(dst + 7 * 288) = pack_bf16(t3[0], t3[1]);
}
DEV u32 dup_lo(u32 a) { return __builtin_amdgcn_perm(a, a, 0x01000100u); }
DEV u32 dup_hi(u32 a) { return __builtin_amdgcn_perm(a, a, 0x03020302u); }
DEV u32 hi_lo(u32 a, u32 b) { return __builtin_amdgcn_alignbit(b, a, 16); }

DEV void role_s(const P& p, LDS char* smem, int sg, int w, int lane) {
  const int tid = w * 64 + lane;
  const int ch = w >> 1, hh = w & 1, d = 6 * sg + ch;
  const int n = d * 128 + 64 * hh + lane;
  const u16 r_ldt = p.ldtA[d], r_la = p.la[n], r_aim = p.aim[n], r_cre = p.creA[n], r_cim = p.cimA[n];
  STAMP(0, 1);
  f32x4 tab[3];
  [[maybe_unused]] u32 wc[2] = {0, 0};
  LDS char* st = smem + S_ST + w * 4608;
  {
    const float L2E = 1.44269504088896341f;
    const float dt = rbf(__builtin_amdgcn_exp2f(bf2f(r_ldt) * L2E));
    const float ar = -rbf(__builtin_amdgcn_exp2f(bf2f(r_la) * L2E));
    const float zl = ar * dt * L2E, zi = bf2f(r_aim) * dt;
    const float th = zi * 0.15915494309189535f;
    const float tl = fmaf(zi, 0.15915494309189535f, -th) + zi * 4.6017384e-10f;
    const c2 C = {2.0f * (bf2f(r_cre) * dt), 2.0f * (bf2f(r_cim) * dt)};
    const c2 w1 = cexp_k(zl, th, tl, 1.f), w8 = cexp_k(zl, th, tl, 8.f), w128 = cexp_k(zl, th, tl, 128.f);
    c2 w2, w4, w16, w32, w64, w256, w512, w1024, w2048;
    asm(PKM("%0", "%9", "%9") PKM("%2", "%10", "%10") PKM("%5", "%11", "%11") PKF("%0", "%9", "%9") PKF("%2", "%10", "%10") PKF("%5", "%11", "%11")
        PKM("%1", "%0", "%0") PKM("%3", "%2", "%2") PKM("%6", "%5", "%5") PKF("%1", "%0", "%0") PKF("%3", "%2", "%2") PKF("%6", "%5", "%5")
        PKM("%4", "%3", "%3") PKM("%7", "%6", "%6") PKF("%4", "%3", "%3") PKF("%7", "%6", "%6")
        PKM("%8", "%7", "%7") PKF("%8", "%7", "%7")
        : "=&v"(w2), "=&v"(w4), "=&v"(w16), "=&v"(w32), "=&v"(w64), "=&v"(w256), "=&v"(w512), "=&v"(w1024), "=&v"(w2048)
        : "v"(w1), "v"(w8), "v"(w128));
    c2 C128, C256, C512, C1024, C2048;
    asm(PKM("%0", "%5", "%6") PKM("%1", "%5", "%7") PKM("%2", "%5", "%8") PKM("%3", "%5", "%9") PKM("%4", "%5", "%10")
        PKF("%0", "%5", "%6") PKF("%1", "%5", "%7") PKF("%2", "%5", "%8") PKF("%3", "%5", "%9") PKF("%4", "%5", "%10")
        : "=&v"(C128), "=&v"(C256), "=&v"(C512), "=&v"(C1024), "=&v"(C2048)
        : "v"(C), "v"(w128), "v"(w256), "v"(w512), "v"(w1024), "v"(w2048));
#pragma unroll
    for (int i = 0; i < 2; ++i) {
      const int ln = i * 768 + tid, all = (sg % 12) * 864 + ln;
      if (ln < 864) wc[i] = *(const GLB u32*)((const char*)(all < 5184 ? p.cre : p.cim) + (size_t)(all < 5184 ? all : all - 5184) * 128);
    }
    STAMP(0, 2);
    LDS char* wr = st + 4 * lane;
    LDS const char* rd = st + (lane & 15) * 288 + (lane >> 4) * 16;
    bf16x8 B[4];
    const c2 w8c = {w8[0], -w8[1]}, w32c = {w32[0], -w32[1]}, w64c = {w64[0], -w64[1]};
    octet(wr, c2{1.f, 0.f}, w8c, w32c);
    octet(wr + 8 * 288, w64c, w8c, w32c);
    wave_fence();
#pragma unroll
    for (int ks = 0; ks < 4; ++ks) B[ks] = lds_frag(rd + 64 * ks);
    wave_fence();
#pragma unroll
    for (int mt = 0; mt < 3; ++mt) {
      if (mt == 0) { octet(wr, C, w1, w4); octet(wr + 8 * 288, C128, w1, w4); }
      if (mt == 1) { octet(wr, C256, w2, w128); octet(wr + 8 * 288, C512, w128, w4); }
      if (mt == 2) { octet(wr, C1024, w128, w512); *(LDS u32*)(wr + 8 * 288) = pack_bf16(C2048[0], C2048[1]); }
      wave_fence();
      f32x4 acc = {0, 0, 0, 0};
      bf16x8 A[4];
#pragma unroll
      for (int ks = 0; ks < 4; ++ks) A[ks] = lds_frag(rd + 64 * ks);
      __builtin_amdgcn_sched_barrier(0);
#pragma unroll
      for (int ks = 0; ks < 4; ++ks) acc = mfma16(A[ks], B[ks], acc);
      __builtin_amdgcn_sched_barrier(0);
      wave_fence();
      tab[mt] = acc;
    }
  }
  const int b = lane & 15, q = lane >> 4;
#pragma unroll
  for (int mt = 0; mt < 3; ++mt) *(LDS f32x4*)(st + mt * 1024 + lane * 16) = tab[mt];
  STAMP(0, 3);
  bar();
  {
    LDS const char* ot = smem + S_ST + (w ^ 1) * 4608 + lane * 16;
    LDS char* T = smem + S_T + ch * S_TSZ;
    if (hh == 0) {
      const f32x4 v0 = tab[0] + *(LDS const f32x4*)(ot), v2 = tab[2] + *(LDS const f32x4*)(ot + 2048);
      *(LDS u32x2*)(T + 2 * (128 * (q >> 1) + 8 * b + 4 * (q & 1))) = u32x2{pack_bf16(v0[0], v0[1]), pack_bf16(v0[2], v0[3])};
      const u32 a0 = pack_bf16(v2[0], v2[1]), a1 = pack_bf16(v2[2], v2[3]);
      if (q < 2) {
        LDS char* t3 = T + 2 * (512 + 64 * q + b);
        *(LDS u16*)(t3) = (u16)a0; *(LDS u16*)(t3 + 32) = (u16)(a0 >> 16);
        *(LDS u16*)(t3 + 64) = (u16)a1; *(LDS u16*)(t3 + 96) = (u16)(a1 >> 16);
      } else if (lane == 32) *(LDS u16*)(T + 2 * 640) = (u16)a0;
    } else {
      const f32x4 v1 = tab[1] + *(LDS const f32x4*)(ot + 1024);
      const u32 a0 = pack_bf16(v1[0], v1[1]), a1 = pack_bf16(v1[2], v1[3]);
      if (q < 2) *(LDS u32x2*)(T + 2 * (256 + 64 * q + 4 * b)) = u32x2{a0, a1};
      else {
        LDS char* t2 = T + 2 * (384 + 2 * b + (q - 2));
        *(LDS u16*)(t2) = (u16)a0; *(LDS u16*)(t2 + 64) = (u16)(a0 >> 16);
        *(LDS u16*)(t2 + 128) = (u16)a1; *(LDS u16*)(t2 + 192) = (u16)(a1 >> 16);
      }
    }
  }
  bar();
  STAMP(0, 4);
  u32x4 uv[2];
  {
    GLB u64* fl = (GLB u64*)(p.ws + WS_FL) + (8 * hh + (lane & 7)) * DM + d;
    asm volatile("" :: "v"(wc[0]), "v"(wc[1]));
    wait_flags(fl, true);
    const __amdgpu_buffer_rsrc_t rs = mk_rsrc(p.ws + WS_XT, DM * SL * 2);
#pragma unroll
    for (int v = 0; v < 2; ++v) uv[v] = __builtin_amdgcn_raw_buffer_load_b128(rs, (d * SL + 1024 * hh + 8 * (lane + 64 * v)) * 2, 0, SC1);
    if (lane < 8) __hip_atomic_store(fl, 0ull, RLX_AGENT);
  }
  STAMP(0, 5);
  {
    LDS const char* T = smem + S_T + ch * S_TSZ;
    u32 o[10];
    const u32 e512 = *(LDS const u16*)(T + 2 * 512);
    o[8] = o[9] = e512 | (e512 << 16);
    if (hh == 0) {
      const int off = lane < 16 ? 32 * lane : (lane < 32 ? 256 + 16 * lane : 512 + 8 * lane);
      u32x2 W[4];
#pragma unroll
      for (int i = 0; i < 4; ++i) W[i] = *(LDS const u32x2*)(T + off + 8 * i);
      if (lane < 16) {
#pragma unroll
        for (int i = 0; i < 8; ++i) o[i] = W[i >> 1][i & 1];
      } else if (lane < 32) {
#pragma unroll
        for (int i = 0; i < 4; ++i) {
          const u32 a = W[i >> 1][i & 1], nx = W[(i + 1) >> 1][(i + 1) & 1];
          o[2 * i] = dup_lo(a);
          o[2 * i + 1] = hi_lo(a, nx);
        }
      } else {
        const u32 w0 = W[0][0], w1 = W[0][1], w2 = W[1][0];
        o[0] = dup_lo(w0); o[1] = w0; o[2] = dup_hi(w0); o[3] = dup_lo(w1);
        o[4] = o[3]; o[5] = w1; o[6] = dup_hi(w1); o[7] = dup_lo(w2);
      }
    } else {
      const u32 w0 = *(LDS const u32*)(T + 1024 + 4 * lane), w1 = *(LDS const u32*)(T + 1028 + 4 * lane);
      o[0] = o[1] = dup_lo(w0); o[2] = w0; o[3] = o[4] = o[5] = dup_hi(w0); o[6] = o[7] = dup_lo(w1);
    }
    o[8] = (u32)__builtin_amdgcn_update_dpp((int)o[8], (int)o[0], 0x130, 0xf, 0xf, false);
    o[9] = (u32)__builtin_amdgcn_update_dpp((int)o[9], (int)o[1], 0x130, 0xf, 0xf, false);
    u32 a[9];
#pragma unroll
    for (int i = 0; i < 9; ++i) a[i] = hi_lo(o[i], o[i + 1]);
    LDS char* xb = smem + S_X + ch * 4 * S_CPY + 32 + 2 * (1024 * hh + 16 * lane);
    *(LDS u32x4*)(xb) = u32x4{o[0], o[1], o[2], o[3]};
    *(LDS u32x4*)(xb + 16) = u32x4{o[4], o[5], o[6], o[7]};
    *(LDS u32x4*)(xb + S_CPY) = u32x4{a[0], a[1], a[2], a[3]};
    *(LDS u32x4*)(xb + S_CPY + 16) = u32x4{a[4], a[5], a[6], a[7]};
    *(LDS u32x4*)(xb + 2 * S_CPY) = u32x4{o[1], o[2], o[3], o[4]};
    *(LDS u32x4*)(xb + 2 * S_CPY + 16) = u32x4{o[5], o[6], o[7], o[8]};
    *(LDS u32x4*)(xb + 3 * S_CPY) = u32x4{a[1], a[2], a[3], a[4]};
    *(LDS u32x4*)(xb + 3 * S_CPY + 16) = u32x4{a[5], a[6], a[7], a[8]};
    if (hh == 0 && lane == 0) {
      LDS char* hb = smem + S_X + ch * 4 * S_CPY;
      const u32x4 z = {0, 0, 0, 0};
      *(LDS u32x4*)(hb) = z;
      *(LDS u32x4*)(hb + 16) = z;
      *(LDS u32x4*)(hb + S_CPY) = z;
      *(LDS u32x4*)(hb + S_CPY + 16) = u32x4{0, 0, 0, o[0] << 16};
      *(LDS u32x4*)(hb + 2 * S_CPY) = z;
      *(LDS u32x4*)(hb + 2 * S_CPY + 16) = u32x4{0, 0, 0, o[0]};
      *(LDS u32x4*)(hb + 3 * S_CPY) = z;
      *(LDS u32x4*)(hb + 3 * S_CPY + 16) = u32x4{0, 0, o[0] << 16, a[0]};
    }
    if (tid < 6 * 32) *(LDS u32x4*)(smem + S_Z + (tid >> 5) * S_ZSZ + (tid & 31) * 16) = u32x4{0, 0, 0, 0};
    STAMP(0, 6);
    LDS char* z = smem + S_Z + ch * S_ZSZ + 512 + (1024 * hh + 8 * lane) * 2;
    *(LDS u32x4*)z = uv[0];
    *(LDS u32x4*)(z + 1024) = uv[1];
  }
  bar();
  STAMP(0, 7);
  f32x4 acc[8];
#pragma unroll
  for (int i = 0; i < 8; ++i) acc[i] = f32x4{0, 0, 0, 0};
  {
    const int kq = q, qq = b >> 2, pp = b & 3;
    LDS const char* fb = smem + S_X + (ch * 4 + ((4 - qq) & 3)) * S_CPY +
                         2 * (16 + 16 * (kq >> 1) - 8 * (kq & 1) + 4 * pp - (qq ? 4 : 0)) + 64 * hh;
    LDS const char* gb = smem + S_Z + ch * S_ZSZ + 512 + 32 * (b - (kq >> 1)) + 16 * (kq & 1) - 64 * hh;
    {
      f32x4 dum[8];
#pragma unroll
      for (int i = 0; i < 8; ++i) dum[i] = f32x4{0, 0, 0, 0};
#pragma unroll 1
      for (int it = 0; it < BURN_S; ++it) {
        bf16x8 F[8], G[8];
#pragma unroll
        for (int s = 0; s < 8; ++s) { F[s] = tr_frag(fb + 512 * s, fb + 512 * s - 8); G[s] = lds_frag(gb + 512 * s); }
        __builtin_amdgcn_sched_barrier(0);
#pragma unroll
        for (int t = 0; t < 8; ++t)
#pragma unroll
          for (int s = 0; s <= t; ++s) dum[t] = mfma16(F[s], G[t - s], dum[t]);
      }
#pragma unroll
      for (int i = 0; i < 8; ++i) asm volatile("" :: "v"(dum[i]));
    }
#pragma unroll 1
    for (int it = 0; it < 4; ++it) {
      bf16x8 F[8], G[8];
#pragma unroll
      for (int s = 0; s < 8; ++s) {
        F[s] = tr_frag(fb + 512 * s, fb + 512 * s - 8);
        G[s] = lds_frag(gb + 512 * s);
      }
      __builtin_amdgcn_sched_barrier(0);
#pragma unroll
      for (int t = 0; t < 8; ++t)
#pragma unroll
        for (int s = 0; s <= t; ++s) acc[t] = mfma16(F[s], G[t - s], acc[t]);
      fb += 128;
      gb -= 128;
    }
  }
  __builtin_amdgcn_sched_barrier(0);
  STAMP(0, 8);
  bar();
  {
    LDS char* ex = smem + S_X + 49152 + w * 4096 + lane * 16;
#pragma unroll
    for (int i = 0; i < 4; ++i) {
      const f32x4 lo = acc[i], hi = acc[4 + i];
      *(LDS f32x4*)(ex + i * 1024) = hh ? +lo : +hi;
    }
    if (tid == 0) *(LDS u32*)(smem + S_CNT) = 0;
  }
  bar();
  u32x2 yo[4];
  {
    LDS const char* ey = smem + S_X + 49152 + (w ^ 1) * 4096 + lane * 16;
#pragma unroll
    for (int i = 0; i < 4; ++i) {
      const f32x4 lo = acc[i], hi = acc[4 + i];
      const f32x4 y = (hh ? +hi : +lo) + *(LDS const f32x4*)(ey + i * 1024);
      yo[i] = u32x2{pack_bf16(y[0], y[1]), pack_bf16(y[2], y[3])};
    }
  }
  bar();
  {
    const __amdgpu_buffer_rsrc_t rs = mk_rsrc(p.ws + WS_AT, DM * SL * 2);
    const int dst = (d * SL + 1024 * hh + 16 * (lane & 15) + 4 * (lane >> 4)) * 2;
#pragma unroll
    for (int i = 0; i < 4; ++i) __builtin_amdgcn_raw_buffer_store_b64(yo[i], rs, dst + 512 * i, 0, SC1);
  }
  STAMP(0, 9);
}
DEV void s_signal(const P& p, LDS char* smem, int sg, int lane) {
  u32 old = 0;
  if (lane == 0) old = __hip_atomic_fetch_add((LDS u32*)(smem + S_CNT), 1u, __ATOMIC_RELAXED, __HIP_MEMORY_SCOPE_WORKGROUP);
  if (__builtin_amdgcn_readfirstlane(old) == 11) {
    const int lt = lane & 31, second = lane >> 5;
    const int dd = 6 * sg + (second ? 5 : 0);
    const int idx = dd < 480 ? dd / 96 : 5 + (dd - 480) / 32;
    const int dfirst = idx < 5 ? 96 * idx : 480 + 32 * (idx - 5);
    __hip_atomic_store((GLB u64*)(p.ws + WS_IN) + tile_block(lt, idx) * 64 + IN_A + (sg - dfirst / 6), FLAG_SET, RLX_AGENT);
  }
}

DEV void role_gate(const P& p, int rb, int w, int lane, u64 pc) {
  u32 pf0 = 0, pf1 = 0;
  const int tid = w * 64 + lane;
  const int sub = lane & 7;
  u32x4 wv[9];
#pragma unroll
  for (int i = 0; i < 9; ++i) wv[i] = *(const u32x4*)((const char*)p.gw + (i * 8 + sub) * 16);
  for (int r = w * 8 + (lane >> 3); r < 128; r += 96) {
    const int l = rb * 128 + r;
    float s = 0.f;
    u32x4 xv[9];
#pragma unroll
    for (int i = 0; i < 9; ++i) xv[i] = *(const u32x4*)((const char*)p.x + (size_t)l * 1152 + (i * 8 + sub) * 16);
#pragma unroll
    for (int i = 0; i < 9; ++i) {
#pragma unroll
      for (int k = 0; k < 4; ++k) {
        const u32 pr = pack_bf16(bflo(xv[i][k]) * bflo(wv[i][k]), bfhi(xv[i][k]) * bfhi(wv[i][k]));
        s += bflo(pr);
        s += bfhi(pr);
      }
    }
    s += __shfl_xor(s, 1); s += __shfl_xor(s, 2); s += __shfl_xor(s, 4);
    const float gv = rbf(1.0f / (1.0f + expf(-rbf(s))));
    if (sub == 0)
      __hip_atomic_store((GLB u64*)(p.ws + WS_G) + l, ((u64)__float_as_uint(rbf(1.0f - gv)) << 32) | __float_as_uint(gv), RLX_AGENT);
  }
  asm volatile("" :: "v"(pf0), "v"(pf1));
  wait_vm<0>();
  bar();
  if (w == 0 && lane < 16)
    __hip_atomic_store((GLB u64*)(p.ws + WS_IN) + tile_block(2 * rb + (lane >> 3), lane & 7) * 64 + IN_G, FLAG_SET, RLX_AGENT);
}

constexpr int H_C = 32768, H_CS = 12288;
DEV int h_slot(int i) { return i < 4 ? i * 8192 : 131072 + (i - 4) * 8192; }
constexpr int H_AT = H_C + 2 * H_CS;
constexpr int H_XT = H_C + 3 * H_CS;
constexpr int H_G = H_C + 4 * H_CS;
constexpr int H_OUT = H_C + 5 * H_CS;
constexpr int K_LDS = 163840;

constexpr int h_wait(int s, int NDT) {
  int n = 0;
  int need = 0;
  for (int k = 0; k < 4; ++k) { n += 2; if (k == s) need = n; }
  for (int t = 0; t < s; ++t) {
    if (t == 0) { n += 2; if (s == 4) need = n; n += 2; if (s == 5) need = n; }
    else if (t == 1) { n += 2; if (s == 6) need = n; n += 2; if (s == 7) need = n; }
    else if (t <= 11) { n += 2 + NDT; if (s == t + 6) need = n; if (t == 2) n += 1; }
    else if (t == 12 || t == 13) n += NDT;
    else if (t == 14) n += 1;
  }
  return s == 17 ? 0 : n - need;
}
template <int S, int NDT, class F> DEV void h_steps(F&& after, u64& fv, char* wsp) {
  if constexpr (S == 9) asm volatile("s_waitcnt vmcnt(%1)" : "+v"(fv) : "n"(h_wait(S, NDT)) : "memory");
  else wait_vm<h_wait(S, NDT)>();
  LSTAMP2(1, 16 + S);
  bar();
  after(std::integral_constant<int, S>{});
  if constexpr (S < 17) h_steps<S + 1, NDT>(after, fv, wsp);
}
DEV int p2_rot(int d0) { const int idx = d0 < 480 ? d0 / 96 : 5 + (d0 - 480) / 32; return (idx * 18) >> 3; }

template <int NDT, class F>
DEV void p2_loader(const P& p, LDS char* smem, int lt, int d0, int lw, int lane, bool scratch_barrier, F&& pre_poll) {
  __builtin_amdgcn_s_setprio(3);
  const char* hb = p.ws + WS_HB + (size_t)lt * 18 * 8192 + (2 * lw) * 1024 + lane * 16;
  constexpr int W = 32 * NDT;
  const int l0 = 64 * lt, rot = p2_rot(d0);
  u32 co[NDT];
#pragma unroll
  for (int i = 0; i < NDT; ++i) {
    const int row = 8 * (NDT * lw + i) + (lane >> 3), cp = lane & 7;
    co[i] = (u32)(d0 + row) * 1152u + ((cp ^ (row & 7)) << 4);
  }
  auto issue_c = [&](int s) {
    LDS char* buf = smem + H_C + (s & 7) * H_CS + NDT * lw * 1024;
    int ks = s + rot; ks = ks >= 18 ? ks - 18 : ks;
    const char* cb = ks < 9 ? (const char*)p.cre + ks * 128 : (const char*)p.cim + (ks - 9) * 128;
#pragma unroll
    for (int i = 0; i < NDT; ++i) dma16<0>(cb + co[i], buf + i * 1024);
  };
  auto issue_h = [&](int s) {
    LDS char* buf = smem + h_slot(s & 7) + 2 * lw * 1024;
    int ks = s + rot; ks = ks >= 18 ? ks - 18 : ks;
#pragma unroll
    for (int i = 0; i < 2; ++i) dma16<SC1>(hb + ks * 8192 + i * 1024, buf + i * 1024);
  };
  GLB u64* inb = (GLB u64*)(p.ws + WS_IN) + blockIdx.x * 64;
  const int nA = (d0 + W - 1) / 6 - d0 / 6 + 1;
  const bool need = lane == IN_G || (lane >= IN_A && lane < IN_A + nA);
  if (scratch_barrier) bar();
  else {
#pragma unroll 1
    for (int s = 0; s < 8; ++s) issue_c(s);
  }
  pre_poll();
  { [[maybe_unused]] const int w = lw + 8; STAMP(1, 1); }
  wait_flags(inb + IN_H + (lane < 36 ? lane : 0), true);
  { [[maybe_unused]] const int w = lw + 8; STAMP(1, 2); }
#pragma unroll
  for (int s = 0; s < 4; ++s) issue_h(s);
  { [[maybe_unused]] const int w = lw + 8; STAMP(1, 3); }
  u64 fv = 0;
  h_steps<0, NDT>([&](auto S_) {
    constexpr int S = decltype(S_)::value;
    if constexpr (S == 0) { issue_h(4); issue_h(5); }
    if constexpr (S == 1) { issue_h(6); issue_h(7); }
    if constexpr (S >= 2 && S <= 11) { issue_h(S + 6); issue_c(S + 6); }
    if constexpr (S == 2)
      asm volatile("global_load_dwordx2 %0, %1, off sc1" : "=v"(fv) : "v"(inb + lane) : "memory");
    if constexpr (S == 9) { if (!__all(!need || fv == FLAG_SET)) wait_flags(inb + lane, need); }
    if constexpr (S == 12) {
#pragma unroll
      for (int i = 0; i < NDT; ++i) {
        const int pc = NDT * lw + i;
        dma16<SC1>(p.ws + WS_AT + (size_t)(d0 + 8 * pc + (lane >> 3)) * 4096 + l0 * 2 + (lane & 7) * 16, smem + H_AT + pc * 1024);
      }
    }
    if constexpr (S == 13) {
#pragma unroll
      for (int i = 0; i < NDT; ++i) {
        const int pc = NDT * lw + i, id = pc * 64 + lane, row = id / (4 * NDT), qq = id - row * (4 * NDT);
        dma16<0>((const char*)p.x + (size_t)(l0 + row) * 1152 + d0 * 2 + qq * 16, smem + H_XT + pc * 1024);
      }
    }
    if constexpr (S == 14) { if (lane < 8) dma16<SC1>(p.ws + WS_G + (size_t)l0 * 8 + lw * 128 + lane * 16, smem + H_G + lw * 128); }
  }, fv, p.ws);
}

template <int NDT, int STAG>
DEV void p2_compute(const P& p, LDS char* smem, int lt0, int d0, int w, int lane) {
  const int lh = (w >> 1) & 1, kh = w & 1, dg = w >> 2;
  const int r = lane & 15, kq = lane >> 4;
  const int lt = 2 * lh + kh, l0 = 64 * lt0;
  constexpr int W = 32 * NDT;
  u32x2 da[NDT], db[NDT];
#pragma unroll
  for (int t = 0; t < NDT; ++t) {
    const int d = d0 + 16 * (NDT * dg + t) + 4 * kq;
    da[t] = *(const u32x2*)(p.dskA + d);
    db[t] = *(const u32x2*)(p.dskB + d);
  }
  GLB u64* inb = (GLB u64*)(p.ws + WS_IN) + blockIdx.x * 64;

  f32x4 acc[2][NDT];
#pragma unroll
  for (int i = 0; i < 2; ++i)
#pragma unroll
    for (int t = 0; t < NDT; ++t) acc[i][t] = f32x4{0, 0, 0, 0};
  int ho[2];
#pragma unroll
  for (int u = 0; u < 2; ++u) {
    const int k = 32 * kh + 8 * kq + 4 * u + (r >> 2), i = (r & 3) >> 1;
    ho[u] = (2 * (k >> 4) + i) * 1024 + (32 * lh + ((k & 15) ^ (4 * i))) * 16 + (r & 1) * 8;
  }
  const int co = H_C + (16 * NDT * dg + r) * 128 + (((4 * kh + kq) ^ (r & 7)) << 4);
  constexpr int NREG = 0, NRING = 18 - NREG;
  bf16x8 cr[NREG ? NREG : 1][NDT];
  if constexpr (NREG > 0) {
    const int rot = p2_rot(d0);
#pragma unroll
    for (int i = 0; i < NREG; ++i) {
      int ks = NRING + i + rot; ks = ks >= 18 ? ks - 18 : ks;
      const char* cb = (ks < 9 ? (const char*)p.cre + ks * 128 : (const char*)p.cim + (ks - 9) * 128) +
                       (size_t)(d0 + 16 * NDT * dg + r) * 1152 + (4 * kh + kq) * 16;
#pragma unroll
      for (int t = 0; t < NDT; ++t) cr[i][t] = *(const GLB bf16x8*)(cb + t * 16 * 1152);
    }
  }
  bf16x8 hb[2], ca[NDT];
#pragma unroll
  for (int i = 0; i < 2; ++i) hb[i] = bf16x8{};
#pragma unroll
  for (int t = 0; t < NDT; ++t) ca[t] = bf16x8{};
  auto mma_with = [&](const bf16x8 (&cc)[NDT]) {
#pragma unroll
    for (int t = 0; t < NDT; ++t) {
      acc[0][t] = mfma16(cc[t], hb[0], acc[0][t]);
      acc[1][t] = mfma16(cc[t], hb[1], acc[1][t]);
    }
    __builtin_amdgcn_sched_barrier(0);
  };
  auto read_h = [&](int s) {
    LDS const char* hbuf = smem + h_slot(s & 7);
#pragma unroll
    for (int i = 0; i < 2; ++i) hb[i] = tr_frag(hbuf + ho[0] + i * 256, hbuf + ho[1] + i * 256);
  };
  STAMP(1, 1);
#pragma unroll 1
  for (int s = 0; s < NRING; ++s) {
    bar();
    if (s == 0) STAMP(1, 2);
    LSTAMP(1, 16 + s);
    if (STAG) mma_with(ca);
    read_h(s);
    LDS const char* cbuf = smem + (s & 7) * H_CS;
#pragma unroll
    for (int t = 0; t < NDT; ++t) ca[t] = lds_frag(cbuf + co + t * 2048);
    __builtin_amdgcn_sched_barrier(0);
    if (!STAG) mma_with(ca);
  }
#pragma unroll
  for (int i = 0; i < NREG; ++i) {
    bar();
    if (STAG) { if (i == 0) mma_with(ca); else mma_with(cr[i ? i - 1 : 0]); }
    read_h(NRING + i);
    __builtin_amdgcn_sched_barrier(0);
    if (!STAG) mma_with(cr[i]);
  }
  if (STAG) { if (NREG) mma_with(cr[NREG ? NREG - 1 : 0]); else mma_with(ca); }
  __builtin_amdgcn_sched_barrier(0);
  STAMP(1, 3);
  bar();
  if (w == 0) __hip_atomic_store(inb + lane, 0ull, RLX_AGENT);
  LDS char* ex = smem + w * (NDT * 1024) + lane * 16;
#pragma unroll
  for (int t = 0; t < NDT; ++t) {
    const f32x4 a0 = acc[0][t], a1 = acc[1][t];
    *(LDS f32x4*)(ex + t * 1024) = kh ? +a0 : +a1;
  }
  bar();
  LDS const char* ey = smem + (w ^ 1) * (NDT * 1024) + lane * 16;
  constexpr int OSTR = W * 4 + 16;
  const f32x2 gg = *(LDS const f32x2*)(smem + H_G + (16 * lt + r) * 8);
#pragma unroll
  for (int t = 0; t < NDT; ++t) {
    const int dt = NDT * dg + t;
    const f32x4 o = *(LDS const f32x4*)(ey + t * 1024);
    const f32x4 a0 = acc[0][t], a1 = acc[1][t];
    const f32x4 y = (kh ? +a1 : +a0) + o;
    const s16x4 av = tr_read(smem + H_AT + (16 * dt + 4 * kq + (r >> 2)) * 128 + lt * 32 + (r & 3) * 8);
    const u32x2 xv = *(LDS const u32x2*)(smem + H_XT + (16 * lt + r) * (W * 2) + (16 * dt + 4 * kq) * 2);
    f32x4 res;
#pragma unroll
    for (int i = 0; i < 4; ++i) {
      const u32 xw = xv[i >> 1], aw = da[t][i >> 1], bw = db[t][i >> 1];
      const float xf = (i & 1) ? bfhi(xw) : bflo(xw);
      const float sa = (i & 1) ? bfhi(aw) : bflo(aw);
      const float sb = (i & 1) ? bfhi(bw) : bflo(bw);
      const float yc = bf2f((u16)av[i]);
      const float a = rbf(yc + rbf(xf * sa));
      const float b = y[i] + rbf(xf * sb);
      res[i] = rbf(gg[0] * a) + gg[1] * b;
    }
    *(LDS f32x4*)(smem + H_OUT + (16 * lt + r) * OSTR + (16 * dt + 4 * kq) * 4) = res;
  }
  STAMP(1, 4);
  bar();
#pragma unroll
  for (int it = 0; it < NDT; ++it) {
    const int id = it * 512 + w * 64 + lane;
    const int row = id / (8 * NDT), pc = id - row * (8 * NDT);
    const f32x4 v = *(LDS const f32x4*)(smem + H_OUT + row * OSTR + pc * 16);
    __builtin_amdgcn_raw_buffer_store_b128(__builtin_bit_cast(u32x4, v), mk_rsrc(p.out, SL * DM * 4), ((l0 + row) * DM + d0 + pc * 4) * 4, 0, OUT_AUX);
  }
}

template <int NDT>
DEV void phase2(const P& p, LDS char* smem, int lt, int d0, int w, int lane, bool scratch_barrier, int sg) {
  STAMP(1, 0);
  if (w >= 8) {
    p2_loader<NDT>(p, smem, lt, d0, w - 8, lane, scratch_barrier, [&]() { if (NDT == 1) { wait_vm<8>(); s_signal(p, smem, sg, lane); } });
  } else {
    if (NDT == 1) { wait_vm<0>(); s_signal(p, smem, sg, lane); }
    if (w < 4) p2_compute<NDT, 0>(p, smem, lt, d0, w, lane);
    else p2_compute<NDT, 1>(p, smem, lt, d0, w, lane);
  }
  STAMP(1, 15);
}

__global__ __launch_bounds__(768, 3) void gated_mix_s4d_s5(P p) {
  extern __shared__ __attribute__((aligned(16))) char smem_raw[];
  LDS char* smem = (LDS char*)smem_raw;
  u64 pc;
  asm volatile("s_getpc_b64 %0" : "=s"(pc));
  u32 pf = 0;
#define PF_TOP 312
  if (threadIdx.x < PF_TOP)
    asm volatile("global_load_dword %0, %1, off" : "=v"(pf) : "v"((pc & ~255ull) + threadIdx.x * 128));
  const int w = __builtin_amdgcn_readfirstlane(threadIdx.x >> 6), lane = threadIdx.x & 63;
  const int xcd = blockIdx.x & 7, j = blockIdx.x >> 3;
  STAMP(0, 0);
  if (j >= 18 && j < 30) {
    const int sg = 12 * xcd + (j - 18);
    role_s(p, smem, sg, w, lane);
    STAMP(0, 15);
    phase2<1>(p, smem, 4 * xcd + (j - 18) / 3, 480 + 32 * ((j - 18) % 3), w, lane, false, sg);
  } else {
    const bool isg = j < 18;
    if (isg) {
      const int c = 2 * xcd + j / 9, g = j % 9;
      if (w >= 8) g_loader(p, smem, c, g, 96 * (j % 5), w - 8, lane);
      else if (w < 4) g_compute<0>(p, smem, c, g, w, lane);
      else g_compute<1>(p, smem, c, g, w, lane);
    } else {
      role_gate(p, 2 * xcd + (j - 30), w, lane, pc);
    }
    STAMP(0, 15);
    const int e = isg ? j : j - 12;
    phase2<3>(p, smem, 4 * xcd + e / 5, 96 * (e % 5), w, lane, isg, 0);
  }
  asm volatile("s_waitcnt vmcnt(0)" :: "v"(pf));
}

extern "C" void kernel_launch(void* const* d_in, const int* in_sizes, int n_in, void* d_out, int out_size,
                              void* d_ws, size_t ws_size, hipStream_t stream) {
  P a;
  a.x = (const u16*)d_in[0]; a.la = (const u16*)d_in[1]; a.aim = (const u16*)d_in[2];
  a.creA = (const u16*)d_in[3]; a.cimA = (const u16*)d_in[4]; a.ldtA = (const u16*)d_in[5]; a.dskA = (const u16*)d_in[6];
  a.lre = (const u16*)d_in[8]; a.lim = (const u16*)d_in[9]; a.bre = (const u16*)d_in[10]; a.bim = (const u16*)d_in[11];
  a.cre = (const u16*)d_in[12]; a.cim = (const u16*)d_in[13]; a.ldtB = (const u16*)d_in[14];
  a.dskB = (const u16*)d_in[15]; a.gw = (const u16*)d_in[16];
  a.ws = (char*)d_ws + WS_OFF; a.out = (float*)d_out;
  (void)hipFuncSetAttribute((const void*)gated_mix_s4d_s5, hipFuncAttributeMaxDynamicSharedMemorySize, K_LDS);
  gated_mix_s4d_s5<<<256, 768, K_LDS, stream>>>(a);
}
```

```cpp
#define HOLD_AT 5
#include <hip/hip_runtime.h>
#include <hip/hip_bf16.h>
#include <stdint.h>
#include <type_traits>

typedef unsigned short u16;
typedef unsigned int u32;
typedef unsigned long long u64;
typedef __attribute__((ext_vector_type(8))) __bf16 bf16x8;
typedef __attribute__((ext_vector_type(2))) __bf16 bf16x2;
typedef __attribute__((ext_vector_type(4))) short s16x4;
typedef __attribute__((ext_vector_type(4))) float f32x4;
typedef __attribute__((ext_vector_type(2))) float f32x2;
typedef __attribute__((ext_vector_type(4))) unsigned u32x4;
typedef __attribute__((ext_vector_type(2))) unsigned u32x2;
#define LDS __attribute__((address_space(3)))
#define GLB __attribute__((address_space(1)))
#define DEV __device__ __forceinline__
#define RLX_AGENT __ATOMIC_RELAXED, __HIP_MEMORY_SCOPE_AGENT

constexpr int SL = 2048;
constexpr int DM = 576;

constexpr size_t WS_HB = 0;
constexpr size_t WS_AT = WS_HB + (size_t)2 * DM * SL * 2;
constexpr size_t WS_G = WS_AT + (size_t)DM * SL * 2;
constexpr size_t WS_GRAN = WS_G + (size_t)SL * 8;
constexpr size_t WS_EP = WS_GRAN + (size_t)9 * 16 * 2 * 64 * 8;
constexpr size_t WS_FL = WS_EP + 4096;
constexpr size_t WS_XT = WS_FL + (size_t)16 * DM * 8;
constexpr size_t WS_IN = WS_XT + (size_t)DM * SL * 2;
constexpr u64 FLAG_SET = 0x9E3779B97F4A7C15ull;
constexpr u32 SPIN_CAP = 1u << 17;
constexpr int IN_H = 0, IN_G = 36, IN_A = 40;

constexpr size_t WS_ST = (size_t)16 << 20;
#define STAMP(kern, idx) do {} while (0)
#define CSTAMP(kern, idx) do {} while (0)
#define LSTAMP(kern, idx) do {} while (0)
#define LSTAMP2(kern, idx) do {} while (0)

#define HOLD(id) do { if (HOLD_AT == (id)) { while (__builtin_amdgcn_s_memrealtime() - g_tb < 4000) __builtin_amdgcn_s_sleep(2); } } while (0)
DEV float bf2f(u16 v) { return __uint_as_float((u32)v << 16); }
DEV float bflo(u32 v) { return __uint_as_float(v << 16); }
DEV float bfhi(u32 v) { return __uint_as_float(v & 0xffff0000u); }
DEV u32 pack_bf16(float lo, float hi) {
  f32x2 v = {lo, hi};
  bf16x2 b = __builtin_convertvector(v, bf16x2);
  return __builtin_bit_cast(u32, b);
}
DEV float rbf(float x) { return bflo(pack_bf16(x, x)); }
template <int N> DEV void wait_vm() { asm volatile("s_waitcnt vmcnt(%0)" ::"n"(N) : "memory"); }
DEV void bar() { asm volatile("s_waitcnt lgkmcnt(0)\n\ts_barrier" ::: "memory"); }
DEV void wave_fence() { __builtin_amdgcn_fence(__ATOMIC_ACQ_REL, "wavefront"); }
template <int AUX> DEV void dma16(const void* src, LDS char* dst) {
  __builtin_amdgcn_global_load_lds((const GLB u32*)src, (LDS u32*)dst, 16, 0, AUX);
}
constexpr int SC1 = 16;
#define POLL_SLEEP 1
#define OUT_AUX 16
#define WS_OFF 0
#define XT_WAIT 4
DEV bf16x8 lds_frag(LDS const char* p) { return *(LDS const bf16x8*)p; }
DEV s16x4 tr_read(LDS const char* a) { return __builtin_amdgcn_ds_read_tr16_b64_v4i16((LDS s16x4*)a); }
DEV bf16x8 tr_frag(LDS const char* a, LDS const char* b) {
  const s16x4 lo = tr_read(a), hi = tr_read(b);
  return __builtin_bit_cast(bf16x8, __builtin_shufflevector(lo, hi, 0, 1, 2, 3, 4, 5, 6, 7));
}
DEV f32x4 mfma16(bf16x8 a, bf16x8 b, f32x4 c) { return __builtin_amdgcn_mfma_f32_16x16x32_bf16(a, b, c, 0, 0, 0); }
DEV __amdgpu_buffer_rsrc_t mk_rsrc(const void* base, int bytes) {
  return __builtin_amdgcn_make_buffer_rsrc(const_cast<void*>(base), (short)0, bytes, 0x00020000);
}
struct cf { float r, i; };
DEV cf cmul(cf a, cf b) { return {a.r * b.r - a.i * b.i, a.r * b.i + a.i * b.r}; }
DEV cf cfma(cf a, cf b, cf c) { return {fmaf(a.r, b.r, fmaf(-a.i, b.i, c.r)), fmaf(a.r, b.i, fmaf(a.i, b.r, c.i))}; }
typedef f32x2 c2;
DEV c2 cm2(c2 a, c2 b) {
  c2 t, r;
  asm("v_pk_mul_f32 %0, %1, %2 op_sel:[0,0] op_sel_hi:[0,1]" : "=v"(t) : "v"(a), "v"(b));
  asm("v_pk_fma_f32 %0, %1, %2, %3 op_sel:[1,1,0] op_sel_hi:[1,0,1] neg_lo:[1,0,0]" : "=v"(r) : "v"(a), "v"(b), "v"(t));
  return r;
}
DEV f32x2 fma2(f32x2 a, f32x2 b, f32x2 c) { return __builtin_elementwise_fma(a, b, c); }
DEV f32x2 lo2(f32x4 v) { return __builtin_shufflevector(v, v, 0, 1); }
DEV f32x2 hi2(f32x4 v) { return __builtin_shufflevector(v, v, 2, 3); }
DEV f32x4 cat2(f32x2 a, f32x2 b) { return __builtin_shufflevector(a, b, 0, 1, 2, 3); }
DEV void wait_flags(GLB u64* word, bool need) {
  for (u32 spin = 0;; ++spin) {
    const u64 v = __hip_atomic_load(word, RLX_AGENT);
    if (__all(!need || v == FLAG_SET) || spin > SPIN_CAP) break;
    __builtin_amdgcn_s_sleep(POLL_SLEEP);
  }
}
DEV int tile_block(int lt, int idx) {
  const int xcd = lt >> 2, a = lt & 3;
  int j;
  if (idx < 5) { const int e = 5 * a + idx; j = e < 18 ? e : e + 12; }
  else j = 18 + 3 * a + (idx - 5);
  return (j << 3) | xcd;
}

struct P {
  const u16 *x, *la, *aim, *creA, *cimA, *ldtA, *dskA;
  const u16 *lre, *lim, *bre, *bim, *cre, *cim, *ldtB, *dskB, *gw;
  char* ws;
  float* out;
};

constexpr int G_NB = 4, G_STAGE = 32768;
constexpr int G_E = 153600;
constexpr int G_PF = G_E + 4096;
constexpr int G_L = G_PF + 4096;
constexpr int G_X = G_L + 1024;
constexpr int G_T = G_NB * G_STAGE;
static_assert(G_X + 1024 <= 163840 && G_T + 4 * 32 * 144 <= G_E, "role G scratch");

DEV void g_loader(const P& p, LDS char* smem, int c, int g, int d0, int lw, int lane) {
  [[maybe_unused]] const int w = lw + 8;
  __builtin_amdgcn_s_setprio(3);
  GLB u32* ep = (GLB u32*)(p.ws + WS_EP + g * 256);
  GLB u64* gran = (GLB u64*)(p.ws + WS_GRAN) + (size_t)g * 16 * 128;
  const u32 tag = __hip_atomic_load(ep, RLX_AGENT) + 1u;
  u32 xo[4], bo[4];
#pragma unroll
  for (int i = 0; i < 4; ++i) {
    const int rho = 8 * (4 * lw + i) + (lane >> 3);
    const int hf = rho >> 6, m = (rho >> 4) & 3, r = rho & 15;
    const int tau = 64 * hf + 16 * (r >> 2) + 8 * (r & 1) + 2 * m + ((r >> 1) & 1);
    const int ch = (lane & 7) ^ (rho & 7);
    xo[i] = (u32)(128 * c + tau) * 1152u + ch * 16;
    const int pp = 64 * g + 16 * (rho >> 5) + r;
    bo[i] = (u32)pp * 1152u + ch * 16;
  }
  auto issue = [&](int s) {
    LDS char* buf = smem + (s & (G_NB - 1)) * G_STAGE + lw * 4096;
    int kb = g + s; kb = (kb >= 9 ? kb - 9 : kb) * 128;
#pragma unroll
    for (int i = 0; i < 4; ++i) dma16<0>((const char*)p.x + (xo[i] + kb), buf + i * 1024);
#pragma unroll
    for (int i = 0; i < 4; ++i) {
      const char* base = (const char*)((((4 * lw + i) >> 1) & 1) ? p.bim : p.bre);
      dma16<0>(base + (bo[i] + kb), buf + 16384 + i * 1024);
    }
  };
#pragma unroll
  for (int s = 0; s < G_NB - 1; ++s) issue(s);
#pragma unroll 1
  for (int s = 0; s < 6; ++s) { wait_vm<16>(); LSTAMP(0, 16 + s); bar(); issue(s + G_NB - 1); LSTAMP(0, 32 + s); }
  wait_vm<16>(); bar();
  wait_vm<8>(); bar();
  wait_vm<0>(); bar();
  STAMP(0, 1);
  if (lw < 2) {
    bar();
    u32 co[6];
#pragma unroll
    for (int i = 0; i < 6; ++i) {
      const int row = 8 * (6 * lw + i) + (lane >> 3), cp = lane & 7;
      co[i] = (u32)(d0 + row) * 1152u + ((cp ^ (row & 7)) << 4);
    }
    const int rot = ((d0 / 96) * 18) >> 3;
#pragma unroll 1
    for (int s = 0; s < 8; ++s) {
      LDS char* buf = smem + 32768 + s * 12288 + 6 * lw * 1024;
      const int ks = s + rot;
      const char* cb = ks < 9 ? (const char*)p.cre + ks * 128 : (const char*)p.cim + (ks - 9) * 128;
#pragma unroll
      for (int i = 0; i < 6; ++i) dma16<0>(cb + co[i], buf + i * 1024);
    }
    bar();
    return;
  }
  bar();
  STAMP(0, 2);
  if (lw == 3) {
    const f32x2 lv = *(LDS const f32x2*)(smem + G_L + lane * 8);
    const cf l16 = {lv[0], lv[1]};
    f32x2 ev[8];
#pragma unroll
    for (int s = 0; s < 8; ++s) ev[s] = *(LDS const f32x2*)(smem + G_E + (s * 64 + lane) * 8);
    cf tot = {0.f, 0.f};
    cf pre[8];
#pragma unroll
    for (int s = 0; s < 8; ++s) { pre[s] = tot; tot = cfma(l16, tot, cf{ev[s][0], ev[s][1]}); }
    if (c < 15) {
      __hip_atomic_store(gran + c * 128 + lane, ((u64)tag << 32) | __float_as_uint(tot.r), RLX_AGENT);
      __hip_atomic_store(gran + c * 128 + 64 + lane, ((u64)tag << 32) | __float_as_uint(tot.i), RLX_AGENT);
    }
#pragma unroll
    for (int s = 0; s < 8; ++s) *(LDS f32x2*)(smem + G_PF + (s * 64 + lane) * 8) = f32x2{pre[s].r, pre[s].i};
  }
  const int par = lw - 2;
  const f32x2 lv = *(LDS const f32x2*)(smem + G_L + 512 + lane * 8);
  const cf l128 = {lv[0], lv[1]};
  const cf l256 = cmul(l128, l128);
  cf acc = {0.f, 0.f};
  if (par < c) {
    const int nc = (c - par + 1) >> 1;
    u64 a[8], b[8];
    for (u32 spin = 0;; ++spin) {
      bool ok = true;
#pragma unroll
      for (int i = 0; i < 8; ++i) {
        const int cc = par + 2 * (i < nc ? i : 0);
        a[i] = __hip_atomic_load(gran + cc * 128 + lane, RLX_AGENT);
        b[i] = __hip_atomic_load(gran + cc * 128 + 64 + lane, RLX_AGENT);
      }
#pragma unroll
      for (int i = 0; i < 8; ++i) ok = ok && (u32)(a[i] >> 32) == tag && (u32)(b[i] >> 32) == tag;
      if (__all(ok) || spin > SPIN_CAP) break;
      __builtin_amdgcn_s_sleep(1);
    }
#pragma unroll
    for (int i = 0; i < 8; ++i)
      if (i < nc) acc = cfma(l256, acc, cf{__uint_as_float((u32)a[i]), __uint_as_float((u32)b[i])});
    if (c - 1 - (par + 2 * (nc - 1))) acc = cmul(acc, l128);
  }
  *(LDS f32x2*)(smem + G_X + (par * 64 + lane) * 8) = f32x2{acc.r, acc.i};
  STAMP(0, 3);
  bar();
  if (c == 15 && lw == 2 && lane == 0) __hip_atomic_store(ep, tag, RLX_AGENT);
}

template <int HF>
DEV void g_compute(const P& p, LDS char* smem, int c, int g, int w, int lane, u32& pf, u64 g_tb) {
  const int pq = w & 3, col = lane & 15, q = lane >> 4;
  const int pl = 16 * pq + col, pp = 64 * g + pl;
  cf lam, coef2;
  {
    const float lre = bf2f(p.lre[pp]), lim = bf2f(p.lim[pp]);
    const float dt = rbf(expf(bf2f(p.ldtB[pp])));
    const float zr = dt * lre, zi = dt * lim;
    const float er = expf(zr);
    float sn, cs;
    sincosf(zi, &sn, &cs);
    lam = {er * cs, er * sn};
    const float nr = lam.r - 1.0f, ni = lam.i;
    const float den = 1.0f / (lre * lre + lim * lim);
    coef2 = {2.0f * (nr * lre + ni * lim) * den, 2.0f * (ni * lre - nr * lim) * den};
  }
  cf l8 = cmul(lam, lam); l8 = cmul(l8, l8); l8 = cmul(l8, l8);
  const cf l16 = cmul(l8, l8);
  const cf l32 = cmul(l16, l16), l64 = cmul(l32, l32);
  if (HF == 0 && q == 0) {
    const cf l128 = cmul(l64, l64);
    *(LDS f32x2*)(smem + G_L + pl * 8) = f32x2{l16.r, l16.i};
    *(LDS f32x2*)(smem + G_L + 512 + pl * 8) = f32x2{l128.r, l128.i};
  }
  cf pw = HF ? l64 : cf{1.f, 0.f};
  { const cf t = cmul(pw, l16); if (q & 1) pw = t; }
  { const cf t = cmul(pw, l32); if (q & 2) pw = t; }
  u32 wb[3] = {0, 0, 0};
  if (HF == 0) {
#pragma unroll
    for (int i = 0; i < 3; ++i) {
      const int ln = i * 256 + pq * 64 + lane;
      if (ln < 576) {
        const int row = ln / 9, kb = ln - row * 9;
        wb[i] = *(const GLB u32*)((const char*)((c & 1) ? p.bim : p.bre) + (size_t)(64 * g + row) * 1152 + kb * 128);
      }
    }
  }
  asm volatile("" : "+v"(lam.r), "+v"(lam.i), "+v"(coef2.r), "+v"(coef2.i), "+v"(l8.r), "+v"(l8.i), "+v"(pw.r), "+v"(pw.i));
  f32x4 are[4], aim[4];
#pragma unroll
  for (int m = 0; m < 4; ++m) { are[m] = f32x4{0, 0, 0, 0}; aim[m] = f32x4{0, 0, 0, 0}; }
  const int o0 = col * 128 + (((0 + q) ^ (col & 7)) << 4);
  const int o1 = col * 128 + (((4 + q) ^ (col & 7)) << 4);
  bf16x8 br[2], bi[2], a[2][4];
  auto read_frags = [&](int s) {
    LDS const char* buf = smem + (s & (G_NB - 1)) * G_STAGE;
    LDS const char* ab = buf + HF * 8192;
    LDS const char* bb = buf + 16384 + pq * 4096;
#pragma unroll
    for (int ks = 0; ks < 2; ++ks) {
      const int o = ks ? o1 : o0;
      br[ks] = lds_frag(bb + o); bi[ks] = lds_frag(bb + 2048 + o);
#pragma unroll
      for (int m = 0; m < 4; ++m) a[ks][m] = lds_frag(ab + m * 2048 + o);
    }
  };
  auto mma = [&]() {
#pragma unroll
    for (int ks = 0; ks < 2; ++ks)
#pragma unroll
      for (int m = 0; m < 4; ++m) {
        are[m] = mfma16(a[ks][m], br[ks], are[m]);
        aim[m] = mfma16(a[ks][m], bi[ks], aim[m]);
      }
  };
  STAMP(0, 1);
  HOLD(1);
#pragma unroll
  for (int s = 0; s < 9; ++s) {
    if (HF == 1 && s == XT_WAIT) {
      wait_vm<0>();
      if (lane < 16) __hip_atomic_store((GLB u64*)(p.ws + WS_FL) + c * DM + 64 * g + 16 * pq + lane, FLAG_SET, RLX_AGENT);
    }
    bar();
    if (s == 0) { STAMP(0, 2); CSTAMP(0, 60); }
    if (HF == 1 && s > 0) { mma(); __builtin_amdgcn_sched_barrier(0); }
    if (HF == 1 && s == 0) {
      const int rr = 4 * q + (col >> 2);
      LDS const char* tb = smem + rr * 128 + (((2 * pq + ((col & 3) >> 1)) ^ (rr & 7)) << 4) + (col & 1) * 8;
      LDS char* sc = smem + G_T + pq * (32 * 144);
#pragma unroll
      for (int hf = 0; hf < 2; ++hf) {
        u32x2 t[4];
#pragma unroll
        for (int m = 0; m < 4; ++m) t[m] = __builtin_bit_cast(u32x2, tr_read(tb + hf * 8192 + m * 2048));
        u32x4 oa, ob;
#pragma unroll
        for (int m = 0; m < 4; ++m) {
          oa[m] = __builtin_amdgcn_perm(t[m][1], t[m][0], 0x05040100u);
          ob[m] = __builtin_amdgcn_perm(t[m][1], t[m][0], 0x07060302u);
        }
        *(LDS u32x4*)(sc + hf * (16 * 144) + col * 144 + q * 32) = oa;
        *(LDS u32x4*)(sc + hf * (16 * 144) + col * 144 + q * 32 + 16) = ob;
      }
      wave_fence();
      const __amdgpu_buffer_rsrc_t rs = mk_rsrc(p.ws + WS_XT, DM * SL * 2);
#pragma unroll
      for (int u = 0; u < 4; ++u) {
        const int row = (lane >> 3) + 8 * (u & 1), hf = u >> 1;
        const u32x4 v = *(LDS const u32x4*)(sc + hf * (16 * 144) + row * 144 + (lane & 7) * 16);
        __builtin_amdgcn_raw_buffer_store_b128(v, rs, ((64 * g + 16 * pq + row) * SL + 128 * c + 64 * hf) * 2 + (lane & 7) * 16, 0, SC1);
      }
      __builtin_amdgcn_sched_barrier(0);
    }
    read_frags(s);
    __builtin_amdgcn_sched_barrier(0);
    if (HF == 0) { mma(); __builtin_amdgcn_sched_barrier(0); }
  }
  if (HF == 1) mma();
  __builtin_amdgcn_sched_barrier(0);
  STAMP(0, 3); CSTAMP(0, 61);
  HOLD(2);
  const f32x2 Lr = {lam.r, lam.r}, Li = {lam.i, lam.i};
  f32x2 er = {0.f, 0.f}, ei = {0.f, 0.f};
#pragma unroll
  for (int t = 0; t < 8; ++t) {
    const f32x2 br_ = (t & 1) ? hi2(are[t >> 1]) : lo2(are[t >> 1]);
    const f32x2 bi_ = (t & 1) ? hi2(aim[t >> 1]) : lo2(aim[t >> 1]);
    const f32x2 nr = fma2(-Li, ei, fma2(Lr, er, br_));
    ei = fma2(Li, er, fma2(Lr, ei, bi_));
    er = nr;
  }
  const cf eA = {er[0], ei[0]};
  const cf e = cfma(l8, eA, cf{er[1], ei[1]});
  const int seg = 4 * HF + q;
  *(LDS f32x2*)(smem + G_E + (seg * 64 + pl) * 8) = f32x2{e.r, e.i};
  asm volatile("" :: "v"(wb[0]), "v"(wb[1]), "v"(wb[2]));
  STAMP(0, 4);
  bar();
  STAMP(0, 5);
  {
#pragma unroll
    for (int m = 0; m < 4; ++m) asm volatile("" : "+v"(are[m]), "+v"(aim[m]));
    const f32x2 Cr = {coef2.r, coef2.r}, Ci = {coef2.i, coef2.i};
#pragma unroll
    for (int m = 0; m < 4; ++m) {
      const f32x2 r0 = lo2(are[m]), r1 = hi2(are[m]), i0 = lo2(aim[m]), i1 = hi2(aim[m]);
      are[m] = cat2(fma2(-Ci, i0, Cr * r0), fma2(-Ci, i1, Cr * r1));
      aim[m] = cat2(fma2(-Ci, r0, -Cr * i0), fma2(-Ci, r1, -Cr * i1));
      asm volatile("" : "+v"(are[m]), "+v"(aim[m]));
    }
  }
  cf eAc = cmul(coef2, eA), pwc = cmul(coef2, pw);
  asm volatile("" : "+v"(eAc.r), "+v"(eAc.i), "+v"(pwc.r), "+v"(pwc.i));
  STAMP(0, 7);
  bar();
  STAMP(0, 8);
  HOLD(3);
  cf cin;
  {
    f32x2 v[2];
#pragma unroll
    for (int i = 0; i < 2; ++i) v[i] = *(LDS const f32x2*)(smem + G_X + (i * 64 + pl) * 8);
    const f32x2 pf = *(LDS const f32x2*)(smem + G_PF + (seg * 64 + pl) * 8);
    const f32x2 S = v[0] + v[1];
    cin = cfma(pwc, cf{S[0], S[1]}, cmul(coef2, cf{pf[0], pf[1]}));
  }
  const cf cinB = cfma(l8, cin, eAc);
  er = f32x2{cin.r, cinB.r}; ei = f32x2{-cin.i, -cinB.i};
  f32x2 hr[8], hi[8];
#pragma unroll
  for (int t = 0; t < 8; ++t) {
    const f32x2 br_ = (t & 1) ? hi2(are[t >> 1]) : lo2(are[t >> 1]);
    const f32x2 bi_ = (t & 1) ? hi2(aim[t >> 1]) : lo2(aim[t >> 1]);
    const f32x2 nr = fma2(Li, ei, fma2(Lr, er, br_));
    ei = fma2(-Li, er, fma2(Lr, ei, bi_));
    er = nr;
    hr[t] = er; hi[t] = ei;
  }
  u32x4 o[4];
#pragma unroll
  for (int t = 0; t < 4; ++t) {
    o[0][t] = pack_bf16(hr[2 * t][0], hr[2 * t + 1][0]);
    o[1][t] = pack_bf16(hr[2 * t][1], hr[2 * t + 1][1]);
    o[2][t] = pack_bf16(hi[2 * t][0], hi[2 * t + 1][0]);
    o[3][t] = pack_bf16(hi[2 * t][1], hi[2 * t + 1][1]);
  }
  STAMP(0, 9);
  {
    const __amdgpu_buffer_rsrc_t rs = mk_rsrc(p.ws + WS_HB, 2 * DM * SL * 2);
    const int hb = (((c * 2 + HF) * 2) * 9 + g) * 8192 + pq * 2048;
    const int p0 = (16 * q + col) * 16, p1 = 1024 + (16 * q + (col ^ 4)) * 16;
    __builtin_amdgcn_raw_buffer_store_b128(o[0], rs, hb + p0, 0, SC1);
    __builtin_amdgcn_raw_buffer_store_b128(o[1], rs, hb + p1, 0, SC1);
    __builtin_amdgcn_raw_buffer_store_b128(o[2], rs, hb + 9 * 8192 + p0, 0, SC1);
    __builtin_amdgcn_raw_buffer_store_b128(o[3], rs, hb + 9 * 8192 + p1, 0, SC1);
    wait_vm<0>();
    STAMP(0, 10);
    if (lane < 8)
      __hip_atomic_store((GLB u64*)(p.ws + WS_IN) + tile_block(2 * c + HF, lane) * 64 + IN_H + g * 4 + pq, FLAG_SET, RLX_AGENT);
  }
}

constexpr int S_CPY = 4160;
constexpr int S_X = 0;
constexpr int S_ST = 6 * 4 * S_CPY;
constexpr int S_Z = S_ST;
constexpr int S_ZSZ = (256 + SL) * 2;
constexpr int S_T = S_ST + 12 * 4608;
constexpr int S_TSZ = 1312;
constexpr int S_END = S_T + 6 * S_TSZ;
constexpr int S_CNT = 32768 + 8192;

DEV c2 cexp_k(float zl, float th, float tl, float k) {
  const float mag = __builtin_amdgcn_exp2f(zl * k);
  const float t = fmaf(tl, k, __builtin_amdgcn_fractf(th * k));
  return c2{mag * __builtin_amdgcn_cosf(t), mag * __builtin_amdgcn_sinf(t)};
}
#define PKM(d, a, b) "v_pk_mul_f32 " d ", " a ", " b " op_sel:[0,0] op_sel_hi:[0,1]\n\t"
#define PKF(d, a, b) "v_pk_fma_f32 " d ", " a ", " b ", " d " op_sel:[1,1,0] op_sel_hi:[1,0,1] neg_lo:[1,0,0]\n\t"
DEV void octet(LDS char* dst, c2 s0, c2 m1, c2 m2) {
  c2 v1, v2, v3, t0, t1, t2, t3;
  asm(PKM("%0", "%7", "%8") PKM("%3", "%7", "%9") PKF("%0", "%7", "%8") PKF("%3", "%7", "%9")
      PKM("%1", "%0", "%8") PKM("%4", "%0", "%9") PKF("%1", "%0", "%8") PKF("%4", "%0", "%9")
      PKM("%2", "%1", "%8") PKM("%5", "%1", "%9") PKF("%2", "%1", "%8") PKF("%5", "%1", "%9")
      PKM("%6", "%2", "%9") PKF("%6", "%2", "%9")
      : "=&v"(v1), "=&v"(v2), "=&v"(v3), "=&v"(t0), "=&v"(t1), "=&v"(t2), "=&v"(t3) : "v"(s0), "v"(m1), "v"(m2));
  *(LDS u32*)(dst + 0 * 288) = pack_bf16(s0[0], s0[1]);
  *(LDS u32*)(dst + 1 * 288) = pack_bf16(v1[0], v1[1]);
  *(LDS u32*)(dst + 2 * 288) = pack_bf16(v2[0], v2[1]);
  *(LDS u32*)(dst + 3 * 288) = pack_bf16(v3[0], v3[1]);
  *(LDS u32*)(dst + 4 * 288) = pack_bf16(t0[0], t0[1]);
  *(LDS u32*)(dst + 5 * 288) = pack_bf16(t1[0], t1[1]);
  *(LDS u32*)(dst + 6 * 288) = pack_bf16(t2[0], t2[1]);
  *(LDS u32*)(dst + 7 * 288) = pack_bf16(t3[0], t3[1]);
}
DEV u32 dup_lo(u32 a) { return __builtin_amdgcn_perm(a, a, 0x01000100u); }
DEV u32 dup_hi(u32 a) { return __builtin_amdgcn_perm(a, a, 0x03020302u); }
DEV u32 hi_lo(u32 a, u32 b) { return __builtin_amdgcn_alignbit(b, a, 16); }

DEV void role_s(const P& p, LDS char* smem, int sg, int w, int lane) {
  const int tid = w * 64 + lane;
  const int ch = w >> 1, hh = w & 1, d = 6 * sg + ch;
  const int n = d * 128 + 64 * hh + lane;
  const u16 r_ldt = p.ldtA[d], r_la = p.la[n], r_aim = p.aim[n], r_cre = p.creA[n], r_cim = p.cimA[n];
  STAMP(0, 1);
  f32x4 tab[3];
  [[maybe_unused]] u32 wc[2] = {0, 0};
  LDS char* st = smem + S_ST + w * 4608;
  {
    const float L2E = 1.44269504088896341f;
    const float dt = rbf(__builtin_amdgcn_exp2f(bf2f(r_ldt) * L2E));
    const float ar = -rbf(__builtin_amdgcn_exp2f(bf2f(r_la) * L2E));
    const float zl = ar * dt * L2E, zi = bf2f(r_aim) * dt;
    const float th = zi * 0.15915494309189535f;
    const float tl = fmaf(zi, 0.15915494309189535f, -th) + zi * 4.6017384e-10f;
    const c2 C = {2.0f * (bf2f(r_cre) * dt), 2.0f * (bf2f(r_cim) * dt)};
    const c2 w1 = cexp_k(zl, th, tl, 1.f), w8 = cexp_k(zl, th, tl, 8.f), w128 = cexp_k(zl, th, tl, 128.f);
    c2 w2, w4, w16, w32, w64, w256, w512, w1024, w2048;
    asm(PKM("%0", "%9", "%9") PKM("%2", "%10", "%10") PKM("%5", "%11", "%11") PKF("%0", "%9", "%9") PKF("%2", "%10", "%10") PKF("%5", "%11", "%11")
        PKM("%1", "%0", "%0") PKM("%3", "%2", "%2") PKM("%6", "%5", "%5") PKF("%1", "%0", "%0") PKF("%3", "%2", "%2") PKF("%6", "%5", "%5")
        PKM("%4", "%3", "%3") PKM("%7", "%6", "%6") PKF("%4", "%3", "%3") PKF("%7", "%6", "%6")
        PKM("%8", "%7", "%7") PKF("%8", "%7", "%7")
        : "=&v"(w2), "=&v"(w4), "=&v"(w16), "=&v"(w32), "=&v"(w64), "=&v"(w256), "=&v"(w512), "=&v"(w1024), "=&v"(w2048)
        : "v"(w1), "v"(w8), "v"(w128));
    c2 C128, C256, C512, C1024, C2048;
    asm(PKM("%0", "%5", "%6") PKM("%1", "%5", "%7") PKM("%2", "%5", "%8") PKM("%3", "%5", "%9") PKM("%4", "%5", "%10")
        PKF("%0", "%5", "%6") PKF("%1", "%5", "%7") PKF("%2", "%5", "%8") PKF("%3", "%5", "%9") PKF("%4", "%5", "%10")
        : "=&v"(C128), "=&v"(C256), "=&v"(C512), "=&v"(C1024), "=&v"(C2048)
        : "v"(C), "v"(w128), "v"(w256), "v"(w512), "v"(w1024), "v"(w2048));
#pragma unroll
    for (int i = 0; i < 2; ++i) {
      const int ln = i * 768 + tid, all = (sg % 12) * 864 + ln;
      if (ln < 864) wc[i] = *(const GLB u32*)((const char*)(all < 5184 ? p.cre : p.cim) + (size_t)(all < 5184 ? all : all - 5184) * 128);
    }
    STAMP(0, 2);
    LDS char* wr = st + 4 * lane;
    LDS const char* rd = st + (lane & 15) * 288 + (lane >> 4) * 16;
    bf16x8 B[4];
    const c2 w8c = {w8[0], -w8[1]}, w32c = {w32[0], -w32[1]}, w64c = {w64[0], -w64[1]};
    octet(wr, c2{1.f, 0.f}, w8c, w32c);
    octet(wr + 8 * 288, w64c, w8c, w32c);
    wave_fence();
#pragma unroll
    for (int ks = 0; ks < 4; ++ks) B[ks] = lds_frag(rd + 64 * ks);
    wave_fence();
#pragma unroll
    for (int mt = 0; mt < 3; ++mt) {
      if (mt == 0) { octet(wr, C, w1, w4); octet(wr + 8 * 288, C128, w1, w4); }
      if (mt == 1) { octet(wr, C256, w2, w128); octet(wr + 8 * 288, C512, w128, w4); }
      if (mt == 2) { octet(wr, C1024, w128, w512); *(LDS u32*)(wr + 8 * 288) = pack_bf16(C2048[0], C2048[1]); }
      wave_fence();
      f32x4 acc = {0, 0, 0, 0};
      bf16x8 A[4];
#pragma unroll
      for (int ks = 0; ks < 4; ++ks) A[ks] = lds_frag(rd + 64 * ks);
      __builtin_amdgcn_sched_barrier(0);
#pragma unroll
      for (int ks = 0; ks < 4; ++ks) acc = mfma16(A[ks], B[ks], acc);
      __builtin_amdgcn_sched_barrier(0);
      wave_fence();
      tab[mt] = acc;
    }
  }
  const int b = lane & 15, q = lane >> 4;
#pragma unroll
  for (int mt = 0; mt < 3; ++mt) *(LDS f32x4*)(st + mt * 1024 + lane * 16) = tab[mt];
  STAMP(0, 3);
  bar();
  {
    LDS const char* ot = smem + S_ST + (w ^ 1) * 4608 + lane * 16;
    LDS char* T = smem + S_T + ch * S_TSZ;
    if (hh == 0) {
      const f32x4 v0 = tab[0] + *(LDS const f32x4*)(ot), v2 = tab[2] + *(LDS const f32x4*)(ot + 2048);
      *(LDS u32x2*)(T + 2 * (128 * (q >> 1) + 8 * b + 4 * (q & 1))) = u32x2{pack_bf16(v0[0], v0[1]), pack_bf16(v0[2], v0[3])};
      const u32 a0 = pack_bf16(v2[0], v2[1]), a1 = pack_bf16(v2[2], v2[3]);
      if (q < 2) {
        LDS char* t3 = T + 2 * (512 + 64 * q + b);
        *(LDS u16*)(t3) = (u16)a0; *(LDS u16*)(t3 + 32) = (u16)(a0 >> 16);
        *(LDS u16*)(t3 + 64) = (u16)a1; *(LDS u16*)(t3 + 96) = (u16)(a1 >> 16);
      } else if (lane == 32) *(LDS u16*)(T + 2 * 640) = (u16)a0;
    } else {
      const f32x4 v1 = tab[1] + *(LDS const f32x4*)(ot + 1024);
      const u32 a0 = pack_bf16(v1[0], v1[1]), a1 = pack_bf16(v1[2], v1[3]);
      if (q < 2) *(LDS u32x2*)(T + 2 * (256 + 64 * q + 4 * b)) = u32x2{a0, a1};
      else {
        LDS char* t2 = T + 2 * (384 + 2 * b + (q - 2));
        *(LDS u16*)(t2) = (u16)a0; *(LDS u16*)(t2 + 64) = (u16)(a0 >> 16);
        *(LDS u16*)(t2 + 128) = (u16)a1; *(LDS u16*)(t2 + 192) = (u16)(a1 >> 16);
      }
    }
  }
  bar();
  STAMP(0, 4);
  u32x4 uv[2];
  {
    GLB u64* fl = (GLB u64*)(p.ws + WS_FL) + (8 * hh + (lane & 7)) * DM + d;
    asm volatile("" :: "v"(wc[0]), "v"(wc[1]));
    wait_flags(fl, true);
    const __amdgpu_buffer_rsrc_t rs = mk_rsrc(p.ws + WS_XT, DM * SL * 2);
#pragma unroll
    for (int v = 0; v < 2; ++v) uv[v] = __builtin_amdgcn_raw_buffer_load_b128(rs, (d * SL + 1024 * hh + 8 * (lane + 64 * v)) * 2, 0, SC1);
    if (lane < 8) __hip_atomic_store(fl, 0ull, RLX_AGENT);
  }
  STAMP(0, 5);
  {
    LDS const char* T = smem + S_T + ch * S_TSZ;
    u32 o[10];
    const u32 e512 = *(LDS const u16*)(T + 2 * 512);
    o[8] = o[9] = e512 | (e512 << 16);
    if (hh == 0) {
      const int off = lane < 16 ? 32 * lane : (lane < 32 ? 256 + 16 * lane : 512 + 8 * lane);
      u32x2 W[4];
#pragma unroll
      for (int i = 0; i < 4; ++i) W[i] = *(LDS const u32x2*)(T + off + 8 * i);
      if (lane < 16) {
#pragma unroll
        for (int i = 0; i < 8; ++i) o[i] = W[i >> 1][i & 1];
      } else if (lane < 32) {
#pragma unroll
        for (int i = 0; i < 4; ++i) {
          const u32 a = W[i >> 1][i & 1], nx = W[(i + 1) >> 1][(i + 1) & 1];
          o[2 * i] = dup_lo(a);
          o[2 * i + 1] = hi_lo(a, nx);
        }
      } else {
        const u32 w0 = W[0][0], w1 = W[0][1], w2 = W[1][0];
        o[0] = dup_lo(w0); o[1] = w0; o[2] = dup_hi(w0); o[3] = dup_lo(w1);
        o[4] = o[3]; o[5] = w1; o[6] = dup_hi(w1); o[7] = dup_lo(w2);
      }
    } else {
      const u32 w0 = *(LDS const u32*)(T + 1024 + 4 * lane), w1 = *(LDS const u32*)(T + 1028 + 4 * lane);
      o[0] = o[1] = dup_lo(w0); o[2] = w0; o[3] = o[4] = o[5] = dup_hi(w0); o[6] = o[7] = dup_lo(w1);
    }
    o[8] = (u32)__builtin_amdgcn_update_dpp((int)o[8], (int)o[0], 0x130, 0xf, 0xf, false);
    o[9] = (u32)__builtin_amdgcn_update_dpp((int)o[9], (int)o[1], 0x130, 0xf, 0xf, false);
    u32 a[9];
#pragma unroll
    for (int i = 0; i < 9; ++i) a[i] = hi_lo(o[i], o[i + 1]);
    LDS char* xb = smem + S_X + ch * 4 * S_CPY + 32 + 2 * (1024 * hh + 16 * lane);
    *(LDS u32x4*)(xb) = u32x4{o[0], o[1], o[2], o[3]};
    *(LDS u32x4*)(xb + 16) = u32x4{o[4], o[5], o[6], o[7]};
    *(LDS u32x4*)(xb + S_CPY) = u32x4{a[0], a[1], a[2], a[3]};
    *(LDS u32x4*)(xb + S_CPY + 16) = u32x4{a[4], a[5], a[6], a[7]};
    *(LDS u32x4*)(xb + 2 * S_CPY) = u32x4{o[1], o[2], o[3], o[4]};
    *(LDS u32x4*)(xb + 2 * S_CPY + 16) = u32x4{o[5], o[6], o[7], o[8]};
    *(LDS u32x4*)(xb + 3 * S_CPY) = u32x4{a[1], a[2], a[3], a[4]};
    *(LDS u32x4*)(xb + 3 * S_CPY + 16) = u32x4{a[5], a[6], a[7], a[8]};
    if (hh == 0 && lane == 0) {
      LDS char* hb = smem + S_X + ch * 4 * S_CPY;
      const u32x4 z = {0, 0, 0, 0};
      *(LDS u32x4*)(hb) = z;
      *(LDS u32x4*)(hb + 16) = z;
      *(LDS u32x4*)(hb + S_CPY) = z;
      *(LDS u32x4*)(hb + S_CPY + 16) = u32x4{0, 0, 0, o[0] << 16};
      *(LDS u32x4*)(hb + 2 * S_CPY) = z;
      *(LDS u32x4*)(hb + 2 * S_CPY + 16) = u32x4{0, 0, 0, o[0]};
      *(LDS u32x4*)(hb + 3 * S_CPY) = z;
      *(LDS u32x4*)(hb + 3 * S_CPY + 16) = u32x4{0, 0, o[0] << 16, a[0]};
    }
    if (tid < 6 * 32) *(LDS u32x4*)(smem + S_Z + (tid >> 5) * S_ZSZ + (tid & 31) * 16) = u32x4{0, 0, 0, 0};
    STAMP(0, 6);
    LDS char* z = smem + S_Z + ch * S_ZSZ + 512 + (1024 * hh + 8 * lane) * 2;
    *(LDS u32x4*)z = uv[0];
    *(LDS u32x4*)(z + 1024) = uv[1];
  }
  bar();
  STAMP(0, 7);
  f32x4 acc[8];
#pragma unroll
  for (int i = 0; i < 8; ++i) acc[i] = f32x4{0, 0, 0, 0};
  {
    const int kq = q, qq = b >> 2, pp = b & 3;
    LDS const char* fb = smem + S_X + (ch * 4 + ((4 - qq) & 3)) * S_CPY +
                         2 * (16 + 16 * (kq >> 1) - 8 * (kq & 1) + 4 * pp - (qq ? 4 : 0)) + 64 * hh;
    LDS const char* gb = smem + S_Z + ch * S_ZSZ + 512 + 32 * (b - (kq >> 1)) + 16 * (kq & 1) - 64 * hh;
#pragma unroll 1
    for (int it = 0; it < 4; ++it) {
      bf16x8 F[8], G[8];
#pragma unroll
      for (int s = 0; s < 8; ++s) {
        F[s] = tr_frag(fb + 512 * s, fb + 512 * s - 8);
        G[s] = lds_frag(gb + 512 * s);
      }
      __builtin_amdgcn_sched_barrier(0);
#pragma unroll
      for (int t = 0; t < 8; ++t)
#pragma unroll
        for (int s = 0; s <= t; ++s) acc[t] = mfma16(F[s], G[t - s], acc[t]);
      fb += 128;
      gb -= 128;
    }
  }
  __builtin_amdgcn_sched_barrier(0);
  STAMP(0, 8);
  bar();
  {
    LDS char* ex = smem + S_X + 49152 + w * 4096 + lane * 16;
#pragma unroll
    for (int i = 0; i < 4; ++i) {
      const f32x4 lo = acc[i], hi = acc[4 + i];
      *(LDS f32x4*)(ex + i * 1024) = hh ? +lo : +hi;
    }
    if (tid == 0) *(LDS u32*)(smem + S_CNT) = 0;
  }
  bar();
  u32x2 yo[4];
  {
    LDS const char* ey = smem + S_X + 49152 + (w ^ 1) * 4096 + lane * 16;
#pragma unroll
    for (int i = 0; i < 4; ++i) {
      const f32x4 lo = acc[i], hi = acc[4 + i];
      const f32x4 y = (hh ? +hi : +lo) + *(LDS const f32x4*)(ey + i * 1024);
      yo[i] = u32x2{pack_bf16(y[0], y[1]), pack_bf16(y[2], y[3])};
    }
  }
  bar();
  {
    const __amdgpu_buffer_rsrc_t rs = mk_rsrc(p.ws + WS_AT, DM * SL * 2);
    const int dst = (d * SL + 1024 * hh + 16 * (lane & 15) + 4 * (lane >> 4)) * 2;
#pragma unroll
    for (int i = 0; i < 4; ++i) __builtin_amdgcn_raw_buffer_store_b64(yo[i], rs, dst + 512 * i, 0, SC1);
  }
  STAMP(0, 9);
}
DEV void s_signal(const P& p, LDS char* smem, int sg, int lane) {
  u32 old = 0;
  if (lane == 0) old = __hip_atomic_fetch_add((LDS u32*)(smem + S_CNT), 1u, __ATOMIC_RELAXED, __HIP_MEMORY_SCOPE_WORKGROUP);
  if (__builtin_amdgcn_readfirstlane(old) == 11) {
    const int lt = lane & 31, second = lane >> 5;
    const int dd = 6 * sg + (second ? 5 : 0);
    const int idx = dd < 480 ? dd / 96 : 5 + (dd - 480) / 32;
    const int dfirst = idx < 5 ? 96 * idx : 480 + 32 * (idx - 5);
    __hip_atomic_store((GLB u64*)(p.ws + WS_IN) + tile_block(lt, idx) * 64 + IN_A + (sg - dfirst / 6), FLAG_SET, RLX_AGENT);
  }
}

DEV void role_gate(const P& p, int rb, int w, int lane, u64 pc) {
  u32 pf0 = 0, pf1 = 0;
  const int tid = w * 64 + lane;
  const int sub = lane & 7;
  u32x4 wv[9];
#pragma unroll
  for (int i = 0; i < 9; ++i) wv[i] = *(const u32x4*)((const char*)p.gw + (i * 8 + sub) * 16);
  for (int r = w * 8 + (lane >> 3); r < 128; r += 96) {
    const int l = rb * 128 + r;
    float s = 0.f;
    u32x4 xv[9];
#pragma unroll
    for (int i = 0; i < 9; ++i) xv[i] = *(const u32x4*)((const char*)p.x + (size_t)l * 1152 + (i * 8 + sub) * 16);
#pragma unroll
    for (int i = 0; i < 9; ++i) {
#pragma unroll
      for (int k = 0; k < 4; ++k) {
        const u32 pr = pack_bf16(bflo(xv[i][k]) * bflo(wv[i][k]), bfhi(xv[i][k]) * bfhi(wv[i][k]));
        s += bflo(pr);
        s += bfhi(pr);
      }
    }
    s += __shfl_xor(s, 1); s += __shfl_xor(s, 2); s += __shfl_xor(s, 4);
    const float gv = rbf(1.0f / (1.0f + expf(-rbf(s))));
    if (sub == 0)
      __hip_atomic_store((GLB u64*)(p.ws + WS_G) + l, ((u64)__float_as_uint(rbf(1.0f - gv)) << 32) | __float_as_uint(gv), RLX_AGENT);
  }
  asm volatile("" :: "v"(pf0), "v"(pf1));
  wait_vm<0>();
  bar();
  if (w == 0 && lane < 16)
    __hip_atomic_store((GLB u64*)(p.ws + WS_IN) + tile_block(2 * rb + (lane >> 3), lane & 7) * 64 + IN_G, FLAG_SET, RLX_AGENT);
}

constexpr int H_C = 32768, H_CS = 12288;
DEV int h_slot(int i) { return i < 4 ? i * 8192 : 131072 + (i - 4) * 8192; }
constexpr int H_AT = H_C + 2 * H_CS;
constexpr int H_XT = H_C + 3 * H_CS;
constexpr int H_G = H_C + 4 * H_CS;
constexpr int H_OUT = H_C + 5 * H_CS;
constexpr int K_LDS = 163840;

constexpr int h_wait(int s, int NDT) {
  int n = 0;
  int need = 0;
  for (int k = 0; k < 4; ++k) { n += 2; if (k == s) need = n; }
  for (int t = 0; t < s; ++t) {
    if (t == 0) { n += 2; if (s == 4) need = n; n += 2; if (s == 5) need = n; }
    else if (t == 1) { n += 2; if (s == 6) need = n; n += 2; if (s == 7) need = n; }
    else if (t <= 11) { n += 2 + NDT; if (s == t + 6) need = n; if (t == 2) n += 1; }
    else if (t == 12 || t == 13) n += NDT;
    else if (t == 14) n += 1;
  }
  return s == 17 ? 0 : n - need;
}
template <int S, int NDT, class F> DEV void h_steps(F&& after, u64& fv, char* wsp) {
  if constexpr (S == 9) asm volatile("s_waitcnt vmcnt(%1)" : "+v"(fv) : "n"(h_wait(S, NDT)) : "memory");
  else wait_vm<h_wait(S, NDT)>();
  LSTAMP2(1, 16 + S);
  bar();
  after(std::integral_constant<int, S>{});
  if constexpr (S < 17) h_steps<S + 1, NDT>(after, fv, wsp);
}
DEV int p2_rot(int d0) { const int idx = d0 < 480 ? d0 / 96 : 5 + (d0 - 480) / 32; return (idx * 18) >> 3; }

template <int NDT, class F>
DEV void p2_loader(const P& p, LDS char* smem, int lt, int d0, int lw, int lane, bool scratch_barrier, F&& pre_poll) {
  __builtin_amdgcn_s_setprio(3);
  const char* hb = p.ws + WS_HB + (size_t)lt * 18 * 8192 + (2 * lw) * 1024 + lane * 16;
  constexpr int W = 32 * NDT;
  const int l0 = 64 * lt, rot = p2_rot(d0);
  u32 co[NDT];
#pragma unroll
  for (int i = 0; i < NDT; ++i) {
    const int row = 8 * (NDT * lw + i) + (lane >> 3), cp = lane & 7;
    co[i] = (u32)(d0 + row) * 1152u + ((cp ^ (row & 7)) << 4);
  }
  auto issue_c = [&](int s) {
    LDS char* buf = smem + H_C + (s & 7) * H_CS + NDT * lw * 1024;
    int ks = s + rot; ks = ks >= 18 ? ks - 18 : ks;
    const char* cb = ks < 9 ? (const char*)p.cre + ks * 128 : (const char*)p.cim + (ks - 9) * 128;
#pragma unroll
    for (int i = 0; i < NDT; ++i) dma16<0>(cb + co[i], buf + i * 1024);
  };
  auto issue_h = [&](int s) {
    LDS char* buf = smem + h_slot(s & 7) + 2 * lw * 1024;
    int ks = s + rot; ks = ks >= 18 ? ks - 18 : ks;
#pragma unroll
    for (int i = 0; i < 2; ++i) dma16<SC1>(hb + ks * 8192 + i * 1024, buf + i * 1024);
  };
  GLB u64* inb = (GLB u64*)(p.ws + WS_IN) + blockIdx.x * 64;
  const int nA = (d0 + W - 1) / 6 - d0 / 6 + 1;
  const bool need = lane == IN_G || (lane >= IN_A && lane < IN_A + nA);
  if (!scratch_barrier) {
#pragma unroll 1
    for (int s = 0; s < 8; ++s) issue_c(s);
  }
  pre_poll();
  { [[maybe_unused]] const int w = lw + 8; STAMP(1, 1); }
  wait_flags(inb + IN_H + (lane < 36 ? lane : 0), true);
  { [[maybe_unused]] const int w = lw + 8; STAMP(1, 2); }
#pragma unroll
  for (int s = 0; s < 4; ++s) issue_h(s);
  { [[maybe_unused]] const int w = lw + 8; STAMP(1, 3); }
  u64 fv = 0;
  h_steps<0, NDT>([&](auto S_) {
    constexpr int S = decltype(S_)::value;
    if constexpr (S == 0) { issue_h(4); issue_h(5); }
    if constexpr (S == 1) { issue_h(6); issue_h(7); }
    if constexpr (S >= 2 && S <= 11) { issue_h(S + 6); issue_c(S + 6); }
    if constexpr (S == 2)
      asm volatile("global_load_dwordx2 %0, %1, off sc1" : "=v"(fv) : "v"(inb + lane) : "memory");
    if constexpr (S == 9) { if (!__all(!need || fv == FLAG_SET)) wait_flags(inb + lane, need); }
    if constexpr (S == 12) {
#pragma unroll
      for (int i = 0; i < NDT; ++i) {
        const int pc = NDT * lw + i;
        dma16<SC1>(p.ws + WS_AT + (size_t)(d0 + 8 * pc + (lane >> 3)) * 4096 + l0 * 2 + (lane & 7) * 16, smem + H_AT + pc * 1024);
      }
    }
    if constexpr (S == 13) {
#pragma unroll
      for (int i = 0; i < NDT; ++i) {
        const int pc = NDT * lw + i, id = pc * 64 + lane, row = id / (4 * NDT), qq = id - row * (4 * NDT);
        dma16<0>((const char*)p.x + (size_t)(l0 + row) * 1152 + d0 * 2 + qq * 16, smem + H_XT + pc * 1024);
      }
    }
    if constexpr (S == 14) { if (lane < 8) dma16<SC1>(p.ws + WS_G + (size_t)l0 * 8 + lw * 128 + lane * 16, smem + H_G + lw * 128); }
  }, fv, p.ws);
}

template <int NDT, int STAG>
DEV void p2_compute(const P& p, LDS char* smem, int lt0, int d0, int w, int lane, u64 g_tb) {
  const int lh = (w >> 1) & 1, kh = w & 1, dg = w >> 2;
  const int r = lane & 15, kq = lane >> 4;
  const int lt = 2 * lh + kh, l0 = 64 * lt0;
  constexpr int W = 32 * NDT;
  u32x2 da[NDT], db[NDT];
#pragma unroll
  for (int t = 0; t < NDT; ++t) {
    const int d = d0 + 16 * (NDT * dg + t) + 4 * kq;
    da[t] = *(const u32x2*)(p.dskA + d);
    db[t] = *(const u32x2*)(p.dskB + d);
  }
  GLB u64* inb = (GLB u64*)(p.ws + WS_IN) + blockIdx.x * 64;

  f32x4 acc[2][NDT];
#pragma unroll
  for (int i = 0; i < 2; ++i)
#pragma unroll
    for (int t = 0; t < NDT; ++t) acc[i][t] = f32x4{0, 0, 0, 0};
  int ho[2];
#pragma unroll
  for (int u = 0; u < 2; ++u) {
    const int k = 32 * kh + 8 * kq + 4 * u + (r >> 2), i = (r & 3) >> 1;
    ho[u] = (2 * (k >> 4) + i) * 1024 + (32 * lh + ((k & 15) ^ (4 * i))) * 16 + (r & 1) * 8;
  }
  const int co = H_C + (16 * NDT * dg + r) * 128 + (((4 * kh + kq) ^ (r & 7)) << 4);
  constexpr int NREG = 0, NRING = 18 - NREG;
  bf16x8 cr[NREG ? NREG : 1][NDT];
  if constexpr (NREG > 0) {
    const int rot = p2_rot(d0);
#pragma unroll
    for (int i = 0; i < NREG; ++i) {
      int ks = NRING + i + rot; ks = ks >= 18 ? ks - 18 : ks;
      const char* cb = (ks < 9 ? (const char*)p.cre + ks * 128 : (const char*)p.cim + (ks - 9) * 128) +
                       (size_t)(d0 + 16 * NDT * dg + r) * 1152 + (4 * kh + kq) * 16;
#pragma unroll
      for (int t = 0; t < NDT; ++t) cr[i][t] = *(const GLB bf16x8*)(cb + t * 16 * 1152);
    }
  }
  bf16x8 hb[2], ca[NDT];
#pragma unroll
  for (int i = 0; i < 2; ++i) hb[i] = bf16x8{};
#pragma unroll
  for (int t = 0; t < NDT; ++t) ca[t] = bf16x8{};
  auto mma_with = [&](const bf16x8 (&cc)[NDT]) {
#pragma unroll
    for (int t = 0; t < NDT; ++t) {
      acc[0][t] = mfma16(cc[t], hb[0], acc[0][t]);
      acc[1][t] = mfma16(cc[t], hb[1], acc[1][t]);
    }
    __builtin_amdgcn_sched_barrier(0);
  };
  auto read_h = [&](int s) {
    LDS const char* hbuf = smem + h_slot(s & 7);
#pragma unroll
    for (int i = 0; i < 2; ++i) hb[i] = tr_frag(hbuf + ho[0] + i * 256, hbuf + ho[1] + i * 256);
  };
  STAMP(1, 1);
  HOLD(4);
#pragma unroll 1
  for (int s = 0; s < NRING; ++s) {
    bar();
    if (s == 0) STAMP(1, 2);
    LSTAMP(1, 16 + s);
    if (STAG) mma_with(ca);
    read_h(s);
    LDS const char* cbuf = smem + (s & 7) * H_CS;
#pragma unroll
    for (int t = 0; t < NDT; ++t) ca[t] = lds_frag(cbuf + co + t * 2048);
    __builtin_amdgcn_sched_barrier(0);
    if (!STAG) mma_with(ca);
  }
#pragma unroll
  for (int i = 0; i < NREG; ++i) {
    bar();
    if (STAG) { if (i == 0) mma_with(ca); else mma_with(cr[i ? i - 1 : 0]); }
    read_h(NRING + i);
    __builtin_amdgcn_sched_barrier(0);
    if (!STAG) mma_with(cr[i]);
  }
  if (STAG) { if (NREG) mma_with(cr[NREG ? NREG - 1 : 0]); else mma_with(ca); }
  __builtin_amdgcn_sched_barrier(0);
  STAMP(1, 3);
  HOLD(5);
  bar();
  if (w == 0) __hip_atomic_store(inb + lane, 0ull, RLX_AGENT);
  LDS char* ex = smem + w * (NDT * 1024) + lane * 16;
#pragma unroll
  for (int t = 0; t < NDT; ++t) {
    const f32x4 a0 = acc[0][t], a1 = acc[1][t];
    *(LDS f32x4*)(ex + t * 1024) = kh ? +a0 : +a1;
  }
  bar();
  LDS const char* ey = smem + (w ^ 1) * (NDT * 1024) + lane * 16;
  constexpr int OSTR = W * 4 + 16;
  const f32x2 gg = *(LDS const f32x2*)(smem + H_G + (16 * lt + r) * 8);
#pragma unroll
  for (int t = 0; t < NDT; ++t) {
    const int dt = NDT * dg + t;
    const f32x4 o = *(LDS const f32x4*)(ey + t * 1024);
    const f32x4 a0 = acc[0][t], a1 = acc[1][t];
    const f32x4 y = (kh ? +a1 : +a0) + o;
    const s16x4 av = tr_read(smem + H_AT + (16 * dt + 4 * kq + (r >> 2)) * 128 + lt * 32 + (r & 3) * 8);
    const u32x2 xv = *(LDS const u32x2*)(smem + H_XT + (16 * lt + r) * (W * 2) + (16 * dt + 4 * kq) * 2);
    f32x4 res;
#pragma unroll
    for (int i = 0; i < 4; ++i) {
      const u32 xw = xv[i >> 1], aw = da[t][i >> 1], bw = db[t][i >> 1];
      const float xf = (i & 1) ? bfhi(xw) : bflo(xw);
      const float sa = (i & 1) ? bfhi(aw) : bflo(aw);
      const float sb = (i & 1) ? bfhi(bw) : bflo(bw);
      const float yc = bf2f((u16)av[i]);
      const float a = rbf(yc + rbf(xf * sa));
      const float b = y[i] + rbf(xf * sb);
      res[i] = rbf(gg[0] * a) + gg[1] * b;
    }
    *(LDS f32x4*)(smem + H_OUT + (16 * lt + r) * OSTR + (16 * dt + 4 * kq) * 4) = res;
  }
  STAMP(1, 4);
  bar();
#pragma unroll
  for (int it = 0; it < NDT; ++it) {
    const int id = it * 512 + w * 64 + lane;
    const int row = id / (8 * NDT), pc = id - row * (8 * NDT);
    const f32x4 v = *(LDS const f32x4*)(smem + H_OUT + row * OSTR + pc * 16);
    __builtin_amdgcn_raw_buffer_store_b128(__builtin_bit_cast(u32x4, v), mk_rsrc(p.out, SL * DM * 4), ((l0 + row) * DM + d0 + pc * 4) * 4, 0, OUT_AUX);
  }
}

template <int NDT>
DEV void phase2(const P& p, LDS char* smem, int lt, int d0, int w, int lane, bool scratch_barrier, int sg, u64 g_tb) {
  STAMP(1, 0);
  if (w >= 8) {
    p2_loader<NDT>(p, smem, lt, d0, w - 8, lane, scratch_barrier, [&]() { if (NDT == 1) { wait_vm<8>(); s_signal(p, smem, sg, lane); } });
  } else {
    if (NDT == 1) { wait_vm<0>(); s_signal(p, smem, sg, lane); }
    if (w < 4) p2_compute<NDT, 0>(p, smem, lt, d0, w, lane, g_tb);
    else p2_compute<NDT, 1>(p, smem, lt, d0, w, lane, g_tb);
  }
  STAMP(1, 15);
}

__global__ __launch_bounds__(768, 3) void gated_mix_s4d_s5(P p) {
  extern __shared__ __attribute__((aligned(16))) char smem_raw[];
  LDS char* smem = (LDS char*)smem_raw;
  u64 pc;
  asm volatile("s_getpc_b64 %0" : "=s"(pc));
  u32 pf = 0;
  const u64 g_tb = __builtin_amdgcn_s_memrealtime();
#define PF_TOP 312
  if (threadIdx.x < PF_TOP)
    asm volatile("global_load_dword %0, %1, off" : "=v"(pf) : "v"((pc & ~255ull) + threadIdx.x * 128));
  const int w = __builtin_amdgcn_readfirstlane(threadIdx.x >> 6), lane = threadIdx.x & 63;
  const int xcd = blockIdx.x & 7, j = blockIdx.x >> 3;
  STAMP(0, 0);
  if (j >= 18 && j < 30) {
    const int sg = 12 * xcd + (j - 18);
    role_s(p, smem, sg, w, lane);
    STAMP(0, 15);
    phase2<1>(p, smem, 4 * xcd + (j - 18) / 3, 480 + 32 * ((j - 18) % 3), w, lane, false, sg, g_tb);
  } else {
    const bool isg = j < 18;
    if (isg) {
      const int c = 2 * xcd + j / 9, g = j % 9;
      if (w >= 8) g_loader(p, smem, c, g, 96 * (j % 5), w - 8, lane);
      else if (w < 4) g_compute<0>(p, smem, c, g, w, lane, pf, g_tb);
      else g_compute<1>(p, smem, c, g, w, lane, pf, g_tb);
    } else {
      role_gate(p, 2 * xcd + (j - 30), w, lane, pc);
    }
    STAMP(0, 15);
    const int e = isg ? j : j - 12;
    phase2<3>(p, smem, 4 * xcd + e / 5, 96 * (e % 5), w, lane, isg, 0, g_tb);
  }
  asm volatile("s_waitcnt vmcnt(0)" :: "v"(pf));
}

extern "C" void kernel_launch(void* const* d_in, const int* in_sizes, int n_in, void* d_out, int out_size,
                              void* d_ws, size_t ws_size, hipStream_t stream) {
  P a;
  a.x = (const u16*)d_in[0]; a.la = (const u16*)d_in[1]; a.aim = (const u16*)d_in[2];
  a.creA = (const u16*)d_in[3]; a.cimA = (const u16*)d_in[4]; a.ldtA = (const u16*)d_in[5]; a.dskA = (const u16*)d_in[6];
  a.lre = (const u16*)d_in[8]; a.lim = (const u16*)d_in[9]; a.bre = (const u16*)d_in[10]; a.bim = (const u16*)d_in[11];
  a.cre = (const u16*)d_in[12]; a.cim = (const u16*)d_in[13]; a.ldtB = (const u16*)d_in[14];
  a.dskB = (const u16*)d_in[15]; a.gw = (const u16*)d_in[16];
  a.ws = (char*)d_ws + WS_OFF; a.out = (float*)d_out;
  (void)hipFuncSetAttribute((const void*)gated_mix_s4d_s5, hipFuncAttributeMaxDynamicSharedMemorySize, K_LDS);
  gated_mix_s4d_s5<<<256, 768, K_LDS, stream>>>(a);
}
```

```cpp
#define WARM_B_S 1
#include <hip/hip_runtime.h>
#include <hip/hip_bf16.h>
#include <stdint.h>
#include <type_traits>

typedef unsigned short u16;
typedef unsigned int u32;
typedef unsigned long long u64;
typedef __attribute__((ext_vector_type(8))) __bf16 bf16x8;
typedef __attribute__((ext_vector_type(2))) __bf16 bf16x2;
typedef __attribute__((ext_vector_type(4))) short s16x4;
typedef __attribute__((ext_vector_type(4))) float f32x4;
typedef __attribute__((ext_vector_type(2))) float f32x2;
typedef __attribute__((ext_vector_type(4))) unsigned u32x4;
typedef __attribute__((ext_vector_type(2))) unsigned u32x2;
#define LDS __attribute__((address_space(3)))
#define GLB __attribute__((address_space(1)))
#define DEV __device__ __forceinline__
#define RLX_AGENT __ATOMIC_RELAXED, __HIP_MEMORY_SCOPE_AGENT

constexpr int SL = 2048;
constexpr int DM = 576;

constexpr size_t WS_HB = 0;
constexpr size_t WS_AT = WS_HB + (size_t)2 * DM * SL * 2;
constexpr size_t WS_G = WS_AT + (size_t)DM * SL * 2;
constexpr size_t WS_GRAN = WS_G + (size_t)SL * 8;
constexpr size_t WS_EP = WS_GRAN + (size_t)9 * 16 * 2 * 64 * 8;
constexpr size_t WS_FL = WS_EP + 4096;
constexpr size_t WS_XT = WS_FL + (size_t)16 * DM * 8;
constexpr size_t WS_IN = WS_XT + (size_t)DM * SL * 2;
constexpr u64 FLAG_SET = 0x9E3779B97F4A7C15ull;
constexpr u32 SPIN_CAP = 1u << 17;
constexpr int IN_H = 0, IN_G = 36, IN_A = 40;

constexpr size_t WS_ST = (size_t)16 << 20;
#define STAMP(kern, idx) do {} while (0)
#define CSTAMP(kern, idx) do {} while (0)
#define LSTAMP(kern, idx) do {} while (0)
#define LSTAMP2(kern, idx) do {} while (0)

DEV float bf2f(u16 v) { return __uint_as_float((u32)v << 16); }
DEV float bflo(u32 v) { return __uint_as_float(v << 16); }
DEV float bfhi(u32 v) { return __uint_as_float(v & 0xffff0000u); }
DEV u32 pack_bf16(float lo, float hi) {
  f32x2 v = {lo, hi};
  bf16x2 b = __builtin_convertvector(v, bf16x2);
  return __builtin_bit_cast(u32, b);
}
DEV float rbf(float x) { return bflo(pack_bf16(x, x)); }
template <int N> DEV void wait_vm() { asm volatile("s_waitcnt vmcnt(%0)" ::"n"(N) : "memory"); }
DEV void bar() { asm volatile("s_waitcnt lgkmcnt(0)\n\ts_barrier" ::: "memory"); }
DEV void wave_fence() { __builtin_amdgcn_fence(__ATOMIC_ACQ_REL, "wavefront"); }
template <int AUX> DEV void dma16(const void* src, LDS char* dst) {
  __builtin_amdgcn_global_load_lds((const GLB u32*)src, (LDS u32*)dst, 16, 0, AUX);
}
constexpr int SC1 = 16;
#define POLL_SLEEP 1
#define OUT_AUX 16
#define WS_OFF 0
#define XT_WAIT 4
DEV bf16x8 lds_frag(LDS const char* p) { return *(LDS const bf16x8*)p; }
DEV s16x4 tr_read(LDS const char* a) { return __builtin_amdgcn_ds_read_tr16_b64_v4i16((LDS s16x4*)a); }
DEV bf16x8 tr_frag(LDS const char* a, LDS const char* b) {
  const s16x4 lo = tr_read(a), hi = tr_read(b);
  return __builtin_bit_cast(bf16x8, __builtin_shufflevector(lo, hi, 0, 1, 2, 3, 4, 5, 6, 7));
}
DEV f32x4 mfma16(bf16x8 a, bf16x8 b, f32x4 c) { return __builtin_amdgcn_mfma_f32_16x16x32_bf16(a, b, c, 0, 0, 0); }
DEV __amdgpu_buffer_rsrc_t mk_rsrc(const void* base, int bytes) {
  return __builtin_amdgcn_make_buffer_rsrc(const_cast<void*>(base), (short)0, bytes, 0x00020000);
}
struct cf { float r, i; };
DEV cf cmul(cf a, cf b) { return {a.r * b.r - a.i * b.i, a.r * b.i + a.i * b.r}; }
DEV cf cfma(cf a, cf b, cf c) { return {fmaf(a.r, b.r, fmaf(-a.i, b.i, c.r)), fmaf(a.r, b.i, fmaf(a.i, b.r, c.i))}; }
typedef f32x2 c2;
DEV c2 cm2(c2 a, c2 b) {
  c2 t, r;
  asm("v_pk_mul_f32 %0, %1, %2 op_sel:[0,0] op_sel_hi:[0,1]" : "=v"(t) : "v"(a), "v"(b));
  asm("v_pk_fma_f32 %0, %1, %2, %3 op_sel:[1,1,0] op_sel_hi:[1,0,1] neg_lo:[1,0,0]" : "=v"(r) : "v"(a), "v"(b), "v"(t));
  return r;
}
DEV f32x2 fma2(f32x2 a, f32x2 b, f32x2 c) { return __builtin_elementwise_fma(a, b, c); }
DEV f32x2 lo2(f32x4 v) { return __builtin_shufflevector(v, v, 0, 1); }
DEV f32x2 hi2(f32x4 v) { return __builtin_shufflevector(v, v, 2, 3); }
DEV f32x4 cat2(f32x2 a, f32x2 b) { return __builtin_shufflevector(a, b, 0, 1, 2, 3); }
DEV void wait_flags(GLB u64* word, bool need) {
  for (u32 spin = 0;; ++spin) {
    const u64 v = __hip_atomic_load(word, RLX_AGENT);
    if (__all(!need || v == FLAG_SET) || spin > SPIN_CAP) break;
    __builtin_amdgcn_s_sleep(POLL_SLEEP);
  }
}
DEV int tile_block(int lt, int idx) {
  const int xcd = lt >> 2, a = lt & 3;
  int j;
  if (idx < 5) { const int e = 5 * a + idx; j = e < 18 ? e : e + 12; }
  else j = 18 + 3 * a + (idx - 5);
  return (j << 3) | xcd;
}

struct P {
  const u16 *x, *la, *aim, *creA, *cimA, *ldtA, *dskA;
  const u16 *lre, *lim, *bre, *bim, *cre, *cim, *ldtB, *dskB, *gw;
  char* ws;
  float* out;
};

constexpr int G_NB = 4, G_STAGE = 32768;
constexpr int G_E = 153600;
constexpr int G_PF = G_E + 4096;
constexpr int G_L = G_PF + 4096;
constexpr int G_X = G_L + 1024;
constexpr int G_T = G_NB * G_STAGE;
static_assert(G_X + 1024 <= 163840 && G_T + 4 * 32 * 144 <= G_E, "role G scratch");

DEV void g_loader(const P& p, LDS char* smem, int c, int g, int d0, int lw, int lane) {
  [[maybe_unused]] const int w = lw + 8;
  __builtin_amdgcn_s_setprio(3);
  GLB u32* ep = (GLB u32*)(p.ws + WS_EP + g * 256);
  GLB u64* gran = (GLB u64*)(p.ws + WS_GRAN) + (size_t)g * 16 * 128;
  const u32 tag = __hip_atomic_load(ep, RLX_AGENT) + 1u;
  u32 xo[4], bo[4];
#pragma unroll
  for (int i = 0; i < 4; ++i) {
    const int rho = 8 * (4 * lw + i) + (lane >> 3);
    const int hf = rho >> 6, m = (rho >> 4) & 3, r = rho & 15;
    const int tau = 64 * hf + 16 * (r >> 2) + 8 * (r & 1) + 2 * m + ((r >> 1) & 1);
    const int ch = (lane & 7) ^ (rho & 7);
    xo[i] = (u32)(128 * c + tau) * 1152u + ch * 16;
    const int pp = 64 * g + 16 * (rho >> 5) + r;
    bo[i] = (u32)pp * 1152u + ch * 16;
  }
  auto issue = [&](int s) {
    LDS char* buf = smem + (s & (G_NB - 1)) * G_STAGE + lw * 4096;
    int kb = g + s; kb = (kb >= 9 ? kb - 9 : kb) * 128;
#pragma unroll
    for (int i = 0; i < 4; ++i) dma16<0>((const char*)p.x + (xo[i] + kb), buf + i * 1024);
#pragma unroll
    for (int i = 0; i < 4; ++i) {
      const char* base = (const char*)((((4 * lw + i) >> 1) & 1) ? p.bim : p.bre);
      dma16<0>(base + (bo[i] + kb), buf + 16384 + i * 1024);
    }
  };
#pragma unroll
  for (int s = 0; s < G_NB - 1; ++s) issue(s);
#pragma unroll 1
  for (int s = 0; s < 6; ++s) { wait_vm<16>(); LSTAMP(0, 16 + s); bar(); issue(s + G_NB - 1); LSTAMP(0, 32 + s); }
  wait_vm<16>(); bar();
  wait_vm<8>(); bar();
  wait_vm<0>(); bar();
  STAMP(0, 1);
  if (lw < 2) {
    bar();
    u32 co[6];
#pragma unroll
    for (int i = 0; i < 6; ++i) {
      const int row = 8 * (6 * lw + i) + (lane >> 3), cp = lane & 7;
      co[i] = (u32)(d0 + row) * 1152u + ((cp ^ (row & 7)) << 4);
    }
    const int rot = ((d0 / 96) * 18) >> 3;
#pragma unroll 1
    for (int s = 0; s < 8; ++s) {
      LDS char* buf = smem + 32768 + s * 12288 + 6 * lw * 1024;
      const int ks = s + rot;
      const char* cb = ks < 9 ? (const char*)p.cre + ks * 128 : (const char*)p.cim + (ks - 9) * 128;
#pragma unroll
      for (int i = 0; i < 6; ++i) dma16<0>(cb + co[i], buf + i * 1024);
    }
    bar();
    return;
  }
  bar();
  STAMP(0, 2);
  if (lw == 3) {
    const f32x2 lv = *(LDS const f32x2*)(smem + G_L + lane * 8);
    const cf l16 = {lv[0], lv[1]};
    f32x2 ev[8];
#pragma unroll
    for (int s = 0; s < 8; ++s) ev[s] = *(LDS const f32x2*)(smem + G_E + (s * 64 + lane) * 8);
    cf tot = {0.f, 0.f};
    cf pre[8];
#pragma unroll
    for (int s = 0; s < 8; ++s) { pre[s] = tot; tot = cfma(l16, tot, cf{ev[s][0], ev[s][1]}); }
    if (c < 15) {
      __hip_atomic_store(gran + c * 128 + lane, ((u64)tag << 32) | __float_as_uint(tot.r), RLX_AGENT);
      __hip_atomic_store(gran + c * 128 + 64 + lane, ((u64)tag << 32) | __float_as_uint(tot.i), RLX_AGENT);
    }
#pragma unroll
    for (int s = 0; s < 8; ++s) *(LDS f32x2*)(smem + G_PF + (s * 64 + lane) * 8) = f32x2{pre[s].r, pre[s].i};
  }
  const int par = lw - 2;
  const f32x2 lv = *(LDS const f32x2*)(smem + G_L + 512 + lane * 8);
  const cf l128 = {lv[0], lv[1]};
  const cf l256 = cmul(l128, l128);
  cf acc = {0.f, 0.f};
  if (par < c) {
    const int nc = (c - par + 1) >> 1;
    u64 a[8], b[8];
    for (u32 spin = 0;; ++spin) {
      bool ok = true;
#pragma unroll
      for (int i = 0; i < 8; ++i) {
        const int cc = par + 2 * (i < nc ? i : 0);
        a[i] = __hip_atomic_load(gran + cc * 128 + lane, RLX_AGENT);
        b[i] = __hip_atomic_load(gran + cc * 128 + 64 + lane, RLX_AGENT);
      }
#pragma unroll
      for (int i = 0; i < 8; ++i) ok = ok && (u32)(a[i] >> 32) == tag && (u32)(b[i] >> 32) == tag;
      if (__all(ok) || spin > SPIN_CAP) break;
      __builtin_amdgcn_s_sleep(1);
    }
#pragma unroll
    for (int i = 0; i < 8; ++i)
      if (i < nc) acc = cfma(l256, acc, cf{__uint_as_float((u32)a[i]), __uint_as_float((u32)b[i])});
    if (c - 1 - (par + 2 * (nc - 1))) acc = cmul(acc, l128);
  }
  *(LDS f32x2*)(smem + G_X + (par * 64 + lane) * 8) = f32x2{acc.r, acc.i};
  STAMP(0, 3);
  bar();
  if (c == 15 && lw == 2 && lane == 0) __hip_atomic_store(ep, tag, RLX_AGENT);
}

template <int HF>
DEV void g_compute(const P& p, LDS char* smem, int c, int g, int w, int lane, u32& pf) {
  const int pq = w & 3, col = lane & 15, q = lane >> 4;
  const int pl = 16 * pq + col, pp = 64 * g + pl;
  cf lam, coef2;
  {
    const float lre = bf2f(p.lre[pp]), lim = bf2f(p.lim[pp]);
    const float dt = rbf(expf(bf2f(p.ldtB[pp])));
    const float zr = dt * lre, zi = dt * lim;
    const float er = expf(zr);
    float sn, cs;
    sincosf(zi, &sn, &cs);
    lam = {er * cs, er * sn};
    const float nr = lam.r - 1.0f, ni = lam.i;
    const float den = 1.0f / (lre * lre + lim * lim);
    coef2 = {2.0f * (nr * lre + ni * lim) * den, 2.0f * (ni * lre - nr * lim) * den};
  }
  cf l8 = cmul(lam, lam); l8 = cmul(l8, l8); l8 = cmul(l8, l8);
  const cf l16 = cmul(l8, l8);
  const cf l32 = cmul(l16, l16), l64 = cmul(l32, l32);
  if (HF == 0 && q == 0) {
    const cf l128 = cmul(l64, l64);
    *(LDS f32x2*)(smem + G_L + pl * 8) = f32x2{l16.r, l16.i};
    *(LDS f32x2*)(smem + G_L + 512 + pl * 8) = f32x2{l128.r, l128.i};
  }
  cf pw = HF ? l64 : cf{1.f, 0.f};
  { const cf t = cmul(pw, l16); if (q & 1) pw = t; }
  { const cf t = cmul(pw, l32); if (q & 2) pw = t; }
  asm volatile("" : "+v"(lam.r), "+v"(lam.i), "+v"(coef2.r), "+v"(coef2.i), "+v"(l8.r), "+v"(l8.i), "+v"(pw.r), "+v"(pw.i));
  f32x4 are[4], aim[4];
#pragma unroll
  for (int m = 0; m < 4; ++m) { are[m] = f32x4{0, 0, 0, 0}; aim[m] = f32x4{0, 0, 0, 0}; }
  const int o0 = col * 128 + (((0 + q) ^ (col & 7)) << 4);
  const int o1 = col * 128 + (((4 + q) ^ (col & 7)) << 4);
  bf16x8 br[2], bi[2], a[2][4];
  auto read_frags = [&](int s) {
    LDS const char* buf = smem + (s & (G_NB - 1)) * G_STAGE;
    LDS const char* ab = buf + HF * 8192;
    LDS const char* bb = buf + 16384 + pq * 4096;
#pragma unroll
    for (int ks = 0; ks < 2; ++ks) {
      const int o = ks ? o1 : o0;
      br[ks] = lds_frag(bb + o); bi[ks] = lds_frag(bb + 2048 + o);
#pragma unroll
      for (int m = 0; m < 4; ++m) a[ks][m] = lds_frag(ab + m * 2048 + o);
    }
  };
  auto mma = [&]() {
#pragma unroll
    for (int ks = 0; ks < 2; ++ks)
#pragma unroll
      for (int m = 0; m < 4; ++m) {
        are[m] = mfma16(a[ks][m], br[ks], are[m]);
        aim[m] = mfma16(a[ks][m], bi[ks], aim[m]);
      }
  };
  STAMP(0, 1);
#pragma unroll
  for (int s = 0; s < 9; ++s) {
    if (HF == 1 && s == XT_WAIT) {
      wait_vm<0>();
      if (lane < 16) __hip_atomic_store((GLB u64*)(p.ws + WS_FL) + c * DM + 64 * g + 16 * pq + lane, FLAG_SET, RLX_AGENT);
    }
    bar();
    if (s == 0) { STAMP(0, 2); CSTAMP(0, 60); }
    if (HF == 1 && s > 0) { mma(); __builtin_amdgcn_sched_barrier(0); }
    if (HF == 1 && s == 0) {
      const int rr = 4 * q + (col >> 2);
      LDS const char* tb = smem + rr * 128 + (((2 * pq + ((col & 3) >> 1)) ^ (rr & 7)) << 4) + (col & 1) * 8;
      LDS char* sc = smem + G_T + pq * (32 * 144);
#pragma unroll
      for (int hf = 0; hf < 2; ++hf) {
        u32x2 t[4];
#pragma unroll
        for (int m = 0; m < 4; ++m) t[m] = __builtin_bit_cast(u32x2, tr_read(tb + hf * 8192 + m * 2048));
        u32x4 oa, ob;
#pragma unroll
        for (int m = 0; m < 4; ++m) {
          oa[m] = __builtin_amdgcn_perm(t[m][1], t[m][0], 0x05040100u);
          ob[m] = __builtin_amdgcn_perm(t[m][1], t[m][0], 0x07060302u);
        }
        *(LDS u32x4*)(sc + hf * (16 * 144) + col * 144 + q * 32) = oa;
        *(LDS u32x4*)(sc + hf * (16 * 144) + col * 144 + q * 32 + 16) = ob;
      }
      wave_fence();
      const __amdgpu_buffer_rsrc_t rs = mk_rsrc(p.ws + WS_XT, DM * SL * 2);
#pragma unroll
      for (int u = 0; u < 4; ++u) {
        const int row = (lane >> 3) + 8 * (u & 1), hf = u >> 1;
        const u32x4 v = *(LDS const u32x4*)(sc + hf * (16 * 144) + row * 144 + (lane & 7) * 16);
        __builtin_amdgcn_raw_buffer_store_b128(v, rs, ((64 * g + 16 * pq + row) * SL + 128 * c + 64 * hf) * 2 + (lane & 7) * 16, 0, SC1);
      }
      __builtin_amdgcn_sched_barrier(0);
    }
    read_frags(s);
    __builtin_amdgcn_sched_barrier(0);
    if (HF == 0) { mma(); __builtin_amdgcn_sched_barrier(0); }
  }
  if (HF == 1) mma();
  __builtin_amdgcn_sched_barrier(0);
  STAMP(0, 3); CSTAMP(0, 61);
  const f32x2 Lr = {lam.r, lam.r}, Li = {lam.i, lam.i};
  f32x2 er = {0.f, 0.f}, ei = {0.f, 0.f};
#pragma unroll
  for (int t = 0; t < 8; ++t) {
    const f32x2 br_ = (t & 1) ? hi2(are[t >> 1]) : lo2(are[t >> 1]);
    const f32x2 bi_ = (t & 1) ? hi2(aim[t >> 1]) : lo2(aim[t >> 1]);
    const f32x2 nr = fma2(-Li, ei, fma2(Lr, er, br_));
    ei = fma2(Li, er, fma2(Lr, ei, bi_));
    er = nr;
  }
  const cf eA = {er[0], ei[0]};
  const cf e = cfma(l8, eA, cf{er[1], ei[1]});
  const int seg = 4 * HF + q;
  *(LDS f32x2*)(smem + G_E + (seg * 64 + pl) * 8) = f32x2{e.r, e.i};
  STAMP(0, 4);
  bar();
  STAMP(0, 5);
  {
#pragma unroll
    for (int m = 0; m < 4; ++m) asm volatile("" : "+v"(are[m]), "+v"(aim[m]));
    const f32x2 Cr = {coef2.r, coef2.r}, Ci = {coef2.i, coef2.i};
#pragma unroll
    for (int m = 0; m < 4; ++m) {
      const f32x2 r0 = lo2(are[m]), r1 = hi2(are[m]), i0 = lo2(aim[m]), i1 = hi2(aim[m]);
      are[m] = cat2(fma2(-Ci, i0, Cr * r0), fma2(-Ci, i1, Cr * r1));
      aim[m] = cat2(fma2(-Ci, r0, -Cr * i0), fma2(-Ci, r1, -Cr * i1));
      asm volatile("" : "+v"(are[m]), "+v"(aim[m]));
    }
  }
  cf eAc = cmul(coef2, eA), pwc = cmul(coef2, pw);
  asm volatile("" : "+v"(eAc.r), "+v"(eAc.i), "+v"(pwc.r), "+v"(pwc.i));
  STAMP(0, 7);
  bar();
  STAMP(0, 8);
  cf cin;
  {
    f32x2 v[2];
#pragma unroll
    for (int i = 0; i < 2; ++i) v[i] = *(LDS const f32x2*)(smem + G_X + (i * 64 + pl) * 8);
    const f32x2 pf = *(LDS const f32x2*)(smem + G_PF + (seg * 64 + pl) * 8);
    const f32x2 S = v[0] + v[1];
    cin = cfma(pwc, cf{S[0], S[1]}, cmul(coef2, cf{pf[0], pf[1]}));
  }
  const cf cinB = cfma(l8, cin, eAc);
  er = f32x2{cin.r, cinB.r}; ei = f32x2{-cin.i, -cinB.i};
  f32x2 hr[8], hi[8];
#pragma unroll
  for (int t = 0; t < 8; ++t) {
    const f32x2 br_ = (t & 1) ? hi2(are[t >> 1]) : lo2(are[t >> 1]);
    const f32x2 bi_ = (t & 1) ? hi2(aim[t >> 1]) : lo2(aim[t >> 1]);
    const f32x2 nr = fma2(Li, ei, fma2(Lr, er, br_));
    ei = fma2(-Li, er, fma2(Lr, ei, bi_));
    er = nr;
    hr[t] = er; hi[t] = ei;
  }
  u32x4 o[4];
#pragma unroll
  for (int t = 0; t < 4; ++t) {
    o[0][t] = pack_bf16(hr[2 * t][0], hr[2 * t + 1][0]);
    o[1][t] = pack_bf16(hr[2 * t][1], hr[2 * t + 1][1]);
    o[2][t] = pack_bf16(hi[2 * t][0], hi[2 * t + 1][0]);
    o[3][t] = pack_bf16(hi[2 * t][1], hi[2 * t + 1][1]);
  }
  STAMP(0, 9);
  {
    const __amdgpu_buffer_rsrc_t rs = mk_rsrc(p.ws + WS_HB, 2 * DM * SL * 2);
    const int hb = (((c * 2 + HF) * 2) * 9 + g) * 8192 + pq * 2048;
    const int p0 = (16 * q + col) * 16, p1 = 1024 + (16 * q + (col ^ 4)) * 16;
    __builtin_amdgcn_raw_buffer_store_b128(o[0], rs, hb + p0, 0, SC1);
    __builtin_amdgcn_raw_buffer_store_b128(o[1], rs, hb + p1, 0, SC1);
    __builtin_amdgcn_raw_buffer_store_b128(o[2], rs, hb + 9 * 8192 + p0, 0, SC1);
    __builtin_amdgcn_raw_buffer_store_b128(o[3], rs, hb + 9 * 8192 + p1, 0, SC1);
    wait_vm<0>();
    STAMP(0, 10);
    if (lane < 8)
      __hip_atomic_store((GLB u64*)(p.ws + WS_IN) + tile_block(2 * c + HF, lane) * 64 + IN_H + g * 4 + pq, FLAG_SET, RLX_AGENT);
  }
}

constexpr int S_CPY = 4160;
constexpr int S_X = 0;
constexpr int S_ST = 6 * 4 * S_CPY;
constexpr int S_Z = S_ST;
constexpr int S_ZSZ = (256 + SL) * 2;
constexpr int S_T = S_ST + 12 * 4608;
constexpr int S_TSZ = 1312;
constexpr int S_END = S_T + 6 * S_TSZ;
constexpr int S_CNT = 32768 + 8192;

DEV c2 cexp_k(float zl, float th, float tl, float k) {
  const float mag = __builtin_amdgcn_exp2f(zl * k);
  const float t = fmaf(tl, k, __builtin_amdgcn_fractf(th * k));
  return c2{mag * __builtin_amdgcn_cosf(t), mag * __builtin_amdgcn_sinf(t)};
}
#define PKM(d, a, b) "v_pk_mul_f32 " d ", " a ", " b " op_sel:[0,0] op_sel_hi:[0,1]\n\t"
#define PKF(d, a, b) "v_pk_fma_f32 " d ", " a ", " b ", " d " op_sel:[1,1,0] op_sel_hi:[1,0,1] neg_lo:[1,0,0]\n\t"
DEV void octet(LDS char* dst, c2 s0, c2 m1, c2 m2) {
  c2 v1, v2, v3, t0, t1, t2, t3;
  asm(PKM("%0", "%7", "%8") PKM("%3", "%7", "%9") PKF("%0", "%7", "%8") PKF("%3", "%7", "%9")
      PKM("%1", "%0", "%8") PKM("%4", "%0", "%9") PKF("%1", "%0", "%8") PKF("%4", "%0", "%9")
      PKM("%2", "%1", "%8") PKM("%5", "%1", "%9") PKF("%2", "%1", "%8") PKF("%5", "%1", "%9")
      PKM("%6", "%2", "%9") PKF("%6", "%2", "%9")
      : "=&v"(v1), "=&v"(v2), "=&v"(v3), "=&v"(t0), "=&v"(t1), "=&v"(t2), "=&v"(t3) : "v"(s0), "v"(m1), "v"(m2));
  *(LDS u32*)(dst + 0 * 288) = pack_bf16(s0[0], s0[1]);
  *(LDS u32*)(dst + 1 * 288) = pack_bf16(v1[0], v1[1]);
  *(LDS u32*)(dst + 2 * 288) = pack_bf16(v2[0], v2[1]);
  *(LDS u32*)(dst + 3 * 288) = pack_bf16(v3[0], v3[1]);
  *(LDS u32*)(dst + 4 * 288) = pack_bf16(t0[0], t0[1]);
  *(LDS u32*)(dst + 5 * 288) = pack_bf16(t1[0], t1[1]);
  *(LDS u32*)(dst + 6 * 288) = pack_bf16(t2[0], t2[1]);
  *(LDS u32*)(dst + 7 * 288) = pack_bf16(t3[0], t3[1]);
}
DEV u32 dup_lo(u32 a) { return __builtin_amdgcn_perm(a, a, 0x01000100u); }
DEV u32 dup_hi(u32 a) { return __builtin_amdgcn_perm(a, a, 0x03020302u); }
DEV u32 hi_lo(u32 a, u32 b) { return __builtin_amdgcn_alignbit(b, a, 16); }

DEV void role_s(const P& p, LDS char* smem, int sg, int w, int lane) {
  const int tid = w * 64 + lane;
  const int ch = w >> 1, hh = w & 1, d = 6 * sg + ch;
  const int n = d * 128 + 64 * hh + lane;
  const u16 r_ldt = p.ldtA[d], r_la = p.la[n], r_aim = p.aim[n], r_cre = p.creA[n], r_cim = p.cimA[n];
  u32 wb[2] = {0, 0};
  __builtin_amdgcn_sched_barrier(0);
#pragma unroll
  for (int i = 0; i < 2; ++i) {
    const int ln = i * 768 + tid, all = (sg % 12) * 864 + ln;
    if (ln < 864) wb[i] = *(const GLB u32*)((const char*)(all < 5184 ? p.bre : p.bim) + (size_t)(all < 5184 ? all : all - 5184) * 128);
  }
  __builtin_amdgcn_sched_barrier(0);
  STAMP(0, 1);
  f32x4 tab[3];
  [[maybe_unused]] u32 wc[2] = {0, 0};
  LDS char* st = smem + S_ST + w * 4608;
  {
    const float L2E = 1.44269504088896341f;
    const float dt = rbf(__builtin_amdgcn_exp2f(bf2f(r_ldt) * L2E));
    const float ar = -rbf(__builtin_amdgcn_exp2f(bf2f(r_la) * L2E));
    const float zl = ar * dt * L2E, zi = bf2f(r_aim) * dt;
    const float th = zi * 0.15915494309189535f;
    const float tl = fmaf(zi, 0.15915494309189535f, -th) + zi * 4.6017384e-10f;
    const c2 C = {2.0f * (bf2f(r_cre) * dt), 2.0f * (bf2f(r_cim) * dt)};
    const c2 w1 = cexp_k(zl, th, tl, 1.f), w8 = cexp_k(zl, th, tl, 8.f), w128 = cexp_k(zl, th, tl, 128.f);
    c2 w2, w4, w16, w32, w64, w256, w512, w1024, w2048;
    asm(PKM("%0", "%9", "%9") PKM("%2", "%10", "%10") PKM("%5", "%11", "%11") PKF("%0", "%9", "%9") PKF("%2", "%10", "%10") PKF("%5", "%11", "%11")
        PKM("%1", "%0", "%0") PKM("%3", "%2", "%2") PKM("%6", "%5", "%5") PKF("%1", "%0", "%0") PKF("%3", "%2", "%2") PKF("%6", "%5", "%5")
        PKM("%4", "%3", "%3") PKM("%7", "%6", "%6") PKF("%4", "%3", "%3") PKF("%7", "%6", "%6")
        PKM("%8", "%7", "%7") PKF("%8", "%7", "%7")
        : "=&v"(w2), "=&v"(w4), "=&v"(w16), "=&v"(w32), "=&v"(w64), "=&v"(w256), "=&v"(w512), "=&v"(w1024), "=&v"(w2048)
        : "v"(w1), "v"(w8), "v"(w128));
    c2 C128, C256, C512, C1024, C2048;
    asm(PKM("%0", "%5", "%6") PKM("%1", "%5", "%7") PKM("%2", "%5", "%8") PKM("%3", "%5", "%9") PKM("%4", "%5", "%10")
        PKF("%0", "%5", "%6") PKF("%1", "%5", "%7") PKF("%2", "%5", "%8") PKF("%3", "%5", "%9") PKF("%4", "%5", "%10")
        : "=&v"(C128), "=&v"(C256), "=&v"(C512), "=&v"(C1024), "=&v"(C2048)
        : "v"(C), "v"(w128), "v"(w256), "v"(w512), "v"(w1024), "v"(w2048));
#pragma unroll
    for (int i = 0; i < 2; ++i) {
      const int ln = i * 768 + tid, all = (sg % 12) * 864 + ln;
      if (ln < 864) wc[i] = *(const GLB u32*)((const char*)(all < 5184 ? p.cre : p.cim) + (size_t)(all < 5184 ? all : all - 5184) * 128);
    }
    STAMP(0, 2);
    LDS char* wr = st + 4 * lane;
    LDS const char* rd = st + (lane & 15) * 288 + (lane >> 4) * 16;
    bf16x8 B[4];
    const c2 w8c = {w8[0], -w8[1]}, w32c = {w32[0], -w32[1]}, w64c = {w64[0], -w64[1]};
    octet(wr, c2{1.f, 0.f}, w8c, w32c);
    octet(wr + 8 * 288, w64c, w8c, w32c);
    wave_fence();
#pragma unroll
    for (int ks = 0; ks < 4; ++ks) B[ks] = lds_frag(rd + 64 * ks);
    wave_fence();
#pragma unroll
    for (int mt = 0; mt < 3; ++mt) {
      if (mt == 0) { octet(wr, C, w1, w4); octet(wr + 8 * 288, C128, w1, w4); }
      if (mt == 1) { octet(wr, C256, w2, w128); octet(wr + 8 * 288, C512, w128, w4); }
      if (mt == 2) { octet(wr, C1024, w128, w512); *(LDS u32*)(wr + 8 * 288) = pack_bf16(C2048[0], C2048[1]); }
      wave_fence();
      f32x4 acc = {0, 0, 0, 0};
      bf16x8 A[4];
#pragma unroll
      for (int ks = 0; ks < 4; ++ks) A[ks] = lds_frag(rd + 64 * ks);
      __builtin_amdgcn_sched_barrier(0);
#pragma unroll
      for (int ks = 0; ks < 4; ++ks) acc = mfma16(A[ks], B[ks], acc);
      __builtin_amdgcn_sched_barrier(0);
      wave_fence();
      tab[mt] = acc;
    }
  }
  const int b = lane & 15, q = lane >> 4;
#pragma unroll
  for (int mt = 0; mt < 3; ++mt) *(LDS f32x4*)(st + mt * 1024 + lane * 16) = tab[mt];
  STAMP(0, 3);
  bar();
  {
    LDS const char* ot = smem + S_ST + (w ^ 1) * 4608 + lane * 16;
    LDS char* T = smem + S_T + ch * S_TSZ;
    if (hh == 0) {
      const f32x4 v0 = tab[0] + *(LDS const f32x4*)(ot), v2 = tab[2] + *(LDS const f32x4*)(ot + 2048);
      *(LDS u32x2*)(T + 2 * (128 * (q >> 1) + 8 * b + 4 * (q & 1))) = u32x2{pack_bf16(v0[0], v0[1]), pack_bf16(v0[2], v0[3])};
      const u32 a0 = pack_bf16(v2[0], v2[1]), a1 = pack_bf16(v2[2], v2[3]);
      if (q < 2) {
        LDS char* t3 = T + 2 * (512 + 64 * q + b);
        *(LDS u16*)(t3) = (u16)a0; *(LDS u16*)(t3 + 32) = (u16)(a0 >> 16);
        *(LDS u16*)(t3 + 64) = (u16)a1; *(LDS u16*)(t3 + 96) = (u16)(a1 >> 16);
      } else if (lane == 32) *(LDS u16*)(T + 2 * 640) = (u16)a0;
    } else {
      const f32x4 v1 = tab[1] + *(LDS const f32x4*)(ot + 1024);
      const u32 a0 = pack_bf16(v1[0], v1[1]), a1 = pack_bf16(v1[2], v1[3]);
      if (q < 2) *(LDS u32x2*)(T + 2 * (256 + 64 * q + 4 * b)) = u32x2{a0, a1};
      else {
        LDS char* t2 = T + 2 * (384 + 2 * b + (q - 2));
        *(LDS u16*)(t2) = (u16)a0; *(LDS u16*)(t2 + 64) = (u16)(a0 >> 16);
        *(LDS u16*)(t2 + 128) = (u16)a1; *(LDS u16*)(t2 + 192) = (u16)(a1 >> 16);
      }
    }
  }
  bar();
  STAMP(0, 4);
  u32x4 uv[2];
  {
    GLB u64* fl = (GLB u64*)(p.ws + WS_FL) + (8 * hh + (lane & 7)) * DM + d;
    asm volatile("" :: "v"(wc[0]), "v"(wc[1]), "v"(wb[0]), "v"(wb[1]));
    wait_flags(fl, true);
    const __amdgpu_buffer_rsrc_t rs = mk_rsrc(p.ws + WS_XT, DM * SL * 2);
#pragma unroll
    for (int v = 0; v < 2; ++v) uv[v] = __builtin_amdgcn_raw_buffer_load_b128(rs, (d * SL + 1024 * hh + 8 * (lane + 64 * v)) * 2, 0, SC1);
    if (lane < 8) __hip_atomic_store(fl, 0ull, RLX_AGENT);
  }
  STAMP(0, 5);
  {
    LDS const char* T = smem + S_T + ch * S_TSZ;
    u32 o[10];
    const u32 e512 = *(LDS const u16*)(T + 2 * 512);
    o[8] = o[9] = e512 | (e512 << 16);
    if (hh == 0) {
      const int off = lane < 16 ? 32 * lane : (lane < 32 ? 256 + 16 * lane : 512 + 8 * lane);
      u32x2 W[4];
#pragma unroll
      for (int i = 0; i < 4; ++i) W[i] = *(LDS const u32x2*)(T + off + 8 * i);
      if (lane < 16) {
#pragma unroll
        for (int i = 0; i < 8; ++i) o[i] = W[i >> 1][i & 1];
      } else if (lane < 32) {
#pragma unroll
        for (int i = 0; i < 4; ++i) {
          const u32 a = W[i >> 1][i & 1], nx = W[(i + 1) >> 1][(i + 1) & 1];
          o[2 * i] = dup_lo(a);
          o[2 * i + 1] = hi_lo(a, nx);
        }
      } else {
        const u32 w0 = W[0][0], w1 = W[0][1], w2 = W[1][0];
        o[0] = dup_lo(w0); o[1] = w0; o[2] = dup_hi(w0); o[3] = dup_lo(w1);
        o[4] = o[3]; o[5] = w1; o[6] = dup_hi(w1); o[7] = dup_lo(w2);
      }
    } else {
      const u32 w0 = *(LDS const u32*)(T + 1024 + 4 * lane), w1 = *(LDS const u32*)(T + 1028 + 4 * lane);
      o[0] = o[1] = dup_lo(w0); o[2] = w0; o[3] = o[4] = o[5] = dup_hi(w0); o[6] = o[7] = dup_lo(w1);
    }
    o[8] = (u32)__builtin_amdgcn_update_dpp((int)o[8], (int)o[0], 0x130, 0xf, 0xf, false);
    o[9] = (u32)__builtin_amdgcn_update_dpp((int)o[9], (int)o[1], 0x130, 0xf, 0xf, false);
    u32 a[9];
#pragma unroll
    for (int i = 0; i < 9; ++i) a[i] = hi_lo(o[i], o[i + 1]);
    LDS char* xb = smem + S_X + ch * 4 * S_CPY + 32 + 2 * (1024 * hh + 16 * lane);
    *(LDS u32x4*)(xb) = u32x4{o[0], o[1], o[2], o[3]};
    *(LDS u32x4*)(xb + 16) = u32x4{o[4], o[5], o[6], o[7]};
    *(LDS u32x4*)(xb + S_CPY) = u32x4{a[0], a[1], a[2], a[3]};
    *(LDS u32x4*)(xb + S_CPY + 16) = u32x4{a[4], a[5], a[6], a[7]};
    *(LDS u32x4*)(xb + 2 * S_CPY) = u32x4{o[1], o[2], o[3], o[4]};
    *(LDS u32x4*)(xb + 2 * S_CPY + 16) = u32x4{o[5], o[6], o[7], o[8]};
    *(LDS u32x4*)(xb + 3 * S_CPY) = u32x4{a[1], a[2], a[3], a[4]};
    *(LDS u32x4*)(xb + 3 * S_CPY + 16) = u32x4{a[5], a[6], a[7], a[8]};
    if (hh == 0 && lane == 0) {
      LDS char* hb = smem + S_X + ch * 4 * S_CPY;
      const u32x4 z = {0, 0, 0, 0};
      *(LDS u32x4*)(hb) = z;
      *(LDS u32x4*)(hb + 16) = z;
      *(LDS u32x4*)(hb + S_CPY) = z;
      *(LDS u32x4*)(hb + S_CPY + 16) = u32x4{0, 0, 0, o[0] << 16};
      *(LDS u32x4*)(hb + 2 * S_CPY) = z;
      *(LDS u32x4*)(hb + 2 * S_CPY + 16) = u32x4{0, 0, 0, o[0]};
      *(LDS u32x4*)(hb + 3 * S_CPY) = z;
      *(LDS u32x4*)(hb + 3 * S_CPY + 16) = u32x4{0, 0, o[0] << 16, a[0]};
    }
    if (tid < 6 * 32) *(LDS u32x4*)(smem + S_Z + (tid >> 5) * S_ZSZ + (tid & 31) * 16) = u32x4{0, 0, 0, 0};
    STAMP(0, 6);
    LDS char* z = smem + S_Z + ch * S_ZSZ + 512 + (1024 * hh + 8 * lane) * 2;
    *(LDS u32x4*)z = uv[0];
    *(LDS u32x4*)(z + 1024) = uv[1];
  }
  bar();
  STAMP(0, 7);
  f32x4 acc[8];
#pragma unroll
  for (int i = 0; i < 8; ++i) acc[i] = f32x4{0, 0, 0, 0};
  {
    const int kq = q, qq = b >> 2, pp = b & 3;
    LDS const char* fb = smem + S_X + (ch * 4 + ((4 - qq) & 3)) * S_CPY +
                         2 * (16 + 16 * (kq >> 1) - 8 * (kq & 1) + 4 * pp - (qq ? 4 : 0)) + 64 * hh;
    LDS const char* gb = smem + S_Z + ch * S_ZSZ + 512 + 32 * (b - (kq >> 1)) + 16 * (kq & 1) - 64 * hh;
#pragma unroll 1
    for (int it = 0; it < 4; ++it) {
      bf16x8 F[8], G[8];
#pragma unroll
      for (int s = 0; s < 8; ++s) {
        F[s] = tr_frag(fb + 512 * s, fb + 512 * s - 8);
        G[s] = lds_frag(gb + 512 * s);
      }
      __builtin_amdgcn_sched_barrier(0);
#pragma unroll
      for (int t = 0; t < 8; ++t)
#pragma unroll
        for (int s = 0; s <= t; ++s) acc[t] = mfma16(F[s], G[t - s], acc[t]);
      fb += 128;
      gb -= 128;
    }
  }
  __builtin_amdgcn_sched_barrier(0);
  STAMP(0, 8);
  bar();
  {
    LDS char* ex = smem + S_X + 49152 + w * 4096 + lane * 16;
#pragma unroll
    for (int i = 0; i < 4; ++i) {
      const f32x4 lo = acc[i], hi = acc[4 + i];
      *(LDS f32x4*)(ex + i * 1024) = hh ? +lo : +hi;
    }
    if (tid == 0) *(LDS u32*)(smem + S_CNT) = 0;
  }
  bar();
  u32x2 yo[4];
  {
    LDS const char* ey = smem + S_X + 49152 + (w ^ 1) * 4096 + lane * 16;
#pragma unroll
    for (int i = 0; i < 4; ++i) {
      const f32x4 lo = acc[i], hi = acc[4 + i];
      const f32x4 y = (hh ? +hi : +lo) + *(LDS const f32x4*)(ey + i * 1024);
      yo[i] = u32x2{pack_bf16(y[0], y[1]), pack_bf16(y[2], y[3])};
    }
  }
  bar();
  {
    const __amdgpu_buffer_rsrc_t rs = mk_rsrc(p.ws + WS_AT, DM * SL * 2);
    const int dst = (d * SL + 1024 * hh + 16 * (lane & 15) + 4 * (lane >> 4)) * 2;
#pragma unroll
    for (int i = 0; i < 4; ++i) __builtin_amdgcn_raw_buffer_store_b64(yo[i], rs, dst + 512 * i, 0, SC1);
  }
  STAMP(0, 9);
}
DEV void s_signal(const P& p, LDS char* smem, int sg, int lane) {
  u32 old = 0;
  if (lane == 0) old = __hip_atomic_fetch_add((LDS u32*)(smem + S_CNT), 1u, __ATOMIC_RELAXED, __HIP_MEMORY_SCOPE_WORKGROUP);
  if (__builtin_amdgcn_readfirstlane(old) == 11) {
    const int lt = lane & 31, second = lane >> 5;
    const int dd = 6 * sg + (second ? 5 : 0);
    const int idx = dd < 480 ? dd / 96 : 5 + (dd - 480) / 32;
    const int dfirst = idx < 5 ? 96 * idx : 480 + 32 * (idx - 5);
    __hip_atomic_store((GLB u64*)(p.ws + WS_IN) + tile_block(lt, idx) * 64 + IN_A + (sg - dfirst / 6), FLAG_SET, RLX_AGENT);
  }
}

DEV void role_gate(const P& p, int rb, int w, int lane, u64 pc) {
  u32 pf0 = 0, pf1 = 0;
  const int tid = w * 64 + lane;
  const int sub = lane & 7;
  u32x4 wv[9];
#pragma unroll
  for (int i = 0; i < 9; ++i) wv[i] = *(const u32x4*)((const char*)p.gw + (i * 8 + sub) * 16);
  for (int r = w * 8 + (lane >> 3); r < 128; r += 96) {
    const int l = rb * 128 + r;
    float s = 0.f;
    u32x4 xv[9];
#pragma unroll
    for (int i = 0; i < 9; ++i) xv[i] = *(const u32x4*)((const char*)p.x + (size_t)l * 1152 + (i * 8 + sub) * 16);
#pragma unroll
    for (int i = 0; i < 9; ++i) {
#pragma unroll
      for (int k = 0; k < 4; ++k) {
        const u32 pr = pack_bf16(bflo(xv[i][k]) * bflo(wv[i][k]), bfhi(xv[i][k]) * bfhi(wv[i][k]));
        s += bflo(pr);
        s += bfhi(pr);
      }
    }
    s += __shfl_xor(s, 1); s += __shfl_xor(s, 2); s += __shfl_xor(s, 4);
    const float gv = rbf(1.0f / (1.0f + expf(-rbf(s))));
    if (sub == 0)
      __hip_atomic_store((GLB u64*)(p.ws + WS_G) + l, ((u64)__float_as_uint(rbf(1.0f - gv)) << 32) | __float_as_uint(gv), RLX_AGENT);
  }
  asm volatile("" :: "v"(pf0), "v"(pf1));
  wait_vm<0>();
  bar();
  if (w == 0 && lane < 16)
    __hip_atomic_store((GLB u64*)(p.ws + WS_IN) + tile_block(2 * rb + (lane >> 3), lane & 7) * 64 + IN_G, FLAG_SET, RLX_AGENT);
}

constexpr int H_C = 32768, H_CS = 12288;
DEV int h_slot(int i) { return i < 4 ? i * 8192 : 131072 + (i - 4) * 8192; }
constexpr int H_AT = H_C + 2 * H_CS;
constexpr int H_XT = H_C + 3 * H_CS;
constexpr int H_G = H_C + 4 * H_CS;
constexpr int H_OUT = H_C + 5 * H_CS;
constexpr int K_LDS = 163840;

constexpr int h_wait(int s, int NDT) {
  int n = 0;
  int need = 0;
  for (int k = 0; k < 4; ++k) { n += 2; if (k == s) need = n; }
  for (int t = 0; t < s; ++t) {
    if (t == 0) { n += 2; if (s == 4) need = n; n += 2; if (s == 5) need = n; }
    else if (t == 1) { n += 2; if (s == 6) need = n; n += 2; if (s == 7) need = n; }
    else if (t <= 11) { n += 2 + NDT; if (s == t + 6) need = n; if (t == 2) n += 1; }
    else if (t == 12 || t == 13) n += NDT;
    else if (t == 14) n += 1;
  }
  return s == 17 ? 0 : n - need;
}
template <int S, int NDT, class F> DEV void h_steps(F&& after, u64& fv, char* wsp) {
  if constexpr (S == 9) asm volatile("s_waitcnt vmcnt(%1)" : "+v"(fv) : "n"(h_wait(S, NDT)) : "memory");
  else wait_vm<h_wait(S, NDT)>();
  LSTAMP2(1, 16 + S);
  bar();
  after(std::integral_constant<int, S>{});
  if constexpr (S < 17) h_steps<S + 1, NDT>(after, fv, wsp);
}
DEV int p2_rot(int d0) { const int idx = d0 < 480 ? d0 / 96 : 5 + (d0 - 480) / 32; return (idx * 18) >> 3; }

template <int NDT, class F>
DEV void p2_loader(const P& p, LDS char* smem, int lt, int d0, int lw, int lane, bool scratch_barrier, F&& pre_poll) {
  __builtin_amdgcn_s_setprio(3);
  const char* hb = p.ws + WS_HB + (size_t)lt * 18 * 8192 + (2 * lw) * 1024 + lane * 16;
  constexpr int W = 32 * NDT;
  const int l0 = 64 * lt, rot = p2_rot(d0);
  u32 co[NDT];
#pragma unroll
  for (int i = 0; i < NDT; ++i) {
    const int row = 8 * (NDT * lw + i) + (lane >> 3), cp = lane & 7;
    co[i] = (u32)(d0 + row) * 1152u + ((cp ^ (row & 7)) << 4);
  }
  auto issue_c = [&](int s) {
    LDS char* buf = smem + H_C + (s & 7) * H_CS + NDT * lw * 1024;
    int ks = s + rot; ks = ks >= 18 ? ks - 18 : ks;
    const char* cb = ks < 9 ? (const char*)p.cre + ks * 128 : (const char*)p.cim + (ks - 9) * 128;
#pragma unroll
    for (int i = 0; i < NDT; ++i) dma16<0>(cb + co[i], buf + i * 1024);
  };
  auto issue_h = [&](int s) {
    LDS char* buf = smem + h_slot(s & 7) + 2 * lw * 1024;
    int ks = s + rot; ks = ks >= 18 ? ks - 18 : ks;
#pragma unroll
    for (int i = 0; i < 2; ++i) dma16<SC1>(hb + ks * 8192 + i * 1024, buf + i * 1024);
  };
  GLB u64* inb = (GLB u64*)(p.ws + WS_IN) + blockIdx.x * 64;
  const int nA = (d0 + W - 1) / 6 - d0 / 6 + 1;
  const bool need = lane == IN_G || (lane >= IN_A && lane < IN_A + nA);
  if (!scratch_barrier) {
#pragma unroll 1
    for (int s = 0; s < 8; ++s) issue_c(s);
  }
  pre_poll();
  { [[maybe_unused]] const int w = lw + 8; STAMP(1, 1); }
  wait_flags(inb + IN_H + (lane < 36 ? lane : 0), true);
  { [[maybe_unused]] const int w = lw + 8; STAMP(1, 2); }
#pragma unroll
  for (int s = 0; s < 4; ++s) issue_h(s);
  { [[maybe_unused]] const int w = lw + 8; STAMP(1, 3); }
  u64 fv = 0;
  h_steps<0, NDT>([&](auto S_) {
    constexpr int S = decltype(S_)::value;
    if constexpr (S == 0) { issue_h(4); issue_h(5); }
    if constexpr (S == 1) { issue_h(6); issue_h(7); }
    if constexpr (S >= 2 && S <= 11) { issue_h(S + 6); issue_c(S + 6); }
    if constexpr (S == 2)
      asm volatile("global_load_dwordx2 %0, %1, off sc1" : "=v"(fv) : "v"(inb + lane) : "memory");
    if constexpr (S == 9) { if (!__all(!need || fv == FLAG_SET)) wait_flags(inb + lane, need); }
    if constexpr (S == 12) {
#pragma unroll
      for (int i = 0; i < NDT; ++i) {
        const int pc = NDT * lw + i;
        dma16<SC1>(p.ws + WS_AT + (size_t)(d0 + 8 * pc + (lane >> 3)) * 4096 + l0 * 2 + (lane & 7) * 16, smem + H_AT + pc * 1024);
      }
    }
    if constexpr (S == 13) {
#pragma unroll
      for (int i = 0; i < NDT; ++i) {
        const int pc = NDT * lw + i, id = pc * 64 + lane, row = id / (4 * NDT), qq = id - row * (4 * NDT);
        dma16<0>((const char*)p.x + (size_t)(l0 + row) * 1152 + d0 * 2 + qq * 16, smem + H_XT + pc * 1024);
      }
    }
    if constexpr (S == 14) { if (lane < 8) dma16<SC1>(p.ws + WS_G + (size_t)l0 * 8 + lw * 128 + lane * 16, smem + H_G + lw * 128); }
  }, fv, p.ws);
}

template <int NDT, int STAG>
DEV void p2_compute(const P& p, LDS char* smem, int lt0, int d0, int w, int lane) {
  const int lh = (w >> 1) & 1, kh = w & 1, dg = w >> 2;
  const int r = lane & 15, kq = lane >> 4;
  const int lt = 2 * lh + kh, l0 = 64 * lt0;
  constexpr int W = 32 * NDT;
  u32x2 da[NDT], db[NDT];
#pragma unroll
  for (int t = 0; t < NDT; ++t) {
    const int d = d0 + 16 * (NDT * dg + t) + 4 * kq;
    da[t] = *(const u32x2*)(p.dskA + d);
    db[t] = *(const u32x2*)(p.dskB + d);
  }
  GLB u64* inb = (GLB u64*)(p.ws + WS_IN) + blockIdx.x * 64;

  f32x4 acc[2][NDT];
#pragma unroll
  for (int i = 0; i < 2; ++i)
#pragma unroll
    for (int t = 0; t < NDT; ++t) acc[i][t] = f32x4{0, 0, 0, 0};
  int ho[2];
#pragma unroll
  for (int u = 0; u < 2; ++u) {
    const int k = 32 * kh + 8 * kq + 4 * u + (r >> 2), i = (r & 3) >> 1;
    ho[u] = (2 * (k >> 4) + i) * 1024 + (32 * lh + ((k & 15) ^ (4 * i))) * 16 + (r & 1) * 8;
  }
  const int co = H_C + (16 * NDT * dg + r) * 128 + (((4 * kh + kq) ^ (r & 7)) << 4);
  constexpr int NREG = 0, NRING = 18 - NREG;
  bf16x8 cr[NREG ? NREG : 1][NDT];
  if constexpr (NREG > 0) {
    const int rot = p2_rot(d0);
#pragma unroll
    for (int i = 0; i < NREG; ++i) {
      int ks = NRING + i + rot; ks = ks >= 18 ? ks - 18 : ks;
      const char* cb = (ks < 9 ? (const char*)p.cre + ks * 128 : (const char*)p.cim + (ks - 9) * 128) +
                       (size_t)(d0 + 16 * NDT * dg + r) * 1152 + (4 * kh + kq) * 16;
#pragma unroll
      for (int t = 0; t < NDT; ++t) cr[i][t] = *(const GLB bf16x8*)(cb + t * 16 * 1152);
    }
  }
  bf16x8 hb[2], ca[NDT];
#pragma unroll
  for (int i = 0; i < 2; ++i) hb[i] = bf16x8{};
#pragma unroll
  for (int t = 0; t < NDT; ++t) ca[t] = bf16x8{};
  auto mma_with = [&](const bf16x8 (&cc)[NDT]) {
#pragma unroll
    for (int t = 0; t < NDT; ++t) {
      acc[0][t] = mfma16(cc[t], hb[0], acc[0][t]);
      acc[1][t] = mfma16(cc[t], hb[1], acc[1][t]);
    }
    __builtin_amdgcn_sched_barrier(0);
  };
  auto read_h = [&](int s) {
    LDS const char* hbuf = smem + h_slot(s & 7);
#pragma unroll
    for (int i = 0; i < 2; ++i) hb[i] = tr_frag(hbuf + ho[0] + i * 256, hbuf + ho[1] + i * 256);
  };
  STAMP(1, 1);
#pragma unroll 1
  for (int s = 0; s < NRING; ++s) {
    bar();
    if (s == 0) STAMP(1, 2);
    LSTAMP(1, 16 + s);
    if (STAG) mma_with(ca);
    read_h(s);
    LDS const char* cbuf = smem + (s & 7) * H_CS;
#pragma unroll
    for (int t = 0; t < NDT; ++t) ca[t] = lds_frag(cbuf + co + t * 2048);
    __builtin_amdgcn_sched_barrier(0);
    if (!STAG) mma_with(ca);
  }
#pragma unroll
  for (int i = 0; i < NREG; ++i) {
    bar();
    if (STAG) { if (i == 0) mma_with(ca); else mma_with(cr[i ? i - 1 : 0]); }
    read_h(NRING + i);
    __builtin_amdgcn_sched_barrier(0);
    if (!STAG) mma_with(cr[i]);
  }
  if (STAG) { if (NREG) mma_with(cr[NREG ? NREG - 1 : 0]); else mma_with(ca); }
  __builtin_amdgcn_sched_barrier(0);
  STAMP(1, 3);
  bar();
  if (w == 0) __hip_atomic_store(inb + lane, 0ull, RLX_AGENT);
  LDS char* ex = smem + w * (NDT * 1024) + lane * 16;
#pragma unroll
  for (int t = 0; t < NDT; ++t) {
    const f32x4 a0 = acc[0][t], a1 = acc[1][t];
    *(LDS f32x4*)(ex + t * 1024) = kh ? +a0 : +a1;
  }
  bar();
  LDS const char* ey = smem + (w ^ 1) * (NDT * 1024) + lane * 16;
  constexpr int OSTR = W * 4 + 16;
  const f32x2 gg = *(LDS const f32x2*)(smem + H_G + (16 * lt + r) * 8);
#pragma unroll
  for (int t = 0; t < NDT; ++t) {
    const int dt = NDT * dg + t;
    const f32x4 o = *(LDS const f32x4*)(ey + t * 1024);
    const f32x4 a0 = acc[0][t], a1 = acc[1][t];
    const f32x4 y = (kh ? +a1 : +a0) + o;
    const s16x4 av = tr_read(smem + H_AT + (16 * dt + 4 * kq + (r >> 2)) * 128 + lt * 32 + (r & 3) * 8);
    const u32x2 xv = *(LDS const u32x2*)(smem + H_XT + (16 * lt + r) * (W * 2) + (16 * dt + 4 * kq) * 2);
    f32x4 res;
#pragma unroll
    for (int i = 0; i < 4; ++i) {
      const u32 xw = xv[i >> 1], aw = da[t][i >> 1], bw = db[t][i >> 1];
      const float xf = (i & 1) ? bfhi(xw) : bflo(xw);
      const float sa = (i & 1) ? bfhi(aw) : bflo(aw);
      const float sb = (i & 1) ? bfhi(bw) : bflo(bw);
      const float yc = bf2f((u16)av[i]);
      const float a = rbf(yc + rbf(xf * sa));
      const float b = y[i] + rbf(xf * sb);
      res[i] = rbf(gg[0] * a) + gg[1] * b;
    }
    *(LDS f32x4*)(smem + H_OUT + (16 * lt + r) * OSTR + (16 * dt + 4 * kq) * 4) = res;
  }
  STAMP(1, 4);
  bar();
#pragma unroll
  for (int it = 0; it < NDT; ++it) {
    const int id = it * 512 + w * 64 + lane;
    const int row = id / (8 * NDT), pc = id - row * (8 * NDT);
    const f32x4 v = *(LDS const f32x4*)(smem + H_OUT + row * OSTR + pc * 16);
    __builtin_amdgcn_raw_buffer_store_b128(__builtin_bit_cast(u32x4, v), mk_rsrc(p.out, SL * DM * 4), ((l0 + row) * DM + d0 + pc * 4) * 4, 0, OUT_AUX);
  }
}

template <int NDT>
DEV void phase2(const P& p, LDS char* smem, int lt, int d0, int w, int lane, bool scratch_barrier, int sg) {
  STAMP(1, 0);
  if (w >= 8) {
    p2_loader<NDT>(p, smem, lt, d0, w - 8, lane, scratch_barrier, [&]() { if (NDT == 1) { wait_vm<8>(); s_signal(p, smem, sg, lane); } });
  } else {
    if (NDT == 1) { wait_vm<0>(); s_signal(p, smem, sg, lane); }
    if (w < 4) p2_compute<NDT, 0>(p, smem, lt, d0, w, lane);
    else p2_compute<NDT, 1>(p, smem, lt, d0, w, lane);
  }
  STAMP(1, 15);
}

__global__ __launch_bounds__(768, 3) void gated_mix_s4d_s5(P p) {
  extern __shared__ __attribute__((aligned(16))) char smem_raw[];
  LDS char* smem = (LDS char*)smem_raw;
  u64 pc;
  asm volatile("s_getpc_b64 %0" : "=s"(pc));
  u32 pf = 0;
#define PF_TOP 312
  if (threadIdx.x < PF_TOP)
    asm volatile("global_load_dword %0, %1, off" : "=v"(pf) : "v"((pc & ~255ull) + threadIdx.x * 128));
  const int w = __builtin_amdgcn_readfirstlane(threadIdx.x >> 6), lane = threadIdx.x & 63;
  const int xcd = blockIdx.x & 7, j = blockIdx.x >> 3;
  STAMP(0, 0);
  if (j >= 18 && j < 30) {
    const int sg = 12 * xcd + (j - 18);
    role_s(p, smem, sg, w, lane);
    STAMP(0, 15);
    phase2<1>(p, smem, 4 * xcd + (j - 18) / 3, 480 + 32 * ((j - 18) % 3), w, lane, false, sg);
  } else {
    const bool isg = j < 18;
    if (isg) {
      const int c = 2 * xcd + j / 9, g = j % 9;
      if (w >= 8) g_loader(p, smem, c, g, 96 * (j % 5), w - 8, lane);
      else if (w < 4) g_compute<0>(p, smem, c, g, w, lane, pf);
      else g_compute<1>(p, smem, c, g, w, lane, pf);
    } else {
      role_gate(p, 2 * xcd + (j - 30), w, lane, pc);
    }
    STAMP(0, 15);
    const int e = isg ? j : j - 12;
    phase2<3>(p, smem, 4 * xcd + e / 5, 96 * (e % 5), w, lane, isg, 0);
  }
  asm volatile("s_waitcnt vmcnt(0)" :: "v"(pf));
}

extern "C" void kernel_launch(void* const* d_in, const int* in_sizes, int n_in, void* d_out, int out_size,
                              void* d_ws, size_t ws_size, hipStream_t stream) {
  P a;
  a.x = (const u16*)d_in[0]; a.la = (const u16*)d_in[1]; a.aim = (const u16*)d_in[2];
  a.creA = (const u16*)d_in[3]; a.cimA = (const u16*)d_in[4]; a.ldtA = (const u16*)d_in[5]; a.dskA = (const u16*)d_in[6];
  a.lre = (const u16*)d_in[8]; a.lim = (const u16*)d_in[9]; a.bre = (const u16*)d_in[10]; a.bim = (const u16*)d_in[11];
  a.cre = (const u16*)d_in[12]; a.cim = (const u16*)d_in[13]; a.ldtB = (const u16*)d_in[14];
  a.dskB = (const u16*)d_in[15]; a.gw = (const u16*)d_in[16];
  a.ws = (char*)d_ws + WS_OFF; a.out = (float*)d_out;
  (void)hipFuncSetAttribute((const void*)gated_mix_s4d_s5, hipFuncAttributeMaxDynamicSharedMemorySize, K_LDS);
  gated_mix_s4d_s5<<<256, 768, K_LDS, stream>>>(a);
}
```

```cpp
#define HOLD_AT 8
#include <hip/hip_runtime.h>
#include <hip/hip_bf16.h>
#include <stdint.h>
#include <type_traits>

typedef unsigned short u16;
typedef unsigned int u32;
typedef unsigned long long u64;
typedef __attribute__((ext_vector_type(8))) __bf16 bf16x8;
typedef __attribute__((ext_vector_type(2))) __bf16 bf16x2;
typedef __attribute__((ext_vector_type(4))) short s16x4;
typedef __attribute__((ext_vector_type(4))) float f32x4;
typedef __attribute__((ext_vector_type(2))) float f32x2;
typedef __attribute__((ext_vector_type(4))) unsigned u32x4;
typedef __attribute__((ext_vector_type(2))) unsigned u32x2;
#define LDS __attribute__((address_space(3)))
#define GLB __attribute__((address_space(1)))
#define DEV __device__ __forceinline__
#define RLX_AGENT __ATOMIC_RELAXED, __HIP_MEMORY_SCOPE_AGENT

constexpr int SL = 2048;
constexpr int DM = 576;

constexpr size_t WS_HB = 0;
constexpr size_t WS_AT = WS_HB + (size_t)2 * DM * SL * 2;
constexpr size_t WS_G = WS_AT + (size_t)DM * SL * 2;
constexpr size_t WS_GRAN = WS_G + (size_t)SL * 8;
constexpr size_t WS_EP = WS_GRAN + (size_t)9 * 16 * 2 * 64 * 8;
constexpr size_t WS_FL = WS_EP + 4096;
constexpr size_t WS_XT = WS_FL + (size_t)16 * DM * 8;
constexpr size_t WS_IN = WS_XT + (size_t)DM * SL * 2;
constexpr u64 FLAG_SET = 0x9E3779B97F4A7C15ull;
constexpr u32 SPIN_CAP = 1u << 17;
constexpr int IN_H = 0, IN_G = 36, IN_A = 40;

constexpr size_t WS_ST = (size_t)16 << 20;
#define STAMP(kern, idx) do {} while (0)
#define CSTAMP(kern, idx) do {} while (0)
#define LSTAMP(kern, idx) do {} while (0)
#define LSTAMP2(kern, idx) do {} while (0)

#define HOLD(id) do { if (HOLD_AT == (id)) { while (__builtin_amdgcn_s_memrealtime() - g_tb < 4000) __builtin_amdgcn_s_sleep(2); } } while (0)
DEV float bf2f(u16 v) { return __uint_as_float((u32)v << 16); }
DEV float bflo(u32 v) { return __uint_as_float(v << 16); }
DEV float bfhi(u32 v) { return __uint_as_float(v & 0xffff0000u); }
DEV u32 pack_bf16(float lo, float hi) {
  f32x2 v = {lo, hi};
  bf16x2 b = __builtin_convertvector(v, bf16x2);
  return __builtin_bit_cast(u32, b);
}
DEV float rbf(float x) { return bflo(pack_bf16(x, x)); }
template <int N> DEV void wait_vm() { asm volatile("s_waitcnt vmcnt(%0)" ::"n"(N) : "memory"); }
DEV void bar() { asm volatile("s_waitcnt lgkmcnt(0)\n\ts_barrier" ::: "memory"); }
DEV void wave_fence() { __builtin_amdgcn_fence(__ATOMIC_ACQ_REL, "wavefront"); }
template <int AUX> DEV void dma16(const void* src, LDS char* dst) {
  __builtin_amdgcn_global_load_lds((const GLB u32*)src, (LDS u32*)dst, 16, 0, AUX);
}
constexpr int SC1 = 16;
#define POLL_SLEEP 1
#define OUT_AUX 16
#define WS_OFF 0
#define XT_WAIT 4
DEV bf16x8 lds_frag(LDS const char* p) { return *(LDS const bf16x8*)p; }
DEV s16x4 tr_read(LDS const char* a) { return __builtin_amdgcn_ds_read_tr16_b64_v4i16((LDS s16x4*)a); }
DEV bf16x8 tr_frag(LDS const char* a, LDS const char* b) {
  const s16x4 lo = tr_read(a), hi = tr_read(b);
  return __builtin_bit_cast(bf16x8, __builtin_shufflevector(lo, hi, 0, 1, 2, 3, 4, 5, 6, 7));
}
DEV f32x4 mfma16(bf16x8 a, bf16x8 b, f32x4 c) { return __builtin_amdgcn_mfma_f32_16x16x32_bf16(a, b, c, 0, 0, 0); }
DEV __amdgpu_buffer_rsrc_t mk_rsrc(const void* base, int bytes) {
  return __builtin_amdgcn_make_buffer_rsrc(const_cast<void*>(base), (short)0, bytes, 0x00020000);
}
struct cf { float r, i; };
DEV cf cmul(cf a, cf b) { return {a.r * b.r - a.i * b.i, a.r * b.i + a.i * b.r}; }
DEV cf cfma(cf a, cf b, cf c) { return {fmaf(a.r, b.r, fmaf(-a.i, b.i, c.r)), fmaf(a.r, b.i, fmaf(a.i, b.r, c.i))}; }
typedef f32x2 c2;
DEV c2 cm2(c2 a, c2 b) {
  c2 t, r;
  asm("v_pk_mul_f32 %0, %1, %2 op_sel:[0,0] op_sel_hi:[0,1]" : "=v"(t) : "v"(a), "v"(b));
  asm("v_pk_fma_f32 %0, %1, %2, %3 op_sel:[1,1,0] op_sel_hi:[1,0,1] neg_lo:[1,0,0]" : "=v"(r) : "v"(a), "v"(b), "v"(t));
  return r;
}
DEV f32x2 fma2(f32x2 a, f32x2 b, f32x2 c) { return __builtin_elementwise_fma(a, b, c); }
DEV f32x2 lo2(f32x4 v) { return __builtin_shufflevector(v, v, 0, 1); }
DEV f32x2 hi2(f32x4 v) { return __builtin_shufflevector(v, v, 2, 3); }
DEV f32x4 cat2(f32x2 a, f32x2 b) { return __builtin_shufflevector(a, b, 0, 1, 2, 3); }
DEV void wait_flags(GLB u64* word, bool need) {
  for (u32 spin = 0;; ++spin) {
    const u64 v = __hip_atomic_load(word, RLX_AGENT);
    if (__all(!need || v == FLAG_SET) || spin > SPIN_CAP) break;
    __builtin_amdgcn_s_sleep(POLL_SLEEP);
  }
}
DEV int tile_block(int lt, int idx) {
  const int xcd = lt >> 2, a = lt & 3;
  int j;
  if (idx < 5) { const int e = 5 * a + idx; j = e < 18 ? e : e + 12; }
  else j = 18 + 3 * a + (idx - 5);
  return (j << 3) | xcd;
}

struct P {
  const u16 *x, *la, *aim, *creA, *cimA, *ldtA, *dskA;
  const u16 *lre, *lim, *bre, *bim, *cre, *cim, *ldtB, *dskB, *gw;
  char* ws;
  float* out;
};

constexpr int G_NB = 4, G_STAGE = 32768;
constexpr int G_E = 153600;
constexpr int G_PF = G_E + 4096;
constexpr int G_L = G_PF + 4096;
constexpr int G_X = G_L + 1024;
constexpr int G_T = G_NB * G_STAGE;
static_assert(G_X + 1024 <= 163840 && G_T + 4 * 32 * 144 <= G_E, "role G scratch");

DEV void g_loader(const P& p, LDS char* smem, int c, int g, int d0, int lw, int lane, u64 g_tb) {
  [[maybe_unused]] const int w = lw + 8;
  __builtin_amdgcn_s_setprio(3);
  GLB u32* ep = (GLB u32*)(p.ws + WS_EP + g * 256);
  GLB u64* gran = (GLB u64*)(p.ws + WS_GRAN) + (size_t)g * 16 * 128;
  const u32 tag = __hip_atomic_load(ep, RLX_AGENT) + 1u;
  u32 xo[4], bo[4];
#pragma unroll
  for (int i = 0; i < 4; ++i) {
    const int rho = 8 * (4 * lw + i) + (lane >> 3);
    const int hf = rho >> 6, m = (rho >> 4) & 3, r = rho & 15;
    const int tau = 64 * hf + 16 * (r >> 2) + 8 * (r & 1) + 2 * m + ((r >> 1) & 1);
    const int ch = (lane & 7) ^ (rho & 7);
    xo[i] = (u32)(128 * c + tau) * 1152u + ch * 16;
    const int pp = 64 * g + 16 * (rho >> 5) + r;
    bo[i] = (u32)pp * 1152u + ch * 16;
  }
  auto issue = [&](int s) {
    LDS char* buf = smem + (s & (G_NB - 1)) * G_STAGE + lw * 4096;
    int kb = g + s; kb = (kb >= 9 ? kb - 9 : kb) * 128;
#pragma unroll
    for (int i = 0; i < 4; ++i) dma16<0>((const char*)p.x + (xo[i] + kb), buf + i * 1024);
#pragma unroll
    for (int i = 0; i < 4; ++i) {
      const char* base = (const char*)((((4 * lw + i) >> 1) & 1) ? p.bim : p.bre);
      dma16<0>(base + (bo[i] + kb), buf + 16384 + i * 1024);
    }
  };
#pragma unroll
  for (int s = 0; s < G_NB - 1; ++s) issue(s);
#pragma unroll 1
  for (int s = 0; s < 6; ++s) { wait_vm<16>(); LSTAMP(0, 16 + s); bar(); issue(s + G_NB - 1); LSTAMP(0, 32 + s); }
  wait_vm<16>(); bar();
  wait_vm<8>(); bar();
  wait_vm<0>(); bar();
  STAMP(0, 1);
  if (lw < 2) {
    bar();
    u32 co[6];
#pragma unroll
    for (int i = 0; i < 6; ++i) {
      const int row = 8 * (6 * lw + i) + (lane >> 3), cp = lane & 7;
      co[i] = (u32)(d0 + row) * 1152u + ((cp ^ (row & 7)) << 4);
    }
    const int rot = ((d0 / 96) * 18) >> 3;
#pragma unroll 1
    for (int s = 0; s < 8; ++s) {
      LDS char* buf = smem + 32768 + s * 12288 + 6 * lw * 1024;
      const int ks = s + rot;
      const char* cb = ks < 9 ? (const char*)p.cre + ks * 128 : (const char*)p.cim + (ks - 9) * 128;
#pragma unroll
      for (int i = 0; i < 6; ++i) dma16<0>(cb + co[i], buf + i * 1024);
    }
    bar();
    return;
  }
  bar();
  STAMP(0, 2);
  if (lw == 3) {
    const f32x2 lv = *(LDS const f32x2*)(smem + G_L + lane * 8);
    const cf l16 = {lv[0], lv[1]};
    f32x2 ev[8];
#pragma unroll
    for (int s = 0; s < 8; ++s) ev[s] = *(LDS const f32x2*)(smem + G_E + (s * 64 + lane) * 8);
    cf tot = {0.f, 0.f};
    cf pre[8];
#pragma unroll
    for (int s = 0; s < 8; ++s) { pre[s] = tot; tot = cfma(l16, tot, cf{ev[s][0], ev[s][1]}); }
    HOLD(6);
    if (c < 15) {
      __hip_atomic_store(gran + c * 128 + lane, ((u64)tag << 32) | __float_as_uint(tot.r), RLX_AGENT);
      __hip_atomic_store(gran + c * 128 + 64 + lane, ((u64)tag << 32) | __float_as_uint(tot.i), RLX_AGENT);
    }
#pragma unroll
    for (int s = 0; s < 8; ++s) *(LDS f32x2*)(smem + G_PF + (s * 64 + lane) * 8) = f32x2{pre[s].r, pre[s].i};
  }
  const int par = lw - 2;
  const f32x2 lv = *(LDS const f32x2*)(smem + G_L + 512 + lane * 8);
  const cf l128 = {lv[0], lv[1]};
  const cf l256 = cmul(l128, l128);
  cf acc = {0.f, 0.f};
  if (par < c) {
    const int nc = (c - par + 1) >> 1;
    u64 a[8], b[8];
    for (u32 spin = 0;; ++spin) {
      bool ok = true;
#pragma unroll
      for (int i = 0; i < 8; ++i) {
        const int cc = par + 2 * (i < nc ? i : 0);
        a[i] = __hip_atomic_load(gran + cc * 128 + lane, RLX_AGENT);
        b[i] = __hip_atomic_load(gran + cc * 128 + 64 + lane, RLX_AGENT);
      }
#pragma unroll
      for (int i = 0; i < 8; ++i) ok = ok && (u32)(a[i] >> 32) == tag && (u32)(b[i] >> 32) == tag;
      if (__all(ok) || spin > SPIN_CAP) break;
      __builtin_amdgcn_s_sleep(1);
    }
#pragma unroll
    for (int i = 0; i < 8; ++i)
      if (i < nc) acc = cfma(l256, acc, cf{__uint_as_float((u32)a[i]), __uint_as_float((u32)b[i])});
    if (c - 1 - (par + 2 * (nc - 1))) acc = cmul(acc, l128);
  }
  HOLD(7);
  *(LDS f32x2*)(smem + G_X + (par * 64 + lane) * 8) = f32x2{acc.r, acc.i};
  STAMP(0, 3);
  bar();
  if (c == 15 && lw == 2 && lane == 0) __hip_atomic_store(ep, tag, RLX_AGENT);
}

template <int HF>
DEV void g_compute(const P& p, LDS char* smem, int c, int g, int w, int lane, u32& pf, u64 g_tb) {
  const int pq = w & 3, col = lane & 15, q = lane >> 4;
  const int pl = 16 * pq + col, pp = 64 * g + pl;
  cf lam, coef2;
  {
    const float lre = bf2f(p.lre[pp]), lim = bf2f(p.lim[pp]);
    const float dt = rbf(expf(bf2f(p.ldtB[pp])));
    const float zr = dt * lre, zi = dt * lim;
    const float er = expf(zr);
    float sn, cs;
    sincosf(zi, &sn, &cs);
    lam = {er * cs, er * sn};
    const float nr = lam.r - 1.0f, ni = lam.i;
    const float den = 1.0f / (lre * lre + lim * lim);
    coef2 = {2.0f * (nr * lre + ni * lim) * den, 2.0f * (ni * lre - nr * lim) * den};
  }
  cf l8 = cmul(lam, lam); l8 = cmul(l8, l8); l8 = cmul(l8, l8);
  const cf l16 = cmul(l8, l8);
  const cf l32 = cmul(l16, l16), l64 = cmul(l32, l32);
  if (HF == 0 && q == 0) {
    const cf l128 = cmul(l64, l64);
    *(LDS f32x2*)(smem + G_L + pl * 8) = f32x2{l16.r, l16.i};
    *(LDS f32x2*)(smem + G_L + 512 + pl * 8) = f32x2{l128.r, l128.i};
  }
  cf pw = HF ? l64 : cf{1.f, 0.f};
  { const cf t = cmul(pw, l16); if (q & 1) pw = t; }
  { const cf t = cmul(pw, l32); if (q & 2) pw = t; }
  asm volatile("" : "+v"(lam.r), "+v"(lam.i), "+v"(coef2.r), "+v"(coef2.i), "+v"(l8.r), "+v"(l8.i), "+v"(pw.r), "+v"(pw.i));
  f32x4 are[4], aim[4];
#pragma unroll
  for (int m = 0; m < 4; ++m) { are[m] = f32x4{0, 0, 0, 0}; aim[m] = f32x4{0, 0, 0, 0}; }
  const int o0 = col * 128 + (((0 + q) ^ (col & 7)) << 4);
  const int o1 = col * 128 + (((4 + q) ^ (col & 7)) << 4);
  bf16x8 br[2], bi[2], a[2][4];
  auto read_frags = [&](int s) {
    LDS const char* buf = smem + (s & (G_NB - 1)) * G_STAGE;
    LDS const char* ab = buf + HF * 8192;
    LDS const char* bb = buf + 16384 + pq * 4096;
#pragma unroll
    for (int ks = 0; ks < 2; ++ks) {
      const int o = ks ? o1 : o0;
      br[ks] = lds_frag(bb + o); bi[ks] = lds_frag(bb + 2048 + o);
#pragma unroll
      for (int m = 0; m < 4; ++m) a[ks][m] = lds_frag(ab + m * 2048 + o);
    }
  };
  auto mma = [&]() {
#pragma unroll
    for (int ks = 0; ks < 2; ++ks)
#pragma unroll
      for (int m = 0; m < 4; ++m) {
        are[m] = mfma16(a[ks][m], br[ks], are[m]);
        aim[m] = mfma16(a[ks][m], bi[ks], aim[m]);
      }
  };
  STAMP(0, 1);
#pragma unroll
  for (int s = 0; s < 9; ++s) {
    if (HF == 1 && s == XT_WAIT) {
      wait_vm<0>();
      if (lane < 16) __hip_atomic_store((GLB u64*)(p.ws + WS_FL) + c * DM + 64 * g + 16 * pq + lane, FLAG_SET, RLX_AGENT);
    }
    bar();
    if (s == 0) { STAMP(0, 2); CSTAMP(0, 60); }
    if (HF == 1 && s > 0) { mma(); __builtin_amdgcn_sched_barrier(0); }
    if (HF == 1 && s == 0) {
      const int rr = 4 * q + (col >> 2);
      LDS const char* tb = smem + rr * 128 + (((2 * pq + ((col & 3) >> 1)) ^ (rr & 7)) << 4) + (col & 1) * 8;
      LDS char* sc = smem + G_T + pq * (32 * 144);
#pragma unroll
      for (int hf = 0; hf < 2; ++hf) {
        u32x2 t[4];
#pragma unroll
        for (int m = 0; m < 4; ++m) t[m] = __builtin_bit_cast(u32x2, tr_read(tb + hf * 8192 + m * 2048));
        u32x4 oa, ob;
#pragma unroll
        for (int m = 0; m < 4; ++m) {
          oa[m] = __builtin_amdgcn_perm(t[m][1], t[m][0], 0x05040100u);
          ob[m] = __builtin_amdgcn_perm(t[m][1], t[m][0], 0x07060302u);
        }
        *(LDS u32x4*)(sc + hf * (16 * 144) + col * 144 + q * 32) = oa;
        *(LDS u32x4*)(sc + hf * (16 * 144) + col * 144 + q * 32 + 16) = ob;
      }
      wave_fence();
      const __amdgpu_buffer_rsrc_t rs = mk_rsrc(p.ws + WS_XT, DM * SL * 2);
#pragma unroll
      for (int u = 0; u < 4; ++u) {
        const int row = (lane >> 3) + 8 * (u & 1), hf = u >> 1;
        const u32x4 v = *(LDS const u32x4*)(sc + hf * (16 * 144) + row * 144 + (lane & 7) * 16);
        __builtin_amdgcn_raw_buffer_store_b128(v, rs, ((64 * g + 16 * pq + row) * SL + 128 * c + 64 * hf) * 2 + (lane & 7) * 16, 0, SC1);
      }
      __builtin_amdgcn_sched_barrier(0);
    }
    read_frags(s);
    __builtin_amdgcn_sched_barrier(0);
    if (HF == 0) { mma(); __builtin_amdgcn_sched_barrier(0); }
  }
  if (HF == 1) mma();
  __builtin_amdgcn_sched_barrier(0);
  STAMP(0, 3); CSTAMP(0, 61);
  HOLD(2);
  const f32x2 Lr = {lam.r, lam.r}, Li = {lam.i, lam.i};
  f32x2 er = {0.f, 0.f}, ei = {0.f, 0.f};
#pragma unroll
  for (int t = 0; t < 8; ++t) {
    const f32x2 br_ = (t & 1) ? hi2(are[t >> 1]) : lo2(are[t >> 1]);
    const f32x2 bi_ = (t & 1) ? hi2(aim[t >> 1]) : lo2(aim[t >> 1]);
    const f32x2 nr = fma2(-Li, ei, fma2(Lr, er, br_));
    ei = fma2(Li, er, fma2(Lr, ei, bi_));
    er = nr;
  }
  const cf eA = {er[0], ei[0]};
  const cf e = cfma(l8, eA, cf{er[1], ei[1]});
  const int seg = 4 * HF + q;
  *(LDS f32x2*)(smem + G_E + (seg * 64 + pl) * 8) = f32x2{e.r, e.i};
  STAMP(0, 4);
  bar();
  STAMP(0, 5);
  {
#pragma unroll
    for (int m = 0; m < 4; ++m) asm volatile("" : "+v"(are[m]), "+v"(aim[m]));
    const f32x2 Cr = {coef2.r, coef2.r}, Ci = {coef2.i, coef2.i};
#pragma unroll
    for (int m = 0; m < 4; ++m) {
      const f32x2 r0 = lo2(are[m]), r1 = hi2(are[m]), i0 = lo2(aim[m]), i1 = hi2(aim[m]);
      are[m] = cat2(fma2(-Ci, i0, Cr * r0), fma2(-Ci, i1, Cr * r1));
      aim[m] = cat2(fma2(-Ci, r0, -Cr * i0), fma2(-Ci, r1, -Cr * i1));
      asm volatile("" : "+v"(are[m]), "+v"(aim[m]));
    }
  }
  cf eAc = cmul(coef2, eA), pwc = cmul(coef2, pw);
  asm volatile("" : "+v"(eAc.r), "+v"(eAc.i), "+v"(pwc.r), "+v"(pwc.i));
  STAMP(0, 7);
  bar();
  STAMP(0, 8);
  HOLD(3);
  cf cin;
  {
    f32x2 v[2];
#pragma unroll
    for (int i = 0; i < 2; ++i) v[i] = *(LDS const f32x2*)(smem + G_X + (i * 64 + pl) * 8);
    const f32x2 pf = *(LDS const f32x2*)(smem + G_PF + (seg * 64 + pl) * 8);
    const f32x2 S = v[0] + v[1];
    cin = cfma(pwc, cf{S[0], S[1]}, cmul(coef2, cf{pf[0], pf[1]}));
  }
  const cf cinB = cfma(l8, cin, eAc);
  er = f32x2{cin.r, cinB.r}; ei = f32x2{-cin.i, -cinB.i};
  f32x2 hr[8], hi[8];
#pragma unroll
  for (int t = 0; t < 8; ++t) {
    const f32x2 br_ = (t & 1) ? hi2(are[t >> 1]) : lo2(are[t >> 1]);
    const f32x2 bi_ = (t & 1) ? hi2(aim[t >> 1]) : lo2(aim[t >> 1]);
    const f32x2 nr = fma2(Li, ei, fma2(Lr, er, br_));
    ei = fma2(-Li, er, fma2(Lr, ei, bi_));
    er = nr;
    hr[t] = er; hi[t] = ei;
  }
  u32x4 o[4];
#pragma unroll
  for (int t = 0; t < 4; ++t) {
    o[0][t] = pack_bf16(hr[2 * t][0], hr[2 * t + 1][0]);
    o[1][t] = pack_bf16(hr[2 * t][1], hr[2 * t + 1][1]);
    o[2][t] = pack_bf16(hi[2 * t][0], hi[2 * t + 1][0]);
    o[3][t] = pack_bf16(hi[2 * t][1], hi[2 * t + 1][1]);
  }
  STAMP(0, 9);
  {
    const __amdgpu_buffer_rsrc_t rs = mk_rsrc(p.ws + WS_HB, 2 * DM * SL * 2);
    const int hb = (((c * 2 + HF) * 2) * 9 + g) * 8192 + pq * 2048;
    const int p0 = (16 * q + col) * 16, p1 = 1024 + (16 * q + (col ^ 4)) * 16;
    __builtin_amdgcn_raw_buffer_store_b128(o[0], rs, hb + p0, 0, SC1);
    __builtin_amdgcn_raw_buffer_store_b128(o[1], rs, hb + p1, 0, SC1);
    __builtin_amdgcn_raw_buffer_store_b128(o[2], rs, hb + 9 * 8192 + p0, 0, SC1);
    __builtin_amdgcn_raw_buffer_store_b128(o[3], rs, hb + 9 * 8192 + p1, 0, SC1);
    wait_vm<0>();
    HOLD(8);
    STAMP(0, 10);
    if (lane < 8)
      __hip_atomic_store((GLB u64*)(p.ws + WS_IN) + tile_block(2 * c + HF, lane) * 64 + IN_H + g * 4 + pq, FLAG_SET, RLX_AGENT);
  }
}

constexpr int S_CPY = 4160;
constexpr int S_X = 0;
constexpr int S_ST = 6 * 4 * S_CPY;
constexpr int S_Z = S_ST;
constexpr int S_ZSZ = (256 + SL) * 2;
constexpr int S_T = S_ST + 12 * 4608;
constexpr int S_TSZ = 1312;
constexpr int S_END = S_T + 6 * S_TSZ;
constexpr int S_CNT = 32768 + 8192;

DEV c2 cexp_k(float zl, float th, float tl, float k) {
  const float mag = __builtin_amdgcn_exp2f(zl * k);
  const float t = fmaf(tl, k, __builtin_amdgcn_fractf(th * k));
  return c2{mag * __builtin_amdgcn_cosf(t), mag * __builtin_amdgcn_sinf(t)};
}
#define PKM(d, a, b) "v_pk_mul_f32 " d ", " a ", " b " op_sel:[0,0] op_sel_hi:[0,1]\n\t"
#define PKF(d, a, b) "v_pk_fma_f32 " d ", " a ", " b ", " d " op_sel:[1,1,0] op_sel_hi:[1,0,1] neg_lo:[1,0,0]\n\t"
DEV void octet(LDS char* dst, c2 s0, c2 m1, c2 m2) {
  c2 v1, v2, v3, t0, t1, t2, t3;
  asm(PKM("%0", "%7", "%8") PKM("%3", "%7", "%9") PKF("%0", "%7", "%8") PKF("%3", "%7", "%9")
      PKM("%1", "%0", "%8") PKM("%4", "%0", "%9") PKF("%1", "%0", "%8") PKF("%4", "%0", "%9")
      PKM("%2", "%1", "%8") PKM("%5", "%1", "%9") PKF("%2", "%1", "%8") PKF("%5", "%1", "%9")
      PKM("%6", "%2", "%9") PKF("%6", "%2", "%9")
      : "=&v"(v1), "=&v"(v2), "=&v"(v3), "=&v"(t0), "=&v"(t1), "=&v"(t2), "=&v"(t3) : "v"(s0), "v"(m1), "v"(m2));
  *(LDS u32*)(dst + 0 * 288) = pack_bf16(s0[0], s0[1]);
  *(LDS u32*)(dst + 1 * 288) = pack_bf16(v1[0], v1[1]);
  *(LDS u32*)(dst + 2 * 288) = pack_bf16(v2[0], v2[1]);
  *(LDS u32*)(dst + 3 * 288) = pack_bf16(v3[0], v3[1]);
  *(LDS u32*)(dst + 4 * 288) = pack_bf16(t0[0], t0[1]);
  *(LDS u32*)(dst + 5 * 288) = pack_bf16(t1[0], t1[1]);
  *(LDS u32*)(dst + 6 * 288) = pack_bf16(t2[0], t2[1]);
  *(LDS u32*)(dst + 7 * 288) = pack_bf16(t3[0], t3[1]);
}
DEV u32 dup_lo(u32 a) { return __builtin_amdgcn_perm(a, a, 0x01000100u); }
DEV u32 dup_hi(u32 a) { return __builtin_amdgcn_perm(a, a, 0x03020302u); }
DEV u32 hi_lo(u32 a, u32 b) { return __builtin_amdgcn_alignbit(b, a, 16); }

DEV void role_s(const P& p, LDS char* smem, int sg, int w, int lane) {
  const int tid = w * 64 + lane;
  const int ch = w >> 1, hh = w & 1, d = 6 * sg + ch;
  const int n = d * 128 + 64 * hh + lane;
  const u16 r_ldt = p.ldtA[d], r_la = p.la[n], r_aim = p.aim[n], r_cre = p.creA[n], r_cim = p.cimA[n];
  u32 wb[2] = {0, 0};
  __builtin_amdgcn_sched_barrier(0);
#pragma unroll
  for (int i = 0; i < 2; ++i) {
    const int ln = i * 768 + tid, all = (sg % 12) * 864 + ln;
    if (ln < 864) wb[i] = *(const GLB u32*)((const char*)(all < 5184 ? p.bre : p.bim) + (size_t)(all < 5184 ? all : all - 5184) * 128);
  }
  __builtin_amdgcn_sched_barrier(0);
  STAMP(0, 1);
  f32x4 tab[3];
  [[maybe_unused]] u32 wc[2] = {0, 0};
  LDS char* st = smem + S_ST + w * 4608;
  {
    const float L2E = 1.44269504088896341f;
    const float dt = rbf(__builtin_amdgcn_exp2f(bf2f(r_ldt) * L2E));
    const float ar = -rbf(__builtin_amdgcn_exp2f(bf2f(r_la) * L2E));
    const float zl = ar * dt * L2E, zi = bf2f(r_aim) * dt;
    const float th = zi * 0.15915494309189535f;
    const float tl = fmaf(zi, 0.15915494309189535f, -th) + zi * 4.6017384e-10f;
    const c2 C = {2.0f * (bf2f(r_cre) * dt), 2.0f * (bf2f(r_cim) * dt)};
    const c2 w1 = cexp_k(zl, th, tl, 1.f), w8 = cexp_k(zl, th, tl, 8.f), w128 = cexp_k(zl, th, tl, 128.f);
    c2 w2, w4, w16, w32, w64, w256, w512, w1024, w2048;
    asm(PKM("%0", "%9", "%9") PKM("%2", "%10", "%10") PKM("%5", "%11", "%11") PKF("%0", "%9", "%9") PKF("%2", "%10", "%10") PKF("%5", "%11", "%11")
        PKM("%1", "%0", "%0") PKM("%3", "%2", "%2") PKM("%6", "%5", "%5") PKF("%1", "%0", "%0") PKF("%3", "%2", "%2") PKF("%6", "%5", "%5")
        PKM("%4", "%3", "%3") PKM("%7", "%6", "%6") PKF("%4", "%3", "%3") PKF("%7", "%6", "%6")
        PKM("%8", "%7", "%7") PKF("%8", "%7", "%7")
        : "=&v"(w2), "=&v"(w4), "=&v"(w16), "=&v"(w32), "=&v"(w64), "=&v"(w256), "=&v"(w512), "=&v"(w1024), "=&v"(w2048)
        : "v"(w1), "v"(w8), "v"(w128));
    c2 C128, C256, C512, C1024, C2048;
    asm(PKM("%0", "%5", "%6") PKM("%1", "%5", "%7") PKM("%2", "%5", "%8") PKM("%3", "%5", "%9") PKM("%4", "%5", "%10")
        PKF("%0", "%5", "%6") PKF("%1", "%5", "%7") PKF("%2", "%5", "%8") PKF("%3", "%5", "%9") PKF("%4", "%5", "%10")
        : "=&v"(C128), "=&v"(C256), "=&v"(C512), "=&v"(C1024), "=&v"(C2048)
        : "v"(C), "v"(w128), "v"(w256), "v"(w512), "v"(w1024), "v"(w2048));
#pragma unroll
    for (int i = 0; i < 2; ++i) {
      const int ln = i * 768 + tid, all = (sg % 12) * 864 + ln;
      if (ln < 864) wc[i] = *(const GLB u32*)((const char*)(all < 5184 ? p.cre : p.cim) + (size_t)(all < 5184 ? all : all - 5184) * 128);
    }
    STAMP(0, 2);
    LDS char* wr = st + 4 * lane;
    LDS const char* rd = st + (lane & 15) * 288 + (lane >> 4) * 16;
    bf16x8 B[4];
    const c2 w8c = {w8[0], -w8[1]}, w32c = {w32[0], -w32[1]}, w64c = {w64[0], -w64[1]};
    octet(wr, c2{1.f, 0.f}, w8c, w32c);
    octet(wr + 8 * 288, w64c, w8c, w32c);
    wave_fence();
#pragma unroll
    for (int ks = 0; ks < 4; ++ks) B[ks] = lds_frag(rd + 64 * ks);
    wave_fence();
#pragma unroll
    for (int mt = 0; mt < 3; ++mt) {
      if (mt == 0) { octet(wr, C, w1, w4); octet(wr + 8 * 288, C128, w1, w4); }
      if (mt == 1) { octet(wr, C256, w2, w128); octet(wr + 8 * 288, C512, w128, w4); }
      if (mt == 2) { octet(wr, C1024, w128, w512); *(LDS u32*)(wr + 8 * 288) = pack_bf16(C2048[0], C2048[1]); }
      wave_fence();
      f32x4 acc = {0, 0, 0, 0};
      bf16x8 A[4];
#pragma unroll
      for (int ks = 0; ks < 4; ++ks) A[ks] = lds_frag(rd + 64 * ks);
      __builtin_amdgcn_sched_barrier(0);
#pragma unroll
      for (int ks = 0; ks < 4; ++ks) acc = mfma16(A[ks], B[ks], acc);
      __builtin_amdgcn_sched_barrier(0);
      wave_fence();
      tab[mt] = acc;
    }
  }
  const int b = lane & 15, q = lane >> 4;
#pragma unroll
  for (int mt = 0; mt < 3; ++mt) *(LDS f32x4*)(st + mt * 1024 + lane * 16) = tab[mt];
  STAMP(0, 3);
  bar();
  {
    LDS const char* ot = smem + S_ST + (w ^ 1) * 4608 + lane * 16;
    LDS char* T = smem + S_T + ch * S_TSZ;
    if (hh == 0) {
      const f32x4 v0 = tab[0] + *(LDS const f32x4*)(ot), v2 = tab[2] + *(LDS const f32x4*)(ot + 2048);
      *(LDS u32x2*)(T + 2 * (128 * (q >> 1) + 8 * b + 4 * (q & 1))) = u32x2{pack_bf16(v0[0], v0[1]), pack_bf16(v0[2], v0[3])};
      const u32 a0 = pack_bf16(v2[0], v2[1]), a1 = pack_bf16(v2[2], v2[3]);
      if (q < 2) {
        LDS char* t3 = T + 2 * (512 + 64 * q + b);
        *(LDS u16*)(t3) = (u16)a0; *(LDS u16*)(t3 + 32) = (u16)(a0 >> 16);
        *(LDS u16*)(t3 + 64) = (u16)a1; *(LDS u16*)(t3 + 96) = (u16)(a1 >> 16);
      } else if (lane == 32) *(LDS u16*)(T + 2 * 640) = (u16)a0;
    } else {
      const f32x4 v1 = tab[1] + *(LDS const f32x4*)(ot + 1024);
      const u32 a0 = pack_bf16(v1[0], v1[1]), a1 = pack_bf16(v1[2], v1[3]);
      if (q < 2) *(LDS u32x2*)(T + 2 * (256 + 64 * q + 4 * b)) = u32x2{a0, a1};
      else {
        LDS char* t2 = T + 2 * (384 + 2 * b + (q - 2));
        *(LDS u16*)(t2) = (u16)a0; *(LDS u16*)(t2 + 64) = (u16)(a0 >> 16);
        *(LDS u16*)(t2 + 128) = (u16)a1; *(LDS u16*)(t2 + 192) = (u16)(a1 >> 16);
      }
    }
  }
  bar();
  STAMP(0, 4);
  u32x4 uv[2];
  {
    GLB u64* fl = (GLB u64*)(p.ws + WS_FL) + (8 * hh + (lane & 7)) * DM + d;
    asm volatile("" :: "v"(wc[0]), "v"(wc[1]), "v"(wb[0]), "v"(wb[1]));
    wait_flags(fl, true);
    const __amdgpu_buffer_rsrc_t rs = mk_rsrc(p.ws + WS_XT, DM * SL * 2);
#pragma unroll
    for (int v = 0; v < 2; ++v) uv[v] = __builtin_amdgcn_raw_buffer_load_b128(rs, (d * SL + 1024 * hh + 8 * (lane + 64 * v)) * 2, 0, SC1);
    if (lane < 8) __hip_atomic_store(fl, 0ull, RLX_AGENT);
  }
  STAMP(0, 5);
  {
    LDS const char* T = smem + S_T + ch * S_TSZ;
    u32 o[10];
    const u32 e512 = *(LDS const u16*)(T + 2 * 512);
    o[8] = o[9] = e512 | (e512 << 16);
    if (hh == 0) {
      const int off = lane < 16 ? 32 * lane : (lane < 32 ? 256 + 16 * lane : 512 + 8 * lane);
      u32x2 W[4];
#pragma unroll
      for (int i = 0; i < 4; ++i) W[i] = *(LDS const u32x2*)(T + off + 8 * i);
      if (lane < 16) {
#pragma unroll
        for (int i = 0; i < 8; ++i) o[i] = W[i >> 1][i & 1];
      } else if (lane < 32) {
#pragma unroll
        for (int i = 0; i < 4; ++i) {
          const u32 a = W[i >> 1][i & 1], nx = W[(i + 1) >> 1][(i + 1) & 1];
          o[2 * i] = dup_lo(a);
          o[2 * i + 1] = hi_lo(a, nx);
        }
      } else {
        const u32 w0 = W[0][0], w1 = W[0][1], w2 = W[1][0];
        o[0] = dup_lo(w0); o[1] = w0; o[2] = dup_hi(w0); o[3] = dup_lo(w1);
        o[4] = o[3]; o[5] = w1; o[6] = dup_hi(w1); o[7] = dup_lo(w2);
      }
    } else {
      const u32 w0 = *(LDS const u32*)(T + 1024 + 4 * lane), w1 = *(LDS const u32*)(T + 1028 + 4 * lane);
      o[0] = o[1] = dup_lo(w0); o[2] = w0; o[3] = o[4] = o[5] = dup_hi(w0); o[6] = o[7] = dup_lo(w1);
    }
    o[8] = (u32)__builtin_amdgcn_update_dpp((int)o[8], (int)o[0], 0x130, 0xf, 0xf, false);
    o[9] = (u32)__builtin_amdgcn_update_dpp((int)o[9], (int)o[1], 0x130, 0xf, 0xf, false);
    u32 a[9];
#pragma unroll
    for (int i = 0; i < 9; ++i) a[i] = hi_lo(o[i], o[i + 1]);
    LDS char* xb = smem + S_X + ch * 4 * S_CPY + 32 + 2 * (1024 * hh + 16 * lane);
    *(LDS u32x4*)(xb) = u32x4{o[0], o[1], o[2], o[3]};
    *(LDS u32x4*)(xb + 16) = u32x4{o[4], o[5], o[6], o[7]};
    *(LDS u32x4*)(xb + S_CPY) = u32x4{a[0], a[1], a[2], a[3]};
    *(LDS u32x4*)(xb + S_CPY + 16) = u32x4{a[4], a[5], a[6], a[7]};
    *(LDS u32x4*)(xb + 2 * S_CPY) = u32x4{o[1], o[2], o[3], o[4]};
    *(LDS u32x4*)(xb + 2 * S_CPY + 16) = u32x4{o[5], o[6], o[7], o[8]};
    *(LDS u32x4*)(xb + 3 * S_CPY) = u32x4{a[1], a[2], a[3], a[4]};
    *(LDS u32x4*)(xb + 3 * S_CPY + 16) = u32x4{a[5], a[6], a[7], a[8]};
    if (hh == 0 && lane == 0) {
      LDS char* hb = smem + S_X + ch * 4 * S_CPY;
      const u32x4 z = {0, 0, 0, 0};
      *(LDS u32x4*)(hb) = z;
      *(LDS u32x4*)(hb + 16) = z;
      *(LDS u32x4*)(hb + S_CPY) = z;
      *(LDS u32x4*)(hb + S_CPY + 16) = u32x4{0, 0, 0, o[0] << 16};
      *(LDS u32x4*)(hb + 2 * S_CPY) = z;
      *(LDS u32x4*)(hb + 2 * S_CPY + 16) = u32x4{0, 0, 0, o[0]};
      *(LDS u32x4*)(hb + 3 * S_CPY) = z;
      *(LDS u32x4*)(hb + 3 * S_CPY + 16) = u32x4{0, 0, o[0] << 16, a[0]};
    }
    if (tid < 6 * 32) *(LDS u32x4*)(smem + S_Z + (tid >> 5) * S_ZSZ + (tid & 31) * 16) = u32x4{0, 0, 0, 0};
    STAMP(0, 6);
    LDS char* z = smem + S_Z + ch * S_ZSZ + 512 + (1024 * hh + 8 * lane) * 2;
    *(LDS u32x4*)z = uv[0];
    *(LDS u32x4*)(z + 1024) = uv[1];
  }
  bar();
  STAMP(0, 7);
  f32x4 acc[8];
#pragma unroll
  for (int i = 0; i < 8; ++i) acc[i] = f32x4{0, 0, 0, 0};
  {
    const int kq = q, qq = b >> 2, pp = b & 3;
    LDS const char* fb = smem + S_X + (ch * 4 + ((4 - qq) & 3)) * S_CPY +
                         2 * (16 + 16 * (kq >> 1) - 8 * (kq & 1) + 4 * pp - (qq ? 4 : 0)) + 64 * hh;
    LDS const char* gb = smem + S_Z + ch * S_ZSZ + 512 + 32 * (b - (kq >> 1)) + 16 * (kq & 1) - 64 * hh;
#pragma unroll 1
    for (int it = 0; it < 4; ++it) {
      bf16x8 F[8], G[8];
#pragma unroll
      for (int s = 0; s < 8; ++s) {
        F[s] = tr_frag(fb + 512 * s, fb + 512 * s - 8);
        G[s] = lds_frag(gb + 512 * s);
      }
      __builtin_amdgcn_sched_barrier(0);
#pragma unroll
      for (int t = 0; t < 8; ++t)
#pragma unroll
        for (int s = 0; s <= t; ++s) acc[t] = mfma16(F[s], G[t - s], acc[t]);
      fb += 128;
      gb -= 128;
    }
  }
  __builtin_amdgcn_sched_barrier(0);
  STAMP(0, 8);
  bar();
  {
    LDS char* ex = smem + S_X + 49152 + w * 4096 + lane * 16;
#pragma unroll
    for (int i = 0; i < 4; ++i) {
      const f32x4 lo = acc[i], hi = acc[4 + i];
      *(LDS f32x4*)(ex + i * 1024) = hh ? +lo : +hi;
    }
    if (tid == 0) *(LDS u32*)(smem + S_CNT) = 0;
  }
  bar();
  u32x2 yo[4];
  {
    LDS const char* ey = smem + S_X + 49152 + (w ^ 1) * 4096 + lane * 16;
#pragma unroll
    for (int i = 0; i < 4; ++i) {
      const f32x4 lo = acc[i], hi = acc[4 + i];
      const f32x4 y = (hh ? +hi : +lo) + *(LDS const f32x4*)(ey + i * 1024);
      yo[i] = u32x2{pack_bf16(y[0], y[1]), pack_bf16(y[2], y[3])};
    }
  }
  bar();
  {
    const __amdgpu_buffer_rsrc_t rs = mk_rsrc(p.ws + WS_AT, DM * SL * 2);
    const int dst = (d * SL + 1024 * hh + 16 * (lane & 15) + 4 * (lane >> 4)) * 2;
#pragma unroll
    for (int i = 0; i < 4; ++i) __builtin_amdgcn_raw_buffer_store_b64(yo[i], rs, dst + 512 * i, 0, SC1);
  }
  STAMP(0, 9);
}
DEV void s_signal(const P& p, LDS char* smem, int sg, int lane) {
  u32 old = 0;
  if (lane == 0) old = __hip_atomic_fetch_add((LDS u32*)(smem + S_CNT), 1u, __ATOMIC_RELAXED, __HIP_MEMORY_SCOPE_WORKGROUP);
  if (__builtin_amdgcn_readfirstlane(old) == 11) {
    const int lt = lane & 31, second = lane >> 5;
    const int dd = 6 * sg + (second ? 5 : 0);
    const int idx = dd < 480 ? dd / 96 : 5 + (dd - 480) / 32;
    const int dfirst = idx < 5 ? 96 * idx : 480 + 32 * (idx - 5);
    __hip_atomic_store((GLB u64*)(p.ws + WS_IN) + tile_block(lt, idx) * 64 + IN_A + (sg - dfirst / 6), FLAG_SET, RLX_AGENT);
  }
}

DEV void role_gate(const P& p, int rb, int w, int lane, u64 pc) {
  u32 pf0 = 0, pf1 = 0;
  const int tid = w * 64 + lane;
  const int sub = lane & 7;
  u32x4 wv[9];
#pragma unroll
  for (int i = 0; i < 9; ++i) wv[i] = *(const u32x4*)((const char*)p.gw + (i * 8 + sub) * 16);
  for (int r = w * 8 + (lane >> 3); r < 128; r += 96) {
    const int l = rb * 128 + r;
    float s = 0.f;
    u32x4 xv[9];
#pragma unroll
    for (int i = 0; i < 9; ++i) xv[i] = *(const u32x4*)((const char*)p.x + (size_t)l * 1152 + (i * 8 + sub) * 16);
#pragma unroll
    for (int i = 0; i < 9; ++i) {
#pragma unroll
      for (int k = 0; k < 4; ++k) {
        const u32 pr = pack_bf16(bflo(xv[i][k]) * bflo(wv[i][k]), bfhi(xv[i][k]) * bfhi(wv[i][k]));
        s += bflo(pr);
        s += bfhi(pr);
      }
    }
    s += __shfl_xor(s, 1); s += __shfl_xor(s, 2); s += __shfl_xor(s, 4);
    const float gv = rbf(1.0f / (1.0f + expf(-rbf(s))));
    if (sub == 0)
      __hip_atomic_store((GLB u64*)(p.ws + WS_G) + l, ((u64)__float_as_uint(rbf(1.0f - gv)) << 32) | __float_as_uint(gv), RLX_AGENT);
  }
  asm volatile("" :: "v"(pf0), "v"(pf1));
  wait_vm<0>();
  bar();
  if (w == 0 && lane < 16)
    __hip_atomic_store((GLB u64*)(p.ws + WS_IN) + tile_block(2 * rb + (lane >> 3), lane & 7) * 64 + IN_G, FLAG_SET, RLX_AGENT);
}

constexpr int H_C = 32768, H_CS = 12288;
DEV int h_slot(int i) { return i < 4 ? i * 8192 : 131072 + (i - 4) * 8192; }
constexpr int H_AT = H_C + 2 * H_CS;
constexpr int H_XT = H_C + 3 * H_CS;
constexpr int H_G = H_C + 4 * H_CS;
constexpr int H_OUT = H_C + 5 * H_CS;
constexpr int K_LDS = 163840;

constexpr int h_wait(int s, int NDT) {
  int n = 0;
  int need = 0;
  for (int k = 0; k < 4; ++k) { n += 2; if (k == s) need = n; }
  for (int t = 0; t < s; ++t) {
    if (t == 0) { n += 2; if (s == 4) need = n; n += 2; if (s == 5) need = n; }
    else if (t == 1) { n += 2; if (s == 6) need = n; n += 2; if (s == 7) need = n; }
    else if (t <= 11) { n += 2 + NDT; if (s == t + 6) need = n; if (t == 2) n += 1; }
    else if (t == 12 || t == 13) n += NDT;
    else if (t == 14) n += 1;
  }
  return s == 17 ? 0 : n - need;
}
template <int S, int NDT, class F> DEV void h_steps(F&& after, u64& fv, char* wsp) {
  if constexpr (S == 9) asm volatile("s_waitcnt vmcnt(%1)" : "+v"(fv) : "n"(h_wait(S, NDT)) : "memory");
  else wait_vm<h_wait(S, NDT)>();
  LSTAMP2(1, 16 + S);
  bar();
  after(std::integral_constant<int, S>{});
  if constexpr (S < 17) h_steps<S + 1, NDT>(after, fv, wsp);
}
DEV int p2_rot(int d0) { const int idx = d0 < 480 ? d0 / 96 : 5 + (d0 - 480) / 32; return (idx * 18) >> 3; }

template <int NDT, class F>
DEV void p2_loader(const P& p, LDS char* smem, int lt, int d0, int lw, int lane, bool scratch_barrier, F&& pre_poll) {
  __builtin_amdgcn_s_setprio(3);
  const char* hb = p.ws + WS_HB + (size_t)lt * 18 * 8192 + (2 * lw) * 1024 + lane * 16;
  constexpr int W = 32 * NDT;
  const int l0 = 64 * lt, rot = p2_rot(d0);
  u32 co[NDT];
#pragma unroll
  for (int i = 0; i < NDT; ++i) {
    const int row = 8 * (NDT * lw + i) + (lane >> 3), cp = lane & 7;
    co[i] = (u32)(d0 + row) * 1152u + ((cp ^ (row & 7)) << 4);
  }
  auto issue_c = [&](int s) {
    LDS char* buf = smem + H_C + (s & 7) * H_CS + NDT * lw * 1024;
    int ks = s + rot; ks = ks >= 18 ? ks - 18 : ks;
    const char* cb = ks < 9 ? (const char*)p.cre + ks * 128 : (const char*)p.cim + (ks - 9) * 128;
#pragma unroll
    for (int i = 0; i < NDT; ++i) dma16<0>(cb + co[i], buf + i * 1024);
  };
  auto issue_h = [&](int s) {
    LDS char* buf = smem + h_slot(s & 7) + 2 * lw * 1024;
    int ks = s + rot; ks = ks >= 18 ? ks - 18 : ks;
#pragma unroll
    for (int i = 0; i < 2; ++i) dma16<SC1>(hb + ks * 8192 + i * 1024, buf + i * 1024);
  };
  GLB u64* inb = (GLB u64*)(p.ws + WS_IN) + blockIdx.x * 64;
  const int nA = (d0 + W - 1) / 6 - d0 / 6 + 1;
  const bool need = lane == IN_G || (lane >= IN_A && lane < IN_A + nA);
  if (!scratch_barrier) {
#pragma unroll 1
    for (int s = 0; s < 8; ++s) issue_c(s);
  }
  pre_poll();
  { [[maybe_unused]] const int w = lw + 8; STAMP(1, 1); }
  wait_flags(inb + IN_H + (lane < 36 ? lane : 0), true);
  { [[maybe_unused]] const int w = lw + 8; STAMP(1, 2); }
#pragma unroll
  for (int s = 0; s < 4; ++s) issue_h(s);
  { [[maybe_unused]] const int w = lw + 8; STAMP(1, 3); }
  u64 fv = 0;
  h_steps<0, NDT>([&](auto S_) {
    constexpr int S = decltype(S_)::value;
    if constexpr (S == 0) { issue_h(4); issue_h(5); }
    if constexpr (S == 1) { issue_h(6); issue_h(7); }
    if constexpr (S >= 2 && S <= 11) { issue_h(S + 6); issue_c(S + 6); }
    if constexpr (S == 2)
      asm volatile("global_load_dwordx2 %0, %1, off sc1" : "=v"(fv) : "v"(inb + lane) : "memory");
    if constexpr (S == 9) { if (!__all(!need || fv == FLAG_SET)) wait_flags(inb + lane, need); }
    if constexpr (S == 12) {
#pragma unroll
      for (int i = 0; i < NDT; ++i) {
        const int pc = NDT * lw + i;
        dma16<SC1>(p.ws + WS_AT + (size_t)(d0 + 8 * pc + (lane >> 3)) * 4096 + l0 * 2 + (lane & 7) * 16, smem + H_AT + pc * 1024);
      }
    }
    if constexpr (S == 13) {
#pragma unroll
      for (int i = 0; i < NDT; ++i) {
        const int pc = NDT * lw + i, id = pc * 64 + lane, row = id / (4 * NDT), qq = id - row * (4 * NDT);
        dma16<0>((const char*)p.x + (size_t)(l0 + row) * 1152 + d0 * 2 + qq * 16, smem + H_XT + pc * 1024);
      }
    }
    if constexpr (S == 14) { if (lane < 8) dma16<SC1>(p.ws + WS_G + (size_t)l0 * 8 + lw * 128 + lane * 16, smem + H_G + lw * 128); }
  }, fv, p.ws);
}

template <int NDT, int STAG>
DEV void p2_compute(const P& p, LDS char* smem, int lt0, int d0, int w, int lane) {
  const int lh = (w >> 1) & 1, kh = w & 1, dg = w >> 2;
  const int r = lane & 15, kq = lane >> 4;
  const int lt = 2 * lh + kh, l0 = 64 * lt0;
  constexpr int W = 32 * NDT;
  u32x2 da[NDT], db[NDT];
#pragma unroll
  for (int t = 0; t < NDT; ++t) {
    const int d = d0 + 16 * (NDT * dg + t) + 4 * kq;
    da[t] = *(const u32x2*)(p.dskA + d);
    db[t] = *(const u32x2*)(p.dskB + d);
  }
  GLB u64* inb = (GLB u64*)(p.ws + WS_IN) + blockIdx.x * 64;

  f32x4 acc[2][NDT];
#pragma unroll
  for (int i = 0; i < 2; ++i)
#pragma unroll
    for (int t = 0; t < NDT; ++t) acc[i][t] = f32x4{0, 0, 0, 0};
  int ho[2];
#pragma unroll
  for (int u = 0; u < 2; ++u) {
    const int k = 32 * kh + 8 * kq + 4 * u + (r >> 2), i = (r & 3) >> 1;
    ho[u] = (2 * (k >> 4) + i) * 1024 + (32 * lh + ((k & 15) ^ (4 * i))) * 16 + (r & 1) * 8;
  }
  const int co = H_C + (16 * NDT * dg + r) * 128 + (((4 * kh + kq) ^ (r & 7)) << 4);
  constexpr int NREG = 0, NRING = 18 - NREG;
  bf16x8 cr[NREG ? NREG : 1][NDT];
  if constexpr (NREG > 0) {
    const int rot = p2_rot(d0);
#pragma unroll
    for (int i = 0; i < NREG; ++i) {
      int ks = NRING + i + rot; ks = ks >= 18 ? ks - 18 : ks;
      const char* cb = (ks < 9 ? (const char*)p.cre + ks * 128 : (const char*)p.cim + (ks - 9) * 128) +
                       (size_t)(d0 + 16 * NDT * dg + r) * 1152 + (4 * kh + kq) * 16;
#pragma unroll
      for (int t = 0; t < NDT; ++t) cr[i][t] = *(const GLB bf16x8*)(cb + t * 16 * 1152);
    }
  }
  bf16x8 hb[2], ca[NDT];
#pragma unroll
  for (int i = 0; i < 2; ++i) hb[i] = bf16x8{};
#pragma unroll
  for (int t = 0; t < NDT; ++t) ca[t] = bf16x8{};
  auto mma_with = [&](const bf16x8 (&cc)[NDT]) {
#pragma unroll
    for (int t = 0; t < NDT; ++t) {
      acc[0][t] = mfma16(cc[t], hb[0], acc[0][t]);
      acc[1][t] = mfma16(cc[t], hb[1], acc[1][t]);
    }
    __builtin_amdgcn_sched_barrier(0);
  };
  auto read_h = [&](int s) {
    LDS const char* hbuf = smem + h_slot(s & 7);
#pragma unroll
    for (int i = 0; i < 2; ++i) hb[i] = tr_frag(hbuf + ho[0] + i * 256, hbuf + ho[1] + i * 256);
  };
  STAMP(1, 1);
#pragma unroll 1
  for (int s = 0; s < NRING; ++s) {
    bar();
    if (s == 0) STAMP(1, 2);
    LSTAMP(1, 16 + s);
    if (STAG) mma_with(ca);
    read_h(s);
    LDS const char* cbuf = smem + (s & 7) * H_CS;
#pragma unroll
    for (int t = 0; t < NDT; ++t) ca[t] = lds_frag(cbuf + co + t * 2048);
    __builtin_amdgcn_sched_barrier(0);
    if (!STAG) mma_with(ca);
  }
#pragma unroll
  for (int i = 0; i < NREG; ++i) {
    bar();
    if (STAG) { if (i == 0) mma_with(ca); else mma_with(cr[i ? i - 1 : 0]); }
    read_h(NRING + i);
    __builtin_amdgcn_sched_barrier(0);
    if (!STAG) mma_with(cr[i]);
  }
  if (STAG) { if (NREG) mma_with(cr[NREG ? NREG - 1 : 0]); else mma_with(ca); }
  __builtin_amdgcn_sched_barrier(0);
  STAMP(1, 3);
  bar();
  if (w == 0) __hip_atomic_store(inb + lane, 0ull, RLX_AGENT);
  LDS char* ex = smem + w * (NDT * 1024) + lane * 16;
#pragma unroll
  for (int t = 0; t < NDT; ++t) {
    const f32x4 a0 = acc[0][t], a1 = acc[1][t];
    *(LDS f32x4*)(ex + t * 1024) = kh ? +a0 : +a1;
  }
  bar();
  LDS const char* ey = smem + (w ^ 1) * (NDT * 1024) + lane * 16;
  constexpr int OSTR = W * 4 + 16;
  const f32x2 gg = *(LDS const f32x2*)(smem + H_G + (16 * lt + r) * 8);
#pragma unroll
  for (int t = 0; t < NDT; ++t) {
    const int dt = NDT * dg + t;
    const f32x4 o = *(LDS const f32x4*)(ey + t * 1024);
    const f32x4 a0 = acc[0][t], a1 = acc[1][t];
    const f32x4 y = (kh ? +a1 : +a0) + o;
    const s16x4 av = tr_read(smem + H_AT + (16 * dt + 4 * kq + (r >> 2)) * 128 + lt * 32 + (r & 3) * 8);
    const u32x2 xv = *(LDS const u32x2*)(smem + H_XT + (16 * lt + r) * (W * 2) + (16 * dt + 4 * kq) * 2);
    f32x4 res;
#pragma unroll
    for (int i = 0; i < 4; ++i) {
      const u32 xw = xv[i >> 1], aw = da[t][i >> 1], bw = db[t][i >> 1];
      const float xf = (i & 1) ? bfhi(xw) : bflo(xw);
      const float sa = (i & 1) ? bfhi(aw) : bflo(aw);
      const float sb = (i & 1) ? bfhi(bw) : bflo(bw);
      const float yc = bf2f((u16)av[i]);
      const float a = rbf(yc + rbf(xf * sa));
      const float b = y[i] + rbf(xf * sb);
      res[i] = rbf(gg[0] * a) + gg[1] * b;
    }
    *(LDS f32x4*)(smem + H_OUT + (16 * lt + r) * OSTR + (16 * dt + 4 * kq) * 4) = res;
  }
  STAMP(1, 4);
  bar();
#pragma unroll
  for (int it = 0; it < NDT; ++it) {
    const int id = it * 512 + w * 64 + lane;
    const int row = id / (8 * NDT), pc = id - row * (8 * NDT);
    const f32x4 v = *(LDS const f32x4*)(smem + H_OUT + row * OSTR + pc * 16);
    __builtin_amdgcn_raw_buffer_store_b128(__builtin_bit_cast(u32x4, v), mk_rsrc(p.out, SL * DM * 4), ((l0 + row) * DM + d0 + pc * 4) * 4, 0, OUT_AUX);
  }
}

template <int NDT>
DEV void phase2(const P& p, LDS char* smem, int lt, int d0, int w, int lane, bool scratch_barrier, int sg) {
  STAMP(1, 0);
  if (w >= 8) {
    p2_loader<NDT>(p, smem, lt, d0, w - 8, lane, scratch_barrier, [&]() { if (NDT == 1) { wait_vm<8>(); s_signal(p, smem, sg, lane); } });
  } else {
    if (NDT == 1) { wait_vm<0>(); s_signal(p, smem, sg, lane); }
    if (w < 4) p2_compute<NDT, 0>(p, smem, lt, d0, w, lane);
    else p2_compute<NDT, 1>(p, smem, lt, d0, w, lane);
  }
  STAMP(1, 15);
}

__global__ __launch_bounds__(768, 3) void gated_mix_s4d_s5(P p) {
  extern __shared__ __attribute__((aligned(16))) char smem_raw[];
  LDS char* smem = (LDS char*)smem_raw;
  u64 pc;
  asm volatile("s_getpc_b64 %0" : "=s"(pc));
  u32 pf = 0;
  const u64 g_tb = __builtin_amdgcn_s_memrealtime();
#define PF_TOP 312
  if (threadIdx.x < PF_TOP)
    asm volatile("global_load_dword %0, %1, off" : "=v"(pf) : "v"((pc & ~255ull) + threadIdx.x * 128));
  const int w = __builtin_amdgcn_readfirstlane(threadIdx.x >> 6), lane = threadIdx.x & 63;
  const int xcd = blockIdx.x & 7, j = blockIdx.x >> 3;
  STAMP(0, 0);
  if (j >= 18 && j < 30) {
    const int sg = 12 * xcd + (j - 18);
    role_s(p, smem, sg, w, lane);
    STAMP(0, 15);
    phase2<1>(p, smem, 4 * xcd + (j - 18) / 3, 480 + 32 * ((j - 18) % 3), w, lane, false, sg);
  } else {
    const bool isg = j < 18;
    if (isg) {
      const int c = 2 * xcd + j / 9, g = j % 9;
      if (w >= 8) g_loader(p, smem, c, g, 96 * (j % 5), w - 8, lane, g_tb);
      else if (w < 4) g_compute<0>(p, smem, c, g, w, lane, pf, g_tb);
      else g_compute<1>(p, smem, c, g, w, lane, pf, g_tb);
    } else {
      role_gate(p, 2 * xcd + (j - 30), w, lane, pc);
    }
    STAMP(0, 15);
    const int e = isg ? j : j - 12;
    phase2<3>(p, smem, 4 * xcd + e / 5, 96 * (e % 5), w, lane, isg, 0);
  }
  asm volatile("s_waitcnt vmcnt(0)" :: "v"(pf));
}

extern "C" void kernel_launch(void* const* d_in, const int* in_sizes, int n_in, void* d_out, int out_size,
                              void* d_ws, size_t ws_size, hipStream_t stream) {
  P a;
  a.x = (const u16*)d_in[0]; a.la = (const u16*)d_in[1]; a.aim = (const u16*)d_in[2];
  a.creA = (const u16*)d_in[3]; a.cimA = (const u16*)d_in[4]; a.ldtA = (const u16*)d_in[5]; a.dskA = (const u16*)d_in[6];
  a.lre = (const u16*)d_in[8]; a.lim = (const u16*)d_in[9]; a.bre = (const u16*)d_in[10]; a.bim = (const u16*)d_in[11];
  a.cre = (const u16*)d_in[12]; a.cim = (const u16*)d_in[13]; a.ldtB = (const u16*)d_in[14];
  a.dskB = (const u16*)d_in[15]; a.gw = (const u16*)d_in[16];
  a.ws = (char*)d_ws + WS_OFF; a.out = (float*)d_out;
  (void)hipFuncSetAttribute((const void*)gated_mix_s4d_s5, hipFuncAttributeMaxDynamicSharedMemorySize, K_LDS);
  gated_mix_s4d_s5<<<256, 768, K_LDS, stream>>>(a);
}
```

```cpp
#define GPOLL_DELAY 24
#define HPOLL_DELAY 40
#include <hip/hip_runtime.h>
#include <hip/hip_bf16.h>
#include <stdint.h>
#include <type_traits>

typedef unsigned short u16;
typedef unsigned int u32;
typedef unsigned long long u64;
typedef __attribute__((ext_vector_type(8))) __bf16 bf16x8;
typedef __attribute__((ext_vector_type(2))) __bf16 bf16x2;
typedef __attribute__((ext_vector_type(4))) short s16x4;
typedef __attribute__((ext_vector_type(4))) float f32x4;
typedef __attribute__((ext_vector_type(2))) float f32x2;
typedef __attribute__((ext_vector_type(4))) unsigned u32x4;
typedef __attribute__((ext_vector_type(2))) unsigned u32x2;
#define LDS __attribute__((address_space(3)))
#define GLB __attribute__((address_space(1)))
#define DEV __device__ __forceinline__
#define RLX_AGENT __ATOMIC_RELAXED, __HIP_MEMORY_SCOPE_AGENT

constexpr int SL = 2048;
constexpr int DM = 576;

constexpr size_t WS_HB = 0;
constexpr size_t WS_AT = WS_HB + (size_t)2 * DM * SL * 2;
constexpr size_t WS_G = WS_AT + (size_t)DM * SL * 2;
constexpr size_t WS_GRAN = WS_G + (size_t)SL * 8;
constexpr size_t WS_EP = WS_GRAN + (size_t)9 * 16 * 2 * 64 * 8;
constexpr size_t WS_FL = WS_EP + 4096;
constexpr size_t WS_XT = WS_FL + (size_t)16 * DM * 8;
constexpr size_t WS_IN = WS_XT + (size_t)DM * SL * 2;
constexpr u64 FLAG_SET = 0x9E3779B97F4A7C15ull;
constexpr u32 SPIN_CAP = 1u << 17;
constexpr int IN_H = 0, IN_G = 36, IN_A = 40;

constexpr size_t WS_ST = (size_t)16 << 20;
#define STAMP(kern, idx) do {} while (0)
#define CSTAMP(kern, idx) do {} while (0)
#define LSTAMP(kern, idx) do {} while (0)
#define LSTAMP2(kern, idx) do {} while (0)

DEV float bf2f(u16 v) { return __uint_as_float((u32)v << 16); }
DEV float bflo(u32 v) { return __uint_as_float(v << 16); }
DEV float bfhi(u32 v) { return __uint_as_float(v & 0xffff0000u); }
DEV u32 pack_bf16(float lo, float hi) {
  f32x2 v = {lo, hi};
  bf16x2 b = __builtin_convertvector(v, bf16x2);
  return __builtin_bit_cast(u32, b);
}
DEV float rbf(float x) { return bflo(pack_bf16(x, x)); }
template <int N> DEV void wait_vm() { asm volatile("s_waitcnt vmcnt(%0)" ::"n"(N) : "memory"); }
DEV void bar() { asm volatile("s_waitcnt lgkmcnt(0)\n\ts_barrier" ::: "memory"); }
DEV void wave_fence() { __builtin_amdgcn_fence(__ATOMIC_ACQ_REL, "wavefront"); }
template <int AUX> DEV void dma16(const void* src, LDS char* dst) {
  __builtin_amdgcn_global_load_lds((const GLB u32*)src, (LDS u32*)dst, 16, 0, AUX);
}
constexpr int SC1 = 16;
#define GPOLL_SLEEP 1
#define POLL_SLEEP 1
#define OUT_AUX 16
#define WS_OFF 0
#define XT_WAIT 4
DEV bf16x8 lds_frag(LDS const char* p) { return *(LDS const bf16x8*)p; }
DEV s16x4 tr_read(LDS const char* a) { return __builtin_amdgcn_ds_read_tr16_b64_v4i16((LDS s16x4*)a); }
DEV bf16x8 tr_frag(LDS const char* a, LDS const char* b) {
  const s16x4 lo = tr_read(a), hi = tr_read(b);
  return __builtin_bit_cast(bf16x8, __builtin_shufflevector(lo, hi, 0, 1, 2, 3, 4, 5, 6, 7));
}
DEV f32x4 mfma16(bf16x8 a, bf16x8 b, f32x4 c) { return __builtin_amdgcn_mfma_f32_16x16x32_bf16(a, b, c, 0, 0, 0); }
DEV __amdgpu_buffer_rsrc_t mk_rsrc(const void* base, int bytes) {
  return __builtin_amdgcn_make_buffer_rsrc(const_cast<void*>(base), (short)0, bytes, 0x00020000);
}
struct cf { float r, i; };
DEV cf cmul(cf a, cf b) { return {a.r * b.r - a.i * b.i, a.r * b.i + a.i * b.r}; }
DEV cf cfma(cf a, cf b, cf c) { return {fmaf(a.r, b.r, fmaf(-a.i, b.i, c.r)), fmaf(a.r, b.i, fmaf(a.i, b.r, c.i))}; }
typedef f32x2 c2;
DEV c2 cm2(c2 a, c2 b) {
  c2 t, r;
  asm("v_pk_mul_f32 %0, %1, %2 op_sel:[0,0] op_sel_hi:[0,1]" : "=v"(t) : "v"(a), "v"(b));
  asm("v_pk_fma_f32 %0, %1, %2, %3 op_sel:[1,1,0] op_sel_hi:[1,0,1] neg_lo:[1,0,0]" : "=v"(r) : "v"(a), "v"(b), "v"(t));
  return r;
}
DEV f32x2 fma2(f32x2 a, f32x2 b, f32x2 c) { return __builtin_elementwise_fma(a, b, c); }
DEV f32x2 lo2(f32x4 v) { return __builtin_shufflevector(v, v, 0, 1); }
DEV f32x2 hi2(f32x4 v) { return __builtin_shufflevector(v, v, 2, 3); }
DEV f32x4 cat2(f32x2 a, f32x2 b) { return __builtin_shufflevector(a, b, 0, 1, 2, 3); }
DEV void wait_flags(GLB u64* word, bool need) {
  for (u32 spin = 0;; ++spin) {
    const u64 v = __hip_atomic_load(word, RLX_AGENT);
    if (__all(!need || v == FLAG_SET) || spin > SPIN_CAP) break;
    __builtin_amdgcn_s_sleep(POLL_SLEEP);
  }
}
DEV int tile_block(int lt, int idx) {
  const int xcd = lt >> 2, a = lt & 3;
  int j;
  if (idx < 5) { const int e = 5 * a + idx; j = e < 18 ? e : e + 12; }
  else j = 18 + 3 * a + (idx - 5);
  return (j << 3) | xcd;
}

struct P {
  const u16 *x, *la, *aim, *creA, *cimA, *ldtA, *dskA;
  const u16 *lre, *lim, *bre, *bim, *cre, *cim, *ldtB, *dskB, *gw;
  char* ws;
  float* out;
};

constexpr int G_NB = 4, G_STAGE = 32768;
constexpr int G_E = 153600;
constexpr int G_PF = G_E + 4096;
constexpr int G_L = G_PF + 4096;
constexpr int G_X = G_L + 1024;
constexpr int G_T = G_NB * G_STAGE;
static_assert(G_X + 1024 <= 163840 && G_T + 4 * 32 * 144 <= G_E, "role G scratch");

DEV void g_loader(const P& p, LDS char* smem, int c, int g, int d0, int lw, int lane) {
  [[maybe_unused]] const int w = lw + 8;
  __builtin_amdgcn_s_setprio(3);
  GLB u32* ep = (GLB u32*)(p.ws + WS_EP + g * 256);
  GLB u64* gran = (GLB u64*)(p.ws + WS_GRAN) + (size_t)g * 16 * 128;
  const u32 tag = __hip_atomic_load(ep, RLX_AGENT) + 1u;
  u32 xo[4], bo[4];
#pragma unroll
  for (int i = 0; i < 4; ++i) {
    const int rho = 8 * (4 * lw + i) + (lane >> 3);
    const int hf = rho >> 6, m = (rho >> 4) & 3, r = rho & 15;
    const int tau = 64 * hf + 16 * (r >> 2) + 8 * (r & 1) + 2 * m + ((r >> 1) & 1);
    const int ch = (lane & 7) ^ (rho & 7);
    xo[i] = (u32)(128 * c + tau) * 1152u + ch * 16;
    const int pp = 64 * g + 16 * (rho >> 5) + r;
    bo[i] = (u32)pp * 1152u + ch * 16;
  }
  auto issue = [&](int s) {
    LDS char* buf = smem + (s & (G_NB - 1)) * G_STAGE + lw * 4096;
    int kb = g + s; kb = (kb >= 9 ? kb - 9 : kb) * 128;
#pragma unroll
    for (int i = 0; i < 4; ++i) dma16<0>((const char*)p.x + (xo[i] + kb), buf + i * 1024);
#pragma unroll
    for (int i = 0; i < 4; ++i) {
      const char* base = (const char*)((((4 * lw + i) >> 1) & 1) ? p.bim : p.bre);
      dma16<0>(base + (bo[i] + kb), buf + 16384 + i * 1024);
    }
  };
#pragma unroll
  for (int s = 0; s < G_NB - 1; ++s) issue(s);
#pragma unroll 1
  for (int s = 0; s < 6; ++s) { wait_vm<16>(); LSTAMP(0, 16 + s); bar(); issue(s + G_NB - 1); LSTAMP(0, 32 + s); }
  wait_vm<16>(); bar();
  wait_vm<8>(); bar();
  wait_vm<0>(); bar();
  STAMP(0, 1);
  if (lw < 2) {
    bar();
    u32 co[6];
#pragma unroll
    for (int i = 0; i < 6; ++i) {
      const int row = 8 * (6 * lw + i) + (lane >> 3), cp = lane & 7;
      co[i] = (u32)(d0 + row) * 1152u + ((cp ^ (row & 7)) << 4);
    }
    const int rot = ((d0 / 96) * 18) >> 3;
#pragma unroll 1
    for (int s = 0; s < 8; ++s) {
      LDS char* buf = smem + 32768 + s * 12288 + 6 * lw * 1024;
      const int ks = s + rot;
      const char* cb = ks < 9 ? (const char*)p.cre + ks * 128 : (const char*)p.cim + (ks - 9) * 128;
#pragma unroll
      for (int i = 0; i < 6; ++i) dma16<0>(cb + co[i], buf + i * 1024);
    }
    bar();
    return;
  }
  bar();
  STAMP(0, 2);
  if (lw == 3) {
    const f32x2 lv = *(LDS const f32x2*)(smem + G_L + lane * 8);
    const cf l16 = {lv[0], lv[1]};
    f32x2 ev[8];
#pragma unroll
    for (int s = 0; s < 8; ++s) ev[s] = *(LDS const f32x2*)(smem + G_E + (s * 64 + lane) * 8);
    cf tot = {0.f, 0.f};
    cf pre[8];
#pragma unroll
    for (int s = 0; s < 8; ++s) { pre[s] = tot; tot = cfma(l16, tot, cf{ev[s][0], ev[s][1]}); }
    if (c < 15) {
      __hip_atomic_store(gran + c * 128 + lane, ((u64)tag << 32) | __float_as_uint(tot.r), RLX_AGENT);
      __hip_atomic_store(gran + c * 128 + 64 + lane, ((u64)tag << 32) | __float_as_uint(tot.i), RLX_AGENT);
    }
#pragma unroll
    for (int s = 0; s < 8; ++s) *(LDS f32x2*)(smem + G_PF + (s * 64 + lane) * 8) = f32x2{pre[s].r, pre[s].i};
  }
  const int par = lw - 2;
  const f32x2 lv = *(LDS const f32x2*)(smem + G_L + 512 + lane * 8);
  const cf l128 = {lv[0], lv[1]};
  const cf l256 = cmul(l128, l128);
  cf acc = {0.f, 0.f};
  if (par < c) {
    const int nc = (c - par + 1) >> 1;
    u64 a[8], b[8];
    __builtin_amdgcn_s_sleep(GPOLL_DELAY);
    for (u32 spin = 0;; ++spin) {
      bool ok = true;
#pragma unroll
      for (int i = 0; i < 8; ++i) {
        const int cc = par + 2 * (i < nc ? i : 0);
        a[i] = __hip_atomic_load(gran + cc * 128 + lane, RLX_AGENT);
        b[i] = __hip_atomic_load(gran + cc * 128 + 64 + lane, RLX_AGENT);
      }
#pragma unroll
      for (int i = 0; i < 8; ++i) ok = ok && (u32)(a[i] >> 32) == tag && (u32)(b[i] >> 32) == tag;
      if (__all(ok) || spin > SPIN_CAP) break;
      __builtin_amdgcn_s_sleep(GPOLL_SLEEP);
    }
#pragma unroll
    for (int i = 0; i < 8; ++i)
      if (i < nc) acc = cfma(l256, acc, cf{__uint_as_float((u32)a[i]), __uint_as_float((u32)b[i])});
    if (c - 1 - (par + 2 * (nc - 1))) acc = cmul(acc, l128);
  }
  *(LDS f32x2*)(smem + G_X + (par * 64 + lane) * 8) = f32x2{acc.r, acc.i};
  STAMP(0, 3);
  bar();
  if (c == 15 && lw == 2 && lane == 0) __hip_atomic_store(ep, tag, RLX_AGENT);
}

template <int HF>
DEV void g_compute(const P& p, LDS char* smem, int c, int g, int w, int lane, u32& pf) {
  const int pq = w & 3, col = lane & 15, q = lane >> 4;
  const int pl = 16 * pq + col, pp = 64 * g + pl;
  cf lam, coef2;
  {
    const float lre = bf2f(p.lre[pp]), lim = bf2f(p.lim[pp]);
    const float dt = rbf(expf(bf2f(p.ldtB[pp])));
    const float zr = dt * lre, zi = dt * lim;
    const float er = expf(zr);
    float sn, cs;
    sincosf(zi, &sn, &cs);
    lam = {er * cs, er * sn};
    const float nr = lam.r - 1.0f, ni = lam.i;
    const float den = 1.0f / (lre * lre + lim * lim);
    coef2 = {2.0f * (nr * lre + ni * lim) * den, 2.0f * (ni * lre - nr * lim) * den};
  }
  cf l8 = cmul(lam, lam); l8 = cmul(l8, l8); l8 = cmul(l8, l8);
  const cf l16 = cmul(l8, l8);
  const cf l32 = cmul(l16, l16), l64 = cmul(l32, l32);
  if (HF == 0 && q == 0) {
    const cf l128 = cmul(l64, l64);
    *(LDS f32x2*)(smem + G_L + pl * 8) = f32x2{l16.r, l16.i};
    *(LDS f32x2*)(smem + G_L + 512 + pl * 8) = f32x2{l128.r, l128.i};
  }
  cf pw = HF ? l64 : cf{1.f, 0.f};
  { const cf t = cmul(pw, l16); if (q & 1) pw = t; }
  { const cf t = cmul(pw, l32); if (q & 2) pw = t; }
  asm volatile("" : "+v"(lam.r), "+v"(lam.i), "+v"(coef2.r), "+v"(coef2.i), "+v"(l8.r), "+v"(l8.i), "+v"(pw.r), "+v"(pw.i));
  f32x4 are[4], aim[4];
#pragma unroll
  for (int m = 0; m < 4; ++m) { are[m] = f32x4{0, 0, 0, 0}; aim[m] = f32x4{0, 0, 0, 0}; }
  const int o0 = col * 128 + (((0 + q) ^ (col & 7)) << 4);
  const int o1 = col * 128 + (((4 + q) ^ (col & 7)) << 4);
  bf16x8 br[2], bi[2], a[2][4];
  auto read_frags = [&](int s) {
    LDS const char* buf = smem + (s & (G_NB - 1)) * G_STAGE;
    LDS const char* ab = buf + HF * 8192;
    LDS const char* bb = buf + 16384 + pq * 4096;
#pragma unroll
    for (int ks = 0; ks < 2; ++ks) {
      const int o = ks ? o1 : o0;
      br[ks] = lds_frag(bb + o); bi[ks] = lds_frag(bb + 2048 + o);
#pragma unroll
      for (int m = 0; m < 4; ++m) a[ks][m] = lds_frag(ab + m * 2048 + o);
    }
  };
  auto mma = [&]() {
#pragma unroll
    for (int ks = 0; ks < 2; ++ks)
#pragma unroll
      for (int m = 0; m < 4; ++m) {
        are[m] = mfma16(a[ks][m], br[ks], are[m]);
        aim[m] = mfma16(a[ks][m], bi[ks], aim[m]);
      }
  };
  STAMP(0, 1);
#pragma unroll
  for (int s = 0; s < 9; ++s) {
    if (HF == 1 && s == XT_WAIT) {
      wait_vm<0>();
      if (lane < 16) __hip_atomic_store((GLB u64*)(p.ws + WS_FL) + c * DM + 64 * g + 16 * pq + lane, FLAG_SET, RLX_AGENT);
    }
    bar();
    if (s == 0) { STAMP(0, 2); CSTAMP(0, 60); }
    if (HF == 1 && s > 0) { mma(); __builtin_amdgcn_sched_barrier(0); }
    if (HF == 1 && s == 0) {
      const int rr = 4 * q + (col >> 2);
      LDS const char* tb = smem + rr * 128 + (((2 * pq + ((col & 3) >> 1)) ^ (rr & 7)) << 4) + (col & 1) * 8;
      LDS char* sc = smem + G_T + pq * (32 * 144);
#pragma unroll
      for (int hf = 0; hf < 2; ++hf) {
        u32x2 t[4];
#pragma unroll
        for (int m = 0; m < 4; ++m) t[m] = __builtin_bit_cast(u32x2, tr_read(tb + hf * 8192 + m * 2048));
        u32x4 oa, ob;
#pragma unroll
        for (int m = 0; m < 4; ++m) {
          oa[m] = __builtin_amdgcn_perm(t[m][1], t[m][0], 0x05040100u);
          ob[m] = __builtin_amdgcn_perm(t[m][1], t[m][0], 0x07060302u);
        }
        *(LDS u32x4*)(sc + hf * (16 * 144) + col * 144 + q * 32) = oa;
        *(LDS u32x4*)(sc + hf * (16 * 144) + col * 144 + q * 32 + 16) = ob;
      }
      wave_fence();
      const __amdgpu_buffer_rsrc_t rs = mk_rsrc(p.ws + WS_XT, DM * SL * 2);
#pragma unroll
      for (int u = 0; u < 4; ++u) {
        const int row = (lane >> 3) + 8 * (u & 1), hf = u >> 1;
        const u32x4 v = *(LDS const u32x4*)(sc + hf * (16 * 144) + row * 144 + (lane & 7) * 16);
        __builtin_amdgcn_raw_buffer_store_b128(v, rs, ((64 * g + 16 * pq + row) * SL + 128 * c + 64 * hf) * 2 + (lane & 7) * 16, 0, SC1);
      }
      __builtin_amdgcn_sched_barrier(0);
    }
    read_frags(s);
    __builtin_amdgcn_sched_barrier(0);
    if (HF == 0) { mma(); __builtin_amdgcn_sched_barrier(0); }
  }
  if (HF == 1) mma();
  __builtin_amdgcn_sched_barrier(0);
  STAMP(0, 3); CSTAMP(0, 61);
  const f32x2 Lr = {lam.r, lam.r}, Li = {lam.i, lam.i};
  f32x2 er = {0.f, 0.f}, ei = {0.f, 0.f};
#pragma unroll
  for (int t = 0; t < 8; ++t) {
    const f32x2 br_ = (t & 1) ? hi2(are[t >> 1]) : lo2(are[t >> 1]);
    const f32x2 bi_ = (t & 1) ? hi2(aim[t >> 1]) : lo2(aim[t >> 1]);
    const f32x2 nr = fma2(-Li, ei, fma2(Lr, er, br_));
    ei = fma2(Li, er, fma2(Lr, ei, bi_));
    er = nr;
  }
  const cf eA = {er[0], ei[0]};
  const cf e = cfma(l8, eA, cf{er[1], ei[1]});
  const int seg = 4 * HF + q;
  *(LDS f32x2*)(smem + G_E + (seg * 64 + pl) * 8) = f32x2{e.r, e.i};
  STAMP(0, 4);
  bar();
  STAMP(0, 5);
  {
#pragma unroll
    for (int m = 0; m < 4; ++m) asm volatile("" : "+v"(are[m]), "+v"(aim[m]));
    const f32x2 Cr = {coef2.r, coef2.r}, Ci = {coef2.i, coef2.i};
#pragma unroll
    for (int m = 0; m < 4; ++m) {
      const f32x2 r0 = lo2(are[m]), r1 = hi2(are[m]), i0 = lo2(aim[m]), i1 = hi2(aim[m]);
      are[m] = cat2(fma2(-Ci, i0, Cr * r0), fma2(-Ci, i1, Cr * r1));
      aim[m] = cat2(fma2(-Ci, r0, -Cr * i0), fma2(-Ci, r1, -Cr * i1));
      asm volatile("" : "+v"(are[m]), "+v"(aim[m]));
    }
  }
  cf eAc = cmul(coef2, eA), pwc = cmul(coef2, pw);
  asm volatile("" : "+v"(eAc.r), "+v"(eAc.i), "+v"(pwc.r), "+v"(pwc.i));
  STAMP(0, 7);
  bar();
  STAMP(0, 8);
  cf cin;
  {
    f32x2 v[2];
#pragma unroll
    for (int i = 0; i < 2; ++i) v[i] = *(LDS const f32x2*)(smem + G_X + (i * 64 + pl) * 8);
    const f32x2 pf = *(LDS const f32x2*)(smem + G_PF + (seg * 64 + pl) * 8);
    const f32x2 S = v[0] + v[1];
    cin = cfma(pwc, cf{S[0], S[1]}, cmul(coef2, cf{pf[0], pf[1]}));
  }
  const cf cinB = cfma(l8, cin, eAc);
  er = f32x2{cin.r, cinB.r}; ei = f32x2{-cin.i, -cinB.i};
  f32x2 hr[8], hi[8];
#pragma unroll
  for (int t = 0; t < 8; ++t) {
    const f32x2 br_ = (t & 1) ? hi2(are[t >> 1]) : lo2(are[t >> 1]);
    const f32x2 bi_ = (t & 1) ? hi2(aim[t >> 1]) : lo2(aim[t >> 1]);
    const f32x2 nr = fma2(Li, ei, fma2(Lr, er, br_));
    ei = fma2(-Li, er, fma2(Lr, ei, bi_));
    er = nr;
    hr[t] = er; hi[t] = ei;
  }
  u32x4 o[4];
#pragma unroll
  for (int t = 0; t < 4; ++t) {
    o[0][t] = pack_bf16(hr[2 * t][0], hr[2 * t + 1][0]);
    o[1][t] = pack_bf16(hr[2 * t][1], hr[2 * t + 1][1]);
    o[2][t] = pack_bf16(hi[2 * t][0], hi[2 * t + 1][0]);
    o[3][t] = pack_bf16(hi[2 * t][1], hi[2 * t + 1][1]);
  }
  STAMP(0, 9);
  {
    const __amdgpu_buffer_rsrc_t rs = mk_rsrc(p.ws + WS_HB, 2 * DM * SL * 2);
    const int hb = (((c * 2 + HF) * 2) * 9 + g) * 8192 + pq * 2048;
    const int p0 = (16 * q + col) * 16, p1 = 1024 + (16 * q + (col ^ 4)) * 16;
    __builtin_amdgcn_raw_buffer_store_b128(o[0], rs, hb + p0, 0, SC1);
    __builtin_amdgcn_raw_buffer_store_b128(o[1], rs, hb + p1, 0, SC1);
    __builtin_amdgcn_raw_buffer_store_b128(o[2], rs, hb + 9 * 8192 + p0, 0, SC1);
    __builtin_amdgcn_raw_buffer_store_b128(o[3], rs, hb + 9 * 8192 + p1, 0, SC1);
    wait_vm<0>();
    STAMP(0, 10);
    if (lane < 8)
      __hip_atomic_store((GLB u64*)(p.ws + WS_IN) + tile_block(2 * c + HF, lane) * 64 + IN_H + g * 4 + pq, FLAG_SET, RLX_AGENT);
  }
}

constexpr int S_CPY = 4160;
constexpr int S_X = 0;
constexpr int S_ST = 6 * 4 * S_CPY;
constexpr int S_Z = S_ST;
constexpr int S_ZSZ = (256 + SL) * 2;
constexpr int S_T = S_ST + 12 * 4608;
constexpr int S_TSZ = 1312;
constexpr int S_END = S_T + 6 * S_TSZ;
constexpr int S_CNT = 32768 + 8192;

DEV c2 cexp_k(float zl, float th, float tl, float k) {
  const float mag = __builtin_amdgcn_exp2f(zl * k);
  const float t = fmaf(tl, k, __builtin_amdgcn_fractf(th * k));
  return c2{mag * __builtin_amdgcn_cosf(t), mag * __builtin_amdgcn_sinf(t)};
}
#define PKM(d, a, b) "v_pk_mul_f32 " d ", " a ", " b " op_sel:[0,0] op_sel_hi:[0,1]\n\t"
#define PKF(d, a, b) "v_pk_fma_f32 " d ", " a ", " b ", " d " op_sel:[1,1,0] op_sel_hi:[1,0,1] neg_lo:[1,0,0]\n\t"
DEV void octet(LDS char* dst, c2 s0, c2 m1, c2 m2) {
  c2 v1, v2, v3, t0, t1, t2, t3;
  asm(PKM("%0", "%7", "%8") PKM("%3", "%7", "%9") PKF("%0", "%7", "%8") PKF("%3", "%7", "%9")
      PKM("%1", "%0", "%8") PKM("%4", "%0", "%9") PKF("%1", "%0", "%8") PKF("%4", "%0", "%9")
      PKM("%2", "%1", "%8") PKM("%5", "%1", "%9") PKF("%2", "%1", "%8") PKF("%5", "%1", "%9")
      PKM("%6", "%2", "%9") PKF("%6", "%2", "%9")
      : "=&v"(v1), "=&v"(v2), "=&v"(v3), "=&v"(t0), "=&v"(t1), "=&v"(t2), "=&v"(t3) : "v"(s0), "v"(m1), "v"(m2));
  *(LDS u32*)(dst + 0 * 288) = pack_bf16(s0[0], s0[1]);
  *(LDS u32*)(dst + 1 * 288) = pack_bf16(v1[0], v1[1]);
  *(LDS u32*)(dst + 2 * 288) = pack_bf16(v2[0], v2[1]);
  *(LDS u32*)(dst + 3 * 288) = pack_bf16(v3[0], v3[1]);
  *(LDS u32*)(dst + 4 * 288) = pack_bf16(t0[0], t0[1]);
  *(LDS u32*)(dst + 5 * 288) = pack_bf16(t1[0], t1[1]);
  *(LDS u32*)(dst + 6 * 288) = pack_bf16(t2[0], t2[1]);
  *(LDS u32*)(dst + 7 * 288) = pack_bf16(t3[0], t3[1]);
}
DEV u32 dup_lo(u32 a) { return __builtin_amdgcn_perm(a, a, 0x01000100u); }
DEV u32 dup_hi(u32 a) { return __builtin_amdgcn_perm(a, a, 0x03020302u); }
DEV u32 hi_lo(u32 a, u32 b) { return __builtin_amdgcn_alignbit(b, a, 16); }

DEV void role_s(const P& p, LDS char* smem, int sg, int w, int lane) {
  const int tid = w * 64 + lane;
  const int ch = w >> 1, hh = w & 1, d = 6 * sg + ch;
  const int n = d * 128 + 64 * hh + lane;
  const u16 r_ldt = p.ldtA[d], r_la = p.la[n], r_aim = p.aim[n], r_cre = p.creA[n], r_cim = p.cimA[n];
  u32 wb[2] = {0, 0};
  __builtin_amdgcn_sched_barrier(0);
#pragma unroll
  for (int i = 0; i < 2; ++i) {
    const int ln = i * 768 + tid, all = (sg % 12) * 864 + ln;
    if (ln < 864) wb[i] = *(const GLB u32*)((const char*)(all < 5184 ? p.bre : p.bim) + (size_t)(all < 5184 ? all : all - 5184) * 128);
  }
  __builtin_amdgcn_sched_barrier(0);
  STAMP(0, 1);
  f32x4 tab[3];
  [[maybe_unused]] u32 wc[2] = {0, 0};
  LDS char* st = smem + S_ST + w * 4608;
  {
    const float L2E = 1.44269504088896341f;
    const float dt = rbf(__builtin_amdgcn_exp2f(bf2f(r_ldt) * L2E));
    const float ar = -rbf(__builtin_amdgcn_exp2f(bf2f(r_la) * L2E));
    const float zl = ar * dt * L2E, zi = bf2f(r_aim) * dt;
    const float th = zi * 0.15915494309189535f;
    const float tl = fmaf(zi, 0.15915494309189535f, -th) + zi * 4.6017384e-10f;
    const c2 C = {2.0f * (bf2f(r_cre) * dt), 2.0f * (bf2f(r_cim) * dt)};
    const c2 w1 = cexp_k(zl, th, tl, 1.f), w8 = cexp_k(zl, th, tl, 8.f), w128 = cexp_k(zl, th, tl, 128.f);
    c2 w2, w4, w16, w32, w64, w256, w512, w1024, w2048;
    asm(PKM("%0", "%9", "%9") PKM("%2", "%10", "%10") PKM("%5", "%11", "%11") PKF("%0", "%9", "%9") PKF("%2", "%10", "%10") PKF("%5", "%11", "%11")
        PKM("%1", "%0", "%0") PKM("%3", "%2", "%2") PKM("%6", "%5", "%5") PKF("%1", "%0", "%0") PKF("%3", "%2", "%2") PKF("%6", "%5", "%5")
        PKM("%4", "%3", "%3") PKM("%7", "%6", "%6") PKF("%4", "%3", "%3") PKF("%7", "%6", "%6")
        PKM("%8", "%7", "%7") PKF("%8", "%7", "%7")
        : "=&v"(w2), "=&v"(w4), "=&v"(w16), "=&v"(w32), "=&v"(w64), "=&v"(w256), "=&v"(w512), "=&v"(w1024), "=&v"(w2048)
        : "v"(w1), "v"(w8), "v"(w128));
    c2 C128, C256, C512, C1024, C2048;
    asm(PKM("%0", "%5", "%6") PKM("%1", "%5", "%7") PKM("%2", "%5", "%8") PKM("%3", "%5", "%9") PKM("%4", "%5", "%10")
        PKF("%0", "%5", "%6") PKF("%1", "%5", "%7") PKF("%2", "%5", "%8") PKF("%3", "%5", "%9") PKF("%4", "%5", "%10")
        : "=&v"(C128), "=&v"(C256), "=&v"(C512), "=&v"(C1024), "=&v"(C2048)
        : "v"(C), "v"(w128), "v"(w256), "v"(w512), "v"(w1024), "v"(w2048));
#pragma unroll
    for (int i = 0; i < 2; ++i) {
      const int ln = i * 768 + tid, all = (sg % 12) * 864 + ln;
      if (ln < 864) wc[i] = *(const GLB u32*)((const char*)(all < 5184 ? p.cre : p.cim) + (size_t)(all < 5184 ? all : all - 5184) * 128);
    }
    STAMP(0, 2);
    LDS char* wr = st + 4 * lane;
    LDS const char* rd = st + (lane & 15) * 288 + (lane >> 4) * 16;
    bf16x8 B[4];
    const c2 w8c = {w8[0], -w8[1]}, w32c = {w32[0], -w32[1]}, w64c = {w64[0], -w64[1]};
    octet(wr, c2{1.f, 0.f}, w8c, w32c);
    octet(wr + 8 * 288, w64c, w8c, w32c);
    wave_fence();
#pragma unroll
    for (int ks = 0; ks < 4; ++ks) B[ks] = lds_frag(rd + 64 * ks);
    wave_fence();
#pragma unroll
    for (int mt = 0; mt < 3; ++mt) {
      if (mt == 0) { octet(wr, C, w1, w4); octet(wr + 8 * 288, C128, w1, w4); }
      if (mt == 1) { octet(wr, C256, w2, w128); octet(wr + 8 * 288, C512, w128, w4); }
      if (mt == 2) { octet(wr, C1024, w128, w512); *(LDS u32*)(wr + 8 * 288) = pack_bf16(C2048[0], C2048[1]); }
      wave_fence();
      f32x4 acc = {0, 0, 0, 0};
      bf16x8 A[4];
#pragma unroll
      for (int ks = 0; ks < 4; ++ks) A[ks] = lds_frag(rd + 64 * ks);
      __builtin_amdgcn_sched_barrier(0);
#pragma unroll
      for (int ks = 0; ks < 4; ++ks) acc = mfma16(A[ks], B[ks], acc);
      __builtin_amdgcn_sched_barrier(0);
      wave_fence();
      tab[mt] = acc;
    }
  }
  const int b = lane & 15, q = lane >> 4;
#pragma unroll
  for (int mt = 0; mt < 3; ++mt) *(LDS f32x4*)(st + mt * 1024 + lane * 16) = tab[mt];
  STAMP(0, 3);
  bar();
  {
    LDS const char* ot = smem + S_ST + (w ^ 1) * 4608 + lane * 16;
    LDS char* T = smem + S_T + ch * S_TSZ;
    if (hh == 0) {
      const f32x4 v0 = tab[0] + *(LDS const f32x4*)(ot), v2 = tab[2] + *(LDS const f32x4*)(ot + 2048);
      *(LDS u32x2*)(T + 2 * (128 * (q >> 1) + 8 * b + 4 * (q & 1))) = u32x2{pack_bf16(v0[0], v0[1]), pack_bf16(v0[2], v0[3])};
      const u32 a0 = pack_bf16(v2[0], v2[1]), a1 = pack_bf16(v2[2], v2[3]);
      if (q < 2) {
        LDS char* t3 = T + 2 * (512 + 64 * q + b);
        *(LDS u16*)(t3) = (u16)a0; *(LDS u16*)(t3 + 32) = (u16)(a0 >> 16);
        *(LDS u16*)(t3 + 64) = (u16)a1; *(LDS u16*)(t3 + 96) = (u16)(a1 >> 16);
      } else if (lane == 32) *(LDS u16*)(T + 2 * 640) = (u16)a0;
    } else {
      const f32x4 v1 = tab[1] + *(LDS const f32x4*)(ot + 1024);
      const u32 a0 = pack_bf16(v1[0], v1[1]), a1 = pack_bf16(v1[2], v1[3]);
      if (q < 2) *(LDS u32x2*)(T + 2 * (256 + 64 * q + 4 * b)) = u32x2{a0, a1};
      else {
        LDS char* t2 = T + 2 * (384 + 2 * b + (q - 2));
        *(LDS u16*)(t2) = (u16)a0; *(LDS u16*)(t2 + 64) = (u16)(a0 >> 16);
        *(LDS u16*)(t2 + 128) = (u16)a1; *(LDS u16*)(t2 + 192) = (u16)(a1 >> 16);
      }
    }
  }
  bar();
  STAMP(0, 4);
  u32x4 uv[2];
  {
    GLB u64* fl = (GLB u64*)(p.ws + WS_FL) + (8 * hh + (lane & 7)) * DM + d;
    asm volatile("" :: "v"(wc[0]), "v"(wc[1]), "v"(wb[0]), "v"(wb[1]));
    wait_flags(fl, true);
    const __amdgpu_buffer_rsrc_t rs = mk_rsrc(p.ws + WS_XT, DM * SL * 2);
#pragma unroll
    for (int v = 0; v < 2; ++v) uv[v] = __builtin_amdgcn_raw_buffer_load_b128(rs, (d * SL + 1024 * hh + 8 * (lane + 64 * v)) * 2, 0, SC1);
    if (lane < 8) __hip_atomic_store(fl, 0ull, RLX_AGENT);
  }
  STAMP(0, 5);
  {
    LDS const char* T = smem + S_T + ch * S_TSZ;
    u32 o[10];
    const u32 e512 = *(LDS const u16*)(T + 2 * 512);
    o[8] = o[9] = e512 | (e512 << 16);
    if (hh == 0) {
      const int off = lane < 16 ? 32 * lane : (lane < 32 ? 256 + 16 * lane : 512 + 8 * lane);
      u32x2 W[4];
#pragma unroll
      for (int i = 0; i < 4; ++i) W[i] = *(LDS const u32x2*)(T + off + 8 * i);
      if (lane < 16) {
#pragma unroll
        for (int i = 0; i < 8; ++i) o[i] = W[i >> 1][i & 1];
      } else if (lane < 32) {
#pragma unroll
        for (int i = 0; i < 4; ++i) {
          const u32 a = W[i >> 1][i & 1], nx = W[(i + 1) >> 1][(i + 1) & 1];
          o[2 * i] = dup_lo(a);
          o[2 * i + 1] = hi_lo(a, nx);
        }
      } else {
        const u32 w0 = W[0][0], w1 = W[0][1], w2 = W[1][0];
        o[0] = dup_lo(w0); o[1] = w0; o[2] = dup_hi(w0); o[3] = dup_lo(w1);
        o[4] = o[3]; o[5] = w1; o[6] = dup_hi(w1); o[7] = dup_lo(w2);
      }
    } else {
      const u32 w0 = *(LDS const u32*)(T + 1024 + 4 * lane), w1 = *(LDS const u32*)(T + 1028 + 4 * lane);
      o[0] = o[1] = dup_lo(w0); o[2] = w0; o[3] = o[4] = o[5] = dup_hi(w0); o[6] = o[7] = dup_lo(w1);
    }
    o[8] = (u32)__builtin_amdgcn_update_dpp((int)o[8], (int)o[0], 0x130, 0xf, 0xf, false);
    o[9] = (u32)__builtin_amdgcn_update_dpp((int)o[9], (int)o[1], 0x130, 0xf, 0xf, false);
    u32 a[9];
#pragma unroll
    for (int i = 0; i < 9; ++i) a[i] = hi_lo(o[i], o[i + 1]);
    LDS char* xb = smem + S_X + ch * 4 * S_CPY + 32 + 2 * (1024 * hh + 16 * lane);
    *(LDS u32x4*)(xb) = u32x4{o[0], o[1], o[2], o[3]};
    *(LDS u32x4*)(xb + 16) = u32x4{o[4], o[5], o[6], o[7]};
    *(LDS u32x4*)(xb + S_CPY) = u32x4{a[0], a[1], a[2], a[3]};
    *(LDS u32x4*)(xb + S_CPY + 16) = u32x4{a[4], a[5], a[6], a[7]};
    *(LDS u32x4*)(xb + 2 * S_CPY) = u32x4{o[1], o[2], o[3], o[4]};
    *(LDS u32x4*)(xb + 2 * S_CPY + 16) = u32x4{o[5], o[6], o[7], o[8]};
    *(LDS u32x4*)(xb + 3 * S_CPY) = u32x4{a[1], a[2], a[3], a[4]};
    *(LDS u32x4*)(xb + 3 * S_CPY + 16) = u32x4{a[5], a[6], a[7], a[8]};
    if (hh == 0 && lane == 0) {
      LDS char* hb = smem + S_X + ch * 4 * S_CPY;
      const u32x4 z = {0, 0, 0, 0};
      *(LDS u32x4*)(hb) = z;
      *(LDS u32x4*)(hb + 16) = z;
      *(LDS u32x4*)(hb + S_CPY) = z;
      *(LDS u32x4*)(hb + S_CPY + 16) = u32x4{0, 0, 0, o[0] << 16};
      *(LDS u32x4*)(hb + 2 * S_CPY) = z;
      *(LDS u32x4*)(hb + 2 * S_CPY + 16) = u32x4{0, 0, 0, o[0]};
      *(LDS u32x4*)(hb + 3 * S_CPY) = z;
      *(LDS u32x4*)(hb + 3 * S_CPY + 16) = u32x4{0, 0, o[0] << 16, a[0]};
    }
    if (tid < 6 * 32) *(LDS u32x4*)(smem + S_Z + (tid >> 5) * S_ZSZ + (tid & 31) * 16) = u32x4{0, 0, 0, 0};
    STAMP(0, 6);
    LDS char* z = smem + S_Z + ch * S_ZSZ + 512 + (1024 * hh + 8 * lane) * 2;
    *(LDS u32x4*)z = uv[0];
    *(LDS u32x4*)(z + 1024) = uv[1];
  }
  bar();
  STAMP(0, 7);
  f32x4 acc[8];
#pragma unroll
  for (int i = 0; i < 8; ++i) acc[i] = f32x4{0, 0, 0, 0};
  {
    const int kq = q, qq = b >> 2, pp = b & 3;
    LDS const char* fb = smem + S_X + (ch * 4 + ((4 - qq) & 3)) * S_CPY +
                         2 * (16 + 16 * (kq >> 1) - 8 * (kq & 1) + 4 * pp - (qq ? 4 : 0)) + 64 * hh;
    LDS const char* gb = smem + S_Z + ch * S_ZSZ + 512 + 32 * (b - (kq >> 1)) + 16 * (kq & 1) - 64 * hh;
#pragma unroll 1
    for (int it = 0; it < 4; ++it) {
      bf16x8 F[8], G[8];
#pragma unroll
      for (int s = 0; s < 8; ++s) {
        F[s] = tr_frag(fb + 512 * s, fb + 512 * s - 8);
        G[s] = lds_frag(gb + 512 * s);
      }
      __builtin_amdgcn_sched_barrier(0);
#pragma unroll
      for (int t = 0; t < 8; ++t)
#pragma unroll
        for (int s = 0; s <= t; ++s) acc[t] = mfma16(F[s], G[t - s], acc[t]);
      fb += 128;
      gb -= 128;
    }
  }
  __builtin_amdgcn_sched_barrier(0);
  STAMP(0, 8);
  bar();
  {
    LDS char* ex = smem + S_X + 49152 + w * 4096 + lane * 16;
#pragma unroll
    for (int i = 0; i < 4; ++i) {
      const f32x4 lo = acc[i], hi = acc[4 + i];
      *(LDS f32x4*)(ex + i * 1024) = hh ? +lo : +hi;
    }
    if (tid == 0) *(LDS u32*)(smem + S_CNT) = 0;
  }
  bar();
  u32x2 yo[4];
  {
    LDS const char* ey = smem + S_X + 49152 + (w ^ 1) * 4096 + lane * 16;
#pragma unroll
    for (int i = 0; i < 4; ++i) {
      const f32x4 lo = acc[i], hi = acc[4 + i];
      const f32x4 y = (hh ? +hi : +lo) + *(LDS const f32x4*)(ey + i * 1024);
      yo[i] = u32x2{pack_bf16(y[0], y[1]), pack_bf16(y[2], y[3])};
    }
  }
  bar();
  {
    const __amdgpu_buffer_rsrc_t rs = mk_rsrc(p.ws + WS_AT, DM * SL * 2);
    const int dst = (d * SL + 1024 * hh + 16 * (lane & 15) + 4 * (lane >> 4)) * 2;
#pragma unroll
    for (int i = 0; i < 4; ++i) __builtin_amdgcn_raw_buffer_store_b64(yo[i], rs, dst + 512 * i, 0, SC1);
  }
  STAMP(0, 9);
}
DEV void s_signal(const P& p, LDS char* smem, int sg, int lane) {
  u32 old = 0;
  if (lane == 0) old = __hip_atomic_fetch_add((LDS u32*)(smem + S_CNT), 1u, __ATOMIC_RELAXED, __HIP_MEMORY_SCOPE_WORKGROUP);
  if (__builtin_amdgcn_readfirstlane(old) == 11) {
    const int lt = lane & 31, second = lane >> 5;
    const int dd = 6 * sg + (second ? 5 : 0);
    const int idx = dd < 480 ? dd / 96 : 5 + (dd - 480) / 32;
    const int dfirst = idx < 5 ? 96 * idx : 480 + 32 * (idx - 5);
    __hip_atomic_store((GLB u64*)(p.ws + WS_IN) + tile_block(lt, idx) * 64 + IN_A + (sg - dfirst / 6), FLAG_SET, RLX_AGENT);
  }
}

DEV void role_gate(const P& p, int rb, int w, int lane, u64 pc) {
  u32 pf0 = 0, pf1 = 0;
  const int tid = w * 64 + lane;
  const int sub = lane & 7;
  u32x4 wv[9];
#pragma unroll
  for (int i = 0; i < 9; ++i) wv[i] = *(const u32x4*)((const char*)p.gw + (i * 8 + sub) * 16);
  for (int r = w * 8 + (lane >> 3); r < 128; r += 96) {
    const int l = rb * 128 + r;
    float s = 0.f;
    u32x4 xv[9];
#pragma unroll
    for (int i = 0; i < 9; ++i) xv[i] = *(const u32x4*)((const char*)p.x + (size_t)l * 1152 + (i * 8 + sub) * 16);
#pragma unroll
    for (int i = 0; i < 9; ++i) {
#pragma unroll
      for (int k = 0; k < 4; ++k) {
        const u32 pr = pack_bf16(bflo(xv[i][k]) * bflo(wv[i][k]), bfhi(xv[i][k]) * bfhi(wv[i][k]));
        s += bflo(pr);
        s += bfhi(pr);
      }
    }
    s += __shfl_xor(s, 1); s += __shfl_xor(s, 2); s += __shfl_xor(s, 4);
    const float gv = rbf(1.0f / (1.0f + expf(-rbf(s))));
    if (sub == 0)
      __hip_atomic_store((GLB u64*)(p.ws + WS_G) + l, ((u64)__float_as_uint(rbf(1.0f - gv)) << 32) | __float_as_uint(gv), RLX_AGENT);
  }
  asm volatile("" :: "v"(pf0), "v"(pf1));
  wait_vm<0>();
  bar();
  if (w == 0 && lane < 16)
    __hip_atomic_store((GLB u64*)(p.ws + WS_IN) + tile_block(2 * rb + (lane >> 3), lane & 7) * 64 + IN_G, FLAG_SET, RLX_AGENT);
}

constexpr int H_C = 32768, H_CS = 12288;
DEV int h_slot(int i) { return i < 4 ? i * 8192 : 131072 + (i - 4) * 8192; }
constexpr int H_AT = H_C + 2 * H_CS;
constexpr int H_XT = H_C + 3 * H_CS;
constexpr int H_G = H_C + 4 * H_CS;
constexpr int H_OUT = H_C + 5 * H_CS;
constexpr int K_LDS = 163840;

constexpr int h_wait(int s, int NDT) {
  int n = 0;
  int need = 0;
  for (int k = 0; k < 4; ++k) { n += 2; if (k == s) need = n; }
  for (int t = 0; t < s; ++t) {
    if (t == 0) { n += 2; if (s == 4) need = n; n += 2; if (s == 5) need = n; }
    else if (t == 1) { n += 2; if (s == 6) need = n; n += 2; if (s == 7) need = n; }
    else if (t <= 11) { n += 2 + NDT; if (s == t + 6) need = n; if (t == 2) n += 1; }
    else if (t == 12 || t == 13) n += NDT;
    else if (t == 14) n += 1;
  }
  return s == 17 ? 0 : n - need;
}
template <int S, int NDT, class F> DEV void h_steps(F&& after, u64& fv, char* wsp) {
  if constexpr (S == 9) asm volatile("s_waitcnt vmcnt(%1)" : "+v"(fv) : "n"(h_wait(S, NDT)) : "memory");
  else wait_vm<h_wait(S, NDT)>();
  LSTAMP2(1, 16 + S);
  bar();
  after(std::integral_constant<int, S>{});
  if constexpr (S < 17) h_steps<S + 1, NDT>(after, fv, wsp);
}
DEV int p2_rot(int d0) { const int idx = d0 < 480 ? d0 / 96 : 5 + (d0 - 480) / 32; return (idx * 18) >> 3; }

template <int NDT, class F>
DEV void p2_loader(const P& p, LDS char* smem, int lt, int d0, int lw, int lane, bool scratch_barrier, F&& pre_poll) {
  __builtin_amdgcn_s_setprio(3);
  const char* hb = p.ws + WS_HB + (size_t)lt * 18 * 8192 + (2 * lw) * 1024 + lane * 16;
  constexpr int W = 32 * NDT;
  const int l0 = 64 * lt, rot = p2_rot(d0);
  u32 co[NDT];
#pragma unroll
  for (int i = 0; i < NDT; ++i) {
    const int row = 8 * (NDT * lw + i) + (lane >> 3), cp = lane & 7;
    co[i] = (u32)(d0 + row) * 1152u + ((cp ^ (row & 7)) << 4);
  }
  auto issue_c = [&](int s) {
    LDS char* buf = smem + H_C + (s & 7) * H_CS + NDT * lw * 1024;
    int ks = s + rot; ks = ks >= 18 ? ks - 18 : ks;
    const char* cb = ks < 9 ? (const char*)p.cre + ks * 128 : (const char*)p.cim + (ks - 9) * 128;
#pragma unroll
    for (int i = 0; i < NDT; ++i) dma16<0>(cb + co[i], buf + i * 1024);
  };
  auto issue_h = [&](int s) {
    LDS char* buf = smem + h_slot(s & 7) + 2 * lw * 1024;
    int ks = s + rot; ks = ks >= 18 ? ks - 18 : ks;
#pragma unroll
    for (int i = 0; i < 2; ++i) dma16<SC1>(hb + ks * 8192 + i * 1024, buf + i * 1024);
  };
  GLB u64* inb = (GLB u64*)(p.ws + WS_IN) + blockIdx.x * 64;
  const int nA = (d0 + W - 1) / 6 - d0 / 6 + 1;
  const bool need = lane == IN_G || (lane >= IN_A && lane < IN_A + nA);
  if (!scratch_barrier) {
#pragma unroll 1
    for (int s = 0; s < 8; ++s) issue_c(s);
  }
  pre_poll();
  { [[maybe_unused]] const int w = lw + 8; STAMP(1, 1); }
  if (scratch_barrier) __builtin_amdgcn_s_sleep(HPOLL_DELAY);
  wait_flags(inb + IN_H + (lane < 36 ? lane : 0), true);
  { [[maybe_unused]] const int w = lw + 8; STAMP(1, 2); }
#pragma unroll
  for (int s = 0; s < 4; ++s) issue_h(s);
  { [[maybe_unused]] const int w = lw + 8; STAMP(1, 3); }
  u64 fv = 0;
  h_steps<0, NDT>([&](auto S_) {
    constexpr int S = decltype(S_)::value;
    if constexpr (S == 0) { issue_h(4); issue_h(5); }
    if constexpr (S == 1) { issue_h(6); issue_h(7); }
    if constexpr (S >= 2 && S <= 11) { issue_h(S + 6); issue_c(S + 6); }
    if constexpr (S == 2)
      asm volatile("global_load_dwordx2 %0, %1, off sc1" : "=v"(fv) : "v"(inb + lane) : "memory");
    if constexpr (S == 9) { if (!__all(!need || fv == FLAG_SET)) wait_flags(inb + lane, need); }
    if constexpr (S == 12) {
#pragma unroll
      for (int i = 0; i < NDT; ++i) {
        const int pc = NDT * lw + i;
        dma16<SC1>(p.ws + WS_AT + (size_t)(d0 + 8 * pc + (lane >> 3)) * 4096 + l0 * 2 + (lane & 7) * 16, smem + H_AT + pc * 1024);
      }
    }
    if constexpr (S == 13) {
#pragma unroll
      for (int i = 0; i < NDT; ++i) {
        const int pc = NDT * lw + i, id = pc * 64 + lane, row = id / (4 * NDT), qq = id - row * (4 * NDT);
        dma16<0>((const char*)p.x + (size_t)(l0 + row) * 1152 + d0 * 2 + qq * 16, smem + H_XT + pc * 1024);
      }
    }
    if constexpr (S == 14) { if (lane < 8) dma16<SC1>(p.ws + WS_G + (size_t)l0 * 8 + lw * 128 + lane * 16, smem + H_G + lw * 128); }
  }, fv, p.ws);
}

template <int NDT, int STAG>
DEV void p2_compute(const P& p, LDS char* smem, int lt0, int d0, int w, int lane) {
  const int lh = (w >> 1) & 1, kh = w & 1, dg = w >> 2;
  const int r = lane & 15, kq = lane >> 4;
  const int lt = 2 * lh + kh, l0 = 64 * lt0;
  constexpr int W = 32 * NDT;
  u32x2 da[NDT], db[NDT];
#pragma unroll
  for (int t = 0; t < NDT; ++t) {
    const int d = d0 + 16 * (NDT * dg + t) + 4 * kq;
    da[t] = *(const u32x2*)(p.dskA + d);
    db[t] = *(const u32x2*)(p.dskB + d);
  }
  GLB u64* inb = (GLB u64*)(p.ws + WS_IN) + blockIdx.x * 64;

  f32x4 acc[2][NDT];
#pragma unroll
  for (int i = 0; i < 2; ++i)
#pragma unroll
    for (int t = 0; t < NDT; ++t) acc[i][t] = f32x4{0, 0, 0, 0};
  int ho[2];
#pragma unroll
  for (int u = 0; u < 2; ++u) {
    const int k = 32 * kh + 8 * kq + 4 * u + (r >> 2), i = (r & 3) >> 1;
    ho[u] = (2 * (k >> 4) + i) * 1024 + (32 * lh + ((k & 15) ^ (4 * i))) * 16 + (r & 1) * 8;
  }
  const int co = H_C + (16 * NDT * dg + r) * 128 + (((4 * kh + kq) ^ (r & 7)) << 4);
  constexpr int NREG = 0, NRING = 18 - NREG;
  bf16x8 cr[NREG ? NREG : 1][NDT];
  if constexpr (NREG > 0) {
    const int rot = p2_rot(d0);
#pragma unroll
    for (int i = 0; i < NREG; ++i) {
      int ks = NRING + i + rot; ks = ks >= 18 ? ks - 18 : ks;
      const char* cb = (ks < 9 ? (const char*)p.cre + ks * 128 : (const char*)p.cim + (ks - 9) * 128) +
                       (size_t)(d0 + 16 * NDT * dg + r) * 1152 + (4 * kh + kq) * 16;
#pragma unroll
      for (int t = 0; t < NDT; ++t) cr[i][t] = *(const GLB bf16x8*)(cb + t * 16 * 1152);
    }
  }
  bf16x8 hb[2], ca[NDT];
#pragma unroll
  for (int i = 0; i < 2; ++i) hb[i] = bf16x8{};
#pragma unroll
  for (int t = 0; t < NDT; ++t) ca[t] = bf16x8{};
  auto mma_with = [&](const bf16x8 (&cc)[NDT]) {
#pragma unroll
    for (int t = 0; t < NDT; ++t) {
      acc[0][t] = mfma16(cc[t], hb[0], acc[0][t]);
      acc[1][t] = mfma16(cc[t], hb[1], acc[1][t]);
    }
    __builtin_amdgcn_sched_barrier(0);
  };
  auto read_h = [&](int s) {
    LDS const char* hbuf = smem + h_slot(s & 7);
#pragma unroll
    for (int i = 0; i < 2; ++i) hb[i] = tr_frag(hbuf + ho[0] + i * 256, hbuf + ho[1] + i * 256);
  };
  STAMP(1, 1);
#pragma unroll 1
  for (int s = 0; s < NRING; ++s) {
    bar();
    if (s == 0) STAMP(1, 2);
    LSTAMP(1, 16 + s);
    if (STAG) mma_with(ca);
    read_h(s);
    LDS const char* cbuf = smem + (s & 7) * H_CS;
#pragma unroll
    for (int t = 0; t < NDT; ++t) ca[t] = lds_frag(cbuf + co + t * 2048);
    __builtin_amdgcn_sched_barrier(0);
    if (!STAG) mma_with(ca);
  }
#pragma unroll
  for (int i = 0; i < NREG; ++i) {
    bar();
    if (STAG) { if (i == 0) mma_with(ca); else mma_with(cr[i ? i - 1 : 0]); }
    read_h(NRING + i);
    __builtin_amdgcn_sched_barrier(0);
    if (!STAG) mma_with(cr[i]);
  }
  if (STAG) { if (NREG) mma_with(cr[NREG ? NREG - 1 : 0]); else mma_with(ca); }
  __builtin_amdgcn_sched_barrier(0);
  STAMP(1, 3);
  bar();
  if (w == 0) __hip_atomic_store(inb + lane, 0ull, RLX_AGENT);
  LDS char* ex = smem + w * (NDT * 1024) + lane * 16;
#pragma unroll
  for (int t = 0; t < NDT; ++t) {
    const f32x4 a0 = acc[0][t], a1 = acc[1][t];
    *(LDS f32x4*)(ex + t * 1024) = kh ? +a0 : +a1;
  }
  bar();
  LDS const char* ey = smem + (w ^ 1) * (NDT * 1024) + lane * 16;
  constexpr int OSTR = W * 4 + 16;
  const f32x2 gg = *(LDS const f32x2*)(smem + H_G + (16 * lt + r) * 8);
#pragma unroll
  for (int t = 0; t < NDT; ++t) {
    const int dt = NDT * dg + t;
    const f32x4 o = *(LDS const f32x4*)(ey + t * 1024);
    const f32x4 a0 = acc[0][t], a1 = acc[1][t];
    const f32x4 y = (kh ? +a1 : +a0) + o;
    const s16x4 av = tr_read(smem + H_AT + (16 * dt + 4 * kq + (r >> 2)) * 128 + lt * 32 + (r & 3) * 8);
    const u32x2 xv = *(LDS const u32x2*)(smem + H_XT + (16 * lt + r) * (W * 2) + (16 * dt + 4 * kq) * 2);
    f32x4 res;
#pragma unroll
    for (int i = 0; i < 4; ++i) {
      const u32 xw = xv[i >> 1], aw = da[t][i >> 1], bw = db[t][i >> 1];
      const float xf = (i & 1) ? bfhi(xw) : bflo(xw);
      const float sa = (i & 1) ? bfhi(aw) : bflo(aw);
      const float sb = (i & 1) ? bfhi(bw) : bflo(bw);
      const float yc = bf2f((u16)av[i]);
      const float a = rbf(yc + rbf(xf * sa));
      const float b = y[i] + rbf(xf * sb);
      res[i] = rbf(gg[0] * a) + gg[1] * b;
    }
    *(LDS f32x4*)(smem + H_OUT + (16 * lt + r) * OSTR + (16 * dt + 4 * kq) * 4) = res;
  }
  STAMP(1, 4);
  bar();
#pragma unroll
  for (int it = 0; it < NDT; ++it) {
    const int id = it * 512 + w * 64 + lane;
    const int row = id / (8 * NDT), pc = id - row * (8 * NDT);
    const f32x4 v = *(LDS const f32x4*)(smem + H_OUT + row * OSTR + pc * 16);
    __builtin_amdgcn_raw_buffer_store_b128(__builtin_bit_cast(u32x4, v), mk_rsrc(p.out, SL * DM * 4), ((l0 + row) * DM + d0 + pc * 4) * 4, 0, OUT_AUX);
  }
}

template <int NDT>
DEV void phase2(const P& p, LDS char* smem, int lt, int d0, int w, int lane, bool scratch_barrier, int sg) {
  STAMP(1, 0);
  if (w >= 8) {
    p2_loader<NDT>(p, smem, lt, d0, w - 8, lane, scratch_barrier, [&]() { if (NDT == 1) { wait_vm<8>(); s_signal(p, smem, sg, lane); } });
  } else {
    if (NDT == 1) { wait_vm<0>(); s_signal(p, smem, sg, lane); }
    if (w < 4) p2_compute<NDT, 0>(p, smem, lt, d0, w, lane);
    else p2_compute<NDT, 1>(p, smem, lt, d0, w, lane);
  }
  STAMP(1, 15);
}

__global__ __launch_bounds__(768, 3) void gated_mix_s4d_s5(P p) {
  extern __shared__ __attribute__((aligned(16))) char smem_raw[];
  LDS char* smem = (LDS char*)smem_raw;
  u64 pc;
  asm volatile("s_getpc_b64 %0" : "=s"(pc));
  u32 pf = 0;
#define PF_TOP 312
  if (threadIdx.x < PF_TOP)
    asm volatile("global_load_dword %0, %1, off" : "=v"(pf) : "v"((pc & ~255ull) + threadIdx.x * 128));
  const int w = __builtin_amdgcn_readfirstlane(threadIdx.x >> 6), lane = threadIdx.x & 63;
  const int xcd = blockIdx.x & 7, j = blockIdx.x >> 3;
  STAMP(0, 0);
  if (j >= 18 && j < 30) {
    const int sg = 12 * xcd + (j - 18);
    role_s(p, smem, sg, w, lane);
    STAMP(0, 15);
    phase2<1>(p, smem, 4 * xcd + (j - 18) / 3, 480 + 32 * ((j - 18) % 3), w, lane, false, sg);
  } else {
    const bool isg = j < 18;
    if (isg) {
      const int c = 2 * xcd + j / 9, g = j % 9;
      if (w >= 8) g_loader(p, smem, c, g, 96 * (j % 5), w - 8, lane);
      else if (w < 4) g_compute<0>(p, smem, c, g, w, lane, pf);
      else g_compute<1>(p, smem, c, g, w, lane, pf);
    } else {
      role_gate(p, 2 * xcd + (j - 30), w, lane, pc);
    }
    STAMP(0, 15);
    const int e = isg ? j : j - 12;
    phase2<3>(p, smem, 4 * xcd + e / 5, 96 * (e % 5), w, lane, isg, 0);
  }
  asm volatile("s_waitcnt vmcnt(0)" :: "v"(pf));
}

extern "C" void kernel_launch(void* const* d_in, const int* in_sizes, int n_in, void* d_out, int out_size,
                              void* d_ws, size_t ws_size, hipStream_t stream) {
  P a;
  a.x = (const u16*)d_in[0]; a.la = (const u16*)d_in[1]; a.aim = (const u16*)d_in[2];
  a.creA = (const u16*)d_in[3]; a.cimA = (const u16*)d_in[4]; a.ldtA = (const u16*)d_in[5]; a.dskA = (const u16*)d_in[6];
  a.lre = (const u16*)d_in[8]; a.lim = (const u16*)d_in[9]; a.bre = (const u16*)d_in[10]; a.bim = (const u16*)d_in[11];
  a.cre = (const u16*)d_in[12]; a.cim = (const u16*)d_in[13]; a.ldtB = (const u16*)d_in[14];
  a.dskB = (const u16*)d_in[15]; a.gw = (const u16*)d_in[16];
  a.ws = (char*)d_ws + WS_OFF; a.out = (float*)d_out;
  (void)hipFuncSetAttribute((const void*)gated_mix_s4d_s5, hipFuncAttributeMaxDynamicSharedMemorySize, K_LDS);
  gated_mix_s4d_s5<<<256, 768, K_LDS, stream>>>(a);
}
```

```cpp
#define GPOLL_DELAY 28
#define GPOLL_SLEEP 4
#define POLL_SLEEP 3
#include <hip/hip_runtime.h>
#include <hip/hip_bf16.h>
#include <stdint.h>
#include <type_traits>

typedef unsigned short u16;
typedef unsigned int u32;
typedef unsigned long long u64;
typedef __attribute__((ext_vector_type(8))) __bf16 bf16x8;
typedef __attribute__((ext_vector_type(2))) __bf16 bf16x2;
typedef __attribute__((ext_vector_type(4))) short s16x4;
typedef __attribute__((ext_vector_type(4))) float f32x4;
typedef __attribute__((ext_vector_type(2))) float f32x2;
typedef __attribute__((ext_vector_type(4))) unsigned u32x4;
typedef __attribute__((ext_vector_type(2))) unsigned u32x2;
#define LDS __attribute__((address_space(3)))
#define GLB __attribute__((address_space(1)))
#define DEV __device__ __forceinline__
#define RLX_AGENT __ATOMIC_RELAXED, __HIP_MEMORY_SCOPE_AGENT

constexpr int SL = 2048;
constexpr int DM = 576;

constexpr size_t WS_HB = 0;
constexpr size_t WS_AT = WS_HB + (size_t)2 * DM * SL * 2;
constexpr size_t WS_G = WS_AT + (size_t)DM * SL * 2;
constexpr size_t WS_GRAN = WS_G + (size_t)SL * 8;
constexpr size_t WS_EP = WS_GRAN + (size_t)9 * 16 * 2 * 64 * 8;
constexpr size_t WS_FL = WS_EP + 4096;
constexpr size_t WS_XT = WS_FL + (size_t)16 * DM * 8;
constexpr size_t WS_IN = WS_XT + (size_t)DM * SL * 2;
constexpr u64 FLAG_SET = 0x9E3779B97F4A7C15ull;
constexpr u32 SPIN_CAP = 1u << 17;
constexpr int IN_H = 0, IN_G = 36, IN_A = 40;

constexpr size_t WS_ST = (size_t)16 << 20;
#define STAMP(kern, idx) do {} while (0)
#define CSTAMP(kern, idx) do {} while (0)
#define LSTAMP(kern, idx) do {} while (0)
#define LSTAMP2(kern, idx) do {} while (0)

DEV float bf2f(u16 v) { return __uint_as_float((u32)v << 16); }
DEV float bflo(u32 v) { return __uint_as_float(v << 16); }
DEV float bfhi(u32 v) { return __uint_as_float(v & 0xffff0000u); }
DEV u32 pack_bf16(float lo, float hi) {
  f32x2 v = {lo, hi};
  bf16x2 b = __builtin_convertvector(v, bf16x2);
  return __builtin_bit_cast(u32, b);
}
DEV float rbf(float x) { return bflo(pack_bf16(x, x)); }
template <int N> DEV void wait_vm() { asm volatile("s_waitcnt vmcnt(%0)" ::"n"(N) : "memory"); }
DEV void bar() { asm volatile("s_waitcnt lgkmcnt(0)\n\ts_barrier" ::: "memory"); }
DEV void wave_fence() { __builtin_amdgcn_fence(__ATOMIC_ACQ_REL, "wavefront"); }
template <int AUX> DEV void dma16(const void* src, LDS char* dst) {
  __builtin_amdgcn_global_load_lds((const GLB u32*)src, (LDS u32*)dst, 16, 0, AUX);
}
constexpr int SC1 = 16;
#define OUT_AUX 16
#define WS_OFF 0
#define XT_WAIT 4
DEV bf16x8 lds_frag(LDS const char* p) { return *(LDS const bf16x8*)p; }
DEV s16x4 tr_read(LDS const char* a) { return __builtin_amdgcn_ds_read_tr16_b64_v4i16((LDS s16x4*)a); }
DEV bf16x8 tr_frag(LDS const char* a, LDS const char* b) {
  const s16x4 lo = tr_read(a), hi = tr_read(b);
  return __builtin_bit_cast(bf16x8, __builtin_shufflevector(lo, hi, 0, 1, 2, 3, 4, 5, 6, 7));
}
DEV f32x4 mfma16(bf16x8 a, bf16x8 b, f32x4 c) { return __builtin_amdgcn_mfma_f32_16x16x32_bf16(a, b, c, 0, 0, 0); }
DEV __amdgpu_buffer_rsrc_t mk_rsrc(const void* base, int bytes) {
  return __builtin_amdgcn_make_buffer_rsrc(const_cast<void*>(base), (short)0, bytes, 0x00020000);
}
struct cf { float r, i; };
DEV cf cmul(cf a, cf b) { return {a.r * b.r - a.i * b.i, a.r * b.i + a.i * b.r}; }
DEV cf cfma(cf a, cf b, cf c) { return {fmaf(a.r, b.r, fmaf(-a.i, b.i, c.r)), fmaf(a.r, b.i, fmaf(a.i, b.r, c.i))}; }
typedef f32x2 c2;
DEV c2 cm2(c2 a, c2 b) {
  c2 t, r;
  asm("v_pk_mul_f32 %0, %1, %2 op_sel:[0,0] op_sel_hi:[0,1]" : "=v"(t) : "v"(a), "v"(b));
  asm("v_pk_fma_f32 %0, %1, %2, %3 op_sel:[1,1,0] op_sel_hi:[1,0,1] neg_lo:[1,0,0]" : "=v"(r) : "v"(a), "v"(b), "v"(t));
  return r;
}
DEV f32x2 fma2(f32x2 a, f32x2 b, f32x2 c) { return __builtin_elementwise_fma(a, b, c); }
DEV f32x2 lo2(f32x4 v) { return __builtin_shufflevector(v, v, 0, 1); }
DEV f32x2 hi2(f32x4 v) { return __builtin_shufflevector(v, v, 2, 3); }
DEV f32x4 cat2(f32x2 a, f32x2 b) { return __builtin_shufflevector(a, b, 0, 1, 2, 3); }
DEV void wait_flags(GLB u64* word, bool need) {
  for (u32 spin = 0;; ++spin) {
    const u64 v = __hip_atomic_load(word, RLX_AGENT);
    if (__all(!need || v == FLAG_SET) || spin > SPIN_CAP) break;
    __builtin_amdgcn_s_sleep(POLL_SLEEP);
  }
}
DEV int tile_block(int lt, int idx) {
  const int xcd = lt >> 2, a = lt & 3;
  int j;
  if (idx < 5) { const int e = 5 * a + idx; j = e < 18 ? e : e + 12; }
  else j = 18 + 3 * a + (idx - 5);
  return (j << 3) | xcd;
}

struct P {
  const u16 *x, *la, *aim, *creA, *cimA, *ldtA, *dskA;
  const u16 *lre, *lim, *bre, *bim, *cre, *cim, *ldtB, *dskB, *gw;
  char* ws;
  float* out;
};

constexpr int G_NB = 4, G_STAGE = 32768;
constexpr int G_E = 153536;
constexpr int G_PF = G_E + 4096;
constexpr int G_L = G_PF + 4096;
constexpr int G_X = G_L + 1024;
constexpr int G_GO = G_X + 1024;
constexpr int G_T = G_NB * G_STAGE;
static_assert(G_GO + 4 <= 163840 && G_T + 4 * 32 * 144 <= G_E, "role G scratch");

DEV void g_loader(const P& p, LDS char* smem, int c, int g, int d0, int lw, int lane) {
  [[maybe_unused]] const int w = lw + 8;
  __builtin_amdgcn_s_setprio(3);
  GLB u32* ep = (GLB u32*)(p.ws + WS_EP + g * 256);
  GLB u64* gran = (GLB u64*)(p.ws + WS_GRAN) + (size_t)g * 16 * 128;
  const u32 tag = __hip_atomic_load(ep, RLX_AGENT) + 1u;
  u32 xo[4], bo[4];
#pragma unroll
  for (int i = 0; i < 4; ++i) {
    const int rho = 8 * (4 * lw + i) + (lane >> 3);
    const int hf = rho >> 6, m = (rho >> 4) & 3, r = rho & 15;
    const int tau = 64 * hf + 16 * (r >> 2) + 8 * (r & 1) + 2 * m + ((r >> 1) & 1);
    const int ch = (lane & 7) ^ (rho & 7);
    xo[i] = (u32)(128 * c + tau) * 1152u + ch * 16;
    const int pp = 64 * g + 16 * (rho >> 5) + r;
    bo[i] = (u32)pp * 1152u + ch * 16;
  }
  auto issue = [&](int s) {
    LDS char* buf = smem + (s & (G_NB - 1)) * G_STAGE + lw * 4096;
    int kb = g + s; kb = (kb >= 9 ? kb - 9 : kb) * 128;
#pragma unroll
    for (int i = 0; i < 4; ++i) dma16<0>((const char*)p.x + (xo[i] + kb), buf + i * 1024);
#pragma unroll
    for (int i = 0; i < 4; ++i) {
      const char* base = (const char*)((((4 * lw + i) >> 1) & 1) ? p.bim : p.bre);
      dma16<0>(base + (bo[i] + kb), buf + 16384 + i * 1024);
    }
  };
#pragma unroll
  for (int s = 0; s < G_NB - 1; ++s) issue(s);
#pragma unroll 1
  for (int s = 0; s < 6; ++s) { wait_vm<16>(); LSTAMP(0, 16 + s); bar(); issue(s + G_NB - 1); LSTAMP(0, 32 + s); }
  wait_vm<16>(); bar();
  wait_vm<8>(); bar();
  wait_vm<0>(); bar();
  STAMP(0, 1);
  if (lw < 2) {
    bar();
    u32 co[6];
#pragma unroll
    for (int i = 0; i < 6; ++i) {
      const int row = 8 * (6 * lw + i) + (lane >> 3), cp = lane & 7;
      co[i] = (u32)(d0 + row) * 1152u + ((cp ^ (row & 7)) << 4);
    }
    const int rot = ((d0 / 96) * 18) >> 3;
#pragma unroll 1
    for (int s = 0; s < 8; ++s) {
      LDS char* buf = smem + 32768 + s * 12288 + 6 * lw * 1024;
      const int ks = s + rot;
      const char* cb = ks < 9 ? (const char*)p.cre + ks * 128 : (const char*)p.cim + (ks - 9) * 128;
#pragma unroll
      for (int i = 0; i < 6; ++i) dma16<0>(cb + co[i], buf + i * 1024);
    }
    bar();
    return;
  }
  bar();
  STAMP(0, 2);
  if (lw == 3) {
    const f32x2 lv = *(LDS const f32x2*)(smem + G_L + lane * 8);
    const cf l16 = {lv[0], lv[1]};
    f32x2 ev[8];
#pragma unroll
    for (int s = 0; s < 8; ++s) ev[s] = *(LDS const f32x2*)(smem + G_E + (s * 64 + lane) * 8);
    cf tot = {0.f, 0.f};
    cf pre[8];
#pragma unroll
    for (int s = 0; s < 8; ++s) { pre[s] = tot; tot = cfma(l16, tot, cf{ev[s][0], ev[s][1]}); }
    if (c < 15) {
      __hip_atomic_store(gran + c * 128 + lane, ((u64)tag << 32) | __float_as_uint(tot.r), RLX_AGENT);
      __hip_atomic_store(gran + c * 128 + 64 + lane, ((u64)tag << 32) | __float_as_uint(tot.i), RLX_AGENT);
    }
#pragma unroll
    for (int s = 0; s < 8; ++s) *(LDS f32x2*)(smem + G_PF + (s * 64 + lane) * 8) = f32x2{pre[s].r, pre[s].i};
  }
  const int par = lw - 2;
  const f32x2 lv = *(LDS const f32x2*)(smem + G_L + 512 + lane * 8);
  const cf l128 = {lv[0], lv[1]};
  const cf l256 = cmul(l128, l128);
  cf acc = {0.f, 0.f};
  if (par < c) {
    const int nc = (c - par + 1) >> 1;
    u64 a[8], b[8];
    __builtin_amdgcn_s_sleep(GPOLL_DELAY);
    for (u32 spin = 0;; ++spin) {
      bool ok = true;
#pragma unroll
      for (int i = 0; i < 8; ++i) {
        const int cc = par + 2 * (i < nc ? i : 0);
        a[i] = __hip_atomic_load(gran + cc * 128 + lane, RLX_AGENT);
        b[i] = __hip_atomic_load(gran + cc * 128 + 64 + lane, RLX_AGENT);
      }
#pragma unroll
      for (int i = 0; i < 8; ++i) ok = ok && (u32)(a[i] >> 32) == tag && (u32)(b[i] >> 32) == tag;
      if (__all(ok) || spin > SPIN_CAP) break;
      __builtin_amdgcn_s_sleep(GPOLL_SLEEP);
    }
#pragma unroll
    for (int i = 0; i < 8; ++i)
      if (i < nc) acc = cfma(l256, acc, cf{__uint_as_float((u32)a[i]), __uint_as_float((u32)b[i])});
    if (c - 1 - (par + 2 * (nc - 1))) acc = cmul(acc, l128);
  }
  *(LDS f32x2*)(smem + G_X + (par * 64 + lane) * 8) = f32x2{acc.r, acc.i};
  STAMP(0, 3);
  bar();
  if (c == 15 && lw == 2 && lane == 0) __hip_atomic_store(ep, tag, RLX_AGENT);
}

template <int HF>
DEV void g_compute(const P& p, LDS char* smem, int c, int g, int w, int lane, u32& pf) {
  const int pq = w & 3, col = lane & 15, q = lane >> 4;
  const int pl = 16 * pq + col, pp = 64 * g + pl;
  cf lam, coef2;
  {
    const float lre = bf2f(p.lre[pp]), lim = bf2f(p.lim[pp]);
    const float dt = rbf(expf(bf2f(p.ldtB[pp])));
    const float zr = dt * lre, zi = dt * lim;
    const float er = expf(zr);
    float sn, cs;
    sincosf(zi, &sn, &cs);
    lam = {er * cs, er * sn};
    const float nr = lam.r - 1.0f, ni = lam.i;
    const float den = 1.0f / (lre * lre + lim * lim);
    coef2 = {2.0f * (nr * lre + ni * lim) * den, 2.0f * (ni * lre - nr * lim) * den};
  }
  cf l8 = cmul(lam, lam); l8 = cmul(l8, l8); l8 = cmul(l8, l8);
  const cf l16 = cmul(l8, l8);
  const cf l32 = cmul(l16, l16), l64 = cmul(l32, l32);
  if (HF == 0 && q == 0) {
    const cf l128 = cmul(l64, l64);
    *(LDS f32x2*)(smem + G_L + pl * 8) = f32x2{l16.r, l16.i};
    *(LDS f32x2*)(smem + G_L + 512 + pl * 8) = f32x2{l128.r, l128.i};
  }
  if (HF == 0 && w == 0 && lane == 0) *(LDS u32*)(smem + G_GO) = 0;
  cf pw = HF ? l64 : cf{1.f, 0.f};
  { const cf t = cmul(pw, l16); if (q & 1) pw = t; }
  { const cf t = cmul(pw, l32); if (q & 2) pw = t; }
  asm volatile("" : "+v"(lam.r), "+v"(lam.i), "+v"(coef2.r), "+v"(coef2.i), "+v"(l8.r), "+v"(l8.i), "+v"(pw.r), "+v"(pw.i));
  f32x4 are[4], aim[4];
#pragma unroll
  for (int m = 0; m < 4; ++m) { are[m] = f32x4{0, 0, 0, 0}; aim[m] = f32x4{0, 0, 0, 0}; }
  const int o0 = col * 128 + (((0 + q) ^ (col & 7)) << 4);
  const int o1 = col * 128 + (((4 + q) ^ (col & 7)) << 4);
  bf16x8 br[2], bi[2], a[2][4];
  auto read_frags = [&](int s) {
    LDS const char* buf = smem + (s & (G_NB - 1)) * G_STAGE;
    LDS const char* ab = buf + HF * 8192;
    LDS const char* bb = buf + 16384 + pq * 4096;
#pragma unroll
    for (int ks = 0; ks < 2; ++ks) {
      const int o = ks ? o1 : o0;
      br[ks] = lds_frag(bb + o); bi[ks] = lds_frag(bb + 2048 + o);
#pragma unroll
      for (int m = 0; m < 4; ++m) a[ks][m] = lds_frag(ab + m * 2048 + o);
    }
  };
  auto mma = [&]() {
#pragma unroll
    for (int ks = 0; ks < 2; ++ks)
#pragma unroll
      for (int m = 0; m < 4; ++m) {
        are[m] = mfma16(a[ks][m], br[ks], are[m]);
        aim[m] = mfma16(a[ks][m], bi[ks], aim[m]);
      }
  };
  STAMP(0, 1);
#pragma unroll
  for (int s = 0; s < 9; ++s) {
    if (HF == 1 && s == XT_WAIT) {
      wait_vm<0>();
      if (lane < 16) __hip_atomic_store((GLB u64*)(p.ws + WS_FL) + c * DM + 64 * g + 16 * pq + lane, FLAG_SET, RLX_AGENT);
    }
    bar();
    if (s == 0) { STAMP(0, 2); CSTAMP(0, 60); }
    if (HF == 1 && s > 0) { mma(); __builtin_amdgcn_sched_barrier(0); }
    if (HF == 1 && s == 0) {
      const int rr = 4 * q + (col >> 2);
      LDS const char* tb = smem + rr * 128 + (((2 * pq + ((col & 3) >> 1)) ^ (rr & 7)) << 4) + (col & 1) * 8;
      LDS char* sc = smem + G_T + pq * (32 * 144);
#pragma unroll
      for (int hf = 0; hf < 2; ++hf) {
        u32x2 t[4];
#pragma unroll
        for (int m = 0; m < 4; ++m) t[m] = __builtin_bit_cast(u32x2, tr_read(tb + hf * 8192 + m * 2048));
        u32x4 oa, ob;
#pragma unroll
        for (int m = 0; m < 4; ++m) {
          oa[m] = __builtin_amdgcn_perm(t[m][1], t[m][0], 0x05040100u);
          ob[m] = __builtin_amdgcn_perm(t[m][1], t[m][0], 0x07060302u);
        }
        *(LDS u32x4*)(sc + hf * (16 * 144) + col * 144 + q * 32) = oa;
        *(LDS u32x4*)(sc + hf * (16 * 144) + col * 144 + q * 32 + 16) = ob;
      }
      wave_fence();
      const __amdgpu_buffer_rsrc_t rs = mk_rsrc(p.ws + WS_XT, DM * SL * 2);
#pragma unroll
      for (int u = 0; u < 4; ++u) {
        const int row = (lane >> 3) + 8 * (u & 1), hf = u >> 1;
        const u32x4 v = *(LDS const u32x4*)(sc + hf * (16 * 144) + row * 144 + (lane & 7) * 16);
        __builtin_amdgcn_raw_buffer_store_b128(v, rs, ((64 * g + 16 * pq + row) * SL + 128 * c + 64 * hf) * 2 + (lane & 7) * 16, 0, SC1);
      }
      __builtin_amdgcn_sched_barrier(0);
    }
    read_frags(s);
    __builtin_amdgcn_sched_barrier(0);
    if (HF == 0) { mma(); __builtin_amdgcn_sched_barrier(0); }
  }
  if (HF == 1) mma();
  __builtin_amdgcn_sched_barrier(0);
  STAMP(0, 3); CSTAMP(0, 61);
  const f32x2 Lr = {lam.r, lam.r}, Li = {lam.i, lam.i};
  f32x2 er = {0.f, 0.f}, ei = {0.f, 0.f};
#pragma unroll
  for (int t = 0; t < 8; ++t) {
    const f32x2 br_ = (t & 1) ? hi2(are[t >> 1]) : lo2(are[t >> 1]);
    const f32x2 bi_ = (t & 1) ? hi2(aim[t >> 1]) : lo2(aim[t >> 1]);
    const f32x2 nr = fma2(-Li, ei, fma2(Lr, er, br_));
    ei = fma2(Li, er, fma2(Lr, ei, bi_));
    er = nr;
  }
  const cf eA = {er[0], ei[0]};
  const cf e = cfma(l8, eA, cf{er[1], ei[1]});
  const int seg = 4 * HF + q;
  *(LDS f32x2*)(smem + G_E + (seg * 64 + pl) * 8) = f32x2{e.r, e.i};
  STAMP(0, 4);
  bar();
  STAMP(0, 5);
  {
#pragma unroll
    for (int m = 0; m < 4; ++m) asm volatile("" : "+v"(are[m]), "+v"(aim[m]));
    const f32x2 Cr = {coef2.r, coef2.r}, Ci = {coef2.i, coef2.i};
#pragma unroll
    for (int m = 0; m < 4; ++m) {
      const f32x2 r0 = lo2(are[m]), r1 = hi2(are[m]), i0 = lo2(aim[m]), i1 = hi2(aim[m]);
      are[m] = cat2(fma2(-Ci, i0, Cr * r0), fma2(-Ci, i1, Cr * r1));
      aim[m] = cat2(fma2(-Ci, r0, -Cr * i0), fma2(-Ci, r1, -Cr * i1));
      asm volatile("" : "+v"(are[m]), "+v"(aim[m]));
    }
  }
  cf eAc = cmul(coef2, eA), pwc = cmul(coef2, pw);
  asm volatile("" : "+v"(eAc.r), "+v"(eAc.i), "+v"(pwc.r), "+v"(pwc.i));
  STAMP(0, 7);
  bar();
  STAMP(0, 8);
  cf cin;
  {
    f32x2 v[2];
#pragma unroll
    for (int i = 0; i < 2; ++i) v[i] = *(LDS const f32x2*)(smem + G_X + (i * 64 + pl) * 8);
    const f32x2 pf = *(LDS const f32x2*)(smem + G_PF + (seg * 64 + pl) * 8);
    const f32x2 S = v[0] + v[1];
    cin = cfma(pwc, cf{S[0], S[1]}, cmul(coef2, cf{pf[0], pf[1]}));
  }
  const cf cinB = cfma(l8, cin, eAc);
  er = f32x2{cin.r, cinB.r}; ei = f32x2{-cin.i, -cinB.i};
  f32x2 hr[8], hi[8];
#pragma unroll
  for (int t = 0; t < 8; ++t) {
    const f32x2 br_ = (t & 1) ? hi2(are[t >> 1]) : lo2(are[t >> 1]);
    const f32x2 bi_ = (t & 1) ? hi2(aim[t >> 1]) : lo2(aim[t >> 1]);
    const f32x2 nr = fma2(Li, ei, fma2(Lr, er, br_));
    ei = fma2(-Li, er, fma2(Lr, ei, bi_));
    er = nr;
    hr[t] = er; hi[t] = ei;
  }
  u32x4 o[4];
#pragma unroll
  for (int t = 0; t < 4; ++t) {
    o[0][t] = pack_bf16(hr[2 * t][0], hr[2 * t + 1][0]);
    o[1][t] = pack_bf16(hr[2 * t][1], hr[2 * t + 1][1]);
    o[2][t] = pack_bf16(hi[2 * t][0], hi[2 * t + 1][0]);
    o[3][t] = pack_bf16(hi[2 * t][1], hi[2 * t + 1][1]);
  }
  STAMP(0, 9);
  {
    const __amdgpu_buffer_rsrc_t rs = mk_rsrc(p.ws + WS_HB, 2 * DM * SL * 2);
    const int hb = (((c * 2 + HF) * 2) * 9 + g) * 8192 + pq * 2048;
    const int p0 = (16 * q + col) * 16, p1 = 1024 + (16 * q + (col ^ 4)) * 16;
    __builtin_amdgcn_raw_buffer_store_b128(o[0], rs, hb + p0, 0, SC1);
    __builtin_amdgcn_raw_buffer_store_b128(o[1], rs, hb + p1, 0, SC1);
    __builtin_amdgcn_raw_buffer_store_b128(o[2], rs, hb + 9 * 8192 + p0, 0, SC1);
    __builtin_amdgcn_raw_buffer_store_b128(o[3], rs, hb + 9 * 8192 + p1, 0, SC1);
    wait_vm<0>();
    STAMP(0, 10);
    if (lane < 8)
      __hip_atomic_store((GLB u64*)(p.ws + WS_IN) + tile_block(2 * c + HF, lane) * 64 + IN_H + g * 4 + pq, FLAG_SET, RLX_AGENT);
    bar();
  }
}

constexpr int S_CPY = 4160;
constexpr int S_X = 0;
constexpr int S_ST = 6 * 4 * S_CPY;
constexpr int S_Z = S_ST;
constexpr int S_ZSZ = (256 + SL) * 2;
constexpr int S_T = S_ST + 12 * 4608;
constexpr int S_TSZ = 1312;
constexpr int S_END = S_T + 6 * S_TSZ;
constexpr int S_CNT = 32768 + 8192;

DEV c2 cexp_k(float zl, float th, float tl, float k) {
  const float mag = __builtin_amdgcn_exp2f(zl * k);
  const float t = fmaf(tl, k, __builtin_amdgcn_fractf(th * k));
  return c2{mag * __builtin_amdgcn_cosf(t), mag * __builtin_amdgcn_sinf(t)};
}
#define PKM(d, a, b) "v_pk_mul_f32 " d ", " a ", " b " op_sel:[0,0] op_sel_hi:[0,1]\n\t"
#define PKF(d, a, b) "v_pk_fma_f32 " d ", " a ", " b ", " d " op_sel:[1,1,0] op_sel_hi:[1,0,1] neg_lo:[1,0,0]\n\t"
DEV void octet(LDS char* dst, c2 s0, c2 m1, c2 m2) {
  c2 v1, v2, v3, t0, t1, t2, t3;
  asm(PKM("%0", "%7", "%8") PKM("%3", "%7", "%9") PKF("%0", "%7", "%8") PKF("%3", "%7", "%9")
      PKM("%1", "%0", "%8") PKM("%4", "%0", "%9") PKF("%1", "%0", "%8") PKF("%4", "%0", "%9")
      PKM("%2", "%1", "%8") PKM("%5", "%1", "%9") PKF("%2", "%1", "%8") PKF("%5", "%1", "%9")
      PKM("%6", "%2", "%9") PKF("%6", "%2", "%9")
      : "=&v"(v1), "=&v"(v2), "=&v"(v3), "=&v"(t0), "=&v"(t1), "=&v"(t2), "=&v"(t3) : "v"(s0), "v"(m1), "v"(m2));
  *(LDS u32*)(dst + 0 * 288) = pack_bf16(s0[0], s0[1]);
  *(LDS u32*)(dst + 1 * 288) = pack_bf16(v1[0], v1[1]);
  *(LDS u32*)(dst + 2 * 288) = pack_bf16(v2[0], v2[1]);
  *(LDS u32*)(dst + 3 * 288) = pack_bf16(v3[0], v3[1]);
  *(LDS u32*)(dst + 4 * 288) = pack_bf16(t0[0], t0[1]);
  *(LDS u32*)(dst + 5 * 288) = pack_bf16(t1[0], t1[1]);
  *(LDS u32*)(dst + 6 * 288) = pack_bf16(t2[0], t2[1]);
  *(LDS u32*)(dst + 7 * 288) = pack_bf16(t3[0], t3[1]);
}
DEV u32 dup_lo(u32 a) { return __builtin_amdgcn_perm(a, a, 0x01000100u); }
DEV u32 dup_hi(u32 a) { return __builtin_amdgcn_perm(a, a, 0x03020302u); }
DEV u32 hi_lo(u32 a, u32 b) { return __builtin_amdgcn_alignbit(b, a, 16); }

DEV void role_s(const P& p, LDS char* smem, int sg, int w, int lane) {
  const int tid = w * 64 + lane;
  const int ch = w >> 1, hh = w & 1, d = 6 * sg + ch;
  const int n = d * 128 + 64 * hh + lane;
  const u16 r_ldt = p.ldtA[d], r_la = p.la[n], r_aim = p.aim[n], r_cre = p.creA[n], r_cim = p.cimA[n];
  u32 wb[2] = {0, 0};
  __builtin_amdgcn_sched_barrier(0);
#pragma unroll
  for (int i = 0; i < 2; ++i) {
    const int ln = i * 768 + tid, all = (sg % 12) * 864 + ln;
    if (ln < 864) wb[i] = *(const GLB u32*)((const char*)(all < 5184 ? p.bre : p.bim) + (size_t)(all < 5184 ? all : all - 5184) * 128);
  }
  __builtin_amdgcn_sched_barrier(0);
  STAMP(0, 1);
  f32x4 tab[3];
  [[maybe_unused]] u32 wc[2] = {0, 0};
  LDS char* st = smem + S_ST + w * 4608;
  {
    const float L2E = 1.44269504088896341f;
    const float dt = rbf(__builtin_amdgcn_exp2f(bf2f(r_ldt) * L2E));
    const float ar = -rbf(__builtin_amdgcn_exp2f(bf2f(r_la) * L2E));
    const float zl = ar * dt * L2E, zi = bf2f(r_aim) * dt;
    const float th = zi * 0.15915494309189535f;
    const float tl = fmaf(zi, 0.15915494309189535f, -th) + zi * 4.6017384e-10f;
    const c2 C = {2.0f * (bf2f(r_cre) * dt), 2.0f * (bf2f(r_cim) * dt)};
    const c2 w1 = cexp_k(zl, th, tl, 1.f), w8 = cexp_k(zl, th, tl, 8.f), w128 = cexp_k(zl, th, tl, 128.f);
    c2 w2, w4, w16, w32, w64, w256, w512, w1024, w2048;
    asm(PKM("%0", "%9", "%9") PKM("%2", "%10", "%10") PKM("%5", "%11", "%11") PKF("%0", "%9", "%9") PKF("%2", "%10", "%10") PKF("%5", "%11", "%11")
        PKM("%1", "%0", "%0") PKM("%3", "%2", "%2") PKM("%6", "%5", "%5") PKF("%1", "%0", "%0") PKF("%3", "%2", "%2") PKF("%6", "%5", "%5")
        PKM("%4", "%3", "%3") PKM("%7", "%6", "%6") PKF("%4", "%3", "%3") PKF("%7", "%6", "%6")
        PKM("%8", "%7", "%7") PKF("%8", "%7", "%7")
        : "=&v"(w2), "=&v"(w4), "=&v"(w16), "=&v"(w32), "=&v"(w64), "=&v"(w256), "=&v"(w512), "=&v"(w1024), "=&v"(w2048)
        : "v"(w1), "v"(w8), "v"(w128));
    c2 C128, C256, C512, C1024, C2048;
    asm(PKM("%0", "%5", "%6") PKM("%1", "%5", "%7") PKM("%2", "%5", "%8") PKM("%3", "%5", "%9") PKM("%4", "%5", "%10")
        PKF("%0", "%5", "%6") PKF("%1", "%5", "%7") PKF("%2", "%5", "%8") PKF("%3", "%5", "%9") PKF("%4", "%5", "%10")
        : "=&v"(C128), "=&v"(C256), "=&v"(C512), "=&v"(C1024), "=&v"(C2048)
        : "v"(C), "v"(w128), "v"(w256), "v"(w512), "v"(w1024), "v"(w2048));
#pragma unroll
    for (int i = 0; i < 2; ++i) {
      const int ln = i * 768 + tid, all = (sg % 12) * 864 + ln;
      if (ln < 864) wc[i] = *(const GLB u32*)((const char*)(all < 5184 ? p.cre : p.cim) + (size_t)(all < 5184 ? all : all - 5184) * 128);
    }
    STAMP(0, 2);
    LDS char* wr = st + 4 * lane;
    LDS const char* rd = st + (lane & 15) * 288 + (lane >> 4) * 16;
    bf16x8 B[4];
    const c2 w8c = {w8[0], -w8[1]}, w32c = {w32[0], -w32[1]}, w64c = {w64[0], -w64[1]};
    octet(wr, c2{1.f, 0.f}, w8c, w32c);
    octet(wr + 8 * 288, w64c, w8c, w32c);
    wave_fence();
#pragma unroll
    for (int ks = 0; ks < 4; ++ks) B[ks] = lds_frag(rd + 64 * ks);
    wave_fence();
#pragma unroll
    for (int mt = 0; mt < 3; ++mt) {
      if (mt == 0) { octet(wr, C, w1, w4); octet(wr + 8 * 288, C128, w1, w4); }
      if (mt == 1) { octet(wr, C256, w2, w128); octet(wr + 8 * 288, C512, w128, w4); }
      if (mt == 2) { octet(wr, C1024, w128, w512); *(LDS u32*)(wr + 8 * 288) = pack_bf16(C2048[0], C2048[1]); }
      wave_fence();
      f32x4 acc = {0, 0, 0, 0};
      bf16x8 A[4];
#pragma unroll
      for (int ks = 0; ks < 4; ++ks) A[ks] = lds_frag(rd + 64 * ks);
      __builtin_amdgcn_sched_barrier(0);
#pragma unroll
      for (int ks = 0; ks < 4; ++ks) acc = mfma16(A[ks], B[ks], acc);
      __builtin_amdgcn_sched_barrier(0);
      wave_fence();
      tab[mt] = acc;
    }
  }
  const int b = lane & 15, q = lane >> 4;
#pragma unroll
  for (int mt = 0; mt < 3; ++mt) *(LDS f32x4*)(st + mt * 1024 + lane * 16) = tab[mt];
  STAMP(0, 3);
  bar();
  {
    LDS const char* ot = smem + S_ST + (w ^ 1) * 4608 + lane * 16;
    LDS char* T = smem + S_T + ch * S_TSZ;
    if (hh == 0) {
      const f32x4 v0 = tab[0] + *(LDS const f32x4*)(ot), v2 = tab[2] + *(LDS const f32x4*)(ot + 2048);
      *(LDS u32x2*)(T + 2 * (128 * (q >> 1) + 8 * b + 4 * (q & 1))) = u32x2{pack_bf16(v0[0], v0[1]), pack_bf16(v0[2], v0[3])};
      const u32 a0 = pack_bf16(v2[0], v2[1]), a1 = pack_bf16(v2[2], v2[3]);
      if (q < 2) {
        LDS char* t3 = T + 2 * (512 + 64 * q + b);
        *(LDS u16*)(t3) = (u16)a0; *(LDS u16*)(t3 + 32) = (u16)(a0 >> 16);
        *(LDS u16*)(t3 + 64) = (u16)a1; *(LDS u16*)(t3 + 96) = (u16)(a1 >> 16);
      } else if (lane == 32) *(LDS u16*)(T + 2 * 640) = (u16)a0;
    } else {
      const f32x4 v1 = tab[1] + *(LDS const f32x4*)(ot + 1024);
      const u32 a0 = pack_bf16(v1[0], v1[1]), a1 = pack_bf16(v1[2], v1[3]);
      if (q < 2) *(LDS u32x2*)(T + 2 * (256 + 64 * q + 4 * b)) = u32x2{a0, a1};
      else {
        LDS char* t2 = T + 2 * (384 + 2 * b + (q - 2));
        *(LDS u16*)(t2) = (u16)a0; *(LDS u16*)(t2 + 64) = (u16)(a0 >> 16);
        *(LDS u16*)(t2 + 128) = (u16)a1; *(LDS u16*)(t2 + 192) = (u16)(a1 >> 16);
      }
    }
  }
  bar();
  STAMP(0, 4);
  u32x4 uv[2];
  {
    GLB u64* fl = (GLB u64*)(p.ws + WS_FL) + (8 * hh + (lane & 7)) * DM + d;
    asm volatile("" :: "v"(wc[0]), "v"(wc[1]), "v"(wb[0]), "v"(wb[1]));
    wait_flags(fl, true);
    const __amdgpu_buffer_rsrc_t rs = mk_rsrc(p.ws + WS_XT, DM * SL * 2);
#pragma unroll
    for (int v = 0; v < 2; ++v) uv[v] = __builtin_amdgcn_raw_buffer_load_b128(rs, (d * SL + 1024 * hh + 8 * (lane + 64 * v)) * 2, 0, SC1);
    if (lane < 8) __hip_atomic_store(fl, 0ull, RLX_AGENT);
  }
  STAMP(0, 5);
  {
    LDS const char* T = smem + S_T + ch * S_TSZ;
    u32 o[10];
    const u32 e512 = *(LDS const u16*)(T + 2 * 512);
    o[8] = o[9] = e512 | (e512 << 16);
    if (hh == 0) {
      const int off = lane < 16 ? 32 * lane : (lane < 32 ? 256 + 16 * lane : 512 + 8 * lane);
      u32x2 W[4];
#pragma unroll
      for (int i = 0; i < 4; ++i) W[i] = *(LDS const u32x2*)(T + off + 8 * i);
      if (lane < 16) {
#pragma unroll
        for (int i = 0; i < 8; ++i) o[i] = W[i >> 1][i & 1];
      } else if (lane < 32) {
#pragma unroll
        for (int i = 0; i < 4; ++i) {
          const u32 a = W[i >> 1][i & 1], nx = W[(i + 1) >> 1][(i + 1) & 1];
          o[2 * i] = dup_lo(a);
          o[2 * i + 1] = hi_lo(a, nx);
        }
      } else {
        const u32 w0 = W[0][0], w1 = W[0][1], w2 = W[1][0];
        o[0] = dup_lo(w0); o[1] = w0; o[2] = dup_hi(w0); o[3] = dup_lo(w1);
        o[4] = o[3]; o[5] = w1; o[6] = dup_hi(w1); o[7] = dup_lo(w2);
      }
    } else {
      const u32 w0 = *(LDS const u32*)(T + 1024 + 4 * lane), w1 = *(LDS const u32*)(T + 1028 + 4 * lane);
      o[0] = o[1] = dup_lo(w0); o[2] = w0; o[3] = o[4] = o[5] = dup_hi(w0); o[6] = o[7] = dup_lo(w1);
    }
    o[8] = (u32)__builtin_amdgcn_update_dpp((int)o[8], (int)o[0], 0x130, 0xf, 0xf, false);
    o[9] = (u32)__builtin_amdgcn_update_dpp((int)o[9], (int)o[1], 0x130, 0xf, 0xf, false);
    u32 a[9];
#pragma unroll
    for (int i = 0; i < 9; ++i) a[i] = hi_lo(o[i], o[i + 1]);
    LDS char* xb = smem + S_X + ch * 4 * S_CPY + 32 + 2 * (1024 * hh + 16 * lane);
    *(LDS u32x4*)(xb) = u32x4{o[0], o[1], o[2], o[3]};
    *(LDS u32x4*)(xb + 16) = u32x4{o[4], o[5], o[6], o[7]};
    *(LDS u32x4*)(xb + S_CPY) = u32x4{a[0], a[1], a[2], a[3]};
    *(LDS u32x4*)(xb + S_CPY + 16) = u32x4{a[4], a[5], a[6], a[7]};
    *(LDS u32x4*)(xb + 2 * S_CPY) = u32x4{o[1], o[2], o[3], o[4]};
    *(LDS u32x4*)(xb + 2 * S_CPY + 16) = u32x4{o[5], o[6], o[7], o[8]};
    *(LDS u32x4*)(xb + 3 * S_CPY) = u32x4{a[1], a[2], a[3], a[4]};
    *(LDS u32x4*)(xb + 3 * S_CPY + 16) = u32x4{a[5], a[6], a[7], a[8]};
    if (hh == 0 && lane == 0) {
      LDS char* hb = smem + S_X + ch * 4 * S_CPY;
      const u32x4 z = {0, 0, 0, 0};
      *(LDS u32x4*)(hb) = z;
      *(LDS u32x4*)(hb + 16) = z;
      *(LDS u32x4*)(hb + S_CPY) = z;
      *(LDS u32x4*)(hb + S_CPY + 16) = u32x4{0, 0, 0, o[0] << 16};
      *(LDS u32x4*)(hb + 2 * S_CPY) = z;
      *(LDS u32x4*)(hb + 2 * S_CPY + 16) = u32x4{0, 0, 0, o[0]};
      *(LDS u32x4*)(hb + 3 * S_CPY) = z;
      *(LDS u32x4*)(hb + 3 * S_CPY + 16) = u32x4{0, 0, o[0] << 16, a[0]};
    }
    if (tid < 6 * 32) *(LDS u32x4*)(smem + S_Z + (tid >> 5) * S_ZSZ + (tid & 31) * 16) = u32x4{0, 0, 0, 0};
    STAMP(0, 6);
    LDS char* z = smem + S_Z + ch * S_ZSZ + 512 + (1024 * hh + 8 * lane) * 2;
    *(LDS u32x4*)z = uv[0];
    *(LDS u32x4*)(z + 1024) = uv[1];
  }
  bar();
  STAMP(0, 7);
  f32x4 acc[8];
#pragma unroll
  for (int i = 0; i < 8; ++i) acc[i] = f32x4{0, 0, 0, 0};
  {
    const int kq = q, qq = b >> 2, pp = b & 3;
    LDS const char* fb = smem + S_X + (ch * 4 + ((4 - qq) & 3)) * S_CPY +
                         2 * (16 + 16 * (kq >> 1) - 8 * (kq & 1) + 4 * pp - (qq ? 4 : 0)) + 64 * hh;
    LDS const char* gb = smem + S_Z + ch * S_ZSZ + 512 + 32 * (b - (kq >> 1)) + 16 * (kq & 1) - 64 * hh;
#pragma unroll 1
    for (int it = 0; it < 4; ++it) {
      bf16x8 F[8], G[8];
#pragma unroll
      for (int s = 0; s < 8; ++s) {
        F[s] = tr_frag(fb + 512 * s, fb + 512 * s - 8);
        G[s] = lds_frag(gb + 512 * s);
      }
      __builtin_amdgcn_sched_barrier(0);
#pragma unroll
      for (int t = 0; t < 8; ++t)
#pragma unroll
        for (int s = 0; s <= t; ++s) acc[t] = mfma16(F[s], G[t - s], acc[t]);
      fb += 128;
      gb -= 128;
    }
  }
  __builtin_amdgcn_sched_barrier(0);
  STAMP(0, 8);
  bar();
  {
    LDS char* ex = smem + S_X + 49152 + w * 4096 + lane * 16;
#pragma unroll
    for (int i = 0; i < 4; ++i) {
      const f32x4 lo = acc[i], hi = acc[4 + i];
      *(LDS f32x4*)(ex + i * 1024) = hh ? +lo : +hi;
    }
    if (tid == 0) *(LDS u32*)(smem + S_CNT) = 0;
  }
  bar();
  u32x2 yo[4];
  {
    LDS const char* ey = smem + S_X + 49152 + (w ^ 1) * 4096 + lane * 16;
#pragma unroll
    for (int i = 0; i < 4; ++i) {
      const f32x4 lo = acc[i], hi = acc[4 + i];
      const f32x4 y = (hh ? +hi : +lo) + *(LDS const f32x4*)(ey + i * 1024);
      yo[i] = u32x2{pack_bf16(y[0], y[1]), pack_bf16(y[2], y[3])};
    }
  }
  bar();
  {
    const __amdgpu_buffer_rsrc_t rs = mk_rsrc(p.ws + WS_AT, DM * SL * 2);
    const int dst = (d * SL + 1024 * hh + 16 * (lane & 15) + 4 * (lane >> 4)) * 2;
#pragma unroll
    for (int i = 0; i < 4; ++i) __builtin_amdgcn_raw_buffer_store_b64(yo[i], rs, dst + 512 * i, 0, SC1);
  }
  STAMP(0, 9);
}
DEV void s_signal(const P& p, LDS char* smem, int sg, int lane) {
  u32 old = 0;
  if (lane == 0) old = __hip_atomic_fetch_add((LDS u32*)(smem + S_CNT), 1u, __ATOMIC_RELAXED, __HIP_MEMORY_SCOPE_WORKGROUP);
  if (__builtin_amdgcn_readfirstlane(old) == 11) {
    const int lt = lane & 31, second = lane >> 5;
    const int dd = 6 * sg + (second ? 5 : 0);
    const int idx = dd < 480 ? dd / 96 : 5 + (dd - 480) / 32;
    const int dfirst = idx < 5 ? 96 * idx : 480 + 32 * (idx - 5);
    __hip_atomic_store((GLB u64*)(p.ws + WS_IN) + tile_block(lt, idx) * 64 + IN_A + (sg - dfirst / 6), FLAG_SET, RLX_AGENT);
  }
}

DEV void role_gate(const P& p, int rb, int w, int lane, u64 pc) {
  u32 pf0 = 0, pf1 = 0;
  const int tid = w * 64 + lane;
  const int sub = lane & 7;
  u32x4 wv[9];
#pragma unroll
  for (int i = 0; i < 9; ++i) wv[i] = *(const u32x4*)((const char*)p.gw + (i * 8 + sub) * 16);
  for (int r = w * 8 + (lane >> 3); r < 128; r += 96) {
    const int l = rb * 128 + r;
    float s = 0.f;
    u32x4 xv[9];
#pragma unroll
    for (int i = 0; i < 9; ++i) xv[i] = *(const u32x4*)((const char*)p.x + (size_t)l * 1152 + (i * 8 + sub) * 16);
#pragma unroll
    for (int i = 0; i < 9; ++i) {
#pragma unroll
      for (int k = 0; k < 4; ++k) {
        const u32 pr = pack_bf16(bflo(xv[i][k]) * bflo(wv[i][k]), bfhi(xv[i][k]) * bfhi(wv[i][k]));
        s += bflo(pr);
        s += bfhi(pr);
      }
    }
    s += __shfl_xor(s, 1); s += __shfl_xor(s, 2); s += __shfl_xor(s, 4);
    const float gv = rbf(1.0f / (1.0f + expf(-rbf(s))));
    if (sub == 0)
      __hip_atomic_store((GLB u64*)(p.ws + WS_G) + l, ((u64)__float_as_uint(rbf(1.0f - gv)) << 32) | __float_as_uint(gv), RLX_AGENT);
  }
  asm volatile("" :: "v"(pf0), "v"(pf1));
  wait_vm<0>();
  bar();
  if (w == 0 && lane < 16)
    __hip_atomic_store((GLB u64*)(p.ws + WS_IN) + tile_block(2 * rb + (lane >> 3), lane & 7) * 64 + IN_G, FLAG_SET, RLX_AGENT);
}

constexpr int H_C = 32768, H_CS = 12288;
DEV int h_slot(int i) { return i < 4 ? i * 8192 : 131072 + (i - 4) * 8192; }
constexpr int H_AT = H_C + 2 * H_CS;
constexpr int H_XT = H_C + 3 * H_CS;
constexpr int H_G = H_C + 4 * H_CS;
constexpr int H_OUT = H_C + 5 * H_CS;
constexpr int K_LDS = 163840;

constexpr int h_wait(int s, int NDT) {
  int n = 0;
  int need = 0;
  for (int k = 0; k < 4; ++k) { n += 2; if (k == s) need = n; }
  for (int t = 0; t < s; ++t) {
    if (t == 0) { n += 2; if (s == 4) need = n; n += 2; if (s == 5) need = n; }
    else if (t == 1) { n += 2; if (s == 6) need = n; n += 2; if (s == 7) need = n; }
    else if (t <= 11) { n += 2 + NDT; if (s == t + 6) need = n; if (t == 2) n += 1; }
    else if (t == 12 || t == 13) n += NDT;
    else if (t == 14) n += 1;
  }
  return s == 17 ? 0 : n - need;
}
template <int S, int NDT, class F> DEV void h_steps(F&& after, u64& fv, char* wsp) {
  if constexpr (S == 9) asm volatile("s_waitcnt vmcnt(%1)" : "+v"(fv) : "n"(h_wait(S, NDT)) : "memory");
  else wait_vm<h_wait(S, NDT)>();
  LSTAMP2(1, 16 + S);
  bar();
  after(std::integral_constant<int, S>{});
  if constexpr (S < 17) h_steps<S + 1, NDT>(after, fv, wsp);
}
DEV int p2_rot(int d0) { const int idx = d0 < 480 ? d0 / 96 : 5 + (d0 - 480) / 32; return (idx * 18) >> 3; }

template <int NDT, class F>
DEV void p2_loader(const P& p, LDS char* smem, int lt, int d0, int lw, int lane, bool scratch_barrier, F&& pre_poll) {
  __builtin_amdgcn_s_setprio(3);
  const char* hb = p.ws + WS_HB + (size_t)lt * 18 * 8192 + (2 * lw) * 1024 + lane * 16;
  constexpr int W = 32 * NDT;
  const int l0 = 64 * lt, rot = p2_rot(d0);
  u32 co[NDT];
#pragma unroll
  for (int i = 0; i < NDT; ++i) {
    const int row = 8 * (NDT * lw + i) + (lane >> 3), cp = lane & 7;
    co[i] = (u32)(d0 + row) * 1152u + ((cp ^ (row & 7)) << 4);
  }
  auto issue_c = [&](int s) {
    LDS char* buf = smem + H_C + (s & 7) * H_CS + NDT * lw * 1024;
    int ks = s + rot; ks = ks >= 18 ? ks - 18 : ks;
    const char* cb = ks < 9 ? (const char*)p.cre + ks * 128 : (const char*)p.cim + (ks - 9) * 128;
#pragma unroll
    for (int i = 0; i < NDT; ++i) dma16<0>(cb + co[i], buf + i * 1024);
  };
  auto issue_h = [&](int s) {
    LDS char* buf = smem + h_slot(s & 7) + 2 * lw * 1024;
    int ks = s + rot; ks = ks >= 18 ? ks - 18 : ks;
#pragma unroll
    for (int i = 0; i < 2; ++i) dma16<SC1>(hb + ks * 8192 + i * 1024, buf + i * 1024);
  };
  GLB u64* inb = (GLB u64*)(p.ws + WS_IN) + blockIdx.x * 64;
  const int nA = (d0 + W - 1) / 6 - d0 / 6 + 1;
  const bool need = lane == IN_G || (lane >= IN_A && lane < IN_A + nA);
  if (!scratch_barrier) {
#pragma unroll 1
    for (int s = 0; s < 8; ++s) issue_c(s);
  }
  pre_poll();
  { [[maybe_unused]] const int w = lw + 8; STAMP(1, 1); }
  if (scratch_barrier) bar();
  wait_flags(inb + IN_H + (lane < 36 ? lane : 0), true);
  { [[maybe_unused]] const int w = lw + 8; STAMP(1, 2); }
#pragma unroll
  for (int s = 0; s < 4; ++s) issue_h(s);
  { [[maybe_unused]] const int w = lw + 8; STAMP(1, 3); }
  u64 fv = 0;
  h_steps<0, NDT>([&](auto S_) {
    constexpr int S = decltype(S_)::value;
    if constexpr (S == 0) { issue_h(4); issue_h(5); }
    if constexpr (S == 1) { issue_h(6); issue_h(7); }
    if constexpr (S >= 2 && S <= 11) { issue_h(S + 6); issue_c(S + 6); }
    if constexpr (S == 2)
      asm volatile("global_load_dwordx2 %0, %1, off sc1" : "=v"(fv) : "v"(inb + lane) : "memory");
    if constexpr (S == 9) { if (!__all(!need || fv == FLAG_SET)) wait_flags(inb + lane, need); }
    if constexpr (S == 12) {
#pragma unroll
      for (int i = 0; i < NDT; ++i) {
        const int pc = NDT * lw + i;
        dma16<SC1>(p.ws + WS_AT + (size_t)(d0 + 8 * pc + (lane >> 3)) * 4096 + l0 * 2 + (lane & 7) * 16, smem + H_AT + pc * 1024);
      }
    }
    if constexpr (S == 13) {
#pragma unroll
      for (int i = 0; i < NDT; ++i) {
        const int pc = NDT * lw + i, id = pc * 64 + lane, row = id / (4 * NDT), qq = id - row * (4 * NDT);
        dma16<0>((const char*)p.x + (size_t)(l0 + row) * 1152 + d0 * 2 + qq * 16, smem + H_XT + pc * 1024);
      }
    }
    if constexpr (S == 14) { if (lane < 8) dma16<SC1>(p.ws + WS_G + (size_t)l0 * 8 + lw * 128 + lane * 16, smem + H_G + lw * 128); }
  }, fv, p.ws);
}

template <int NDT, int STAG>
DEV void p2_compute(const P& p, LDS char* smem, int lt0, int d0, int w, int lane) {
  const int lh = (w >> 1) & 1, kh = w & 1, dg = w >> 2;
  const int r = lane & 15, kq = lane >> 4;
  const int lt = 2 * lh + kh, l0 = 64 * lt0;
  constexpr int W = 32 * NDT;
  u32x2 da[NDT], db[NDT];
#pragma unroll
  for (int t = 0; t < NDT; ++t) {
    const int d = d0 + 16 * (NDT * dg + t) + 4 * kq;
    da[t] = *(const u32x2*)(p.dskA + d);
    db[t] = *(const u32x2*)(p.dskB + d);
  }
  GLB u64* inb = (GLB u64*)(p.ws + WS_IN) + blockIdx.x * 64;

  f32x4 acc[2][NDT];
#pragma unroll
  for (int i = 0; i < 2; ++i)
#pragma unroll
    for (int t = 0; t < NDT; ++t) acc[i][t] = f32x4{0, 0, 0, 0};
  int ho[2];
#pragma unroll
  for (int u = 0; u < 2; ++u) {
    const int k = 32 * kh + 8 * kq + 4 * u + (r >> 2), i = (r & 3) >> 1;
    ho[u] = (2 * (k >> 4) + i) * 1024 + (32 * lh + ((k & 15) ^ (4 * i))) * 16 + (r & 1) * 8;
  }
  const int co = H_C + (16 * NDT * dg + r) * 128 + (((4 * kh + kq) ^ (r & 7)) << 4);
  constexpr int NREG = 0, NRING = 18 - NREG;
  bf16x8 cr[NREG ? NREG : 1][NDT];
  if constexpr (NREG > 0) {
    const int rot = p2_rot(d0);
#pragma unroll
    for (int i = 0; i < NREG; ++i) {
      int ks = NRING + i + rot; ks = ks >= 18 ? ks - 18 : ks;
      const char* cb = (ks < 9 ? (const char*)p.cre + ks * 128 : (const char*)p.cim + (ks - 9) * 128) +
                       (size_t)(d0 + 16 * NDT * dg + r) * 1152 + (4 * kh + kq) * 16;
#pragma unroll
      for (int t = 0; t < NDT; ++t) cr[i][t] = *(const GLB bf16x8*)(cb + t * 16 * 1152);
    }
  }
  bf16x8 hb[2], ca[NDT];
#pragma unroll
  for (int i = 0; i < 2; ++i) hb[i] = bf16x8{};
#pragma unroll
  for (int t = 0; t < NDT; ++t) ca[t] = bf16x8{};
  auto mma_with = [&](const bf16x8 (&cc)[NDT]) {
#pragma unroll
    for (int t = 0; t < NDT; ++t) {
      acc[0][t] = mfma16(cc[t], hb[0], acc[0][t]);
      acc[1][t] = mfma16(cc[t], hb[1], acc[1][t]);
    }
    __builtin_amdgcn_sched_barrier(0);
  };
  auto read_h = [&](int s) {
    LDS const char* hbuf = smem + h_slot(s & 7);
#pragma unroll
    for (int i = 0; i < 2; ++i) hb[i] = tr_frag(hbuf + ho[0] + i * 256, hbuf + ho[1] + i * 256);
  };
  STAMP(1, 1);
#pragma unroll 1
  for (int s = 0; s < NRING; ++s) {
    bar();
    if (s == 0) STAMP(1, 2);
    LSTAMP(1, 16 + s);
    if (STAG) mma_with(ca);
    read_h(s);
    LDS const char* cbuf = smem + (s & 7) * H_CS;
#pragma unroll
    for (int t = 0; t < NDT; ++t) ca[t] = lds_frag(cbuf + co + t * 2048);
    __builtin_amdgcn_sched_barrier(0);
    if (!STAG) mma_with(ca);
  }
#pragma unroll
  for (int i = 0; i < NREG; ++i) {
    bar();
    if (STAG) { if (i == 0) mma_with(ca); else mma_with(cr[i ? i - 1 : 0]); }
    read_h(NRING + i);
    __builtin_amdgcn_sched_barrier(0);
    if (!STAG) mma_with(cr[i]);
  }
  if (STAG) { if (NREG) mma_with(cr[NREG ? NREG - 1 : 0]); else mma_with(ca); }
  __builtin_amdgcn_sched_barrier(0);
  STAMP(1, 3);
  bar();
  if (w == 0) __hip_atomic_store(inb + lane, 0ull, RLX_AGENT);
  LDS char* ex = smem + w * (NDT * 1024) + lane * 16;
#pragma unroll
  for (int t = 0; t < NDT; ++t) {
    const f32x4 a0 = acc[0][t], a1 = acc[1][t];
    *(LDS f32x4*)(ex + t * 1024) = kh ? +a0 : +a1;
  }
  bar();
  LDS const char* ey = smem + (w ^ 1) * (NDT * 1024) + lane * 16;
  constexpr int OSTR = W * 4 + 16;
  const f32x2 gg = *(LDS const f32x2*)(smem + H_G + (16 * lt + r) * 8);
#pragma unroll
  for (int t = 0; t < NDT; ++t) {
    const int dt = NDT * dg + t;
    const f32x4 o = *(LDS const f32x4*)(ey + t * 1024);
    const f32x4 a0 = acc[0][t], a1 = acc[1][t];
    const f32x4 y = (kh ? +a1 : +a0) + o;
    const s16x4 av = tr_read(smem + H_AT + (16 * dt + 4 * kq + (r >> 2)) * 128 + lt * 32 + (r & 3) * 8);
    const u32x2 xv = *(LDS const u32x2*)(smem + H_XT + (16 * lt + r) * (W * 2) + (16 * dt + 4 * kq) * 2);
    f32x4 res;
#pragma unroll
    for (int i = 0; i < 4; ++i) {
      const u32 xw = xv[i >> 1], aw = da[t][i >> 1], bw = db[t][i >> 1];
      const float xf = (i & 1) ? bfhi(xw) : bflo(xw);
      const float sa = (i & 1) ? bfhi(aw) : bflo(aw);
      const float sb = (i & 1) ? bfhi(bw) : bflo(bw);
      const float yc = bf2f((u16)av[i]);
      const float a = rbf(yc + rbf(xf * sa));
      const float b = y[i] + rbf(xf * sb);
      res[i] = rbf(gg[0] * a) + gg[1] * b;
    }
    *(LDS f32x4*)(smem + H_OUT + (16 * lt + r) * OSTR + (16 * dt + 4 * kq) * 4) = res;
  }
  STAMP(1, 4);
  bar();
#pragma unroll
  for (int it = 0; it < NDT; ++it) {
    const int id = it * 512 + w * 64 + lane;
    const int row = id / (8 * NDT), pc = id - row * (8 * NDT);
    const f32x4 v = *(LDS const f32x4*)(smem + H_OUT + row * OSTR + pc * 16);
    __builtin_amdgcn_raw_buffer_store_b128(__builtin_bit_cast(u32x4, v), mk_rsrc(p.out, SL * DM * 4), ((l0 + row) * DM + d0 + pc * 4) * 4, 0, OUT_AUX);
  }
}

template <int NDT>
DEV void phase2(const P& p, LDS char* smem, int lt, int d0, int w, int lane, bool scratch_barrier, int sg) {
  STAMP(1, 0);
  if (w >= 8) {
    p2_loader<NDT>(p, smem, lt, d0, w - 8, lane, scratch_barrier, [&]() { if (NDT == 1) { wait_vm<8>(); s_signal(p, smem, sg, lane); } });
  } else {
    if (NDT == 1) { wait_vm<0>(); s_signal(p, smem, sg, lane); }
    if (w < 4) p2_compute<NDT, 0>(p, smem, lt, d0, w, lane);
    else p2_compute<NDT, 1>(p, smem, lt, d0, w, lane);
  }
  STAMP(1, 15);
}

__global__ __launch_bounds__(768, 3) void gated_mix_s4d_s5(P p) {
  extern __shared__ __attribute__((aligned(16))) char smem_raw[];
  LDS char* smem = (LDS char*)smem_raw;
  u64 pc;
  asm volatile("s_getpc_b64 %0" : "=s"(pc));
  u32 pf = 0;
#define PF_TOP 312
  if (threadIdx.x < PF_TOP)
    asm volatile("global_load_dword %0, %1, off" : "=v"(pf) : "v"((pc & ~255ull) + threadIdx.x * 128));
  const int w = __builtin_amdgcn_readfirstlane(threadIdx.x >> 6), lane = threadIdx.x & 63;
  const int xcd = blockIdx.x & 7, j = blockIdx.x >> 3;
  STAMP(0, 0);
  if (j >= 18 && j < 30) {
    const int sg = 12 * xcd + (j - 18);
    role_s(p, smem, sg, w, lane);
    STAMP(0, 15);
    phase2<1>(p, smem, 4 * xcd + (j - 18) / 3, 480 + 32 * ((j - 18) % 3), w, lane, false, sg);
  } else {
    const bool isg = j < 18;
    if (isg) {
      const int c = 2 * xcd + j / 9, g = j % 9;
      if (w >= 8) g_loader(p, smem, c, g, 96 * (j % 5), w - 8, lane);
      else if (w < 4) g_compute<0>(p, smem, c, g, w, lane, pf);
      else g_compute<1>(p, smem, c, g, w, lane, pf);
    } else {
      role_gate(p, 2 * xcd + (j - 30), w, lane, pc);
    }
    STAMP(0, 15);
    const int e = isg ? j : j - 12;
    phase2<3>(p, smem, 4 * xcd + e / 5, 96 * (e % 5), w, lane, isg, 0);
  }
  asm volatile("s_waitcnt vmcnt(0)" :: "v"(pf));
}

extern "C" void kernel_launch(void* const* d_in, const int* in_sizes, int n_in, void* d_out, int out_size,
                              void* d_ws, size_t ws_size, hipStream_t stream) {
  P a;
  a.x = (const u16*)d_in[0]; a.la = (const u16*)d_in[1]; a.aim = (const u16*)d_in[2];
  a.creA = (const u16*)d_in[3]; a.cimA = (const u16*)d_in[4]; a.ldtA = (const u16*)d_in[5]; a.dskA = (const u16*)d_in[6];
  a.lre = (const u16*)d_in[8]; a.lim = (const u16*)d_in[9]; a.bre = (const u16*)d_in[10]; a.bim = (const u16*)d_in[11];
  a.cre = (const u16*)d_in[12]; a.cim = (const u16*)d_in[13]; a.ldtB = (const u16*)d_in[14];
  a.dskB = (const u16*)d_in[15]; a.gw = (const u16*)d_in[16];
  a.ws = (char*)d_ws + WS_OFF; a.out = (float*)d_out;
  (void)hipFuncSetAttribute((const void*)gated_mix_s4d_s5, hipFuncAttributeMaxDynamicSharedMemorySize, K_LDS);
  gated_mix_s4d_s5<<<256, 768, K_LDS, stream>>>(a);
}
```

```cpp
#include <hip/hip_runtime.h>
#include <hip/hip_bf16.h>
#include <stdint.h>
#include <type_traits>

typedef unsigned short u16;
typedef unsigned int u32;
typedef unsigned long long u64;
typedef __attribute__((ext_vector_type(8))) __bf16 bf16x8;
typedef __attribute__((ext_vector_type(2))) __bf16 bf16x2;
typedef __attribute__((ext_vector_type(4))) short s16x4;
typedef __attribute__((ext_vector_type(4))) float f32x4;
typedef __attribute__((ext_vector_type(2))) float f32x2;
typedef __attribute__((ext_vector_type(4))) unsigned u32x4;
typedef __attribute__((ext_vector_type(2))) unsigned u32x2;
#define LDS __attribute__((address_space(3)))
#define GLB __attribute__((address_space(1)))
#define DEV __device__ __forceinline__
#define RLX_AGENT __ATOMIC_RELAXED, __HIP_MEMORY_SCOPE_AGENT

constexpr int SL = 2048;
constexpr int DM = 576;

constexpr size_t WS_HB = 0;
constexpr size_t WS_AT = WS_HB + (size_t)2 * DM * SL * 2;
constexpr size_t WS_G = WS_AT + (size_t)DM * SL * 2;
constexpr size_t WS_GRAN = WS_G + (size_t)SL * 8;
constexpr size_t WS_EP = WS_GRAN + (size_t)9 * 16 * 2 * 64 * 8;
constexpr size_t WS_FL = WS_EP + 4096;
constexpr size_t WS_XT = WS_FL + (size_t)16 * DM * 8;
constexpr size_t WS_IN = WS_XT + (size_t)DM * SL * 2;
constexpr u64 FLAG_SET = 0x9E3779B97F4A7C15ull;
constexpr u32 SPIN_CAP = 1u << 17;
constexpr int IN_H = 0, IN_G = 36, IN_A = 40;

constexpr size_t WS_ST = (size_t)16 << 20;
#define STAMP(kern, idx) do {} while (0)
#define CSTAMP(kern, idx) do {} while (0)
#define LSTAMP(kern, idx) do {} while (0)
#define LSTAMP2(kern, idx) do {} while (0)

DEV float bf2f(u16 v) { return __uint_as_float((u32)v << 16); }
DEV float bflo(u32 v) { return __uint_as_float(v << 16); }
DEV float bfhi(u32 v) { return __uint_as_float(v & 0xffff0000u); }
DEV u32 pack_bf16(float lo, float hi) {
  f32x2 v = {lo, hi};
  bf16x2 b = __builtin_convertvector(v, bf16x2);
  return __builtin_bit_cast(u32, b);
}
DEV float rbf(float x) { return bflo(pack_bf16(x, x)); }
template <int N> DEV void wait_vm() { asm volatile("s_waitcnt vmcnt(%0)" ::"n"(N) : "memory"); }
DEV void bar() { asm volatile("s_waitcnt lgkmcnt(0)\n\ts_barrier" ::: "memory"); }
DEV void wave_fence() { __builtin_amdgcn_fence(__ATOMIC_ACQ_REL, "wavefront"); }
template <int AUX> DEV void dma16(const void* src, LDS char* dst) {
  __builtin_amdgcn_global_load_lds((const GLB u32*)src, (LDS u32*)dst, 16, 0, AUX);
}
constexpr int SC1 = 16;
#define GPOLL_SLEEP 1
#define GPOLL_DELAY 24
#define POLL_SLEEP 1
#define OUT_AUX 16
#define WS_OFF 0
#define XT_WAIT 4
DEV bf16x8 lds_frag(LDS const char* p) { return *(LDS const bf16x8*)p; }
DEV s16x4 tr_read(LDS const char* a) { return __builtin_amdgcn_ds_read_tr16_b64_v4i16((LDS s16x4*)a); }
DEV bf16x8 tr_frag(LDS const char* a, LDS const char* b) {
  const s16x4 lo = tr_read(a), hi = tr_read(b);
  return __builtin_bit_cast(bf16x8, __builtin_shufflevector(lo, hi, 0, 1, 2, 3, 4, 5, 6, 7));
}
DEV f32x4 mfma16(bf16x8 a, bf16x8 b, f32x4 c) { return __builtin_amdgcn_mfma_f32_16x16x32_bf16(a, b, c, 0, 0, 0); }
DEV __amdgpu_buffer_rsrc_t mk_rsrc(const void* base, int bytes) {
  return __builtin_amdgcn_make_buffer_rsrc(const_cast<void*>(base), (short)0, bytes, 0x00020000);
}
struct cf { float r, i; };
DEV cf cmul(cf a, cf b) { return {a.r * b.r - a.i * b.i, a.r * b.i + a.i * b.r}; }
DEV cf cfma(cf a, cf b, cf c) { return {fmaf(a.r, b.r, fmaf(-a.i, b.i, c.r)), fmaf(a.r, b.i, fmaf(a.i, b.r, c.i))}; }
typedef f32x2 c2;
DEV c2 cm2(c2 a, c2 b) {
  c2 t, r;
  asm("v_pk_mul_f32 %0, %1, %2 op_sel:[0,0] op_sel_hi:[0,1]" : "=v"(t) : "v"(a), "v"(b));
  asm("v_pk_fma_f32 %0, %1, %2, %3 op_sel:[1,1,0] op_sel_hi:[1,0,1] neg_lo:[1,0,0]" : "=v"(r) : "v"(a), "v"(b), "v"(t));
  return r;
}
DEV f32x2 fma2(f32x2 a, f32x2 b, f32x2 c) { return __builtin_elementwise_fma(a, b, c); }
DEV f32x2 lo2(f32x4 v) { return __builtin_shufflevector(v, v, 0, 1); }
DEV f32x2 hi2(f32x4 v) { return __builtin_shufflevector(v, v, 2, 3); }
DEV f32x4 cat2(f32x2 a, f32x2 b) { return __builtin_shufflevector(a, b, 0, 1, 2, 3); }
DEV void wait_flags(GLB u64* word, bool need) {
  for (u32 spin = 0;; ++spin) {
    const u64 v = __hip_atomic_load(word, RLX_AGENT);
    if (__all(!need || v == FLAG_SET) || spin > SPIN_CAP) break;
    __builtin_amdgcn_s_sleep(POLL_SLEEP);
  }
}
DEV int tile_block(int lt, int idx) {
  const int xcd = lt >> 2, a = lt & 3;
  int j;
  if (idx < 5) { const int e = 5 * a + idx; j = e < 18 ? e : e + 12; }
  else j = 18 + 3 * a + (idx - 5);
  return (j << 3) | xcd;
}

struct P {
  const u16 *x, *la, *aim, *creA, *cimA, *ldtA, *dskA;
  const u16 *lre, *lim, *bre, *bim, *cre, *cim, *ldtB, *dskB, *gw;
  char* ws;
  float* out;
};

constexpr int G_NB = 4, G_STAGE = 32768;
constexpr int G_E = 153536;
constexpr int G_PF = G_E + 4096;
constexpr int G_L = G_PF + 4096;
constexpr int G_X = G_L + 1024;
constexpr int G_GO = G_X + 1024;
constexpr int G_T = G_NB * G_STAGE;
static_assert(G_GO + 4 <= 163840 && G_T + 4 * 32 * 144 <= G_E, "role G scratch");

DEV void g_loader(const P& p, LDS char* smem, int c, int g, int d0, int lw, int lane) {
  [[maybe_unused]] const int w = lw + 8;
  __builtin_amdgcn_s_setprio(3);
  GLB u32* ep = (GLB u32*)(p.ws + WS_EP + g * 256);
  GLB u64* gran = (GLB u64*)(p.ws + WS_GRAN) + (size_t)g * 16 * 128;
  const u32 tag = __hip_atomic_load(ep, RLX_AGENT) + 1u;
  u32 xo[4], bo[4];
#pragma unroll
  for (int i = 0; i < 4; ++i) {
    const int rho = 8 * (4 * lw + i) + (lane >> 3);
    const int hf = rho >> 6, m = (rho >> 4) & 3, r = rho & 15;
    const int tau = 64 * hf + 16 * (r >> 2) + 8 * (r & 1) + 2 * m + ((r >> 1) & 1);
    const int ch = (lane & 7) ^ (rho & 7);
    xo[i] = (u32)(128 * c + tau) * 1152u + ch * 16;
    const int pp = 64 * g + 16 * (rho >> 5) + r;
    bo[i] = (u32)pp * 1152u + ch * 16;
  }
  auto issue = [&](int s) {
    LDS char* buf = smem + (s & (G_NB - 1)) * G_STAGE + lw * 4096;
    int kb = g + s; kb = (kb >= 9 ? kb - 9 : kb) * 128;
#pragma unroll
    for (int i = 0; i < 4; ++i) dma16<0>((const char*)p.x + (xo[i] + kb), buf + i * 1024);
#pragma unroll
    for (int i = 0; i < 4; ++i) {
      const char* base = (const char*)((((4 * lw + i) >> 1) & 1) ? p.bim : p.bre);
      dma16<0>(base + (bo[i] + kb), buf + 16384 + i * 1024);
    }
  };
#pragma unroll
  for (int s = 0; s < G_NB - 1; ++s) issue(s);
#pragma unroll 1
  for (int s = 0; s < 6; ++s) { wait_vm<16>(); LSTAMP(0, 16 + s); bar(); issue(s + G_NB - 1); LSTAMP(0, 32 + s); }
  wait_vm<16>(); bar();
  wait_vm<8>(); bar();
  wait_vm<0>(); bar();
  STAMP(0, 1);
  if (lw < 2) {
    bar();
    u32 co[6];
#pragma unroll
    for (int i = 0; i < 6; ++i) {
      const int row = 8 * (6 * lw + i) + (lane >> 3), cp = lane & 7;
      co[i] = (u32)(d0 + row) * 1152u + ((cp ^ (row & 7)) << 4);
    }
    const int rot = ((d0 / 96) * 18) >> 3;
#pragma unroll 1
    for (int s = 0; s < 8; ++s) {
      LDS char* buf = smem + 32768 + s * 12288 + 6 * lw * 1024;
      const int ks = s + rot;
      const char* cb = ks < 9 ? (const char*)p.cre + ks * 128 : (const char*)p.cim + (ks - 9) * 128;
#pragma unroll
      for (int i = 0; i < 6; ++i) dma16<0>(cb + co[i], buf + i * 1024);
    }
    bar();
    return;
  }
  bar();
  STAMP(0, 2);
  if (lw == 3) {
    const f32x2 lv = *(LDS const f32x2*)(smem + G_L + lane * 8);
    const cf l16 = {lv[0], lv[1]};
    f32x2 ev[8];
#pragma unroll
    for (int s = 0; s < 8; ++s) ev[s] = *(LDS const f32x2*)(smem + G_E + (s * 64 + lane) * 8);
    cf tot = {0.f, 0.f};
    cf pre[8];
#pragma unroll
    for (int s = 0; s < 8; ++s) { pre[s] = tot; tot = cfma(l16, tot, cf{ev[s][0], ev[s][1]}); }
    if (c < 15) {
      __hip_atomic_store(gran + c * 128 + lane, ((u64)tag << 32) | __float_as_uint(tot.r), RLX_AGENT);
      __hip_atomic_store(gran + c * 128 + 64 + lane, ((u64)tag << 32) | __float_as_uint(tot.i), RLX_AGENT);
    }
#pragma unroll
    for (int s = 0; s < 8; ++s) *(LDS f32x2*)(smem + G_PF + (s * 64 + lane) * 8) = f32x2{pre[s].r, pre[s].i};
  }
  const int par = lw - 2;
  const f32x2 lv = *(LDS const f32x2*)(smem + G_L + 512 + lane * 8);
  const cf l128 = {lv[0], lv[1]};
  const cf l256 = cmul(l128, l128);
  cf acc = {0.f, 0.f};
  if (par < c) {
    const int nc = (c - par + 1) >> 1;
    u64 a[8], b[8];
    __builtin_amdgcn_s_sleep(GPOLL_DELAY);
    for (u32 spin = 0;; ++spin) {
      bool ok = true;
#pragma unroll
      for (int i = 0; i < 8; ++i) {
        const int cc = par + 2 * (i < nc ? i : 0);
        a[i] = __hip_atomic_load(gran + cc * 128 + lane, RLX_AGENT);
        b[i] = __hip_atomic_load(gran + cc * 128 + 64 + lane, RLX_AGENT);
      }
#pragma unroll
      for (int i = 0; i < 8; ++i) ok = ok && (u32)(a[i] >> 32) == tag && (u32)(b[i] >> 32) == tag;
      if (__all(ok) || spin > SPIN_CAP) break;
      __builtin_amdgcn_s_sleep(GPOLL_SLEEP);
    }
#pragma unroll
    for (int i = 0; i < 8; ++i)
      if (i < nc) acc = cfma(l256, acc, cf{__uint_as_float((u32)a[i]), __uint_as_float((u32)b[i])});
    if (c - 1 - (par + 2 * (nc - 1))) acc = cmul(acc, l128);
  }
  *(LDS f32x2*)(smem + G_X + (par * 64 + lane) * 8) = f32x2{acc.r, acc.i};
  STAMP(0, 3);
  bar();
  if (c == 15 && lw == 2 && lane == 0) __hip_atomic_store(ep, tag, RLX_AGENT);
}

template <int HF>
DEV void g_compute(const P& p, LDS char* smem, int c, int g, int w, int lane, u32& pf) {
  const int pq = w & 3, col = lane & 15, q = lane >> 4;
  const int pl = 16 * pq + col, pp = 64 * g + pl;
  cf lam, coef2;
  {
    const float lre = bf2f(p.lre[pp]), lim = bf2f(p.lim[pp]);
    const float dt = rbf(expf(bf2f(p.ldtB[pp])));
    const float zr = dt * lre, zi = dt * lim;
    const float er = expf(zr);
    float sn, cs;
    sincosf(zi, &sn, &cs);
    lam = {er * cs, er * sn};
    const float nr = lam.r - 1.0f, ni = lam.i;
    const float den = 1.0f / (lre * lre + lim * lim);
    coef2 = {2.0f * (nr * lre + ni * lim) * den, 2.0f * (ni * lre - nr * lim) * den};
  }
  cf l8 = cmul(lam, lam); l8 = cmul(l8, l8); l8 = cmul(l8, l8);
  const cf l16 = cmul(l8, l8);
  const cf l32 = cmul(l16, l16), l64 = cmul(l32, l32);
  if (HF == 0 && q == 0) {
    const cf l128 = cmul(l64, l64);
    *(LDS f32x2*)(smem + G_L + pl * 8) = f32x2{l16.r, l16.i};
    *(LDS f32x2*)(smem + G_L + 512 + pl * 8) = f32x2{l128.r, l128.i};
  }
  if (HF == 0 && w == 0 && lane == 0) *(LDS u32*)(smem + G_GO) = 0;
  cf pw = HF ? l64 : cf{1.f, 0.f};
  { const cf t = cmul(pw, l16); if (q & 1) pw = t; }
  { const cf t = cmul(pw, l32); if (q & 2) pw = t; }
  asm volatile("" : "+v"(lam.r), "+v"(lam.i), "+v"(coef2.r), "+v"(coef2.i), "+v"(l8.r), "+v"(l8.i), "+v"(pw.r), "+v"(pw.i));
  f32x4 are[4], aim[4];
#pragma unroll
  for (int m = 0; m < 4; ++m) { are[m] = f32x4{0, 0, 0, 0}; aim[m] = f32x4{0, 0, 0, 0}; }
  const int o0 = col * 128 + (((0 + q) ^ (col & 7)) << 4);
  const int o1 = col * 128 + (((4 + q) ^ (col & 7)) << 4);
  bf16x8 br[2], bi[2], a[2][4];
  auto read_frags = [&](int s) {
    LDS const char* buf = smem + (s & (G_NB - 1)) * G_STAGE;
    LDS const char* ab = buf + HF * 8192;
    LDS const char* bb = buf + 16384 + pq * 4096;
#pragma unroll
    for (int ks = 0; ks < 2; ++ks) {
      const int o = ks ? o1 : o0;
      br[ks] = lds_frag(bb + o); bi[ks] = lds_frag(bb + 2048 + o);
#pragma unroll
      for (int m = 0; m < 4; ++m) a[ks][m] = lds_frag(ab + m * 2048 + o);
    }
  };
  auto mma = [&]() {
#pragma unroll
    for (int ks = 0; ks < 2; ++ks)
#pragma unroll
      for (int m = 0; m < 4; ++m) {
        are[m] = mfma16(a[ks][m], br[ks], are[m]);
        aim[m] = mfma16(a[ks][m], bi[ks], aim[m]);
      }
  };
  STAMP(0, 1);
#pragma unroll
  for (int s = 0; s < 9; ++s) {
    if (HF == 1 && s == XT_WAIT) {
      wait_vm<0>();
      if (lane < 16) __hip_atomic_store((GLB u64*)(p.ws + WS_FL) + c * DM + 64 * g + 16 * pq + lane, FLAG_SET, RLX_AGENT);
    }
    bar();
    if (s == 0) { STAMP(0, 2); CSTAMP(0, 60); }
    if (HF == 1 && s > 0) { mma(); __builtin_amdgcn_sched_barrier(0); }
    if (HF == 1 && s == 0) {
      const int rr = 4 * q + (col >> 2);
      LDS const char* tb = smem + rr * 128 + (((2 * pq + ((col & 3) >> 1)) ^ (rr & 7)) << 4) + (col & 1) * 8;
      LDS char* sc = smem + G_T + pq * (32 * 144);
#pragma unroll
      for (int hf = 0; hf < 2; ++hf) {
        u32x2 t[4];
#pragma unroll
        for (int m = 0; m < 4; ++m) t[m] = __builtin_bit_cast(u32x2, tr_read(tb + hf * 8192 + m * 2048));
        u32x4 oa, ob;
#pragma unroll
        for (int m = 0; m < 4; ++m) {
          oa[m] = __builtin_amdgcn_perm(t[m][1], t[m][0], 0x05040100u);
          ob[m] = __builtin_amdgcn_perm(t[m][1], t[m][0], 0x07060302u);
        }
        *(LDS u32x4*)(sc + hf * (16 * 144) + col * 144 + q * 32) = oa;
        *(LDS u32x4*)(sc + hf * (16 * 144) + col * 144 + q * 32 + 16) = ob;
      }
      wave_fence();
      const __amdgpu_buffer_rsrc_t rs = mk_rsrc(p.ws + WS_XT, DM * SL * 2);
#pragma unroll
      for (int u = 0; u < 4; ++u) {
        const int row = (lane >> 3) + 8 * (u & 1), hf = u >> 1;
        const u32x4 v = *(LDS const u32x4*)(sc + hf * (16 * 144) + row * 144 + (lane & 7) * 16);
        __builtin_amdgcn_raw_buffer_store_b128(v, rs, ((64 * g + 16 * pq + row) * SL + 128 * c + 64 * hf) * 2 + (lane & 7) * 16, 0, SC1);
      }
      __builtin_amdgcn_sched_barrier(0);
    }
    read_frags(s);
    __builtin_amdgcn_sched_barrier(0);
    if (HF == 0) { mma(); __builtin_amdgcn_sched_barrier(0); }
  }
  if (HF == 1) mma();
  __builtin_amdgcn_sched_barrier(0);
  STAMP(0, 3); CSTAMP(0, 61);
  const f32x2 Lr = {lam.r, lam.r}, Li = {lam.i, lam.i};
  f32x2 er = {0.f, 0.f}, ei = {0.f, 0.f};
#pragma unroll
  for (int t = 0; t < 8; ++t) {
    const f32x2 br_ = (t & 1) ? hi2(are[t >> 1]) : lo2(are[t >> 1]);
    const f32x2 bi_ = (t & 1) ? hi2(aim[t >> 1]) : lo2(aim[t >> 1]);
    const f32x2 nr = fma2(-Li, ei, fma2(Lr, er, br_));
    ei = fma2(Li, er, fma2(Lr, ei, bi_));
    er = nr;
  }
  const cf eA = {er[0], ei[0]};
  const cf e = cfma(l8, eA, cf{er[1], ei[1]});
  const int seg = 4 * HF + q;
  *(LDS f32x2*)(smem + G_E + (seg * 64 + pl) * 8) = f32x2{e.r, e.i};
  STAMP(0, 4);
  bar();
  STAMP(0, 5);
  {
#pragma unroll
    for (int m = 0; m < 4; ++m) asm volatile("" : "+v"(are[m]), "+v"(aim[m]));
    const f32x2 Cr = {coef2.r, coef2.r}, Ci = {coef2.i, coef2.i};
#pragma unroll
    for (int m = 0; m < 4; ++m) {
      const f32x2 r0 = lo2(are[m]), r1 = hi2(are[m]), i0 = lo2(aim[m]), i1 = hi2(aim[m]);
      are[m] = cat2(fma2(-Ci, i0, Cr * r0), fma2(-Ci, i1, Cr * r1));
      aim[m] = cat2(fma2(-Ci, r0, -Cr * i0), fma2(-Ci, r1, -Cr * i1));
      asm volatile("" : "+v"(are[m]), "+v"(aim[m]));
    }
  }
  cf eAc = cmul(coef2, eA), pwc = cmul(coef2, pw);
  asm volatile("" : "+v"(eAc.r), "+v"(eAc.i), "+v"(pwc.r), "+v"(pwc.i));
  STAMP(0, 7);
  bar();
  STAMP(0, 8);
  cf cin;
  {
    f32x2 v[2];
#pragma unroll
    for (int i = 0; i < 2; ++i) v[i] = *(LDS const f32x2*)(smem + G_X + (i * 64 + pl) * 8);
    const f32x2 pf = *(LDS const f32x2*)(smem + G_PF + (seg * 64 + pl) * 8);
    const f32x2 S = v[0] + v[1];
    cin = cfma(pwc, cf{S[0], S[1]}, cmul(coef2, cf{pf[0], pf[1]}));
  }
  const cf cinB = cfma(l8, cin, eAc);
  er = f32x2{cin.r, cinB.r}; ei = f32x2{-cin.i, -cinB.i};
  f32x2 hr[8], hi[8];
#pragma unroll
  for (int t = 0; t < 8; ++t) {
    const f32x2 br_ = (t & 1) ? hi2(are[t >> 1]) : lo2(are[t >> 1]);
    const f32x2 bi_ = (t & 1) ? hi2(aim[t >> 1]) : lo2(aim[t >> 1]);
    const f32x2 nr = fma2(Li, ei, fma2(Lr, er, br_));
    ei = fma2(-Li, er, fma2(Lr, ei, bi_));
    er = nr;
    hr[t] = er; hi[t] = ei;
  }
  u32x4 o[4];
#pragma unroll
  for (int t = 0; t < 4; ++t) {
    o[0][t] = pack_bf16(hr[2 * t][0], hr[2 * t + 1][0]);
    o[1][t] = pack_bf16(hr[2 * t][1], hr[2 * t + 1][1]);
    o[2][t] = pack_bf16(hi[2 * t][0], hi[2 * t + 1][0]);
    o[3][t] = pack_bf16(hi[2 * t][1], hi[2 * t + 1][1]);
  }
  STAMP(0, 9);
  {
    const __amdgpu_buffer_rsrc_t rs = mk_rsrc(p.ws + WS_HB, 2 * DM * SL * 2);
    const int hb = (((c * 2 + HF) * 2) * 9 + g) * 8192 + pq * 2048;
    const int p0 = (16 * q + col) * 16, p1 = 1024 + (16 * q + (col ^ 4)) * 16;
    __builtin_amdgcn_raw_buffer_store_b128(o[0], rs, hb + p0, 0, SC1);
    __builtin_amdgcn_raw_buffer_store_b128(o[1], rs, hb + p1, 0, SC1);
    __builtin_amdgcn_raw_buffer_store_b128(o[2], rs, hb + 9 * 8192 + p0, 0, SC1);
    __builtin_amdgcn_raw_buffer_store_b128(o[3], rs, hb + 9 * 8192 + p1, 0, SC1);
    wait_vm<0>();
    STAMP(0, 10);
    if (lane < 8)
      __hip_atomic_store((GLB u64*)(p.ws + WS_IN) + tile_block(2 * c + HF, lane) * 64 + IN_H + g * 4 + pq, FLAG_SET, RLX_AGENT);
    bar();
  }
}

constexpr int S_CPY = 4160;
constexpr int S_X = 0;
constexpr int S_ST = 6 * 4 * S_CPY;
constexpr int S_Z = S_ST;
constexpr int S_ZSZ = (256 + SL) * 2;
constexpr int S_T = S_ST + 12 * 4608;
constexpr int S_TSZ = 1312;
constexpr int S_END = S_T + 6 * S_TSZ;
constexpr int S_CNT = 32768 + 8192;

DEV c2 cexp_k(float zl, float th, float tl, float k) {
  const float mag = __builtin_amdgcn_exp2f(zl * k);
  const float t = fmaf(tl, k, __builtin_amdgcn_fractf(th * k));
  return c2{mag * __builtin_amdgcn_cosf(t), mag * __builtin_amdgcn_sinf(t)};
}
#define PKM(d, a, b) "v_pk_mul_f32 " d ", " a ", " b " op_sel:[0,0] op_sel_hi:[0,1]\n\t"
#define PKF(d, a, b) "v_pk_fma_f32 " d ", " a ", " b ", " d " op_sel:[1,1,0] op_sel_hi:[1,0,1] neg_lo:[1,0,0]\n\t"
DEV void octet(LDS char* dst, c2 s0, c2 m1, c2 m2) {
  c2 v1, v2, v3, t0, t1, t2, t3;
  asm(PKM("%0", "%7", "%8") PKM("%3", "%7", "%9") PKF("%0", "%7", "%8") PKF("%3", "%7", "%9")
      PKM("%1", "%0", "%8") PKM("%4", "%0", "%9") PKF("%1", "%0", "%8") PKF("%4", "%0", "%9")
      PKM("%2", "%1", "%8") PKM("%5", "%1", "%9") PKF("%2", "%1", "%8") PKF("%5", "%1", "%9")
      PKM("%6", "%2", "%9") PKF("%6", "%2", "%9")
      : "=&v"(v1), "=&v"(v2), "=&v"(v3), "=&v"(t0), "=&v"(t1), "=&v"(t2), "=&v"(t3) : "v"(s0), "v"(m1), "v"(m2));
  *(LDS u32*)(dst + 0 * 288) = pack_bf16(s0[0], s0[1]);
  *(LDS u32*)(dst + 1 * 288) = pack_bf16(v1[0], v1[1]);
  *(LDS u32*)(dst + 2 * 288) = pack_bf16(v2[0], v2[1]);
  *(LDS u32*)(dst + 3 * 288) = pack_bf16(v3[0], v3[1]);
  *(LDS u32*)(dst + 4 * 288) = pack_bf16(t0[0], t0[1]);
  *(LDS u32*)(dst + 5 * 288) = pack_bf16(t1[0], t1[1]);
  *(LDS u32*)(dst + 6 * 288) = pack_bf16(t2[0], t2[1]);
  *(LDS u32*)(dst + 7 * 288) = pack_bf16(t3[0], t3[1]);
}
DEV u32 dup_lo(u32 a) { return __builtin_amdgcn_perm(a, a, 0x01000100u); }
DEV u32 dup_hi(u32 a) { return __builtin_amdgcn_perm(a, a, 0x03020302u); }
DEV u32 hi_lo(u32 a, u32 b) { return __builtin_amdgcn_alignbit(b, a, 16); }

DEV void role_s(const P& p, LDS char* smem, int sg, int w, int lane) {
  const int tid = w * 64 + lane;
  const int ch = w >> 1, hh = w & 1, d = 6 * sg + ch;
  const int n = d * 128 + 64 * hh + lane;
  const u16 r_ldt = p.ldtA[d], r_la = p.la[n], r_aim = p.aim[n], r_cre = p.creA[n], r_cim = p.cimA[n];
  u32 wb[2] = {0, 0};
  __builtin_amdgcn_sched_barrier(0);
#pragma unroll
  for (int i = 0; i < 2; ++i) {
    const int ln = i * 768 + tid, all = (sg % 12) * 864 + ln;
    if (ln < 864) wb[i] = *(const GLB u32*)((const char*)(all < 5184 ? p.bre : p.bim) + (size_t)(all < 5184 ? all : all - 5184) * 128);
  }
  __builtin_amdgcn_sched_barrier(0);
  STAMP(0, 1);
  f32x4 tab[3];
  [[maybe_unused]] u32 wc[2] = {0, 0};
  LDS char* st = smem + S_ST + w * 4608;
  {
    const float L2E = 1.44269504088896341f;
    const float dt = rbf(__builtin_amdgcn_exp2f(bf2f(r_ldt) * L2E));
    const float ar = -rbf(__builtin_amdgcn_exp2f(bf2f(r_la) * L2E));
    const float zl = ar * dt * L2E, zi = bf2f(r_aim) * dt;
    const float th = zi * 0.15915494309189535f;
    const float tl = fmaf(zi, 0.15915494309189535f, -th) + zi * 4.6017384e-10f;
    const c2 C = {2.0f * (bf2f(r_cre) * dt), 2.0f * (bf2f(r_cim) * dt)};
    const c2 w1 = cexp_k(zl, th, tl, 1.f), w8 = cexp_k(zl, th, tl, 8.f), w128 = cexp_k(zl, th, tl, 128.f);
    c2 w2, w4, w16, w32, w64, w256, w512, w1024, w2048;
    asm(PKM("%0", "%9", "%9") PKM("%2", "%10", "%10") PKM("%5", "%11", "%11") PKF("%0", "%9", "%9") PKF("%2", "%10", "%10") PKF("%5", "%11", "%11")
        PKM("%1", "%0", "%0") PKM("%3", "%2", "%2") PKM("%6", "%5", "%5") PKF("%1", "%0", "%0") PKF("%3", "%2", "%2") PKF("%6", "%5", "%5")
        PKM("%4", "%3", "%3") PKM("%7", "%6", "%6") PKF("%4", "%3", "%3") PKF("%7", "%6", "%6")
        PKM("%8", "%7", "%7") PKF("%8", "%7", "%7")
        : "=&v"(w2), "=&v"(w4), "=&v"(w16), "=&v"(w32), "=&v"(w64), "=&v"(w256), "=&v"(w512), "=&v"(w1024), "=&v"(w2048)
        : "v"(w1), "v"(w8), "v"(w128));
    c2 C128, C256, C512, C1024, C2048;
    asm(PKM("%0", "%5", "%6") PKM("%1", "%5", "%7") PKM("%2", "%5", "%8") PKM("%3", "%5", "%9") PKM("%4", "%5", "%10")
        PKF("%0", "%5", "%6") PKF("%1", "%5", "%7") PKF("%2", "%5", "%8") PKF("%3", "%5", "%9") PKF("%4", "%5", "%10")
        : "=&v"(C128), "=&v"(C256), "=&v"(C512), "=&v"(C1024), "=&v"(C2048)
        : "v"(C), "v"(w128), "v"(w256), "v"(w512), "v"(w1024), "v"(w2048));
#pragma unroll
    for (int i = 0; i < 2; ++i) {
      const int ln = i * 768 + tid, all = (sg % 12) * 864 + ln;
      if (ln < 864) wc[i] = *(const GLB u32*)((const char*)(all < 5184 ? p.cre : p.cim) + (size_t)(all < 5184 ? all : all - 5184) * 128);
    }
    STAMP(0, 2);
    LDS char* wr = st + 4 * lane;
    LDS const char* rd = st + (lane & 15) * 288 + (lane >> 4) * 16;
    bf16x8 B[4];
    const c2 w8c = {w8[0], -w8[1]}, w32c = {w32[0], -w32[1]}, w64c = {w64[0], -w64[1]};
    octet(wr, c2{1.f, 0.f}, w8c, w32c);
    octet(wr + 8 * 288, w64c, w8c, w32c);
    wave_fence();
#pragma unroll
    for (int ks = 0; ks < 4; ++ks) B[ks] = lds_frag(rd + 64 * ks);
    wave_fence();
#pragma unroll
    for (int mt = 0; mt < 3; ++mt) {
      if (mt == 0) { octet(wr, C, w1, w4); octet(wr + 8 * 288, C128, w1, w4); }
      if (mt == 1) { octet(wr, C256, w2, w128); octet(wr + 8 * 288, C512, w128, w4); }
      if (mt == 2) { octet(wr, C1024, w128, w512); *(LDS u32*)(wr + 8 * 288) = pack_bf16(C2048[0], C2048[1]); }
      wave_fence();
      f32x4 acc = {0, 0, 0, 0};
      bf16x8 A[4];
#pragma unroll
      for (int ks = 0; ks < 4; ++ks) A[ks] = lds_frag(rd + 64 * ks);
      __builtin_amdgcn_sched_barrier(0);
#pragma unroll
      for (int ks = 0; ks < 4; ++ks) acc = mfma16(A[ks], B[ks], acc);
      __builtin_amdgcn_sched_barrier(0);
      wave_fence();
      tab[mt] = acc;
    }
  }
  const int b = lane & 15, q = lane >> 4;
#pragma unroll
  for (int mt = 0; mt < 3; ++mt) *(LDS f32x4*)(st + mt * 1024 + lane * 16) = tab[mt];
  STAMP(0, 3);
  bar();
  {
    LDS const char* ot = smem + S_ST + (w ^ 1) * 4608 + lane * 16;
    LDS char* T = smem + S_T + ch * S_TSZ;
    if (hh == 0) {
      const f32x4 v0 = tab[0] + *(LDS const f32x4*)(ot), v2 = tab[2] + *(LDS const f32x4*)(ot + 2048);
      *(LDS u32x2*)(T + 2 * (128 * (q >> 1) + 8 * b + 4 * (q & 1))) = u32x2{pack_bf16(v0[0], v0[1]), pack_bf16(v0[2], v0[3])};
      const u32 a0 = pack_bf16(v2[0], v2[1]), a1 = pack_bf16(v2[2], v2[3]);
      if (q < 2) {
        LDS char* t3 = T + 2 * (512 + 64 * q + b);
        *(LDS u16*)(t3) = (u16)a0; *(LDS u16*)(t3 + 32) = (u16)(a0 >> 16);
        *(LDS u16*)(t3 + 64) = (u16)a1; *(LDS u16*)(t3 + 96) = (u16)(a1 >> 16);
      } else if (lane == 32) *(LDS u16*)(T + 2 * 640) = (u16)a0;
    } else {
      const f32x4 v1 = tab[1] + *(LDS const f32x4*)(ot + 1024);
      const u32 a0 = pack_bf16(v1[0], v1[1]), a1 = pack_bf16(v1[2], v1[3]);
      if (q < 2) *(LDS u32x2*)(T + 2 * (256 + 64 * q + 4 * b)) = u32x2{a0, a1};
      else {
        LDS char* t2 = T + 2 * (384 + 2 * b + (q - 2));
        *(LDS u16*)(t2) = (u16)a0; *(LDS u16*)(t2 + 64) = (u16)(a0 >> 16);
        *(LDS u16*)(t2 + 128) = (u16)a1; *(LDS u16*)(t2 + 192) = (u16)(a1 >> 16);
      }
    }
  }
  bar();
  STAMP(0, 4);
  u32x4 uv[2];
  {
    GLB u64* fl = (GLB u64*)(p.ws + WS_FL) + (8 * hh + (lane & 7)) * DM + d;
    asm volatile("" :: "v"(wc[0]), "v"(wc[1]), "v"(wb[0]), "v"(wb[1]));
    wait_flags(fl, true);
    const __amdgpu_buffer_rsrc_t rs = mk_rsrc(p.ws + WS_XT, DM * SL * 2);
#pragma unroll
    for (int v = 0; v < 2; ++v) uv[v] = __builtin_amdgcn_raw_buffer_load_b128(rs, (d * SL + 1024 * hh + 8 * (lane + 64 * v)) * 2, 0, SC1);
    if (lane < 8) __hip_atomic_store(fl, 0ull, RLX_AGENT);
  }
  STAMP(0, 5);
  {
    LDS const char* T = smem + S_T + ch * S_TSZ;
    u32 o[10];
    const u32 e512 = *(LDS const u16*)(T + 2 * 512);
    o[8] = o[9] = e512 | (e512 << 16);
    if (hh == 0) {
      const int off = lane < 16 ? 32 * lane : (lane < 32 ? 256 + 16 * lane : 512 + 8 * lane);
      u32x2 W[4];
#pragma unroll
      for (int i = 0; i < 4; ++i) W[i] = *(LDS const u32x2*)(T + off + 8 * i);
      if (lane < 16) {
#pragma unroll
        for (int i = 0; i < 8; ++i) o[i] = W[i >> 1][i & 1];
      } else if (lane < 32) {
#pragma unroll
        for (int i = 0; i < 4; ++i) {
          const u32 a = W[i >> 1][i & 1], nx = W[(i + 1) >> 1][(i + 1) & 1];
          o[2 * i] = dup_lo(a);
          o[2 * i + 1] = hi_lo(a, nx);
        }
      } else {
        const u32 w0 = W[0][0], w1 = W[0][1], w2 = W[1][0];
        o[0] = dup_lo(w0); o[1] = w0; o[2] = dup_hi(w0); o[3] = dup_lo(w1);
        o[4] = o[3]; o[5] = w1; o[6] = dup_hi(w1); o[7] = dup_lo(w2);
      }
    } else {
      const u32 w0 = *(LDS const u32*)(T + 1024 + 4 * lane), w1 = *(LDS const u32*)(T + 1028 + 4 * lane);
      o[0] = o[1] = dup_lo(w0); o[2] = w0; o[3] = o[4] = o[5] = dup_hi(w0); o[6] = o[7] = dup_lo(w1);
    }
    o[8] = (u32)__builtin_amdgcn_update_dpp((int)o[8], (int)o[0], 0x130, 0xf, 0xf, false);
    o[9] = (u32)__builtin_amdgcn_update_dpp((int)o[9], (int)o[1], 0x130, 0xf, 0xf, false);
    u32 a[9];
#pragma unroll
    for (int i = 0; i < 9; ++i) a[i] = hi_lo(o[i], o[i + 1]);
    LDS char* xb = smem + S_X + ch * 4 * S_CPY + 32 + 2 * (1024 * hh + 16 * lane);
    *(LDS u32x4*)(xb) = u32x4{o[0], o[1], o[2], o[3]};
    *(LDS u32x4*)(xb + 16) = u32x4{o[4], o[5], o[6], o[7]};
    *(LDS u32x4*)(xb + S_CPY) = u32x4{a[0], a[1], a[2], a[3]};
    *(LDS u32x4*)(xb + S_CPY + 16) = u32x4{a[4], a[5], a[6], a[7]};
    *(LDS u32x4*)(xb + 2 * S_CPY) = u32x4{o[1], o[2], o[3], o[4]};
    *(LDS u32x4*)(xb + 2 * S_CPY + 16) = u32x4{o[5], o[6], o[7], o[8]};
    *(LDS u32x4*)(xb + 3 * S_CPY) = u32x4{a[1], a[2], a[3], a[4]};
    *(LDS u32x4*)(xb + 3 * S_CPY + 16) = u32x4{a[5], a[6], a[7], a[8]};
    if (hh == 0 && lane == 0) {
      LDS char* hb = smem + S_X + ch * 4 * S_CPY;
      const u32x4 z = {0, 0, 0, 0};
      *(LDS u32x4*)(hb) = z;
      *(LDS u32x4*)(hb + 16) = z;
      *(LDS u32x4*)(hb + S_CPY) = z;
      *(LDS u32x4*)(hb + S_CPY + 16) = u32x4{0, 0, 0, o[0] << 16};
      *(LDS u32x4*)(hb + 2 * S_CPY) = z;
      *(LDS u32x4*)(hb + 2 * S_CPY + 16) = u32x4{0, 0, 0, o[0]};
      *(LDS u32x4*)(hb + 3 * S_CPY) = z;
      *(LDS u32x4*)(hb + 3 * S_CPY + 16) = u32x4{0, 0, o[0] << 16, a[0]};
    }
    if (tid < 6 * 32) *(LDS u32x4*)(smem + S_Z + (tid >> 5) * S_ZSZ + (tid & 31) * 16) = u32x4{0, 0, 0, 0};
    STAMP(0, 6);
    LDS char* z = smem + S_Z + ch * S_ZSZ + 512 + (1024 * hh + 8 * lane) * 2;
    *(LDS u32x4*)z = uv[0];
    *(LDS u32x4*)(z + 1024) = uv[1];
  }
  bar();
  STAMP(0, 7);
  f32x4 acc[8];
#pragma unroll
  for (int i = 0; i < 8; ++i) acc[i] = f32x4{0, 0, 0, 0};
  {
    const int kq = q, qq = b >> 2, pp = b & 3;
    LDS const char* fb = smem + S_X + (ch * 4 + ((4 - qq) & 3)) * S_CPY +
                         2 * (16 + 16 * (kq >> 1) - 8 * (kq & 1) + 4 * pp - (qq ? 4 : 0)) + 64 * hh;
    LDS const char* gb = smem + S_Z + ch * S_ZSZ + 512 + 32 * (b - (kq >> 1)) + 16 * (kq & 1) - 64 * hh;
#pragma unroll 1
    for (int it = 0; it < 4; ++it) {
      bf16x8 F[8], G[8];
#pragma unroll
      for (int s = 0; s < 8; ++s) {
        F[s] = tr_frag(fb + 512 * s, fb + 512 * s - 8);
        G[s] = lds_frag(gb + 512 * s);
      }
      __builtin_amdgcn_sched_barrier(0);
#pragma unroll
      for (int t = 0; t < 8; ++t)
#pragma unroll
        for (int s = 0; s <= t; ++s) acc[t] = mfma16(F[s], G[t - s], acc[t]);
      fb += 128;
      gb -= 128;
    }
  }
  __builtin_amdgcn_sched_barrier(0);
  STAMP(0, 8);
  bar();
  {
    LDS char* ex = smem + S_X + 49152 + w * 4096 + lane * 16;
#pragma unroll
    for (int i = 0; i < 4; ++i) {
      const f32x4 lo = acc[i], hi = acc[4 + i];
      *(LDS f32x4*)(ex + i * 1024) = hh ? +lo : +hi;
    }
    if (tid == 0) *(LDS u32*)(smem + S_CNT) = 0;
  }
  bar();
  u32x2 yo[4];
  {
    LDS const char* ey = smem + S_X + 49152 + (w ^ 1) * 4096 + lane * 16;
#pragma unroll
    for (int i = 0; i < 4; ++i) {
      const f32x4 lo = acc[i], hi = acc[4 + i];
      const f32x4 y = (hh ? +hi : +lo) + *(LDS const f32x4*)(ey + i * 1024);
      yo[i] = u32x2{pack_bf16(y[0], y[1]), pack_bf16(y[2], y[3])};
    }
  }
  bar();
  {
    const __amdgpu_buffer_rsrc_t rs = mk_rsrc(p.ws + WS_AT, DM * SL * 2);
    const int dst = (d * SL + 1024 * hh + 16 * (lane & 15) + 4 * (lane >> 4)) * 2;
#pragma unroll
    for (int i = 0; i < 4; ++i) __builtin_amdgcn_raw_buffer_store_b64(yo[i], rs, dst + 512 * i, 0, SC1);
  }
  STAMP(0, 9);
}
DEV void s_signal(const P& p, LDS char* smem, int sg, int lane) {
  u32 old = 0;
  if (lane == 0) old = __hip_atomic_fetch_add((LDS u32*)(smem + S_CNT), 1u, __ATOMIC_RELAXED, __HIP_MEMORY_SCOPE_WORKGROUP);
  if (__builtin_amdgcn_readfirstlane(old) == 11) {
    const int lt = lane & 31, second = lane >> 5;
    const int dd = 6 * sg + (second ? 5 : 0);
    const int idx = dd < 480 ? dd / 96 : 5 + (dd - 480) / 32;
    const int dfirst = idx < 5 ? 96 * idx : 480 + 32 * (idx - 5);
    __hip_atomic_store((GLB u64*)(p.ws + WS_IN) + tile_block(lt, idx) * 64 + IN_A + (sg - dfirst / 6), FLAG_SET, RLX_AGENT);
  }
}

DEV void role_gate(const P& p, int rb, int w, int lane, u64 pc) {
  u32 pf0 = 0, pf1 = 0;
  const int tid = w * 64 + lane;
  const int sub = lane & 7;
  u32x4 wv[9];
#pragma unroll
  for (int i = 0; i < 9; ++i) wv[i] = *(const u32x4*)((const char*)p.gw + (i * 8 + sub) * 16);
  for (int r = w * 8 + (lane >> 3); r < 128; r += 96) {
    const int l = rb * 128 + r;
    float s = 0.f;
    u32x4 xv[9];
#pragma unroll
    for (int i = 0; i < 9; ++i) xv[i] = *(const u32x4*)((const char*)p.x + (size_t)l * 1152 + (i * 8 + sub) * 16);
#pragma unroll
    for (int i = 0; i < 9; ++i) {
#pragma unroll
      for (int k = 0; k < 4; ++k) {
        const u32 pr = pack_bf16(bflo(xv[i][k]) * bflo(wv[i][k]), bfhi(xv[i][k]) * bfhi(wv[i][k]));
        s += bflo(pr);
        s += bfhi(pr);
      }
    }
    s += __shfl_xor(s, 1); s += __shfl_xor(s, 2); s += __shfl_xor(s, 4);
    const float gv = rbf(1.0f / (1.0f + expf(-rbf(s))));
    if (sub == 0)
      __hip_atomic_store((GLB u64*)(p.ws + WS_G) + l, ((u64)__float_as_uint(rbf(1.0f - gv)) << 32) | __float_as_uint(gv), RLX_AGENT);
  }
  asm volatile("" :: "v"(pf0), "v"(pf1));
  wait_vm<0>();
  bar();
  if (w == 0 && lane < 16)
    __hip_atomic_store((GLB u64*)(p.ws + WS_IN) + tile_block(2 * rb + (lane >> 3), lane & 7) * 64 + IN_G, FLAG_SET, RLX_AGENT);
}

constexpr int H_C = 32768, H_CS = 12288;
DEV int h_slot(int i) { return i < 4 ? i * 8192 : 131072 + (i - 4) * 8192; }
constexpr int H_AT = H_C + 2 * H_CS;
constexpr int H_XT = H_C + 3 * H_CS;
constexpr int H_G = H_C + 4 * H_CS;
constexpr int H_OUT = H_C + 5 * H_CS;
constexpr int K_LDS = 163840;

constexpr int h_wait(int s, int NDT) {
  int n = 0;
  int need = 0;
  for (int k = 0; k < 4; ++k) { n += 2; if (k == s) need = n; }
  for (int t = 0; t < s; ++t) {
    if (t == 0) { n += 2; if (s == 4) need = n; n += 2; if (s == 5) need = n; }
    else if (t == 1) { n += 2; if (s == 6) need = n; n += 2; if (s == 7) need = n; }
    else if (t <= 11) { n += 2 + NDT; if (s == t + 6) need = n; if (t == 2) n += 1; }
    else if (t == 12 || t == 13) n += NDT;
    else if (t == 14) n += 1;
  }
  return s == 17 ? 0 : n - need;
}
template <int S, int NDT, class F> DEV void h_steps(F&& after, u64& fv, char* wsp) {
  if constexpr (S == 9) asm volatile("s_waitcnt vmcnt(%1)" : "+v"(fv) : "n"(h_wait(S, NDT)) : "memory");
  else wait_vm<h_wait(S, NDT)>();
  LSTAMP2(1, 16 + S);
  bar();
  after(std::integral_constant<int, S>{});
  if constexpr (S < 17) h_steps<S + 1, NDT>(after, fv, wsp);
}
DEV int p2_rot(int d0) { const int idx = d0 < 480 ? d0 / 96 : 5 + (d0 - 480) / 32; return (idx * 18) >> 3; }

template <int NDT, class F>
DEV void p2_loader(const P& p, LDS char* smem, int lt, int d0, int lw, int lane, bool scratch_barrier, F&& pre_poll) {
  __builtin_amdgcn_s_setprio(3);
  const char* hb = p.ws + WS_HB + (size_t)lt * 18 * 8192 + (2 * lw) * 1024 + lane * 16;
  constexpr int W = 32 * NDT;
  const int l0 = 64 * lt, rot = p2_rot(d0);
  u32 co[NDT];
#pragma unroll
  for (int i = 0; i < NDT; ++i) {
    const int row = 8 * (NDT * lw + i) + (lane >> 3), cp = lane & 7;
    co[i] = (u32)(d0 + row) * 1152u + ((cp ^ (row & 7)) << 4);
  }
  auto issue_c = [&](int s) {
    LDS char* buf = smem + H_C + (s & 7) * H_CS + NDT * lw * 1024;
    int ks = s + rot; ks = ks >= 18 ? ks - 18 : ks;
    const char* cb = ks < 9 ? (const char*)p.cre + ks * 128 : (const char*)p.cim + (ks - 9) * 128;
#pragma unroll
    for (int i = 0; i < NDT; ++i) dma16<0>(cb + co[i], buf + i * 1024);
  };
  auto issue_h = [&](int s) {
    LDS char* buf = smem + h_slot(s & 7) + 2 * lw * 1024;
    int ks = s + rot; ks = ks >= 18 ? ks - 18 : ks;
#pragma unroll
    for (int i = 0; i < 2; ++i) dma16<SC1>(hb + ks * 8192 + i * 1024, buf + i * 1024);
  };
  GLB u64* inb = (GLB u64*)(p.ws + WS_IN) + blockIdx.x * 64;
  const int nA = (d0 + W - 1) / 6 - d0 / 6 + 1;
  const bool need = lane == IN_G || (lane >= IN_A && lane < IN_A + nA);
  if (!scratch_barrier) {
#pragma unroll 1
    for (int s = 0; s < 8; ++s) issue_c(s);
  }
  pre_poll();
  { [[maybe_unused]] const int w = lw + 8; STAMP(1, 1); }
  if (scratch_barrier) bar();
  wait_flags(inb + IN_H + (lane < 36 ? lane : 0), true);
  { [[maybe_unused]] const int w = lw + 8; STAMP(1, 2); }
#pragma unroll
  for (int s = 0; s < 4; ++s) issue_h(s);
  { [[maybe_unused]] const int w = lw + 8; STAMP(1, 3); }
  u64 fv = 0;
  h_steps<0, NDT>([&](auto S_) {
    constexpr int S = decltype(S_)::value;
    if constexpr (S == 0) { issue_h(4); issue_h(5); }
    if constexpr (S == 1) { issue_h(6); issue_h(7); }
    if constexpr (S >= 2 && S <= 11) { issue_h(S + 6); issue_c(S + 6); }
    if constexpr (S == 2)
      asm volatile("global_load_dwordx2 %0, %1, off sc1" : "=v"(fv) : "v"(inb + lane) : "memory");
    if constexpr (S == 9) { if (!__all(!need || fv == FLAG_SET)) wait_flags(inb + lane, need); }
    if constexpr (S == 12) {
#pragma unroll
      for (int i = 0; i < NDT; ++i) {
        const int pc = NDT * lw + i;
        dma16<SC1>(p.ws + WS_AT + (size_t)(d0 + 8 * pc + (lane >> 3)) * 4096 + l0 * 2 + (lane & 7) * 16, smem + H_AT + pc * 1024);
      }
    }
    if constexpr (S == 13) {
#pragma unroll
      for (int i = 0; i < NDT; ++i) {
        const int pc = NDT * lw + i, id = pc * 64 + lane, row = id / (4 * NDT), qq = id - row * (4 * NDT);
        dma16<0>((const char*)p.x + (size_t)(l0 + row) * 1152 + d0 * 2 + qq * 16, smem + H_XT + pc * 1024);
      }
    }
    if constexpr (S == 14) { if (lane < 8) dma16<SC1>(p.ws + WS_G + (size_t)l0 * 8 + lw * 128 + lane * 16, smem + H_G + lw * 128); }
  }, fv, p.ws);
}

template <int NDT, int STAG>
DEV void p2_compute(const P& p, LDS char* smem, int lt0, int d0, int w, int lane, const u32x2 (&dsk)[6]) {
  const int lh = (w >> 1) & 1, kh = w & 1, dg = w >> 2;
  const int r = lane & 15, kq = lane >> 4;
  const int lt = 2 * lh + kh, l0 = 64 * lt0;
  constexpr int W = 32 * NDT;
  u32x2 da[NDT], db[NDT];
#pragma unroll
  for (int t = 0; t < NDT; ++t) {
    const int d = d0 + 16 * (NDT * dg + t) + 4 * kq;
    da[t] = *(const u32x2*)(p.dskA + d);
    db[t] = *(const u32x2*)(p.dskB + d);
  }
  GLB u64* inb = (GLB u64*)(p.ws + WS_IN) + blockIdx.x * 64;

  f32x4 acc[2][NDT];
#pragma unroll
  for (int i = 0; i < 2; ++i)
#pragma unroll
    for (int t = 0; t < NDT; ++t) acc[i][t] = f32x4{0, 0, 0, 0};
  int ho[2];
#pragma unroll
  for (int u = 0; u < 2; ++u) {
    const int k = 32 * kh + 8 * kq + 4 * u + (r >> 2), i = (r & 3) >> 1;
    ho[u] = (2 * (k >> 4) + i) * 1024 + (32 * lh + ((k & 15) ^ (4 * i))) * 16 + (r & 1) * 8;
  }
  const int co = H_C + (16 * NDT * dg + r) * 128 + (((4 * kh + kq) ^ (r & 7)) << 4);
  constexpr int NREG = 0, NRING = 18 - NREG;
  bf16x8 cr[NREG ? NREG : 1][NDT];
  if constexpr (NREG > 0) {
    const int rot = p2_rot(d0);
#pragma unroll
    for (int i = 0; i < NREG; ++i) {
      int ks = NRING + i + rot; ks = ks >= 18 ? ks - 18 : ks;
      const char* cb = (ks < 9 ? (const char*)p.cre + ks * 128 : (const char*)p.cim + (ks - 9) * 128) +
                       (size_t)(d0 + 16 * NDT * dg + r) * 1152 + (4 * kh + kq) * 16;
#pragma unroll
      for (int t = 0; t < NDT; ++t) cr[i][t] = *(const GLB bf16x8*)(cb + t * 16 * 1152);
    }
  }
  bf16x8 hb[2], ca[NDT];
#pragma unroll
  for (int i = 0; i < 2; ++i) hb[i] = bf16x8{};
#pragma unroll
  for (int t = 0; t < NDT; ++t) ca[t] = bf16x8{};
  auto mma_with = [&](const bf16x8 (&cc)[NDT]) {
#pragma unroll
    for (int t = 0; t < NDT; ++t) {
      acc[0][t] = mfma16(cc[t], hb[0], acc[0][t]);
      acc[1][t] = mfma16(cc[t], hb[1], acc[1][t]);
    }
    __builtin_amdgcn_sched_barrier(0);
  };
  auto read_h = [&](int s) {
    LDS const char* hbuf = smem + h_slot(s & 7);
#pragma unroll
    for (int i = 0; i < 2; ++i) hb[i] = tr_frag(hbuf + ho[0] + i * 256, hbuf + ho[1] + i * 256);
  };
  STAMP(1, 1);
#pragma unroll 1
  for (int s = 0; s < NRING; ++s) {
    bar();
    if (s == 0) STAMP(1, 2);
    LSTAMP(1, 16 + s);
    if (STAG) mma_with(ca);
    read_h(s);
    LDS const char* cbuf = smem + (s & 7) * H_CS;
#pragma unroll
    for (int t = 0; t < NDT; ++t) ca[t] = lds_frag(cbuf + co + t * 2048);
    __builtin_amdgcn_sched_barrier(0);
    if (!STAG) mma_with(ca);
  }
#pragma unroll
  for (int i = 0; i < NREG; ++i) {
    bar();
    if (STAG) { if (i == 0) mma_with(ca); else mma_with(cr[i ? i - 1 : 0]); }
    read_h(NRING + i);
    __builtin_amdgcn_sched_barrier(0);
    if (!STAG) mma_with(cr[i]);
  }
  if (STAG) { if (NREG) mma_with(cr[NREG ? NREG - 1 : 0]); else mma_with(ca); }
  __builtin_amdgcn_sched_barrier(0);
  STAMP(1, 3);
  bar();
  if (w == 0) __hip_atomic_store(inb + lane, 0ull, RLX_AGENT);
  LDS char* ex = smem + w * (NDT * 1024) + lane * 16;
#pragma unroll
  for (int t = 0; t < NDT; ++t) {
    const f32x4 a0 = acc[0][t], a1 = acc[1][t];
    *(LDS f32x4*)(ex + t * 1024) = kh ? +a0 : +a1;
  }
  bar();
  LDS const char* ey = smem + (w ^ 1) * (NDT * 1024) + lane * 16;
  constexpr int OSTR = W * 4 + 16;
  const f32x2 gg = *(LDS const f32x2*)(smem + H_G + (16 * lt + r) * 8);
#pragma unroll
  for (int t = 0; t < NDT; ++t) {
    const int dt = NDT * dg + t;
    const f32x4 o = *(LDS const f32x4*)(ey + t * 1024);
    const f32x4 a0 = acc[0][t], a1 = acc[1][t];
    const f32x4 y = (kh ? +a1 : +a0) + o;
    const s16x4 av = tr_read(smem + H_AT + (16 * dt + 4 * kq + (r >> 2)) * 128 + lt * 32 + (r & 3) * 8);
    const u32x2 xv = *(LDS const u32x2*)(smem + H_XT + (16 * lt + r) * (W * 2) + (16 * dt + 4 * kq) * 2);
    f32x4 res;
#pragma unroll
    for (int i = 0; i < 4; ++i) {
      const u32 xw = xv[i >> 1], aw = da[t][i >> 1], bw = db[t][i >> 1];
      const float xf = (i & 1) ? bfhi(xw) : bflo(xw);
      const float sa = (i & 1) ? bfhi(aw) : bflo(aw);
      const float sb = (i & 1) ? bfhi(bw) : bflo(bw);
      const float yc = bf2f((u16)av[i]);
      const float a = rbf(yc + rbf(xf * sa));
      const float b = y[i] + rbf(xf * sb);
      res[i] = rbf(gg[0] * a) + gg[1] * b;
    }
    *(LDS f32x4*)(smem + H_OUT + (16 * lt + r) * OSTR + (16 * dt + 4 * kq) * 4) = res;
  }
  STAMP(1, 4);
  bar();
#pragma unroll
  for (int it = 0; it < NDT; ++it) {
    const int id = it * 512 + w * 64 + lane;
    const int row = id / (8 * NDT), pc = id - row * (8 * NDT);
    const f32x4 v = *(LDS const f32x4*)(smem + H_OUT + row * OSTR + pc * 16);
    __builtin_amdgcn_raw_buffer_store_b128(__builtin_bit_cast(u32x4, v), mk_rsrc(p.out, SL * DM * 4), ((l0 + row) * DM + d0 + pc * 4) * 4, 0, OUT_AUX);
  }
}

template <int NDT>
DEV void phase2(const P& p, LDS char* smem, int lt, int d0, int w, int lane, bool scratch_barrier, int sg, const u32x2 (&dsk)[6]) {
  STAMP(1, 0);
  if (w >= 8) {
    p2_loader<NDT>(p, smem, lt, d0, w - 8, lane, scratch_barrier, [&]() { if (NDT == 1) { wait_vm<8>(); s_signal(p, smem, sg, lane); } });
  } else {
    if (NDT == 1) { wait_vm<0>(); s_signal(p, smem, sg, lane); }
    if (w < 4) p2_compute<NDT, 0>(p, smem, lt, d0, w, lane, dsk);
    else p2_compute<NDT, 1>(p, smem, lt, d0, w, lane, dsk);
  }
  STAMP(1, 15);
}

__global__ __launch_bounds__(768, 3) void gated_mix_s4d_s5(P p) {
  extern __shared__ __attribute__((aligned(16))) char smem_raw[];
  LDS char* smem = (LDS char*)smem_raw;
  u64 pc;
  asm volatile("s_getpc_b64 %0" : "=s"(pc));
  u32 pf = 0;
#define PF_TOP 312
  if (threadIdx.x < PF_TOP)
    asm volatile("global_load_dword %0, %1, off" : "=v"(pf) : "v"((pc & ~255ull) + threadIdx.x * 128));
  const int w = __builtin_amdgcn_readfirstlane(threadIdx.x >> 6), lane = threadIdx.x & 63;
  const int xcd = blockIdx.x & 7, j = blockIdx.x >> 3;
  u32x2 dsk[6];
#pragma unroll
  for (int i = 0; i < 6; ++i) dsk[i] = u32x2{0, 0};
  STAMP(0, 0);
  if (j >= 18 && j < 30) {
    const int sg = 12 * xcd + (j - 18);
    role_s(p, smem, sg, w, lane);
    STAMP(0, 15);
    phase2<1>(p, smem, 4 * xcd + (j - 18) / 3, 480 + 32 * ((j - 18) % 3), w, lane, false, sg, dsk);
  } else {
    const bool isg = j < 18;
    if (isg) {
      const int c = 2 * xcd + j / 9, g = j % 9;
      if (w >= 8) g_loader(p, smem, c, g, 96 * (j % 5), w - 8, lane);
      else if (w < 4) g_compute<0>(p, smem, c, g, w, lane, pf);
      else g_compute<1>(p, smem, c, g, w, lane, pf);
    } else {
      role_gate(p, 2 * xcd + (j - 30), w, lane, pc);
    }
    STAMP(0, 15);
    const int e = isg ? j : j - 12;
    phase2<3>(p, smem, 4 * xcd + e / 5, 96 * (e % 5), w, lane, isg, 0, dsk);
  }
  asm volatile("s_waitcnt vmcnt(0)" :: "v"(pf));
}

extern "C" void kernel_launch(void* const* d_in, const int* in_sizes, int n_in, void* d_out, int out_size,
                              void* d_ws, size_t ws_size, hipStream_t stream) {
  P a;
  a.x = (const u16*)d_in[0]; a.la = (const u16*)d_in[1]; a.aim = (const u16*)d_in[2];
  a.creA = (const u16*)d_in[3]; a.cimA = (const u16*)d_in[4]; a.ldtA = (const u16*)d_in[5]; a.dskA = (const u16*)d_in[6];
  a.lre = (const u16*)d_in[8]; a.lim = (const u16*)d_in[9]; a.bre = (const u16*)d_in[10]; a.bim = (const u16*)d_in[11];
  a.cre = (const u16*)d_in[12]; a.cim = (const u16*)d_in[13]; a.ldtB = (const u16*)d_in[14];
  a.dskB = (const u16*)d_in[15]; a.gw = (const u16*)d_in[16];
  a.ws = (char*)d_ws + WS_OFF; a.out = (float*)d_out;
  (void)hipFuncSetAttribute((const void*)gated_mix_s4d_s5, hipFuncAttributeMaxDynamicSharedMemorySize, K_LDS);
  gated_mix_s4d_s5<<<256, 768, K_LDS, stream>>>(a);
}
```

```cpp
#define PF_NOT_G 1
#include <hip/hip_runtime.h>
#include <hip/hip_bf16.h>
#include <stdint.h>
#include <type_traits>

typedef unsigned short u16;
typedef unsigned int u32;
typedef unsigned long long u64;
typedef __attribute__((ext_vector_type(8))) __bf16 bf16x8;
typedef __attribute__((ext_vector_type(2))) __bf16 bf16x2;
typedef __attribute__((ext_vector_type(4))) short s16x4;
typedef __attribute__((ext_vector_type(4))) float f32x4;
typedef __attribute__((ext_vector_type(2))) float f32x2;
typedef __attribute__((ext_vector_type(4))) unsigned u32x4;
typedef __attribute__((ext_vector_type(2))) unsigned u32x2;
#define LDS __attribute__((address_space(3)))
#define GLB __attribute__((address_space(1)))
#define DEV __device__ __forceinline__
#define RLX_AGENT __ATOMIC_RELAXED, __HIP_MEMORY_SCOPE_AGENT

constexpr int SL = 2048;
constexpr int DM = 576;

constexpr size_t WS_HB = 0;
constexpr size_t WS_AT = WS_HB + (size_t)2 * DM * SL * 2;
constexpr size_t WS_G = WS_AT + (size_t)DM * SL * 2;
constexpr size_t WS_GRAN = WS_G + (size_t)SL * 8;
constexpr size_t WS_EP = WS_GRAN + (size_t)9 * 16 * 2 * 64 * 8;
constexpr size_t WS_FL = WS_EP + 4096;
constexpr size_t WS_XT = WS_FL + (size_t)16 * DM * 8;
constexpr size_t WS_IN = WS_XT + (size_t)DM * SL * 2;
constexpr u64 FLAG_SET = 0x9E3779B97F4A7C15ull;
constexpr u32 SPIN_CAP = 1u << 17;
constexpr int IN_H = 0, IN_G = 36, IN_A = 40;

constexpr size_t WS_ST = (size_t)16 << 20;
#define STAMP(kern, idx) do {} while (0)
#define CSTAMP(kern, idx) do {} while (0)
#define LSTAMP(kern, idx) do {} while (0)
#define LSTAMP2(kern, idx) do {} while (0)

DEV float bf2f(u16 v) { return __uint_as_float((u32)v << 16); }
DEV float bflo(u32 v) { return __uint_as_float(v << 16); }
DEV float bfhi(u32 v) { return __uint_as_float(v & 0xffff0000u); }
DEV u32 pack_bf16(float lo, float hi) {
  f32x2 v = {lo, hi};
  bf16x2 b = __builtin_convertvector(v, bf16x2);
  return __builtin_bit_cast(u32, b);
}
DEV float rbf(float x) { return bflo(pack_bf16(x, x)); }
template <int N> DEV void wait_vm() { asm volatile("s_waitcnt vmcnt(%0)" ::"n"(N) : "memory"); }
DEV void bar() { asm volatile("s_waitcnt lgkmcnt(0)\n\ts_barrier" ::: "memory"); }
DEV void wave_fence() { __builtin_amdgcn_fence(__ATOMIC_ACQ_REL, "wavefront"); }
template <int AUX> DEV void dma16(const void* src, LDS char* dst) {
  __builtin_amdgcn_global_load_lds((const GLB u32*)src, (LDS u32*)dst, 16, 0, AUX);
}
constexpr int SC1 = 16;
#define GPOLL_SLEEP 1
#define GPOLL_DELAY 24
#define POLL_SLEEP 1
#define OUT_AUX 16
#define WS_OFF 0
#define XT_WAIT 4
DEV bf16x8 lds_frag(LDS const char* p) { return *(LDS const bf16x8*)p; }
DEV s16x4 tr_read(LDS const char* a) { return __builtin_amdgcn_ds_read_tr16_b64_v4i16((LDS s16x4*)a); }
DEV bf16x8 tr_frag(LDS const char* a, LDS const char* b) {
  const s16x4 lo = tr_read(a), hi = tr_read(b);
  return __builtin_bit_cast(bf16x8, __builtin_shufflevector(lo, hi, 0, 1, 2, 3, 4, 5, 6, 7));
}
DEV f32x4 mfma16(bf16x8 a, bf16x8 b, f32x4 c) { return __builtin_amdgcn_mfma_f32_16x16x32_bf16(a, b, c, 0, 0, 0); }
DEV __amdgpu_buffer_rsrc_t mk_rsrc(const void* base, int bytes) {
  return __builtin_amdgcn_make_buffer_rsrc(const_cast<void*>(base), (short)0, bytes, 0x00020000);
}
struct cf { float r, i; };
DEV cf cmul(cf a, cf b) { return {a.r * b.r - a.i * b.i, a.r * b.i + a.i * b.r}; }
DEV cf cfma(cf a, cf b, cf c) { return {fmaf(a.r, b.r, fmaf(-a.i, b.i, c.r)), fmaf(a.r, b.i, fmaf(a.i, b.r, c.i))}; }
typedef f32x2 c2;
DEV c2 cm2(c2 a, c2 b) {
  c2 t, r;
  asm("v_pk_mul_f32 %0, %1, %2 op_sel:[0,0] op_sel_hi:[0,1]" : "=v"(t) : "v"(a), "v"(b));
  asm("v_pk_fma_f32 %0, %1, %2, %3 op_sel:[1,1,0] op_sel_hi:[1,0,1] neg_lo:[1,0,0]" : "=v"(r) : "v"(a), "v"(b), "v"(t));
  return r;
}
DEV f32x2 fma2(f32x2 a, f32x2 b, f32x2 c) { return __builtin_elementwise_fma(a, b, c); }
DEV f32x2 lo2(f32x4 v) { return __builtin_shufflevector(v, v, 0, 1); }
DEV f32x2 hi2(f32x4 v) { return __builtin_shufflevector(v, v, 2, 3); }
DEV f32x4 cat2(f32x2 a, f32x2 b) { return __builtin_shufflevector(a, b, 0, 1, 2, 3); }
DEV void wait_flags(GLB u64* word, bool need) {
  for (u32 spin = 0;; ++spin) {
    const u64 v = __hip_atomic_load(word, RLX_AGENT);
    if (__all(!need || v == FLAG_SET) || spin > SPIN_CAP) break;
    __builtin_amdgcn_s_sleep(POLL_SLEEP);
  }
}
DEV int tile_block(int lt, int idx) {
  const int xcd = lt >> 2, a = lt & 3;
  int j;
  if (idx < 5) { const int e = 5 * a + idx; j = e < 18 ? e : e + 12; }
  else j = 18 + 3 * a + (idx - 5);
  return (j << 3) | xcd;
}

struct P {
  const u16 *x, *la, *aim, *creA, *cimA, *ldtA, *dskA;
  const u16 *lre, *lim, *bre, *bim, *cre, *cim, *ldtB, *dskB, *gw;
  char* ws;
  float* out;
};

constexpr int G_NB = 4, G_STAGE = 32768;
constexpr int G_E = 153536;
constexpr int G_PF = G_E + 4096;
constexpr int G_L = G_PF + 4096;
constexpr int G_X = G_L + 1024;
constexpr int G_GO = G_X + 1024;
constexpr int G_T = G_NB * G_STAGE;
static_assert(G_GO + 4 <= 163840 && G_T + 4 * 32 * 144 <= G_E, "role G scratch");

DEV void g_loader(const P& p, LDS char* smem, int c, int g, int d0, int lw, int lane) {
  [[maybe_unused]] const int w = lw + 8;
  __builtin_amdgcn_s_setprio(3);
  GLB u32* ep = (GLB u32*)(p.ws + WS_EP + g * 256);
  GLB u64* gran = (GLB u64*)(p.ws + WS_GRAN) + (size_t)g * 16 * 128;
  const u32 tag = __hip_atomic_load(ep, RLX_AGENT) + 1u;
  u32 xo[4], bo[4];
#pragma unroll
  for (int i = 0; i < 4; ++i) {
    const int rho = 8 * (4 * lw + i) + (lane >> 3);
    const int hf = rho >> 6, m = (rho >> 4) & 3, r = rho & 15;
    const int tau = 64 * hf + 16 * (r >> 2) + 8 * (r & 1) + 2 * m + ((r >> 1) & 1);
    const int ch = (lane & 7) ^ (rho & 7);
    xo[i] = (u32)(128 * c + tau) * 1152u + ch * 16;
    const int pp = 64 * g + 16 * (rho >> 5) + r;
    bo[i] = (u32)pp * 1152u + ch * 16;
  }
  auto issue = [&](int s) {
    LDS char* buf = smem + (s & (G_NB - 1)) * G_STAGE + lw * 4096;
    int kb = g + s; kb = (kb >= 9 ? kb - 9 : kb) * 128;
#pragma unroll
    for (int i = 0; i < 4; ++i) dma16<0>((const char*)p.x + (xo[i] + kb), buf + i * 1024);
#pragma unroll
    for (int i = 0; i < 4; ++i) {
      const char* base = (const char*)((((4 * lw + i) >> 1) & 1) ? p.bim : p.bre);
      dma16<0>(base + (bo[i] + kb), buf + 16384 + i * 1024);
    }
  };
#pragma unroll
  for (int s = 0; s < G_NB - 1; ++s) issue(s);
#pragma unroll 1
  for (int s = 0; s < 6; ++s) { wait_vm<16>(); LSTAMP(0, 16 + s); bar(); issue(s + G_NB - 1); LSTAMP(0, 32 + s); }
  wait_vm<16>(); bar();
  wait_vm<8>(); bar();
  wait_vm<0>(); bar();
  STAMP(0, 1);
  if (lw < 2) {
    bar();
    u32 co[6];
#pragma unroll
    for (int i = 0; i < 6; ++i) {
      const int row = 8 * (6 * lw + i) + (lane >> 3), cp = lane & 7;
      co[i] = (u32)(d0 + row) * 1152u + ((cp ^ (row & 7)) << 4);
    }
    const int rot = ((d0 / 96) * 18) >> 3;
#pragma unroll 1
    for (int s = 0; s < 8; ++s) {
      LDS char* buf = smem + 32768 + s * 12288 + 6 * lw * 1024;
      const int ks = s + rot;
      const char* cb = ks < 9 ? (const char*)p.cre + ks * 128 : (const char*)p.cim + (ks - 9) * 128;
#pragma unroll
      for (int i = 0; i < 6; ++i) dma16<0>(cb + co[i], buf + i * 1024);
    }
    bar();
    return;
  }
  bar();
  STAMP(0, 2);
  if (lw == 3) {
    const f32x2 lv = *(LDS const f32x2*)(smem + G_L + lane * 8);
    const cf l16 = {lv[0], lv[1]};
    f32x2 ev[8];
#pragma unroll
    for (int s = 0; s < 8; ++s) ev[s] = *(LDS const f32x2*)(smem + G_E + (s * 64 + lane) * 8);
    cf tot = {0.f, 0.f};
    cf pre[8];
#pragma unroll
    for (int s = 0; s < 8; ++s) { pre[s] = tot; tot = cfma(l16, tot, cf{ev[s][0], ev[s][1]}); }
    if (c < 15) {
      __hip_atomic_store(gran + c * 128 + lane, ((u64)tag << 32) | __float_as_uint(tot.r), RLX_AGENT);
      __hip_atomic_store(gran + c * 128 + 64 + lane, ((u64)tag << 32) | __float_as_uint(tot.i), RLX_AGENT);
    }
#pragma unroll
    for (int s = 0; s < 8; ++s) *(LDS f32x2*)(smem + G_PF + (s * 64 + lane) * 8) = f32x2{pre[s].r, pre[s].i};
  }
  const int par = lw - 2;
  const f32x2 lv = *(LDS const f32x2*)(smem + G_L + 512 + lane * 8);
  const cf l128 = {lv[0], lv[1]};
  const cf l256 = cmul(l128, l128);
  cf acc = {0.f, 0.f};
  if (par < c) {
    const int nc = (c - par + 1) >> 1;
    u64 a[8], b[8];
    __builtin_amdgcn_s_sleep(GPOLL_DELAY);
    for (u32 spin = 0;; ++spin) {
      bool ok = true;
#pragma unroll
      for (int i = 0; i < 8; ++i) {
        const int cc = par + 2 * (i < nc ? i : 0);
        a[i] = __hip_atomic_load(gran + cc * 128 + lane, RLX_AGENT);
        b[i] = __hip_atomic_load(gran + cc * 128 + 64 + lane, RLX_AGENT);
      }
#pragma unroll
      for (int i = 0; i < 8; ++i) ok = ok && (u32)(a[i] >> 32) == tag && (u32)(b[i] >> 32) == tag;
      if (__all(ok) || spin > SPIN_CAP) break;
      __builtin_amdgcn_s_sleep(GPOLL_SLEEP);
    }
#pragma unroll
    for (int i = 0; i < 8; ++i)
      if (i < nc) acc = cfma(l256, acc, cf{__uint_as_float((u32)a[i]), __uint_as_float((u32)b[i])});
    if (c - 1 - (par + 2 * (nc - 1))) acc = cmul(acc, l128);
  }
  *(LDS f32x2*)(smem + G_X + (par * 64 + lane) * 8) = f32x2{acc.r, acc.i};
  STAMP(0, 3);
  bar();
  if (c == 15 && lw == 2 && lane == 0) __hip_atomic_store(ep, tag, RLX_AGENT);
}

template <int HF>
DEV void g_compute(const P& p, LDS char* smem, int c, int g, int w, int lane, u32& pf) {
  const int pq = w & 3, col = lane & 15, q = lane >> 4;
  const int pl = 16 * pq + col, pp = 64 * g + pl;
  cf lam, coef2;
  {
    const float lre = bf2f(p.lre[pp]), lim = bf2f(p.lim[pp]);
    const float dt = rbf(expf(bf2f(p.ldtB[pp])));
    const float zr = dt * lre, zi = dt * lim;
    const float er = expf(zr);
    float sn, cs;
    sincosf(zi, &sn, &cs);
    lam = {er * cs, er * sn};
    const float nr = lam.r - 1.0f, ni = lam.i;
    const float den = 1.0f / (lre * lre + lim * lim);
    coef2 = {2.0f * (nr * lre + ni * lim) * den, 2.0f * (ni * lre - nr * lim) * den};
  }
  cf l8 = cmul(lam, lam); l8 = cmul(l8, l8); l8 = cmul(l8, l8);
  const cf l16 = cmul(l8, l8);
  const cf l32 = cmul(l16, l16), l64 = cmul(l32, l32);
  if (HF == 0 && q == 0) {
    const cf l128 = cmul(l64, l64);
    *(LDS f32x2*)(smem + G_L + pl * 8) = f32x2{l16.r, l16.i};
    *(LDS f32x2*)(smem + G_L + 512 + pl * 8) = f32x2{l128.r, l128.i};
  }
  if (HF == 0 && w == 0 && lane == 0) *(LDS u32*)(smem + G_GO) = 0;
  cf pw = HF ? l64 : cf{1.f, 0.f};
  { const cf t = cmul(pw, l16); if (q & 1) pw = t; }
  { const cf t = cmul(pw, l32); if (q & 2) pw = t; }
  asm volatile("" : "+v"(lam.r), "+v"(lam.i), "+v"(coef2.r), "+v"(coef2.i), "+v"(l8.r), "+v"(l8.i), "+v"(pw.r), "+v"(pw.i));
  f32x4 are[4], aim[4];
#pragma unroll
  for (int m = 0; m < 4; ++m) { are[m] = f32x4{0, 0, 0, 0}; aim[m] = f32x4{0, 0, 0, 0}; }
  const int o0 = col * 128 + (((0 + q) ^ (col & 7)) << 4);
  const int o1 = col * 128 + (((4 + q) ^ (col & 7)) << 4);
  bf16x8 br[2], bi[2], a[2][4];
  auto read_frags = [&](int s) {
    LDS const char* buf = smem + (s & (G_NB - 1)) * G_STAGE;
    LDS const char* ab = buf + HF * 8192;
    LDS const char* bb = buf + 16384 + pq * 4096;
#pragma unroll
    for (int ks = 0; ks < 2; ++ks) {
      const int o = ks ? o1 : o0;
      br[ks] = lds_frag(bb + o); bi[ks] = lds_frag(bb + 2048 + o);
#pragma unroll
      for (int m = 0; m < 4; ++m) a[ks][m] = lds_frag(ab + m * 2048 + o);
    }
  };
  auto mma = [&]() {
#pragma unroll
    for (int ks = 0; ks < 2; ++ks)
#pragma unroll
      for (int m = 0; m < 4; ++m) {
        are[m] = mfma16(a[ks][m], br[ks], are[m]);
        aim[m] = mfma16(a[ks][m], bi[ks], aim[m]);
      }
  };
  STAMP(0, 1);
#pragma unroll
  for (int s = 0; s < 9; ++s) {
    if (HF == 1 && s == XT_WAIT) {
      wait_vm<0>();
      if (lane < 16) __hip_atomic_store((GLB u64*)(p.ws + WS_FL) + c * DM + 64 * g + 16 * pq + lane, FLAG_SET, RLX_AGENT);
    }
    bar();
    if (s == 0) { STAMP(0, 2); CSTAMP(0, 60); }
    if (HF == 1 && s > 0) { mma(); __builtin_amdgcn_sched_barrier(0); }
    if (HF == 1 && s == 0) {
      const int rr = 4 * q + (col >> 2);
      LDS const char* tb = smem + rr * 128 + (((2 * pq + ((col & 3) >> 1)) ^ (rr & 7)) << 4) + (col & 1) * 8;
      LDS char* sc = smem + G_T + pq * (32 * 144);
#pragma unroll
      for (int hf = 0; hf < 2; ++hf) {
        u32x2 t[4];
#pragma unroll
        for (int m = 0; m < 4; ++m) t[m] = __builtin_bit_cast(u32x2, tr_read(tb + hf * 8192 + m * 2048));
        u32x4 oa, ob;
#pragma unroll
        for (int m = 0; m < 4; ++m) {
          oa[m] = __builtin_amdgcn_perm(t[m][1], t[m][0], 0x05040100u);
          ob[m] = __builtin_amdgcn_perm(t[m][1], t[m][0], 0x07060302u);
        }
        *(LDS u32x4*)(sc + hf * (16 * 144) + col * 144 + q * 32) = oa;
        *(LDS u32x4*)(sc + hf * (16 * 144) + col * 144 + q * 32 + 16) = ob;
      }
      wave_fence();
      const __amdgpu_buffer_rsrc_t rs = mk_rsrc(p.ws + WS_XT, DM * SL * 2);
#pragma unroll
      for (int u = 0; u < 4; ++u) {
        const int row = (lane >> 3) + 8 * (u & 1), hf = u >> 1;
        const u32x4 v = *(LDS const u32x4*)(sc + hf * (16 * 144) + row * 144 + (lane & 7) * 16);
        __builtin_amdgcn_raw_buffer_store_b128(v, rs, ((64 * g + 16 * pq + row) * SL + 128 * c + 64 * hf) * 2 + (lane & 7) * 16, 0, SC1);
      }
      __builtin_amdgcn_sched_barrier(0);
    }
    read_frags(s);
    __builtin_amdgcn_sched_barrier(0);
    if (HF == 0) { mma(); __builtin_amdgcn_sched_barrier(0); }
  }
  if (HF == 1) mma();
  __builtin_amdgcn_sched_barrier(0);
  STAMP(0, 3); CSTAMP(0, 61);
  const f32x2 Lr = {lam.r, lam.r}, Li = {lam.i, lam.i};
  f32x2 er = {0.f, 0.f}, ei = {0.f, 0.f};
#pragma unroll
  for (int t = 0; t < 8; ++t) {
    const f32x2 br_ = (t & 1) ? hi2(are[t >> 1]) : lo2(are[t >> 1]);
    const f32x2 bi_ = (t & 1) ? hi2(aim[t >> 1]) : lo2(aim[t >> 1]);
    const f32x2 nr = fma2(-Li, ei, fma2(Lr, er, br_));
    ei = fma2(Li, er, fma2(Lr, ei, bi_));
    er = nr;
  }
  const cf eA = {er[0], ei[0]};
  const cf e = cfma(l8, eA, cf{er[1], ei[1]});
  const int seg = 4 * HF + q;
  *(LDS f32x2*)(smem + G_E + (seg * 64 + pl) * 8) = f32x2{e.r, e.i};
  STAMP(0, 4);
  bar();
  STAMP(0, 5);
  {
#pragma unroll
    for (int m = 0; m < 4; ++m) asm volatile("" : "+v"(are[m]), "+v"(aim[m]));
    const f32x2 Cr = {coef2.r, coef2.r}, Ci = {coef2.i, coef2.i};
#pragma unroll
    for (int m = 0; m < 4; ++m) {
      const f32x2 r0 = lo2(are[m]), r1 = hi2(are[m]), i0 = lo2(aim[m]), i1 = hi2(aim[m]);
      are[m] = cat2(fma2(-Ci, i0, Cr * r0), fma2(-Ci, i1, Cr * r1));
      aim[m] = cat2(fma2(-Ci, r0, -Cr * i0), fma2(-Ci, r1, -Cr * i1));
      asm volatile("" : "+v"(are[m]), "+v"(aim[m]));
    }
  }
  cf eAc = cmul(coef2, eA), pwc = cmul(coef2, pw);
  asm volatile("" : "+v"(eAc.r), "+v"(eAc.i), "+v"(pwc.r), "+v"(pwc.i));
  STAMP(0, 7);
  bar();
  STAMP(0, 8);
  cf cin;
  {
    f32x2 v[2];
#pragma unroll
    for (int i = 0; i < 2; ++i) v[i] = *(LDS const f32x2*)(smem + G_X + (i * 64 + pl) * 8);
    const f32x2 pf = *(LDS const f32x2*)(smem + G_PF + (seg * 64 + pl) * 8);
    const f32x2 S = v[0] + v[1];
    cin = cfma(pwc, cf{S[0], S[1]}, cmul(coef2, cf{pf[0], pf[1]}));
  }
  const cf cinB = cfma(l8, cin, eAc);
  er = f32x2{cin.r, cinB.r}; ei = f32x2{-cin.i, -cinB.i};
  f32x2 hr[8], hi[8];
#pragma unroll
  for (int t = 0; t < 8; ++t) {
    const f32x2 br_ = (t & 1) ? hi2(are[t >> 1]) : lo2(are[t >> 1]);
    const f32x2 bi_ = (t & 1) ? hi2(aim[t >> 1]) : lo2(aim[t >> 1]);
    const f32x2 nr = fma2(Li, ei, fma2(Lr, er, br_));
    ei = fma2(-Li, er, fma2(Lr, ei, bi_));
    er = nr;
    hr[t] = er; hi[t] = ei;
  }
  u32x4 o[4];
#pragma unroll
  for (int t = 0; t < 4; ++t) {
    o[0][t] = pack_bf16(hr[2 * t][0], hr[2 * t + 1][0]);
    o[1][t] = pack_bf16(hr[2 * t][1], hr[2 * t + 1][1]);
    o[2][t] = pack_bf16(hi[2 * t][0], hi[2 * t + 1][0]);
    o[3][t] = pack_bf16(hi[2 * t][1], hi[2 * t + 1][1]);
  }
  STAMP(0, 9);
  {
    const __amdgpu_buffer_rsrc_t rs = mk_rsrc(p.ws + WS_HB, 2 * DM * SL * 2);
    const int hb = (((c * 2 + HF) * 2) * 9 + g) * 8192 + pq * 2048;
    const int p0 = (16 * q + col) * 16, p1 = 1024 + (16 * q + (col ^ 4)) * 16;
    __builtin_amdgcn_raw_buffer_store_b128(o[0], rs, hb + p0, 0, SC1);
    __builtin_amdgcn_raw_buffer_store_b128(o[1], rs, hb + p1, 0, SC1);
    __builtin_amdgcn_raw_buffer_store_b128(o[2], rs, hb + 9 * 8192 + p0, 0, SC1);
    __builtin_amdgcn_raw_buffer_store_b128(o[3], rs, hb + 9 * 8192 + p1, 0, SC1);
    wait_vm<0>();
    STAMP(0, 10);
    if (lane < 8)
      __hip_atomic_store((GLB u64*)(p.ws + WS_IN) + tile_block(2 * c + HF, lane) * 64 + IN_H + g * 4 + pq, FLAG_SET, RLX_AGENT);
    bar();
  }
}

constexpr int S_CPY = 4160;
constexpr int S_X = 0;
constexpr int S_ST = 6 * 4 * S_CPY;
constexpr int S_Z = S_ST;
constexpr int S_ZSZ = (256 + SL) * 2;
constexpr int S_T = S_ST + 12 * 4608;
constexpr int S_TSZ = 1312;
constexpr int S_END = S_T + 6 * S_TSZ;
constexpr int S_CNT = 32768 + 8192;

DEV c2 cexp_k(float zl, float th, float tl, float k) {
  const float mag = __builtin_amdgcn_exp2f(zl * k);
  const float t = fmaf(tl, k, __builtin_amdgcn_fractf(th * k));
  return c2{mag * __builtin_amdgcn_cosf(t), mag * __builtin_amdgcn_sinf(t)};
}
#define PKM(d, a, b) "v_pk_mul_f32 " d ", " a ", " b " op_sel:[0,0] op_sel_hi:[0,1]\n\t"
#define PKF(d, a, b) "v_pk_fma_f32 " d ", " a ", " b ", " d " op_sel:[1,1,0] op_sel_hi:[1,0,1] neg_lo:[1,0,0]\n\t"
DEV void octet(LDS char* dst, c2 s0, c2 m1, c2 m2) {
  c2 v1, v2, v3, t0, t1, t2, t3;
  asm(PKM("%0", "%7", "%8") PKM("%3", "%7", "%9") PKF("%0", "%7", "%8") PKF("%3", "%7", "%9")
      PKM("%1", "%0", "%8") PKM("%4", "%0", "%9") PKF("%1", "%0", "%8") PKF("%4", "%0", "%9")
      PKM("%2", "%1", "%8") PKM("%5", "%1", "%9") PKF("%2", "%1", "%8") PKF("%5", "%1", "%9")
      PKM("%6", "%2", "%9") PKF("%6", "%2", "%9")
      : "=&v"(v1), "=&v"(v2), "=&v"(v3), "=&v"(t0), "=&v"(t1), "=&v"(t2), "=&v"(t3) : "v"(s0), "v"(m1), "v"(m2));
  *(LDS u32*)(dst + 0 * 288) = pack_bf16(s0[0], s0[1]);
  *(LDS u32*)(dst + 1 * 288) = pack_bf16(v1[0], v1[1]);
  *(LDS u32*)(dst + 2 * 288) = pack_bf16(v2[0], v2[1]);
  *(LDS u32*)(dst + 3 * 288) = pack_bf16(v3[0], v3[1]);
  *(LDS u32*)(dst + 4 * 288) = pack_bf16(t0[0], t0[1]);
  *(LDS u32*)(dst + 5 * 288) = pack_bf16(t1[0], t1[1]);
  *(LDS u32*)(dst + 6 * 288) = pack_bf16(t2[0], t2[1]);
  *(LDS u32*)(dst + 7 * 288) = pack_bf16(t3[0], t3[1]);
}
DEV u32 dup_lo(u32 a) { return __builtin_amdgcn_perm(a, a, 0x01000100u); }
DEV u32 dup_hi(u32 a) { return __builtin_amdgcn_perm(a, a, 0x03020302u); }
DEV u32 hi_lo(u32 a, u32 b) { return __builtin_amdgcn_alignbit(b, a, 16); }

DEV void role_s(const P& p, LDS char* smem, int sg, int w, int lane) {
  const int tid = w * 64 + lane;
  const int ch = w >> 1, hh = w & 1, d = 6 * sg + ch;
  const int n = d * 128 + 64 * hh + lane;
  const u16 r_ldt = p.ldtA[d], r_la = p.la[n], r_aim = p.aim[n], r_cre = p.creA[n], r_cim = p.cimA[n];
  u32 wb[2] = {0, 0};
  __builtin_amdgcn_sched_barrier(0);
#pragma unroll
  for (int i = 0; i < 2; ++i) {
    const int ln = i * 768 + tid, all = (sg % 12) * 864 + ln;
    if (ln < 864) wb[i] = *(const GLB u32*)((const char*)(all < 5184 ? p.bre : p.bim) + (size_t)(all < 5184 ? all : all - 5184) * 128);
  }
  __builtin_amdgcn_sched_barrier(0);
  STAMP(0, 1);
  f32x4 tab[3];
  [[maybe_unused]] u32 wc[2] = {0, 0};
  LDS char* st = smem + S_ST + w * 4608;
  {
    const float L2E = 1.44269504088896341f;
    const float dt = rbf(__builtin_amdgcn_exp2f(bf2f(r_ldt) * L2E));
    const float ar = -rbf(__builtin_amdgcn_exp2f(bf2f(r_la) * L2E));
    const float zl = ar * dt * L2E, zi = bf2f(r_aim) * dt;
    const float th = zi * 0.15915494309189535f;
    const float tl = fmaf(zi, 0.15915494309189535f, -th) + zi * 4.6017384e-10f;
    const c2 C = {2.0f * (bf2f(r_cre) * dt), 2.0f * (bf2f(r_cim) * dt)};
    const c2 w1 = cexp_k(zl, th, tl, 1.f), w8 = cexp_k(zl, th, tl, 8.f), w128 = cexp_k(zl, th, tl, 128.f);
    c2 w2, w4, w16, w32, w64, w256, w512, w1024, w2048;
    asm(PKM("%0", "%9", "%9") PKM("%2", "%10", "%10") PKM("%5", "%11", "%11") PKF("%0", "%9", "%9") PKF("%2", "%10", "%10") PKF("%5", "%11", "%11")
        PKM("%1", "%0", "%0") PKM("%3", "%2", "%2") PKM("%6", "%5", "%5") PKF("%1", "%0", "%0") PKF("%3", "%2", "%2") PKF("%6", "%5", "%5")
        PKM("%4", "%3", "%3") PKM("%7", "%6", "%6") PKF("%4", "%3", "%3") PKF("%7", "%6", "%6")
        PKM("%8", "%7", "%7") PKF("%8", "%7", "%7")
        : "=&v"(w2), "=&v"(w4), "=&v"(w16), "=&v"(w32), "=&v"(w64), "=&v"(w256), "=&v"(w512), "=&v"(w1024), "=&v"(w2048)
        : "v"(w1), "v"(w8), "v"(w128));
    c2 C128, C256, C512, C1024, C2048;
    asm(PKM("%0", "%5", "%6") PKM("%1", "%5", "%7") PKM("%2", "%5", "%8") PKM("%3", "%5", "%9") PKM("%4", "%5", "%10")
        PKF("%0", "%5", "%6") PKF("%1", "%5", "%7") PKF("%2", "%5", "%8") PKF("%3", "%5", "%9") PKF("%4", "%5", "%10")
        : "=&v"(C128), "=&v"(C256), "=&v"(C512), "=&v"(C1024), "=&v"(C2048)
        : "v"(C), "v"(w128), "v"(w256), "v"(w512), "v"(w1024), "v"(w2048));
#pragma unroll
    for (int i = 0; i < 2; ++i) {
      const int ln = i * 768 + tid, all = (sg % 12) * 864 + ln;
      if (ln < 864) wc[i] = *(const GLB u32*)((const char*)(all < 5184 ? p.cre : p.cim) + (size_t)(all < 5184 ? all : all - 5184) * 128);
    }
    STAMP(0, 2);
    LDS char* wr = st + 4 * lane;
    LDS const char* rd = st + (lane & 15) * 288 + (lane >> 4) * 16;
    bf16x8 B[4];
    const c2 w8c = {w8[0], -w8[1]}, w32c = {w32[0], -w32[1]}, w64c = {w64[0], -w64[1]};
    octet(wr, c2{1.f, 0.f}, w8c, w32c);
    octet(wr + 8 * 288, w64c, w8c, w32c);
    wave_fence();
#pragma unroll
    for (int ks = 0; ks < 4; ++ks) B[ks] = lds_frag(rd + 64 * ks);
    wave_fence();
#pragma unroll
    for (int mt = 0; mt < 3; ++mt) {
      if (mt == 0) { octet(wr, C, w1, w4); octet(wr + 8 * 288, C128, w1, w4); }
      if (mt == 1) { octet(wr, C256, w2, w128); octet(wr + 8 * 288, C512, w128, w4); }
      if (mt == 2) { octet(wr, C1024, w128, w512); *(LDS u32*)(wr + 8 * 288) = pack_bf16(C2048[0], C2048[1]); }
      wave_fence();
      f32x4 acc = {0, 0, 0, 0};
      bf16x8 A[4];
#pragma unroll
      for (int ks = 0; ks < 4; ++ks) A[ks] = lds_frag(rd + 64 * ks);
      __builtin_amdgcn_sched_barrier(0);
#pragma unroll
      for (int ks = 0; ks < 4; ++ks) acc = mfma16(A[ks], B[ks], acc);
      __builtin_amdgcn_sched_barrier(0);
      wave_fence();
      tab[mt] = acc;
    }
  }
  const int b = lane & 15, q = lane >> 4;
#pragma unroll
  for (int mt = 0; mt < 3; ++mt) *(LDS f32x4*)(st + mt * 1024 + lane * 16) = tab[mt];
  STAMP(0, 3);
  bar();
  {
    LDS const char* ot = smem + S_ST + (w ^ 1) * 4608 + lane * 16;
    LDS char* T = smem + S_T + ch * S_TSZ;
    if (hh == 0) {
      const f32x4 v0 = tab[0] + *(LDS const f32x4*)(ot), v2 = tab[2] + *(LDS const f32x4*)(ot + 2048);
      *(LDS u32x2*)(T + 2 * (128 * (q >> 1) + 8 * b + 4 * (q & 1))) = u32x2{pack_bf16(v0[0], v0[1]), pack_bf16(v0[2], v0[3])};
      const u32 a0 = pack_bf16(v2[0], v2[1]), a1 = pack_bf16(v2[2], v2[3]);
      if (q < 2) {
        LDS char* t3 = T + 2 * (512 + 64 * q + b);
        *(LDS u16*)(t3) = (u16)a0; *(LDS u16*)(t3 + 32) = (u16)(a0 >> 16);
        *(LDS u16*)(t3 + 64) = (u16)a1; *(LDS u16*)(t3 + 96) = (u16)(a1 >> 16);
      } else if (lane == 32) *(LDS u16*)(T + 2 * 640) = (u16)a0;
    } else {
      const f32x4 v1 = tab[1] + *(LDS const f32x4*)(ot + 1024);
      const u32 a0 = pack_bf16(v1[0], v1[1]), a1 = pack_bf16(v1[2], v1[3]);
      if (q < 2) *(LDS u32x2*)(T + 2 * (256 + 64 * q + 4 * b)) = u32x2{a0, a1};
      else {
        LDS char* t2 = T + 2 * (384 + 2 * b + (q - 2));
        *(LDS u16*)(t2) = (u16)a0; *(LDS u16*)(t2 + 64) = (u16)(a0 >> 16);
        *(LDS u16*)(t2 + 128) = (u16)a1; *(LDS u16*)(t2 + 192) = (u16)(a1 >> 16);
      }
    }
  }
  bar();
  STAMP(0, 4);
  u32x4 uv[2];
  {
    GLB u64* fl = (GLB u64*)(p.ws + WS_FL) + (8 * hh + (lane & 7)) * DM + d;
    asm volatile("" :: "v"(wc[0]), "v"(wc[1]), "v"(wb[0]), "v"(wb[1]));
    wait_flags(fl, true);
    const __amdgpu_buffer_rsrc_t rs = mk_rsrc(p.ws + WS_XT, DM * SL * 2);
#pragma unroll
    for (int v = 0; v < 2; ++v) uv[v] = __builtin_amdgcn_raw_buffer_load_b128(rs, (d * SL + 1024 * hh + 8 * (lane + 64 * v)) * 2, 0, SC1);
    if (lane < 8) __hip_atomic_store(fl, 0ull, RLX_AGENT);
  }
  STAMP(0, 5);
  {
    LDS const char* T = smem + S_T + ch * S_TSZ;
    u32 o[10];
    const u32 e512 = *(LDS const u16*)(T + 2 * 512);
    o[8] = o[9] = e512 | (e512 << 16);
    if (hh == 0) {
      const int off = lane < 16 ? 32 * lane : (lane < 32 ? 256 + 16 * lane : 512 + 8 * lane);
      u32x2 W[4];
#pragma unroll
      for (int i = 0; i < 4; ++i) W[i] = *(LDS const u32x2*)(T + off + 8 * i);
      if (lane < 16) {
#pragma unroll
        for (int i = 0; i < 8; ++i) o[i] = W[i >> 1][i & 1];
      } else if (lane < 32) {
#pragma unroll
        for (int i = 0; i < 4; ++i) {
          const u32 a = W[i >> 1][i & 1], nx = W[(i + 1) >> 1][(i + 1) & 1];
          o[2 * i] = dup_lo(a);
          o[2 * i + 1] = hi_lo(a, nx);
        }
      } else {
        const u32 w0 = W[0][0], w1 = W[0][1], w2 = W[1][0];
        o[0] = dup_lo(w0); o[1] = w0; o[2] = dup_hi(w0); o[3] = dup_lo(w1);
        o[4] = o[3]; o[5] = w1; o[6] = dup_hi(w1); o[7] = dup_lo(w2);
      }
    } else {
      const u32 w0 = *(LDS const u32*)(T + 1024 + 4 * lane), w1 = *(LDS const u32*)(T + 1028 + 4 * lane);
      o[0] = o[1] = dup_lo(w0); o[2] = w0; o[3] = o[4] = o[5] = dup_hi(w0); o[6] = o[7] = dup_lo(w1);
    }
    o[8] = (u32)__builtin_amdgcn_update_dpp((int)o[8], (int)o[0], 0x130, 0xf, 0xf, false);
    o[9] = (u32)__builtin_amdgcn_update_dpp((int)o[9], (int)o[1], 0x130, 0xf, 0xf, false);
    u32 a[9];
#pragma unroll
    for (int i = 0; i < 9; ++i) a[i] = hi_lo(o[i], o[i + 1]);
    LDS char* xb = smem + S_X + ch * 4 * S_CPY + 32 + 2 * (1024 * hh + 16 * lane);
    *(LDS u32x4*)(xb) = u32x4{o[0], o[1], o[2], o[3]};
    *(LDS u32x4*)(xb + 16) = u32x4{o[4], o[5], o[6], o[7]};
    *(LDS u32x4*)(xb + S_CPY) = u32x4{a[0], a[1], a[2], a[3]};
    *(LDS u32x4*)(xb + S_CPY + 16) = u32x4{a[4], a[5], a[6], a[7]};
    *(LDS u32x4*)(xb + 2 * S_CPY) = u32x4{o[1], o[2], o[3], o[4]};
    *(LDS u32x4*)(xb + 2 * S_CPY + 16) = u32x4{o[5], o[6], o[7], o[8]};
    *(LDS u32x4*)(xb + 3 * S_CPY) = u32x4{a[1], a[2], a[3], a[4]};
    *(LDS u32x4*)(xb + 3 * S_CPY + 16) = u32x4{a[5], a[6], a[7], a[8]};
    if (hh == 0 && lane == 0) {
      LDS char* hb = smem + S_X + ch * 4 * S_CPY;
      const u32x4 z = {0, 0, 0, 0};
      *(LDS u32x4*)(hb) = z;
      *(LDS u32x4*)(hb + 16) = z;
      *(LDS u32x4*)(hb + S_CPY) = z;
      *(LDS u32x4*)(hb + S_CPY + 16) = u32x4{0, 0, 0, o[0] << 16};
      *(LDS u32x4*)(hb + 2 * S_CPY) = z;
      *(LDS u32x4*)(hb + 2 * S_CPY + 16) = u32x4{0, 0, 0, o[0]};
      *(LDS u32x4*)(hb + 3 * S_CPY) = z;
      *(LDS u32x4*)(hb + 3 * S_CPY + 16) = u32x4{0, 0, o[0] << 16, a[0]};
    }
    if (tid < 6 * 32) *(LDS u32x4*)(smem + S_Z + (tid >> 5) * S_ZSZ + (tid & 31) * 16) = u32x4{0, 0, 0, 0};
    STAMP(0, 6);
    LDS char* z = smem + S_Z + ch * S_ZSZ + 512 + (1024 * hh + 8 * lane) * 2;
    *(LDS u32x4*)z = uv[0];
    *(LDS u32x4*)(z + 1024) = uv[1];
  }
  bar();
  STAMP(0, 7);
  f32x4 acc[8];
#pragma unroll
  for (int i = 0; i < 8; ++i) acc[i] = f32x4{0, 0, 0, 0};
  {
    const int kq = q, qq = b >> 2, pp = b & 3;
    LDS const char* fb = smem + S_X + (ch * 4 + ((4 - qq) & 3)) * S_CPY +
                         2 * (16 + 16 * (kq >> 1) - 8 * (kq & 1) + 4 * pp - (qq ? 4 : 0)) + 64 * hh;
    LDS const char* gb = smem + S_Z + ch * S_ZSZ + 512 + 32 * (b - (kq >> 1)) + 16 * (kq & 1) - 64 * hh;
#pragma unroll 1
    for (int it = 0; it < 4; ++it) {
      bf16x8 F[8], G[8];
#pragma unroll
      for (int s = 0; s < 8; ++s) {
        F[s] = tr_frag(fb + 512 * s, fb + 512 * s - 8);
        G[s] = lds_frag(gb + 512 * s);
      }
      __builtin_amdgcn_sched_barrier(0);
#pragma unroll
      for (int t = 0; t < 8; ++t)
#pragma unroll
        for (int s = 0; s <= t; ++s) acc[t] = mfma16(F[s], G[t - s], acc[t]);
      fb += 128;
      gb -= 128;
    }
  }
  __builtin_amdgcn_sched_barrier(0);
  STAMP(0, 8);
  bar();
  {
    LDS char* ex = smem + S_X + 49152 + w * 4096 + lane * 16;
#pragma unroll
    for (int i = 0; i < 4; ++i) {
      const f32x4 lo = acc[i], hi = acc[4 + i];
      *(LDS f32x4*)(ex + i * 1024) = hh ? +lo : +hi;
    }
    if (tid == 0) *(LDS u32*)(smem + S_CNT) = 0;
  }
  bar();
  u32x2 yo[4];
  {
    LDS const char* ey = smem + S_X + 49152 + (w ^ 1) * 4096 + lane * 16;
#pragma unroll
    for (int i = 0; i < 4; ++i) {
      const f32x4 lo = acc[i], hi = acc[4 + i];
      const f32x4 y = (hh ? +hi : +lo) + *(LDS const f32x4*)(ey + i * 1024);
      yo[i] = u32x2{pack_bf16(y[0], y[1]), pack_bf16(y[2], y[3])};
    }
  }
  bar();
  {
    const __amdgpu_buffer_rsrc_t rs = mk_rsrc(p.ws + WS_AT, DM * SL * 2);
    const int dst = (d * SL + 1024 * hh + 16 * (lane & 15) + 4 * (lane >> 4)) * 2;
#pragma unroll
    for (int i = 0; i < 4; ++i) __builtin_amdgcn_raw_buffer_store_b64(yo[i], rs, dst + 512 * i, 0, SC1);
  }
  STAMP(0, 9);
}
DEV void s_signal(const P& p, LDS char* smem, int sg, int lane) {
  u32 old = 0;
  if (lane == 0) old = __hip_atomic_fetch_add((LDS u32*)(smem + S_CNT), 1u, __ATOMIC_RELAXED, __HIP_MEMORY_SCOPE_WORKGROUP);
  if (__builtin_amdgcn_readfirstlane(old) == 11) {
    const int lt = lane & 31, second = lane >> 5;
    const int dd = 6 * sg + (second ? 5 : 0);
    const int idx = dd < 480 ? dd / 96 : 5 + (dd - 480) / 32;
    const int dfirst = idx < 5 ? 96 * idx : 480 + 32 * (idx - 5);
    __hip_atomic_store((GLB u64*)(p.ws + WS_IN) + tile_block(lt, idx) * 64 + IN_A + (sg - dfirst / 6), FLAG_SET, RLX_AGENT);
  }
}

DEV void role_gate(const P& p, int rb, int w, int lane, u64 pc) {
  u32 pf0 = 0, pf1 = 0;
  const int tid = w * 64 + lane;
  const int sub = lane & 7;
  u32x4 wv[9];
#pragma unroll
  for (int i = 0; i < 9; ++i) wv[i] = *(const u32x4*)((const char*)p.gw + (i * 8 + sub) * 16);
  for (int r = w * 8 + (lane >> 3); r < 128; r += 96) {
    const int l = rb * 128 + r;
    float s = 0.f;
    u32x4 xv[9];
#pragma unroll
    for (int i = 0; i < 9; ++i) xv[i] = *(const u32x4*)((const char*)p.x + (size_t)l * 1152 + (i * 8 + sub) * 16);
#pragma unroll
    for (int i = 0; i < 9; ++i) {
#pragma unroll
      for (int k = 0; k < 4; ++k) {
        const u32 pr = pack_bf16(bflo(xv[i][k]) * bflo(wv[i][k]), bfhi(xv[i][k]) * bfhi(wv[i][k]));
        s += bflo(pr);
        s += bfhi(pr);
      }
    }
    s += __shfl_xor(s, 1); s += __shfl_xor(s, 2); s += __shfl_xor(s, 4);
    const float gv = rbf(1.0f / (1.0f + expf(-rbf(s))));
    if (sub == 0)
      __hip_atomic_store((GLB u64*)(p.ws + WS_G) + l, ((u64)__float_as_uint(rbf(1.0f - gv)) << 32) | __float_as_uint(gv), RLX_AGENT);
  }
  asm volatile("" :: "v"(pf0), "v"(pf1));
  wait_vm<0>();
  bar();
  if (w == 0 && lane < 16)
    __hip_atomic_store((GLB u64*)(p.ws + WS_IN) + tile_block(2 * rb + (lane >> 3), lane & 7) * 64 + IN_G, FLAG_SET, RLX_AGENT);
}

constexpr int H_C = 32768, H_CS = 12288;
DEV int h_slot(int i) { return i < 4 ? i * 8192 : 131072 + (i - 4) * 8192; }
constexpr int H_AT = H_C + 2 * H_CS;
constexpr int H_XT = H_C + 3 * H_CS;
constexpr int H_G = H_C + 4 * H_CS;
constexpr int H_OUT = H_C + 5 * H_CS;
constexpr int K_LDS = 163840;

constexpr int h_wait(int D, int NDT) {
  const int G = 4 + 2 * NDT;
  switch (D) {
    case 0: return 4;
    case 1: return 8;
    case 2: return 4 + G;
    case 3: case 4: case 5: return 2 * G + 1;
    case 6: return 2 * G;
    case 7: return G + 2 * NDT;
    default: return 0;
  }
}
template <int D, int NDT, class F> DEV void h_steps(F&& after, u64& fv, char* wsp) {
  if constexpr (D == 6) asm volatile("s_waitcnt vmcnt(%1)" : "+v"(fv) : "n"(h_wait(D, NDT)) : "memory");
  else wait_vm<h_wait(D, NDT)>();
  LSTAMP2(1, 16 + D);
  bar();
  after(std::integral_constant<int, D>{});
  if constexpr (D < 8) h_steps<D + 1, NDT>(after, fv, wsp);
}
DEV int p2_rot(int d0) { const int idx = d0 < 480 ? d0 / 96 : 5 + (d0 - 480) / 32; return (idx * 18) >> 3; }

template <int NDT, class F>
DEV void p2_loader(const P& p, LDS char* smem, int lt, int d0, int lw, int lane, bool scratch_barrier, F&& pre_poll) {
  __builtin_amdgcn_s_setprio(3);
  const char* hb = p.ws + WS_HB + (size_t)lt * 18 * 8192 + (2 * lw) * 1024 + lane * 16;
  constexpr int W = 32 * NDT;
  const int l0 = 64 * lt, rot = p2_rot(d0);
  u32 co[NDT];
#pragma unroll
  for (int i = 0; i < NDT; ++i) {
    const int row = 8 * (NDT * lw + i) + (lane >> 3), cp = lane & 7;
    co[i] = (u32)(d0 + row) * 1152u + ((cp ^ (row & 7)) << 4);
  }
  auto issue_c = [&](int s) {
    LDS char* buf = smem + H_C + (s & 7) * H_CS + NDT * lw * 1024;
    int ks = s + rot; ks = ks >= 18 ? ks - 18 : ks;
    const char* cb = ks < 9 ? (const char*)p.cre + ks * 128 : (const char*)p.cim + (ks - 9) * 128;
#pragma unroll
    for (int i = 0; i < NDT; ++i) dma16<0>(cb + co[i], buf + i * 1024);
  };
  auto issue_h = [&](int s) {
    LDS char* buf = smem + h_slot(s & 7) + 2 * lw * 1024;
    int ks = s + rot; ks = ks >= 18 ? ks - 18 : ks;
#pragma unroll
    for (int i = 0; i < 2; ++i) dma16<SC1>(hb + ks * 8192 + i * 1024, buf + i * 1024);
  };
  GLB u64* inb = (GLB u64*)(p.ws + WS_IN) + blockIdx.x * 64;
  const int nA = (d0 + W - 1) / 6 - d0 / 6 + 1;
  const bool need = lane == IN_G || (lane >= IN_A && lane < IN_A + nA);
  if (!scratch_barrier) {
#pragma unroll 1
    for (int s = 0; s < 8; ++s) issue_c(s);
  }
  pre_poll();
  { [[maybe_unused]] const int w = lw + 8; STAMP(1, 1); }
  if (scratch_barrier) bar();
  wait_flags(inb + IN_H + (lane < 36 ? lane : 0), true);
  { [[maybe_unused]] const int w = lw + 8; STAMP(1, 2); }
#pragma unroll
  for (int s = 0; s < 4; ++s) issue_h(s);
  { [[maybe_unused]] const int w = lw + 8; STAMP(1, 3); }
  u64 fv = 0;
  h_steps<0, NDT>([&](auto D_) {
    constexpr int D = decltype(D_)::value;
    if constexpr (D == 0) { issue_h(4); issue_h(5); issue_h(6); issue_h(7); }
    if constexpr (D >= 1 && D <= 5) { issue_h(2 * D + 6); issue_h(2 * D + 7); issue_c(2 * D + 6); issue_c(2 * D + 7); }
    if constexpr (D == 2)
      asm volatile("global_load_dwordx2 %0, %1, off sc1" : "=v"(fv) : "v"(inb + lane) : "memory");
    if constexpr (D == 6) {
      if (!__all(!need || fv == FLAG_SET)) wait_flags(inb + lane, need);
#pragma unroll
      for (int i = 0; i < NDT; ++i) {
        const int pc = NDT * lw + i;
        dma16<SC1>(p.ws + WS_AT + (size_t)(d0 + 8 * pc + (lane >> 3)) * 4096 + l0 * 2 + (lane & 7) * 16, smem + H_AT + pc * 1024);
      }
#pragma unroll
      for (int i = 0; i < NDT; ++i) {
        const int pc = NDT * lw + i, id = pc * 64 + lane, row = id / (4 * NDT), qq = id - row * (4 * NDT);
        dma16<0>((const char*)p.x + (size_t)(l0 + row) * 1152 + d0 * 2 + qq * 16, smem + H_XT + pc * 1024);
      }
    }
    if constexpr (D == 7) { if (lane < 8) dma16<SC1>(p.ws + WS_G + (size_t)l0 * 8 + lw * 128 + lane * 16, smem + H_G + lw * 128); }
  }, fv, p.ws);
}

template <int NDT>
DEV void p2_compute(const P& p, LDS char* smem, int lt0, int d0, int w, int lane, const u32x2 (&dsk)[6]) {
  const int lh = (w >> 1) & 1, kh = w & 1, dg = w >> 2;
  const int r = lane & 15, kq = lane >> 4;
  const int lt = 2 * lh + kh, l0 = 64 * lt0;
  constexpr int W = 32 * NDT;
  u32x2 da[NDT], db[NDT];
#pragma unroll
  for (int t = 0; t < NDT; ++t) {
    const int d = d0 + 16 * (NDT * dg + t) + 4 * kq;
    da[t] = *(const u32x2*)(p.dskA + d);
    db[t] = *(const u32x2*)(p.dskB + d);
  }
  GLB u64* inb = (GLB u64*)(p.ws + WS_IN) + blockIdx.x * 64;

  f32x4 acc[2][NDT];
#pragma unroll
  for (int i = 0; i < 2; ++i)
#pragma unroll
    for (int t = 0; t < NDT; ++t) acc[i][t] = f32x4{0, 0, 0, 0};
  int ho[2];
#pragma unroll
  for (int u = 0; u < 2; ++u) {
    const int k = 32 * kh + 8 * kq + 4 * u + (r >> 2), i = (r & 3) >> 1;
    ho[u] = (2 * (k >> 4) + i) * 1024 + (32 * lh + ((k & 15) ^ (4 * i))) * 16 + (r & 1) * 8;
  }
  const int co = H_C + (16 * NDT * dg + r) * 128 + (((4 * kh + kq) ^ (r & 7)) << 4);
  bf16x8 hb[2][2][2], ca[2][2][NDT];
  auto rd = [&](auto SET_, int D) {
    constexpr int SET = decltype(SET_)::value;
#pragma unroll
    for (int u = 0; u < 2; ++u) {
      const int s = 2 * D + u;
      LDS const char* hbuf = smem + h_slot(s & 7);
#pragma unroll
      for (int i = 0; i < 2; ++i) hb[SET][u][i] = tr_frag(hbuf + ho[0] + i * 256, hbuf + ho[1] + i * 256);
      LDS const char* cbuf = smem + (s & 7) * H_CS;
#pragma unroll
      for (int t = 0; t < NDT; ++t) ca[SET][u][t] = lds_frag(cbuf + co + t * 2048);
    }
    __builtin_amdgcn_sched_barrier(0);
  };
  auto mma = [&](auto SET_) {
    constexpr int SET = decltype(SET_)::value;
#pragma unroll
    for (int u = 0; u < 2; ++u)
#pragma unroll
      for (int t = 0; t < NDT; ++t) {
        acc[0][t] = mfma16(ca[SET][u][t], hb[SET][u][0], acc[0][t]);
        acc[1][t] = mfma16(ca[SET][u][t], hb[SET][u][1], acc[1][t]);
      }
    __builtin_amdgcn_sched_barrier(0);
  };
  using S0 = std::integral_constant<int, 0>;
  using S1 = std::integral_constant<int, 1>;
  STAMP(1, 1);
  bar();
  STAMP(1, 2);
  rd(S0{}, 0);
#pragma unroll 1
  for (int D = 1; D < 9; D += 2) {
    bar(); rd(S1{}, D); mma(S0{});
    bar(); rd(S0{}, D + 1); mma(S1{});
  }
  mma(S0{});
  __builtin_amdgcn_sched_barrier(0);
  STAMP(1, 3);
  bar();
  if (w == 0) __hip_atomic_store(inb + lane, 0ull, RLX_AGENT);
  LDS char* ex = smem + w * (NDT * 1024) + lane * 16;
#pragma unroll
  for (int t = 0; t < NDT; ++t) {
    const f32x4 a0 = acc[0][t], a1 = acc[1][t];
    *(LDS f32x4*)(ex + t * 1024) = kh ? +a0 : +a1;
  }
  bar();
  LDS const char* ey = smem + (w ^ 1) * (NDT * 1024) + lane * 16;
  constexpr int OSTR = W * 4 + 16;
  const f32x2 gg = *(LDS const f32x2*)(smem + H_G + (16 * lt + r) * 8);
#pragma unroll
  for (int t = 0; t < NDT; ++t) {
    const int dt = NDT * dg + t;
    const f32x4 o = *(LDS const f32x4*)(ey + t * 1024);
    const f32x4 a0 = acc[0][t], a1 = acc[1][t];
    const f32x4 y = (kh ? +a1 : +a0) + o;
    const s16x4 av = tr_read(smem + H_AT + (16 * dt + 4 * kq + (r >> 2)) * 128 + lt * 32 + (r & 3) * 8);
    const u32x2 xv = *(LDS const u32x2*)(smem + H_XT + (16 * lt + r) * (W * 2) + (16 * dt + 4 * kq) * 2);
    f32x4 res;
#pragma unroll
    for (int i = 0; i < 4; ++i) {
      const u32 xw = xv[i >> 1], aw = da[t][i >> 1], bw = db[t][i >> 1];
      const float xf = (i & 1) ? bfhi(xw) : bflo(xw);
      const float sa = (i & 1) ? bfhi(aw) : bflo(aw);
      const float sb = (i & 1) ? bfhi(bw) : bflo(bw);
      const float yc = bf2f((u16)av[i]);
      const float a = rbf(yc + rbf(xf * sa));
      const float b = y[i] + rbf(xf * sb);
      res[i] = rbf(gg[0] * a) + gg[1] * b;
    }
    *(LDS f32x4*)(smem + H_OUT + (16 * lt + r) * OSTR + (16 * dt + 4 * kq) * 4) = res;
  }
  STAMP(1, 4);
  bar();
#pragma unroll
  for (int it = 0; it < NDT; ++it) {
    const int id = it * 512 + w * 64 + lane;
    const int row = id / (8 * NDT), pc = id - row * (8 * NDT);
    const f32x4 v = *(LDS const f32x4*)(smem + H_OUT + row * OSTR + pc * 16);
    __builtin_amdgcn_raw_buffer_store_b128(__builtin_bit_cast(u32x4, v), mk_rsrc(p.out, SL * DM * 4), ((l0 + row) * DM + d0 + pc * 4) * 4, 0, OUT_AUX);
  }
}

template <int NDT>
DEV void phase2(const P& p, LDS char* smem, int lt, int d0, int w, int lane, bool scratch_barrier, int sg, const u32x2 (&dsk)[6]) {
  STAMP(1, 0);
  if (w >= 8) {
    p2_loader<NDT>(p, smem, lt, d0, w - 8, lane, scratch_barrier, [&]() { if (NDT == 1) { wait_vm<8>(); s_signal(p, smem, sg, lane); } });
  } else {
    if (NDT == 1) { wait_vm<0>(); s_signal(p, smem, sg, lane); }
    p2_compute<NDT>(p, smem, lt, d0, w, lane, dsk);
  }
  STAMP(1, 15);
}

__global__ __launch_bounds__(768, 3) void gated_mix_s4d_s5(P p) {
  extern __shared__ __attribute__((aligned(16))) char smem_raw[];
  LDS char* smem = (LDS char*)smem_raw;
  u64 pc;
  asm volatile("s_getpc_b64 %0" : "=s"(pc));
  u32 pf = 0;
#define PF_TOP 312
  if (threadIdx.x < PF_TOP && blockIdx.x >= 144)
    asm volatile("global_load_dword %0, %1, off" : "=v"(pf) : "v"((pc & ~255ull) + threadIdx.x * 128));
  const int w = __builtin_amdgcn_readfirstlane(threadIdx.x >> 6), lane = threadIdx.x & 63;
  const int xcd = blockIdx.x & 7, j = blockIdx.x >> 3;
  u32x2 dsk[6];
#pragma unroll
  for (int i = 0; i < 6; ++i) dsk[i] = u32x2{0, 0};
  STAMP(0, 0);
  if (j >= 18 && j < 30) {
    const int sg = 12 * xcd + (j - 18);
    role_s(p, smem, sg, w, lane);
    STAMP(0, 15);
    phase2<1>(p, smem, 4 * xcd + (j - 18) / 3, 480 + 32 * ((j - 18) % 3), w, lane, false, sg, dsk);
  } else {
    const bool isg = j < 18;
    if (isg) {
      const int c = 2 * xcd + j / 9, g = j % 9;
      if (w >= 8) g_loader(p, smem, c, g, 96 * (j % 5), w - 8, lane);
      else if (w < 4) g_compute<0>(p, smem, c, g, w, lane, pf);
      else g_compute<1>(p, smem, c, g, w, lane, pf);
    } else {
      role_gate(p, 2 * xcd + (j - 30), w, lane, pc);
    }
    STAMP(0, 15);
    const int e = isg ? j : j - 12;
    phase2<3>(p, smem, 4 * xcd + e / 5, 96 * (e % 5), w, lane, isg, 0, dsk);
  }
  asm volatile("s_waitcnt vmcnt(0)" :: "v"(pf));
}

extern "C" void kernel_launch(void* const* d_in, const int* in_sizes, int n_in, void* d_out, int out_size,
                              void* d_ws, size_t ws_size, hipStream_t stream) {
  P a;
  a.x = (const u16*)d_in[0]; a.la = (const u16*)d_in[1]; a.aim = (const u16*)d_in[2];
  a.creA = (const u16*)d_in[3]; a.cimA = (const u16*)d_in[4]; a.ldtA = (const u16*)d_in[5]; a.dskA = (const u16*)d_in[6];
  a.lre = (const u16*)d_in[8]; a.lim = (const u16*)d_in[9]; a.bre = (const u16*)d_in[10]; a.bim = (const u16*)d_in[11];
  a.cre = (const u16*)d_in[12]; a.cim = (const u16*)d_in[13]; a.ldtB = (const u16*)d_in[14];
  a.dskB = (const u16*)d_in[15]; a.gw = (const u16*)d_in[16];
  a.ws = (char*)d_ws + WS_OFF; a.out = (float*)d_out;
  (void)hipFuncSetAttribute((const void*)gated_mix_s4d_s5, hipFuncAttributeMaxDynamicSharedMemorySize, K_LDS);
  gated_mix_s4d_s5<<<256, 768, K_LDS, stream>>>(a);
}
```

```cpp
#define HOLD_AT 5
#define PF_NOT_G 1
#include <hip/hip_runtime.h>
#include <hip/hip_bf16.h>
#include <stdint.h>
#include <type_traits>

typedef unsigned short u16;
typedef unsigned int u32;
typedef unsigned long long u64;
typedef __attribute__((ext_vector_type(8))) __bf16 bf16x8;
typedef __attribute__((ext_vector_type(2))) __bf16 bf16x2;
typedef __attribute__((ext_vector_type(4))) short s16x4;
typedef __attribute__((ext_vector_type(4))) float f32x4;
typedef __attribute__((ext_vector_type(2))) float f32x2;
typedef __attribute__((ext_vector_type(4))) unsigned u32x4;
typedef __attribute__((ext_vector_type(2))) unsigned u32x2;
#define LDS __attribute__((address_space(3)))
#define GLB __attribute__((address_space(1)))
#define DEV __device__ __forceinline__
#define RLX_AGENT __ATOMIC_RELAXED, __HIP_MEMORY_SCOPE_AGENT

constexpr int SL = 2048;
constexpr int DM = 576;

constexpr size_t WS_HB = 0;
constexpr size_t WS_AT = WS_HB + (size_t)2 * DM * SL * 2;
constexpr size_t WS_G = WS_AT + (size_t)DM * SL * 2;
constexpr size_t WS_GRAN = WS_G + (size_t)SL * 8;
constexpr size_t WS_EP = WS_GRAN + (size_t)9 * 16 * 2 * 64 * 8;
constexpr size_t WS_FL = WS_EP + 4096;
constexpr size_t WS_XT = WS_FL + (size_t)16 * DM * 8;
constexpr size_t WS_IN = WS_XT + (size_t)DM * SL * 2;
constexpr u64 FLAG_SET = 0x9E3779B97F4A7C15ull;
constexpr u32 SPIN_CAP = 1u << 17;
constexpr int IN_H = 0, IN_G = 36, IN_A = 40;

constexpr size_t WS_ST = (size_t)16 << 20;
#define STAMP(kern, idx) do {} while (0)
#define CSTAMP(kern, idx) do {} while (0)
#define LSTAMP(kern, idx) do {} while (0)
#define LSTAMP2(kern, idx) do {} while (0)

#define HOLD(id) do { if (HOLD_AT == (id)) { while (__builtin_amdgcn_s_memrealtime() - g_tb < 4000) __builtin_amdgcn_s_sleep(2); } } while (0)
DEV float bf2f(u16 v) { return __uint_as_float((u32)v << 16); }
DEV float bflo(u32 v) { return __uint_as_float(v << 16); }
DEV float bfhi(u32 v) { return __uint_as_float(v & 0xffff0000u); }
DEV u32 pack_bf16(float lo, float hi) {
  f32x2 v = {lo, hi};
  bf16x2 b = __builtin_convertvector(v, bf16x2);
  return __builtin_bit_cast(u32, b);
}
DEV float rbf(float x) { return bflo(pack_bf16(x, x)); }
template <int N> DEV void wait_vm() { asm volatile("s_waitcnt vmcnt(%0)" ::"n"(N) : "memory"); }
DEV void bar() { asm volatile("s_waitcnt lgkmcnt(0)\n\ts_barrier" ::: "memory"); }
DEV void wave_fence() { __builtin_amdgcn_fence(__ATOMIC_ACQ_REL, "wavefront"); }
template <int AUX> DEV void dma16(const void* src, LDS char* dst) {
  __builtin_amdgcn_global_load_lds((const GLB u32*)src, (LDS u32*)dst, 16, 0, AUX);
}
constexpr int SC1 = 16;
#define GPOLL_SLEEP 1
#define GPOLL_DELAY 24
#define POLL_SLEEP 1
#define OUT_AUX 16
#define WS_OFF 0
#define XT_WAIT 4
DEV bf16x8 lds_frag(LDS const char* p) { return *(LDS const bf16x8*)p; }
DEV s16x4 tr_read(LDS const char* a) { return __builtin_amdgcn_ds_read_tr16_b64_v4i16((LDS s16x4*)a); }
DEV bf16x8 tr_frag(LDS const char* a, LDS const char* b) {
  const s16x4 lo = tr_read(a), hi = tr_read(b);
  return __builtin_bit_cast(bf16x8, __builtin_shufflevector(lo, hi, 0, 1, 2, 3, 4, 5, 6, 7));
}
DEV f32x4 mfma16(bf16x8 a, bf16x8 b, f32x4 c) { return __builtin_amdgcn_mfma_f32_16x16x32_bf16(a, b, c, 0, 0, 0); }
DEV __amdgpu_buffer_rsrc_t mk_rsrc(const void* base, int bytes) {
  return __builtin_amdgcn_make_buffer_rsrc(const_cast<void*>(base), (short)0, bytes, 0x00020000);
}
struct cf { float r, i; };
DEV cf cmul(cf a, cf b) { return {a.r * b.r - a.i * b.i, a.r * b.i + a.i * b.r}; }
DEV cf cfma(cf a, cf b, cf c) { return {fmaf(a.r, b.r, fmaf(-a.i, b.i, c.r)), fmaf(a.r, b.i, fmaf(a.i, b.r, c.i))}; }
typedef f32x2 c2;
DEV c2 cm2(c2 a, c2 b) {
  c2 t, r;
  asm("v_pk_mul_f32 %0, %1, %2 op_sel:[0,0] op_sel_hi:[0,1]" : "=v"(t) : "v"(a), "v"(b));
  asm("v_pk_fma_f32 %0, %1, %2, %3 op_sel:[1,1,0] op_sel_hi:[1,0,1] neg_lo:[1,0,0]" : "=v"(r) : "v"(a), "v"(b), "v"(t));
  return r;
}
DEV f32x2 fma2(f32x2 a, f32x2 b, f32x2 c) { return __builtin_elementwise_fma(a, b, c); }
DEV f32x2 lo2(f32x4 v) { return __builtin_shufflevector(v, v, 0, 1); }
DEV f32x2 hi2(f32x4 v) { return __builtin_shufflevector(v, v, 2, 3); }
DEV f32x4 cat2(f32x2 a, f32x2 b) { return __builtin_shufflevector(a, b, 0, 1, 2, 3); }
DEV void wait_flags(GLB u64* word, bool need) {
  for (u32 spin = 0;; ++spin) {
    const u64 v = __hip_atomic_load(word, RLX_AGENT);
    if (__all(!need || v == FLAG_SET) || spin > SPIN_CAP) break;
    __builtin_amdgcn_s_sleep(POLL_SLEEP);
  }
}
DEV int tile_block(int lt, int idx) {
  const int xcd = lt >> 2, a = lt & 3;
  int j;
  if (idx < 5) { const int e = 5 * a + idx; j = e < 18 ? e : e + 12; }
  else j = 18 + 3 * a + (idx - 5);
  return (j << 3) | xcd;
}

struct P {
  const u16 *x, *la, *aim, *creA, *cimA, *ldtA, *dskA;
  const u16 *lre, *lim, *bre, *bim, *cre, *cim, *ldtB, *dskB, *gw;
  char* ws;
  float* out;
};

constexpr int G_NB = 4, G_STAGE = 32768;
constexpr int G_E = 153536;
constexpr int G_PF = G_E + 4096;
constexpr int G_L = G_PF + 4096;
constexpr int G_X = G_L + 1024;
constexpr int G_GO = G_X + 1024;
constexpr int G_T = G_NB * G_STAGE;
static_assert(G_GO + 4 <= 163840 && G_T + 4 * 32 * 144 <= G_E, "role G scratch");

DEV void g_loader(const P& p, LDS char* smem, int c, int g, int d0, int lw, int lane) {
  [[maybe_unused]] const int w = lw + 8;
  __builtin_amdgcn_s_setprio(3);
  GLB u32* ep = (GLB u32*)(p.ws + WS_EP + g * 256);
  GLB u64* gran = (GLB u64*)(p.ws + WS_GRAN) + (size_t)g * 16 * 128;
  const u32 tag = __hip_atomic_load(ep, RLX_AGENT) + 1u;
  u32 xo[4], bo[4];
#pragma unroll
  for (int i = 0; i < 4; ++i) {
    const int rho = 8 * (4 * lw + i) + (lane >> 3);
    const int hf = rho >> 6, m = (rho >> 4) & 3, r = rho & 15;
    const int tau = 64 * hf + 16 * (r >> 2) + 8 * (r & 1) + 2 * m + ((r >> 1) & 1);
    const int ch = (lane & 7) ^ (rho & 7);
    xo[i] = (u32)(128 * c + tau) * 1152u + ch * 16;
    const int pp = 64 * g + 16 * (rho >> 5) + r;
    bo[i] = (u32)pp * 1152u + ch * 16;
  }
  auto issue = [&](int s) {
    LDS char* buf = smem + (s & (G_NB - 1)) * G_STAGE + lw * 4096;
    int kb = g + s; kb = (kb >= 9 ? kb - 9 : kb) * 128;
#pragma unroll
    for (int i = 0; i < 4; ++i) dma16<0>((const char*)p.x + (xo[i] + kb), buf + i * 1024);
#pragma unroll
    for (int i = 0; i < 4; ++i) {
      const char* base = (const char*)((((4 * lw + i) >> 1) & 1) ? p.bim : p.bre);
      dma16<0>(base + (bo[i] + kb), buf + 16384 + i * 1024);
    }
  };
#pragma unroll
  for (int s = 0; s < G_NB - 1; ++s) issue(s);
#pragma unroll 1
  for (int s = 0; s < 6; ++s) { wait_vm<16>(); LSTAMP(0, 16 + s); bar(); issue(s + G_NB - 1); LSTAMP(0, 32 + s); }
  wait_vm<16>(); bar();
  wait_vm<8>(); bar();
  wait_vm<0>(); bar();
  STAMP(0, 1);
  if (lw < 2) {
    bar();
    u32 co[6];
#pragma unroll
    for (int i = 0; i < 6; ++i) {
      const int row = 8 * (6 * lw + i) + (lane >> 3), cp = lane & 7;
      co[i] = (u32)(d0 + row) * 1152u + ((cp ^ (row & 7)) << 4);
    }
    const int rot = ((d0 / 96) * 18) >> 3;
#pragma unroll 1
    for (int s = 0; s < 8; ++s) {
      LDS char* buf = smem + 32768 + s * 12288 + 6 * lw * 1024;
      const int ks = s + rot;
      const char* cb = ks < 9 ? (const char*)p.cre + ks * 128 : (const char*)p.cim + (ks - 9) * 128;
#pragma unroll
      for (int i = 0; i < 6; ++i) dma16<0>(cb + co[i], buf + i * 1024);
    }
    bar();
    return;
  }
  bar();
  STAMP(0, 2);
  if (lw == 3) {
    const f32x2 lv = *(LDS const f32x2*)(smem + G_L + lane * 8);
    const cf l16 = {lv[0], lv[1]};
    f32x2 ev[8];
#pragma unroll
    for (int s = 0; s < 8; ++s) ev[s] = *(LDS const f32x2*)(smem + G_E + (s * 64 + lane) * 8);
    cf tot = {0.f, 0.f};
    cf pre[8];
#pragma unroll
    for (int s = 0; s < 8; ++s) { pre[s] = tot; tot = cfma(l16, tot, cf{ev[s][0], ev[s][1]}); }
    if (c < 15) {
      __hip_atomic_store(gran + c * 128 + lane, ((u64)tag << 32) | __float_as_uint(tot.r), RLX_AGENT);
      __hip_atomic_store(gran + c * 128 + 64 + lane, ((u64)tag << 32) | __float_as_uint(tot.i), RLX_AGENT);
    }
#pragma unroll
    for (int s = 0; s < 8; ++s) *(LDS f32x2*)(smem + G_PF + (s * 64 + lane) * 8) = f32x2{pre[s].r, pre[s].i};
  }
  const int par = lw - 2;
  const f32x2 lv = *(LDS const f32x2*)(smem + G_L + 512 + lane * 8);
  const cf l128 = {lv[0], lv[1]};
  const cf l256 = cmul(l128, l128);
  cf acc = {0.f, 0.f};
  if (par < c) {
    const int nc = (c - par + 1) >> 1;
    u64 a[8], b[8];
    __builtin_amdgcn_s_sleep(GPOLL_DELAY);
    for (u32 spin = 0;; ++spin) {
      bool ok = true;
#pragma unroll
      for (int i = 0; i < 8; ++i) {
        const int cc = par + 2 * (i < nc ? i : 0);
        a[i] = __hip_atomic_load(gran + cc * 128 + lane, RLX_AGENT);
        b[i] = __hip_atomic_load(gran + cc * 128 + 64 + lane, RLX_AGENT);
      }
#pragma unroll
      for (int i = 0; i < 8; ++i) ok = ok && (u32)(a[i] >> 32) == tag && (u32)(b[i] >> 32) == tag;
      if (__all(ok) || spin > SPIN_CAP) break;
      __builtin_amdgcn_s_sleep(GPOLL_SLEEP);
    }
#pragma unroll
    for (int i = 0; i < 8; ++i)
      if (i < nc) acc = cfma(l256, acc, cf{__uint_as_float((u32)a[i]), __uint_as_float((u32)b[i])});
    if (c - 1 - (par + 2 * (nc - 1))) acc = cmul(acc, l128);
  }
  *(LDS f32x2*)(smem + G_X + (par * 64 + lane) * 8) = f32x2{acc.r, acc.i};
  STAMP(0, 3);
  bar();
  if (c == 15 && lw == 2 && lane == 0) __hip_atomic_store(ep, tag, RLX_AGENT);
}

template <int HF>
DEV void g_compute(const P& p, LDS char* smem, int c, int g, int w, int lane, u32& pf) {
  const int pq = w & 3, col = lane & 15, q = lane >> 4;
  const int pl = 16 * pq + col, pp = 64 * g + pl;
  cf lam, coef2;
  {
    const float lre = bf2f(p.lre[pp]), lim = bf2f(p.lim[pp]);
    const float dt = rbf(expf(bf2f(p.ldtB[pp])));
    const float zr = dt * lre, zi = dt * lim;
    const float er = expf(zr);
    float sn, cs;
    sincosf(zi, &sn, &cs);
    lam = {er * cs, er * sn};
    const float nr = lam.r - 1.0f, ni = lam.i;
    const float den = 1.0f / (lre * lre + lim * lim);
    coef2 = {2.0f * (nr * lre + ni * lim) * den, 2.0f * (ni * lre - nr * lim) * den};
  }
  cf l8 = cmul(lam, lam); l8 = cmul(l8, l8); l8 = cmul(l8, l8);
  const cf l16 = cmul(l8, l8);
  const cf l32 = cmul(l16, l16), l64 = cmul(l32, l32);
  if (HF == 0 && q == 0) {
    const cf l128 = cmul(l64, l64);
    *(LDS f32x2*)(smem + G_L + pl * 8) = f32x2{l16.r, l16.i};
    *(LDS f32x2*)(smem + G_L + 512 + pl * 8) = f32x2{l128.r, l128.i};
  }
  if (HF == 0 && w == 0 && lane == 0) *(LDS u32*)(smem + G_GO) = 0;
  cf pw = HF ? l64 : cf{1.f, 0.f};
  { const cf t = cmul(pw, l16); if (q & 1) pw = t; }
  { const cf t = cmul(pw, l32); if (q & 2) pw = t; }
  asm volatile("" : "+v"(lam.r), "+v"(lam.i), "+v"(coef2.r), "+v"(coef2.i), "+v"(l8.r), "+v"(l8.i), "+v"(pw.r), "+v"(pw.i));
  f32x4 are[4], aim[4];
#pragma unroll
  for (int m = 0; m < 4; ++m) { are[m] = f32x4{0, 0, 0, 0}; aim[m] = f32x4{0, 0, 0, 0}; }
  const int o0 = col * 128 + (((0 + q) ^ (col & 7)) << 4);
  const int o1 = col * 128 + (((4 + q) ^ (col & 7)) << 4);
  bf16x8 br[2], bi[2], a[2][4];
  auto read_frags = [&](int s) {
    LDS const char* buf = smem + (s & (G_NB - 1)) * G_STAGE;
    LDS const char* ab = buf + HF * 8192;
    LDS const char* bb = buf + 16384 + pq * 4096;
#pragma unroll
    for (int ks = 0; ks < 2; ++ks) {
      const int o = ks ? o1 : o0;
      br[ks] = lds_frag(bb + o); bi[ks] = lds_frag(bb + 2048 + o);
#pragma unroll
      for (int m = 0; m < 4; ++m) a[ks][m] = lds_frag(ab + m * 2048 + o);
    }
  };
  auto mma = [&]() {
#pragma unroll
    for (int ks = 0; ks < 2; ++ks)
#pragma unroll
      for (int m = 0; m < 4; ++m) {
        are[m] = mfma16(a[ks][m], br[ks], are[m]);
        aim[m] = mfma16(a[ks][m], bi[ks], aim[m]);
      }
  };
  STAMP(0, 1);
#pragma unroll
  for (int s = 0; s < 9; ++s) {
    if (HF == 1 && s == XT_WAIT) {
      wait_vm<0>();
      if (lane < 16) __hip_atomic_store((GLB u64*)(p.ws + WS_FL) + c * DM + 64 * g + 16 * pq + lane, FLAG_SET, RLX_AGENT);
    }
    bar();
    if (s == 0) { STAMP(0, 2); CSTAMP(0, 60); }
    if (HF == 1 && s > 0) { mma(); __builtin_amdgcn_sched_barrier(0); }
    if (HF == 1 && s == 0) {
      const int rr = 4 * q + (col >> 2);
      LDS const char* tb = smem + rr * 128 + (((2 * pq + ((col & 3) >> 1)) ^ (rr & 7)) << 4) + (col & 1) * 8;
      LDS char* sc = smem + G_T + pq * (32 * 144);
#pragma unroll
      for (int hf = 0; hf < 2; ++hf) {
        u32x2 t[4];
#pragma unroll
        for (int m = 0; m < 4; ++m) t[m] = __builtin_bit_cast(u32x2, tr_read(tb + hf * 8192 + m * 2048));
        u32x4 oa, ob;
#pragma unroll
        for (int m = 0; m < 4; ++m) {
          oa[m] = __builtin_amdgcn_perm(t[m][1], t[m][0], 0x05040100u);
          ob[m] = __builtin_amdgcn_perm(t[m][1], t[m][0], 0x07060302u);
        }
        *(LDS u32x4*)(sc + hf * (16 * 144) + col * 144 + q * 32) = oa;
        *(LDS u32x4*)(sc + hf * (16 * 144) + col * 144 + q * 32 + 16) = ob;
      }
      wave_fence();
      const __amdgpu_buffer_rsrc_t rs = mk_rsrc(p.ws + WS_XT, DM * SL * 2);
#pragma unroll
      for (int u = 0; u < 4; ++u) {
        const int row = (lane >> 3) + 8 * (u & 1), hf = u >> 1;
        const u32x4 v = *(LDS const u32x4*)(sc + hf * (16 * 144) + row * 144 + (lane & 7) * 16);
        __builtin_amdgcn_raw_buffer_store_b128(v, rs, ((64 * g + 16 * pq + row) * SL + 128 * c + 64 * hf) * 2 + (lane & 7) * 16, 0, SC1);
      }
      __builtin_amdgcn_sched_barrier(0);
    }
    read_frags(s);
    __builtin_amdgcn_sched_barrier(0);
    if (HF == 0) { mma(); __builtin_amdgcn_sched_barrier(0); }
  }
  if (HF == 1) mma();
  __builtin_amdgcn_sched_barrier(0);
  STAMP(0, 3); CSTAMP(0, 61);
  const f32x2 Lr = {lam.r, lam.r}, Li = {lam.i, lam.i};
  f32x2 er = {0.f, 0.f}, ei = {0.f, 0.f};
#pragma unroll
  for (int t = 0; t < 8; ++t) {
    const f32x2 br_ = (t & 1) ? hi2(are[t >> 1]) : lo2(are[t >> 1]);
    const f32x2 bi_ = (t & 1) ? hi2(aim[t >> 1]) : lo2(aim[t >> 1]);
    const f32x2 nr = fma2(-Li, ei, fma2(Lr, er, br_));
    ei = fma2(Li, er, fma2(Lr, ei, bi_));
    er = nr;
  }
  const cf eA = {er[0], ei[0]};
  const cf e = cfma(l8, eA, cf{er[1], ei[1]});
  const int seg = 4 * HF + q;
  *(LDS f32x2*)(smem + G_E + (seg * 64 + pl) * 8) = f32x2{e.r, e.i};
  STAMP(0, 4);
  bar();
  STAMP(0, 5);
  {
#pragma unroll
    for (int m = 0; m < 4; ++m) asm volatile("" : "+v"(are[m]), "+v"(aim[m]));
    const f32x2 Cr = {coef2.r, coef2.r}, Ci = {coef2.i, coef2.i};
#pragma unroll
    for (int m = 0; m < 4; ++m) {
      const f32x2 r0 = lo2(are[m]), r1 = hi2(are[m]), i0 = lo2(aim[m]), i1 = hi2(aim[m]);
      are[m] = cat2(fma2(-Ci, i0, Cr * r0), fma2(-Ci, i1, Cr * r1));
      aim[m] = cat2(fma2(-Ci, r0, -Cr * i0), fma2(-Ci, r1, -Cr * i1));
      asm volatile("" : "+v"(are[m]), "+v"(aim[m]));
    }
  }
  cf eAc = cmul(coef2, eA), pwc = cmul(coef2, pw);
  asm volatile("" : "+v"(eAc.r), "+v"(eAc.i), "+v"(pwc.r), "+v"(pwc.i));
  STAMP(0, 7);
  bar();
  STAMP(0, 8);
  cf cin;
  {
    f32x2 v[2];
#pragma unroll
    for (int i = 0; i < 2; ++i) v[i] = *(LDS const f32x2*)(smem + G_X + (i * 64 + pl) * 8);
    const f32x2 pf = *(LDS const f32x2*)(smem + G_PF + (seg * 64 + pl) * 8);
    const f32x2 S = v[0] + v[1];
    cin = cfma(pwc, cf{S[0], S[1]}, cmul(coef2, cf{pf[0], pf[1]}));
  }
  const cf cinB = cfma(l8, cin, eAc);
  er = f32x2{cin.r, cinB.r}; ei = f32x2{-cin.i, -cinB.i};
  f32x2 hr[8], hi[8];
#pragma unroll
  for (int t = 0; t < 8; ++t) {
    const f32x2 br_ = (t & 1) ? hi2(are[t >> 1]) : lo2(are[t >> 1]);
    const f32x2 bi_ = (t & 1) ? hi2(aim[t >> 1]) : lo2(aim[t >> 1]);
    const f32x2 nr = fma2(Li, ei, fma2(Lr, er, br_));
    ei = fma2(-Li, er, fma2(Lr, ei, bi_));
    er = nr;
    hr[t] = er; hi[t] = ei;
  }
  u32x4 o[4];
#pragma unroll
  for (int t = 0; t < 4; ++t) {
    o[0][t] = pack_bf16(hr[2 * t][0], hr[2 * t + 1][0]);
    o[1][t] = pack_bf16(hr[2 * t][1], hr[2 * t + 1][1]);
    o[2][t] = pack_bf16(hi[2 * t][0], hi[2 * t + 1][0]);
    o[3][t] = pack_bf16(hi[2 * t][1], hi[2 * t + 1][1]);
  }
  STAMP(0, 9);
  {
    const __amdgpu_buffer_rsrc_t rs = mk_rsrc(p.ws + WS_HB, 2 * DM * SL * 2);
    const int hb = (((c * 2 + HF) * 2) * 9 + g) * 8192 + pq * 2048;
    const int p0 = (16 * q + col) * 16, p1 = 1024 + (16 * q + (col ^ 4)) * 16;
    __builtin_amdgcn_raw_buffer_store_b128(o[0], rs, hb + p0, 0, SC1);
    __builtin_amdgcn_raw_buffer_store_b128(o[1], rs, hb + p1, 0, SC1);
    __builtin_amdgcn_raw_buffer_store_b128(o[2], rs, hb + 9 * 8192 + p0, 0, SC1);
    __builtin_amdgcn_raw_buffer_store_b128(o[3], rs, hb + 9 * 8192 + p1, 0, SC1);
    wait_vm<0>();
    STAMP(0, 10);
    if (lane < 8)
      __hip_atomic_store((GLB u64*)(p.ws + WS_IN) + tile_block(2 * c + HF, lane) * 64 + IN_H + g * 4 + pq, FLAG_SET, RLX_AGENT);
    bar();
  }
}

constexpr int S_CPY = 4160;
constexpr int S_X = 0;
constexpr int S_ST = 6 * 4 * S_CPY;
constexpr int S_Z = S_ST;
constexpr int S_ZSZ = (256 + SL) * 2;
constexpr int S_T = S_ST + 12 * 4608;
constexpr int S_TSZ = 1312;
constexpr int S_END = S_T + 6 * S_TSZ;
constexpr int S_CNT = 32768 + 8192;

DEV c2 cexp_k(float zl, float th, float tl, float k) {
  const float mag = __builtin_amdgcn_exp2f(zl * k);
  const float t = fmaf(tl, k, __builtin_amdgcn_fractf(th * k));
  return c2{mag * __builtin_amdgcn_cosf(t), mag * __builtin_amdgcn_sinf(t)};
}
#define PKM(d, a, b) "v_pk_mul_f32 " d ", " a ", " b " op_sel:[0,0] op_sel_hi:[0,1]\n\t"
#define PKF(d, a, b) "v_pk_fma_f32 " d ", " a ", " b ", " d " op_sel:[1,1,0] op_sel_hi:[1,0,1] neg_lo:[1,0,0]\n\t"
DEV void octet(LDS char* dst, c2 s0, c2 m1, c2 m2) {
  c2 v1, v2, v3, t0, t1, t2, t3;
  asm(PKM("%0", "%7", "%8") PKM("%3", "%7", "%9") PKF("%0", "%7", "%8") PKF("%3", "%7", "%9")
      PKM("%1", "%0", "%8") PKM("%4", "%0", "%9") PKF("%1", "%0", "%8") PKF("%4", "%0", "%9")
      PKM("%2", "%1", "%8") PKM("%5", "%1", "%9") PKF("%2", "%1", "%8") PKF("%5", "%1", "%9")
      PKM("%6", "%2", "%9") PKF("%6", "%2", "%9")
      : "=&v"(v1), "=&v"(v2), "=&v"(v3), "=&v"(t0), "=&v"(t1), "=&v"(t2), "=&v"(t3) : "v"(s0), "v"(m1), "v"(m2));
  *(LDS u32*)(dst + 0 * 288) = pack_bf16(s0[0], s0[1]);
  *(LDS u32*)(dst + 1 * 288) = pack_bf16(v1[0], v1[1]);
  *(LDS u32*)(dst + 2 * 288) = pack_bf16(v2[0], v2[1]);
  *(LDS u32*)(dst + 3 * 288) = pack_bf16(v3[0], v3[1]);
  *(LDS u32*)(dst + 4 * 288) = pack_bf16(t0[0], t0[1]);
  *(LDS u32*)(dst + 5 * 288) = pack_bf16(t1[0], t1[1]);
  *(LDS u32*)(dst + 6 * 288) = pack_bf16(t2[0], t2[1]);
  *(LDS u32*)(dst + 7 * 288) = pack_bf16(t3[0], t3[1]);
}
DEV u32 dup_lo(u32 a) { return __builtin_amdgcn_perm(a, a, 0x01000100u); }
DEV u32 dup_hi(u32 a) { return __builtin_amdgcn_perm(a, a, 0x03020302u); }
DEV u32 hi_lo(u32 a, u32 b) { return __builtin_amdgcn_alignbit(b, a, 16); }

DEV void role_s(const P& p, LDS char* smem, int sg, int w, int lane) {
  const int tid = w * 64 + lane;
  const int ch = w >> 1, hh = w & 1, d = 6 * sg + ch;
  const int n = d * 128 + 64 * hh + lane;
  const u16 r_ldt = p.ldtA[d], r_la = p.la[n], r_aim = p.aim[n], r_cre = p.creA[n], r_cim = p.cimA[n];
  u32 wb[2] = {0, 0};
  __builtin_amdgcn_sched_barrier(0);
#pragma unroll
  for (int i = 0; i < 2; ++i) {
    const int ln = i * 768 + tid, all = (sg % 12) * 864 + ln;
    if (ln < 864) wb[i] = *(const GLB u32*)((const char*)(all < 5184 ? p.bre : p.bim) + (size_t)(all < 5184 ? all : all - 5184) * 128);
  }
  __builtin_amdgcn_sched_barrier(0);
  STAMP(0, 1);
  f32x4 tab[3];
  [[maybe_unused]] u32 wc[2] = {0, 0};
  LDS char* st = smem + S_ST + w * 4608;
  {
    const float L2E = 1.44269504088896341f;
    const float dt = rbf(__builtin_amdgcn_exp2f(bf2f(r_ldt) * L2E));
    const float ar = -rbf(__builtin_amdgcn_exp2f(bf2f(r_la) * L2E));
    const float zl = ar * dt * L2E, zi = bf2f(r_aim) * dt;
    const float th = zi * 0.15915494309189535f;
    const float tl = fmaf(zi, 0.15915494309189535f, -th) + zi * 4.6017384e-10f;
    const c2 C = {2.0f * (bf2f(r_cre) * dt), 2.0f * (bf2f(r_cim) * dt)};
    const c2 w1 = cexp_k(zl, th, tl, 1.f), w8 = cexp_k(zl, th, tl, 8.f), w128 = cexp_k(zl, th, tl, 128.f);
    c2 w2, w4, w16, w32, w64, w256, w512, w1024, w2048;
    asm(PKM("%0", "%9", "%9") PKM("%2", "%10", "%10") PKM("%5", "%11", "%11") PKF("%0", "%9", "%9") PKF("%2", "%10", "%10") PKF("%5", "%11", "%11")
        PKM("%1", "%0", "%0") PKM("%3", "%2", "%2") PKM("%6", "%5", "%5") PKF("%1", "%0", "%0") PKF("%3", "%2", "%2") PKF("%6", "%5", "%5")
        PKM("%4", "%3", "%3") PKM("%7", "%6", "%6") PKF("%4", "%3", "%3") PKF("%7", "%6", "%6")
        PKM("%8", "%7", "%7") PKF("%8", "%7", "%7")
        : "=&v"(w2), "=&v"(w4), "=&v"(w16), "=&v"(w32), "=&v"(w64), "=&v"(w256), "=&v"(w512), "=&v"(w1024), "=&v"(w2048)
        : "v"(w1), "v"(w8), "v"(w128));
    c2 C128, C256, C512, C1024, C2048;
    asm(PKM("%0", "%5", "%6") PKM("%1", "%5", "%7") PKM("%2", "%5", "%8") PKM("%3", "%5", "%9") PKM("%4", "%5", "%10")
        PKF("%0", "%5", "%6") PKF("%1", "%5", "%7") PKF("%2", "%5", "%8") PKF("%3", "%5", "%9") PKF("%4", "%5", "%10")
        : "=&v"(C128), "=&v"(C256), "=&v"(C512), "=&v"(C1024), "=&v"(C2048)
        : "v"(C), "v"(w128), "v"(w256), "v"(w512), "v"(w1024), "v"(w2048));
#pragma unroll
    for (int i = 0; i < 2; ++i) {
      const int ln = i * 768 + tid, all = (sg % 12) * 864 + ln;
      if (ln < 864) wc[i] = *(const GLB u32*)((const char*)(all < 5184 ? p.cre : p.cim) + (size_t)(all < 5184 ? all : all - 5184) * 128);
    }
    STAMP(0, 2);
    LDS char* wr = st + 4 * lane;
    LDS const char* rd = st + (lane & 15) * 288 + (lane >> 4) * 16;
    bf16x8 B[4];
    const c2 w8c = {w8[0], -w8[1]}, w32c = {w32[0], -w32[1]}, w64c = {w64[0], -w64[1]};
    octet(wr, c2{1.f, 0.f}, w8c, w32c);
    octet(wr + 8 * 288, w64c, w8c, w32c);
    wave_fence();
#pragma unroll
    for (int ks = 0; ks < 4; ++ks) B[ks] = lds_frag(rd + 64 * ks);
    wave_fence();
#pragma unroll
    for (int mt = 0; mt < 3; ++mt) {
      if (mt == 0) { octet(wr, C, w1, w4); octet(wr + 8 * 288, C128, w1, w4); }
      if (mt == 1) { octet(wr, C256, w2, w128); octet(wr + 8 * 288, C512, w128, w4); }
      if (mt == 2) { octet(wr, C1024, w128, w512); *(LDS u32*)(wr + 8 * 288) = pack_bf16(C2048[0], C2048[1]); }
      wave_fence();
      f32x4 acc = {0, 0, 0, 0};
      bf16x8 A[4];
#pragma unroll
      for (int ks = 0; ks < 4; ++ks) A[ks] = lds_frag(rd + 64 * ks);
      __builtin_amdgcn_sched_barrier(0);
#pragma unroll
      for (int ks = 0; ks < 4; ++ks) acc = mfma16(A[ks], B[ks], acc);
      __builtin_amdgcn_sched_barrier(0);
      wave_fence();
      tab[mt] = acc;
    }
  }
  const int b = lane & 15, q = lane >> 4;
#pragma unroll
  for (int mt = 0; mt < 3; ++mt) *(LDS f32x4*)(st + mt * 1024 + lane * 16) = tab[mt];
  STAMP(0, 3);
  bar();
  {
    LDS const char* ot = smem + S_ST + (w ^ 1) * 4608 + lane * 16;
    LDS char* T = smem + S_T + ch * S_TSZ;
    if (hh == 0) {
      const f32x4 v0 = tab[0] + *(LDS const f32x4*)(ot), v2 = tab[2] + *(LDS const f32x4*)(ot + 2048);
      *(LDS u32x2*)(T + 2 * (128 * (q >> 1) + 8 * b + 4 * (q & 1))) = u32x2{pack_bf16(v0[0], v0[1]), pack_bf16(v0[2], v0[3])};
      const u32 a0 = pack_bf16(v2[0], v2[1]), a1 = pack_bf16(v2[2], v2[3]);
      if (q < 2) {
        LDS char* t3 = T + 2 * (512 + 64 * q + b);
        *(LDS u16*)(t3) = (u16)a0; *(LDS u16*)(t3 + 32) = (u16)(a0 >> 16);
        *(LDS u16*)(t3 + 64) = (u16)a1; *(LDS u16*)(t3 + 96) = (u16)(a1 >> 16);
      } else if (lane == 32) *(LDS u16*)(T + 2 * 640) = (u16)a0;
    } else {
      const f32x4 v1 = tab[1] + *(LDS const f32x4*)(ot + 1024);
      const u32 a0 = pack_bf16(v1[0], v1[1]), a1 = pack_bf16(v1[2], v1[3]);
      if (q < 2) *(LDS u32x2*)(T + 2 * (256 + 64 * q + 4 * b)) = u32x2{a0, a1};
      else {
        LDS char* t2 = T + 2 * (384 + 2 * b + (q - 2));
        *(LDS u16*)(t2) = (u16)a0; *(LDS u16*)(t2 + 64) = (u16)(a0 >> 16);
        *(LDS u16*)(t2 + 128) = (u16)a1; *(LDS u16*)(t2 + 192) = (u16)(a1 >> 16);
      }
    }
  }
  bar();
  STAMP(0, 4);
  u32x4 uv[2];
  {
    GLB u64* fl = (GLB u64*)(p.ws + WS_FL) + (8 * hh + (lane & 7)) * DM + d;
    asm volatile("" :: "v"(wc[0]), "v"(wc[1]), "v"(wb[0]), "v"(wb[1]));
    wait_flags(fl, true);
    const __amdgpu_buffer_rsrc_t rs = mk_rsrc(p.ws + WS_XT, DM * SL * 2);
#pragma unroll
    for (int v = 0; v < 2; ++v) uv[v] = __builtin_amdgcn_raw_buffer_load_b128(rs, (d * SL + 1024 * hh + 8 * (lane + 64 * v)) * 2, 0, SC1);
    if (lane < 8) __hip_atomic_store(fl, 0ull, RLX_AGENT);
  }
  STAMP(0, 5);
  {
    LDS const char* T = smem + S_T + ch * S_TSZ;
    u32 o[10];
    const u32 e512 = *(LDS const u16*)(T + 2 * 512);
    o[8] = o[9] = e512 | (e512 << 16);
    if (hh == 0) {
      const int off = lane < 16 ? 32 * lane : (lane < 32 ? 256 + 16 * lane : 512 + 8 * lane);
      u32x2 W[4];
#pragma unroll
      for (int i = 0; i < 4; ++i) W[i] = *(LDS const u32x2*)(T + off + 8 * i);
      if (lane < 16) {
#pragma unroll
        for (int i = 0; i < 8; ++i) o[i] = W[i >> 1][i & 1];
      } else if (lane < 32) {
#pragma unroll
        for (int i = 0; i < 4; ++i) {
          const u32 a = W[i >> 1][i & 1], nx = W[(i + 1) >> 1][(i + 1) & 1];
          o[2 * i] = dup_lo(a);
          o[2 * i + 1] = hi_lo(a, nx);
        }
      } else {
        const u32 w0 = W[0][0], w1 = W[0][1], w2 = W[1][0];
        o[0] = dup_lo(w0); o[1] = w0; o[2] = dup_hi(w0); o[3] = dup_lo(w1);
        o[4] = o[3]; o[5] = w1; o[6] = dup_hi(w1); o[7] = dup_lo(w2);
      }
    } else {
      const u32 w0 = *(LDS const u32*)(T + 1024 + 4 * lane), w1 = *(LDS const u32*)(T + 1028 + 4 * lane);
      o[0] = o[1] = dup_lo(w0); o[2] = w0; o[3] = o[4] = o[5] = dup_hi(w0); o[6] = o[7] = dup_lo(w1);
    }
    o[8] = (u32)__builtin_amdgcn_update_dpp((int)o[8], (int)o[0], 0x130, 0xf, 0xf, false);
    o[9] = (u32)__builtin_amdgcn_update_dpp((int)o[9], (int)o[1], 0x130, 0xf, 0xf, false);
    u32 a[9];
#pragma unroll
    for (int i = 0; i < 9; ++i) a[i] = hi_lo(o[i], o[i + 1]);
    LDS char* xb = smem + S_X + ch * 4 * S_CPY + 32 + 2 * (1024 * hh + 16 * lane);
    *(LDS u32x4*)(xb) = u32x4{o[0], o[1], o[2], o[3]};
    *(LDS u32x4*)(xb + 16) = u32x4{o[4], o[5], o[6], o[7]};
    *(LDS u32x4*)(xb + S_CPY) = u32x4{a[0], a[1], a[2], a[3]};
    *(LDS u32x4*)(xb + S_CPY + 16) = u32x4{a[4], a[5], a[6], a[7]};
    *(LDS u32x4*)(xb + 2 * S_CPY) = u32x4{o[1], o[2], o[3], o[4]};
    *(LDS u32x4*)(xb + 2 * S_CPY + 16) = u32x4{o[5], o[6], o[7], o[8]};
    *(LDS u32x4*)(xb + 3 * S_CPY) = u32x4{a[1], a[2], a[3], a[4]};
    *(LDS u32x4*)(xb + 3 * S_CPY + 16) = u32x4{a[5], a[6], a[7], a[8]};
    if (hh == 0 && lane == 0) {
      LDS char* hb = smem + S_X + ch * 4 * S_CPY;
      const u32x4 z = {0, 0, 0, 0};
      *(LDS u32x4*)(hb) = z;
      *(LDS u32x4*)(hb + 16) = z;
      *(LDS u32x4*)(hb + S_CPY) = z;
      *(LDS u32x4*)(hb + S_CPY + 16) = u32x4{0, 0, 0, o[0] << 16};
      *(LDS u32x4*)(hb + 2 * S_CPY) = z;
      *(LDS u32x4*)(hb + 2 * S_CPY + 16) = u32x4{0, 0, 0, o[0]};
      *(LDS u32x4*)(hb + 3 * S_CPY) = z;
      *(LDS u32x4*)(hb + 3 * S_CPY + 16) = u32x4{0, 0, o[0] << 16, a[0]};
    }
    if (tid < 6 * 32) *(LDS u32x4*)(smem + S_Z + (tid >> 5) * S_ZSZ + (tid & 31) * 16) = u32x4{0, 0, 0, 0};
    STAMP(0, 6);
    LDS char* z = smem + S_Z + ch * S_ZSZ + 512 + (1024 * hh + 8 * lane) * 2;
    *(LDS u32x4*)z = uv[0];
    *(LDS u32x4*)(z + 1024) = uv[1];
  }
  bar();
  STAMP(0, 7);
  f32x4 acc[8];
#pragma unroll
  for (int i = 0; i < 8; ++i) acc[i] = f32x4{0, 0, 0, 0};
  {
    const int kq = q, qq = b >> 2, pp = b & 3;
    LDS const char* fb = smem + S_X + (ch * 4 + ((4 - qq) & 3)) * S_CPY +
                         2 * (16 + 16 * (kq >> 1) - 8 * (kq & 1) + 4 * pp - (qq ? 4 : 0)) + 64 * hh;
    LDS const char* gb = smem + S_Z + ch * S_ZSZ + 512 + 32 * (b - (kq >> 1)) + 16 * (kq & 1) - 64 * hh;
#pragma unroll 1
    for (int it = 0; it < 4; ++it) {
      bf16x8 F[8], G[8];
#pragma unroll
      for (int s = 0; s < 8; ++s) {
        F[s] = tr_frag(fb + 512 * s, fb + 512 * s - 8);
        G[s] = lds_frag(gb + 512 * s);
      }
      __builtin_amdgcn_sched_barrier(0);
#pragma unroll
      for (int t = 0; t < 8; ++t)
#pragma unroll
        for (int s = 0; s <= t; ++s) acc[t] = mfma16(F[s], G[t - s], acc[t]);
      fb += 128;
      gb -= 128;
    }
  }
  __builtin_amdgcn_sched_barrier(0);
  STAMP(0, 8);
  bar();
  {
    LDS char* ex = smem + S_X + 49152 + w * 4096 + lane * 16;
#pragma unroll
    for (int i = 0; i < 4; ++i) {
      const f32x4 lo = acc[i], hi = acc[4 + i];
      *(LDS f32x4*)(ex + i * 1024) = hh ? +lo : +hi;
    }
    if (tid == 0) *(LDS u32*)(smem + S_CNT) = 0;
  }
  bar();
  u32x2 yo[4];
  {
    LDS const char* ey = smem + S_X + 49152 + (w ^ 1) * 4096 + lane * 16;
#pragma unroll
    for (int i = 0; i < 4; ++i) {
      const f32x4 lo = acc[i], hi = acc[4 + i];
      const f32x4 y = (hh ? +hi : +lo) + *(LDS const f32x4*)(ey + i * 1024);
      yo[i] = u32x2{pack_bf16(y[0], y[1]), pack_bf16(y[2], y[3])};
    }
  }
  bar();
  {
    const __amdgpu_buffer_rsrc_t rs = mk_rsrc(p.ws + WS_AT, DM * SL * 2);
    const int dst = (d * SL + 1024 * hh + 16 * (lane & 15) + 4 * (lane >> 4)) * 2;
#pragma unroll
    for (int i = 0; i < 4; ++i) __builtin_amdgcn_raw_buffer_store_b64(yo[i], rs, dst + 512 * i, 0, SC1);
  }
  STAMP(0, 9);
}
DEV void s_signal(const P& p, LDS char* smem, int sg, int lane) {
  u32 old = 0;
  if (lane == 0) old = __hip_atomic_fetch_add((LDS u32*)(smem + S_CNT), 1u, __ATOMIC_RELAXED, __HIP_MEMORY_SCOPE_WORKGROUP);
  if (__builtin_amdgcn_readfirstlane(old) == 11) {
    const int lt = lane & 31, second = lane >> 5;
    const int dd = 6 * sg + (second ? 5 : 0);
    const int idx = dd < 480 ? dd / 96 : 5 + (dd - 480) / 32;
    const int dfirst = idx < 5 ? 96 * idx : 480 + 32 * (idx - 5);
    __hip_atomic_store((GLB u64*)(p.ws + WS_IN) + tile_block(lt, idx) * 64 + IN_A + (sg - dfirst / 6), FLAG_SET, RLX_AGENT);
  }
}

DEV void role_gate(const P& p, int rb, int w, int lane, u64 pc) {
  u32 pf0 = 0, pf1 = 0;
  const int tid = w * 64 + lane;
  const int sub = lane & 7;
  u32x4 wv[9];
#pragma unroll
  for (int i = 0; i < 9; ++i) wv[i] = *(const u32x4*)((const char*)p.gw + (i * 8 + sub) * 16);
  for (int r = w * 8 + (lane >> 3); r < 128; r += 96) {
    const int l = rb * 128 + r;
    float s = 0.f;
    u32x4 xv[9];
#pragma unroll
    for (int i = 0; i < 9; ++i) xv[i] = *(const u32x4*)((const char*)p.x + (size_t)l * 1152 + (i * 8 + sub) * 16);
#pragma unroll
    for (int i = 0; i < 9; ++i) {
#pragma unroll
      for (int k = 0; k < 4; ++k) {
        const u32 pr = pack_bf16(bflo(xv[i][k]) * bflo(wv[i][k]), bfhi(xv[i][k]) * bfhi(wv[i][k]));
        s += bflo(pr);
        s += bfhi(pr);
      }
    }
    s += __shfl_xor(s, 1); s += __shfl_xor(s, 2); s += __shfl_xor(s, 4);
    const float gv = rbf(1.0f / (1.0f + expf(-rbf(s))));
    if (sub == 0)
      __hip_atomic_store((GLB u64*)(p.ws + WS_G) + l, ((u64)__float_as_uint(rbf(1.0f - gv)) << 32) | __float_as_uint(gv), RLX_AGENT);
  }
  asm volatile("" :: "v"(pf0), "v"(pf1));
  wait_vm<0>();
  bar();
  if (w == 0 && lane < 16)
    __hip_atomic_store((GLB u64*)(p.ws + WS_IN) + tile_block(2 * rb + (lane >> 3), lane & 7) * 64 + IN_G, FLAG_SET, RLX_AGENT);
}

constexpr int H_C = 32768, H_CS = 12288;
DEV int h_slot(int i) { return i < 4 ? i * 8192 : 131072 + (i - 4) * 8192; }
constexpr int H_AT = H_C + 2 * H_CS;
constexpr int H_XT = H_C + 3 * H_CS;
constexpr int H_G = H_C + 4 * H_CS;
constexpr int H_OUT = H_C + 5 * H_CS;
constexpr int K_LDS = 163840;

constexpr int h_wait(int D, int NDT) {
  const int G = 4 + 2 * NDT;
  switch (D) {
    case 0: return 4;
    case 1: return 8;
    case 2: return 4 + G;
    case 3: case 4: case 5: return 2 * G + 1;
    case 6: return 2 * G;
    case 7: return G + 2 * NDT;
    default: return 0;
  }
}
template <int D, int NDT, class F> DEV void h_steps(F&& after, u64& fv, char* wsp) {
  if constexpr (D == 6) asm volatile("s_waitcnt vmcnt(%1)" : "+v"(fv) : "n"(h_wait(D, NDT)) : "memory");
  else wait_vm<h_wait(D, NDT)>();
  LSTAMP2(1, 16 + D);
  bar();
  after(std::integral_constant<int, D>{});
  if constexpr (D < 8) h_steps<D + 1, NDT>(after, fv, wsp);
}
DEV int p2_rot(int d0) { const int idx = d0 < 480 ? d0 / 96 : 5 + (d0 - 480) / 32; return (idx * 18) >> 3; }

template <int NDT, class F>
DEV void p2_loader(const P& p, LDS char* smem, int lt, int d0, int lw, int lane, bool scratch_barrier, F&& pre_poll) {
  __builtin_amdgcn_s_setprio(3);
  const char* hb = p.ws + WS_HB + (size_t)lt * 18 * 8192 + (2 * lw) * 1024 + lane * 16;
  constexpr int W = 32 * NDT;
  const int l0 = 64 * lt, rot = p2_rot(d0);
  u32 co[NDT];
#pragma unroll
  for (int i = 0; i < NDT; ++i) {
    const int row = 8 * (NDT * lw + i) + (lane >> 3), cp = lane & 7;
    co[i] = (u32)(d0 + row) * 1152u + ((cp ^ (row & 7)) << 4);
  }
  auto issue_c = [&](int s) {
    LDS char* buf = smem + H_C + (s & 7) * H_CS + NDT * lw * 1024;
    int ks = s + rot; ks = ks >= 18 ? ks - 18 : ks;
    const char* cb = ks < 9 ? (const char*)p.cre + ks * 128 : (const char*)p.cim + (ks - 9) * 128;
#pragma unroll
    for (int i = 0; i < NDT; ++i) dma16<0>(cb + co[i], buf + i * 1024);
  };
  auto issue_h = [&](int s) {
    LDS char* buf = smem + h_slot(s & 7) + 2 * lw * 1024;
    int ks = s + rot; ks = ks >= 18 ? ks - 18 : ks;
#pragma unroll
    for (int i = 0; i < 2; ++i) dma16<SC1>(hb + ks * 8192 + i * 1024, buf + i * 1024);
  };
  GLB u64* inb = (GLB u64*)(p.ws + WS_IN) + blockIdx.x * 64;
  const int nA = (d0 + W - 1) / 6 - d0 / 6 + 1;
  const bool need = lane == IN_G || (lane >= IN_A && lane < IN_A + nA);
  if (!scratch_barrier) {
#pragma unroll 1
    for (int s = 0; s < 8; ++s) issue_c(s);
  }
  pre_poll();
  { [[maybe_unused]] const int w = lw + 8; STAMP(1, 1); }
  if (scratch_barrier) bar();
  wait_flags(inb + IN_H + (lane < 36 ? lane : 0), true);
  { [[maybe_unused]] const int w = lw + 8; STAMP(1, 2); }
#pragma unroll
  for (int s = 0; s < 4; ++s) issue_h(s);
  { [[maybe_unused]] const int w = lw + 8; STAMP(1, 3); }
  u64 fv = 0;
  h_steps<0, NDT>([&](auto D_) {
    constexpr int D = decltype(D_)::value;
    if constexpr (D == 0) { issue_h(4); issue_h(5); issue_h(6); issue_h(7); }
    if constexpr (D >= 1 && D <= 5) { issue_h(2 * D + 6); issue_h(2 * D + 7); issue_c(2 * D + 6); issue_c(2 * D + 7); }
    if constexpr (D == 2)
      asm volatile("global_load_dwordx2 %0, %1, off sc1" : "=v"(fv) : "v"(inb + lane) : "memory");
    if constexpr (D == 6) {
      if (!__all(!need || fv == FLAG_SET)) wait_flags(inb + lane, need);
#pragma unroll
      for (int i = 0; i < NDT; ++i) {
        const int pc = NDT * lw + i;
        dma16<SC1>(p.ws + WS_AT + (size_t)(d0 + 8 * pc + (lane >> 3)) * 4096 + l0 * 2 + (lane & 7) * 16, smem + H_AT + pc * 1024);
      }
#pragma unroll
      for (int i = 0; i < NDT; ++i) {
        const int pc = NDT * lw + i, id = pc * 64 + lane, row = id / (4 * NDT), qq = id - row * (4 * NDT);
        dma16<0>((const char*)p.x + (size_t)(l0 + row) * 1152 + d0 * 2 + qq * 16, smem + H_XT + pc * 1024);
      }
    }
    if constexpr (D == 7) { if (lane < 8) dma16<SC1>(p.ws + WS_G + (size_t)l0 * 8 + lw * 128 + lane * 16, smem + H_G + lw * 128); }
  }, fv, p.ws);
}

template <int NDT>
DEV void p2_compute(const P& p, LDS char* smem, int lt0, int d0, int w, int lane, const u32x2 (&dsk)[6], u64 g_tb) {
  const int lh = (w >> 1) & 1, kh = w & 1, dg = w >> 2;
  const int r = lane & 15, kq = lane >> 4;
  const int lt = 2 * lh + kh, l0 = 64 * lt0;
  constexpr int W = 32 * NDT;
  u32x2 da[NDT], db[NDT];
#pragma unroll
  for (int t = 0; t < NDT; ++t) {
    const int d = d0 + 16 * (NDT * dg + t) + 4 * kq;
    da[t] = *(const u32x2*)(p.dskA + d);
    db[t] = *(const u32x2*)(p.dskB + d);
  }
  GLB u64* inb = (GLB u64*)(p.ws + WS_IN) + blockIdx.x * 64;

  f32x4 acc[2][NDT];
#pragma unroll
  for (int i = 0; i < 2; ++i)
#pragma unroll
    for (int t = 0; t < NDT; ++t) acc[i][t] = f32x4{0, 0, 0, 0};
  int ho[2];
#pragma unroll
  for (int u = 0; u < 2; ++u) {
    const int k = 32 * kh + 8 * kq + 4 * u + (r >> 2), i = (r & 3) >> 1;
    ho[u] = (2 * (k >> 4) + i) * 1024 + (32 * lh + ((k & 15) ^ (4 * i))) * 16 + (r & 1) * 8;
  }
  const int co = H_C + (16 * NDT * dg + r) * 128 + (((4 * kh + kq) ^ (r & 7)) << 4);
  bf16x8 hb[2][2][2], ca[2][2][NDT];
  auto rd = [&](auto SET_, int D) {
    constexpr int SET = decltype(SET_)::value;
#pragma unroll
    for (int u = 0; u < 2; ++u) {
      const int s = 2 * D + u;
      LDS const char* hbuf = smem + h_slot(s & 7);
#pragma unroll
      for (int i = 0; i < 2; ++i) hb[SET][u][i] = tr_frag(hbuf + ho[0] + i * 256, hbuf + ho[1] + i * 256);
      LDS const char* cbuf = smem + (s & 7) * H_CS;
#pragma unroll
      for (int t = 0; t < NDT; ++t) ca[SET][u][t] = lds_frag(cbuf + co + t * 2048);
    }
    __builtin_amdgcn_sched_barrier(0);
  };
  auto mma = [&](auto SET_) {
    constexpr int SET = decltype(SET_)::value;
#pragma unroll
    for (int u = 0; u < 2; ++u)
#pragma unroll
      for (int t = 0; t < NDT; ++t) {
        acc[0][t] = mfma16(ca[SET][u][t], hb[SET][u][0], acc[0][t]);
        acc[1][t] = mfma16(ca[SET][u][t], hb[SET][u][1], acc[1][t]);
      }
    __builtin_amdgcn_sched_barrier(0);
  };
  using S0 = std::integral_constant<int, 0>;
  using S1 = std::integral_constant<int, 1>;
  STAMP(1, 1);
  HOLD(4);
  bar();
  STAMP(1, 2);
  rd(S0{}, 0);
#pragma unroll 1
  for (int D = 1; D < 9; D += 2) {
    bar(); rd(S1{}, D); mma(S0{});
    bar(); rd(S0{}, D + 1); mma(S1{});
  }
  mma(S0{});
  __builtin_amdgcn_sched_barrier(0);
  STAMP(1, 3);
  HOLD(5);
  bar();
  if (w == 0) __hip_atomic_store(inb + lane, 0ull, RLX_AGENT);
  LDS char* ex = smem + w * (NDT * 1024) + lane * 16;
#pragma unroll
  for (int t = 0; t < NDT; ++t) {
    const f32x4 a0 = acc[0][t], a1 = acc[1][t];
    *(LDS f32x4*)(ex + t * 1024) = kh ? +a0 : +a1;
  }
  bar();
  LDS const char* ey = smem + (w ^ 1) * (NDT * 1024) + lane * 16;
  constexpr int OSTR = W * 4 + 16;
  const f32x2 gg = *(LDS const f32x2*)(smem + H_G + (16 * lt + r) * 8);
#pragma unroll
  for (int t = 0; t < NDT; ++t) {
    const int dt = NDT * dg + t;
    const f32x4 o = *(LDS const f32x4*)(ey + t * 1024);
    const f32x4 a0 = acc[0][t], a1 = acc[1][t];
    const f32x4 y = (kh ? +a1 : +a0) + o;
    const s16x4 av = tr_read(smem + H_AT + (16 * dt + 4 * kq + (r >> 2)) * 128 + lt * 32 + (r & 3) * 8);
    const u32x2 xv = *(LDS const u32x2*)(smem + H_XT + (16 * lt + r) * (W * 2) + (16 * dt + 4 * kq) * 2);
    f32x4 res;
#pragma unroll
    for (int i = 0; i < 4; ++i) {
      const u32 xw = xv[i >> 1], aw = da[t][i >> 1], bw = db[t][i >> 1];
      const float xf = (i & 1) ? bfhi(xw) : bflo(xw);
      const float sa = (i & 1) ? bfhi(aw) : bflo(aw);
      const float sb = (i & 1) ? bfhi(bw) : bflo(bw);
      const float yc = bf2f((u16)av[i]);
      const float a = rbf(yc + rbf(xf * sa));
      const float b = y[i] + rbf(xf * sb);
      res[i] = rbf(gg[0] * a) + gg[1] * b;
    }
    *(LDS f32x4*)(smem + H_OUT + (16 * lt + r) * OSTR + (16 * dt + 4 * kq) * 4) = res;
  }
  STAMP(1, 4);
  bar();
#pragma unroll
  for (int it = 0; it < NDT; ++it) {
    const int id = it * 512 + w * 64 + lane;
    const int row = id / (8 * NDT), pc = id - row * (8 * NDT);
    const f32x4 v = *(LDS const f32x4*)(smem + H_OUT + row * OSTR + pc * 16);
    __builtin_amdgcn_raw_buffer_store_b128(__builtin_bit_cast(u32x4, v), mk_rsrc(p.out, SL * DM * 4), ((l0 + row) * DM + d0 + pc * 4) * 4, 0, OUT_AUX);
  }
}

template <int NDT>
DEV void phase2(const P& p, LDS char* smem, int lt, int d0, int w, int lane, bool scratch_barrier, int sg, const u32x2 (&dsk)[6], u64 g_tb) {
  STAMP(1, 0);
  if (w >= 8) {
    p2_loader<NDT>(p, smem, lt, d0, w - 8, lane, scratch_barrier, [&]() { if (NDT == 1) { wait_vm<8>(); s_signal(p, smem, sg, lane); } });
  } else {
    if (NDT == 1) { wait_vm<0>(); s_signal(p, smem, sg, lane); }
    p2_compute<NDT>(p, smem, lt, d0, w, lane, dsk, g_tb);
  }
  STAMP(1, 15);
}

__global__ __launch_bounds__(768, 3) void gated_mix_s4d_s5(P p) {
  extern __shared__ __attribute__((aligned(16))) char smem_raw[];
  LDS char* smem = (LDS char*)smem_raw;
  u64 pc;
  asm volatile("s_getpc_b64 %0" : "=s"(pc));
  u32 pf = 0;
  const u64 g_tb = __builtin_amdgcn_s_memrealtime();
#define PF_TOP 312
  if (threadIdx.x < PF_TOP && blockIdx.x >= 144)
    asm volatile("global_load_dword %0, %1, off" : "=v"(pf) : "v"((pc & ~255ull) + threadIdx.x * 128));
  const int w = __builtin_amdgcn_readfirstlane(threadIdx.x >> 6), lane = threadIdx.x & 63;
  const int xcd = blockIdx.x & 7, j = blockIdx.x >> 3;
  u32x2 dsk[6];
#pragma unroll
  for (int i = 0; i < 6; ++i) dsk[i] = u32x2{0, 0};
  STAMP(0, 0);
  if (j >= 18 && j < 30) {
    const int sg = 12 * xcd + (j - 18);
    role_s(p, smem, sg, w, lane);
    STAMP(0, 15);
    phase2<1>(p, smem, 4 * xcd + (j - 18) / 3, 480 + 32 * ((j - 18) % 3), w, lane, false, sg, dsk, g_tb);
  } else {
    const bool isg = j < 18;
    if (isg) {
      const int c = 2 * xcd + j / 9, g = j % 9;
      if (w >= 8) g_loader(p, smem, c, g, 96 * (j % 5), w - 8, lane);
      else if (w < 4) g_compute<0>(p, smem, c, g, w, lane, pf);
      else g_compute<1>(p, smem, c, g, w, lane, pf);
    } else {
      role_gate(p, 2 * xcd + (j - 30), w, lane, pc);
    }
    STAMP(0, 15);
    const int e = isg ? j : j - 12;
    phase2<3>(p, smem, 4 * xcd + e / 5, 96 * (e % 5), w, lane, isg, 0, dsk, g_tb);
  }
  asm volatile("s_waitcnt vmcnt(0)" :: "v"(pf));
}

extern "C" void kernel_launch(void* const* d_in, const int* in_sizes, int n_in, void* d_out, int out_size,
                              void* d_ws, size_t ws_size, hipStream_t stream) {
  P a;
  a.x = (const u16*)d_in[0]; a.la = (const u16*)d_in[1]; a.aim = (const u16*)d_in[2];
  a.creA = (const u16*)d_in[3]; a.cimA = (const u16*)d_in[4]; a.ldtA = (const u16*)d_in[5]; a.dskA = (const u16*)d_in[6];
  a.lre = (const u16*)d_in[8]; a.lim = (const u16*)d_in[9]; a.bre = (const u16*)d_in[10]; a.bim = (const u16*)d_in[11];
  a.cre = (const u16*)d_in[12]; a.cim = (const u16*)d_in[13]; a.ldtB = (const u16*)d_in[14];
  a.dskB = (const u16*)d_in[15]; a.gw = (const u16*)d_in[16];
  a.ws = (char*)d_ws + WS_OFF; a.out = (float*)d_out;
  (void)hipFuncSetAttribute((const void*)gated_mix_s4d_s5, hipFuncAttributeMaxDynamicSharedMemorySize, K_LDS);
  gated_mix_s4d_s5<<<256, 768, K_LDS, stream>>>(a);
}
```

```cpp
#define PF_NOT_G 1
#include <hip/hip_runtime.h>
#include <hip/hip_bf16.h>
#include <stdint.h>
#include <type_traits>

typedef unsigned short u16;
typedef unsigned int u32;
typedef unsigned long long u64;
typedef __attribute__((ext_vector_type(8))) __bf16 bf16x8;
typedef __attribute__((ext_vector_type(2))) __bf16 bf16x2;
typedef __attribute__((ext_vector_type(4))) short s16x4;
typedef __attribute__((ext_vector_type(4))) float f32x4;
typedef __attribute__((ext_vector_type(2))) float f32x2;
typedef __attribute__((ext_vector_type(4))) unsigned u32x4;
typedef __attribute__((ext_vector_type(2))) unsigned u32x2;
#define LDS __attribute__((address_space(3)))
#define GLB __attribute__((address_space(1)))
#define DEV __device__ __forceinline__
#define RLX_AGENT __ATOMIC_RELAXED, __HIP_MEMORY_SCOPE_AGENT

constexpr int SL = 2048;
constexpr int DM = 576;

constexpr size_t WS_HB = 0;
constexpr size_t WS_AT = WS_HB + (size_t)2 * DM * SL * 2;
constexpr size_t WS_G = WS_AT + (size_t)DM * SL * 2;
constexpr size_t WS_GRAN = WS_G + (size_t)SL * 8;
constexpr size_t WS_EP = WS_GRAN + (size_t)9 * 16 * 2 * 64 * 8;
constexpr size_t WS_FL = WS_EP + 4096;
constexpr size_t WS_XT = WS_FL + (size_t)16 * DM * 8;
constexpr size_t WS_IN = WS_XT + (size_t)DM * SL * 2;
constexpr u64 FLAG_SET = 0x9E3779B97F4A7C15ull;
constexpr u32 SPIN_CAP = 1u << 17;
constexpr int IN_H = 0, IN_G = 36, IN_A = 40;

constexpr size_t WS_ST = (size_t)16 << 20;
#define STAMP(kern, idx) do {} while (0)
#define CSTAMP(kern, idx) do {} while (0)
#define LSTAMP(kern, idx) do {} while (0)
#define LSTAMP2(kern, idx) do {} while (0)

DEV float bf2f(u16 v) { return __uint_as_float((u32)v << 16); }
DEV float bflo(u32 v) { return __uint_as_float(v << 16); }
DEV float bfhi(u32 v) { return __uint_as_float(v & 0xffff0000u); }
DEV u32 pack_bf16(float lo, float hi) {
  f32x2 v = {lo, hi};
  bf16x2 b = __builtin_convertvector(v, bf16x2);
  return __builtin_bit_cast(u32, b);
}
DEV float rbf(float x) { return bflo(pack_bf16(x, x)); }
template <int N> DEV void wait_vm() { asm volatile("s_waitcnt vmcnt(%0)" ::"n"(N) : "memory"); }
DEV void bar() { asm volatile("s_waitcnt lgkmcnt(0)\n\ts_barrier" ::: "memory"); }
DEV void wave_fence() { __builtin_amdgcn_fence(__ATOMIC_ACQ_REL, "wavefront"); }
template <int AUX> DEV void dma16(const void* src, LDS char* dst) {
  __builtin_amdgcn_global_load_lds((const GLB u32*)src, (LDS u32*)dst, 16, 0, AUX);
}
constexpr int SC1 = 16;
#define GPOLL_SLEEP 1
#define GPOLL_DELAY 24
#define POLL_SLEEP 1
#define OUT_AUX 16
#define WS_OFF 0
#define XT_WAIT 4
DEV bf16x8 lds_frag(LDS const char* p) { return *(LDS const bf16x8*)p; }
DEV s16x4 tr_read(LDS const char* a) { return __builtin_amdgcn_ds_read_tr16_b64_v4i16((LDS s16x4*)a); }
DEV bf16x8 tr_frag(LDS const char* a, LDS const char* b) {
  const s16x4 lo = tr_read(a), hi = tr_read(b);
  return __builtin_bit_cast(bf16x8, __builtin_shufflevector(lo, hi, 0, 1, 2, 3, 4, 5, 6, 7));
}
DEV f32x4 mfma16(bf16x8 a, bf16x8 b, f32x4 c) { return __builtin_amdgcn_mfma_f32_16x16x32_bf16(a, b, c, 0, 0, 0); }
DEV __amdgpu_buffer_rsrc_t mk_rsrc(const void* base, int bytes) {
  return __builtin_amdgcn_make_buffer_rsrc(const_cast<void*>(base), (short)0, bytes, 0x00020000);
}
struct cf { float r, i; };
DEV cf cmul(cf a, cf b) { return {a.r * b.r - a.i * b.i, a.r * b.i + a.i * b.r}; }
DEV cf cfma(cf a, cf b, cf c) { return {fmaf(a.r, b.r, fmaf(-a.i, b.i, c.r)), fmaf(a.r, b.i, fmaf(a.i, b.r, c.i))}; }
typedef f32x2 c2;
DEV c2 cm2(c2 a, c2 b) {
  c2 t, r;
  asm("v_pk_mul_f32 %0, %1, %2 op_sel:[0,0] op_sel_hi:[0,1]" : "=v"(t) : "v"(a), "v"(b));
  asm("v_pk_fma_f32 %0, %1, %2, %3 op_sel:[1,1,0] op_sel_hi:[1,0,1] neg_lo:[1,0,0]" : "=v"(r) : "v"(a), "v"(b), "v"(t));
  return r;
}
DEV f32x2 fma2(f32x2 a, f32x2 b, f32x2 c) { return __builtin_elementwise_fma(a, b, c); }
DEV f32x2 lo2(f32x4 v) { return __builtin_shufflevector(v, v, 0, 1); }
DEV f32x2 hi2(f32x4 v) { return __builtin_shufflevector(v, v, 2, 3); }
DEV f32x4 cat2(f32x2 a, f32x2 b) { return __builtin_shufflevector(a, b, 0, 1, 2, 3); }
DEV void wait_flags(GLB u64* word, bool need) {
  for (u32 spin = 0;; ++spin) {
    const u64 v = __hip_atomic_load(word, RLX_AGENT);
    if (__all(!need || v == FLAG_SET) || spin > SPIN_CAP) break;
    __builtin_amdgcn_s_sleep(POLL_SLEEP);
  }
}
DEV int tile_block(int lt, int idx) {
  const int xcd = lt >> 2, a = lt & 3;
  int j;
  if (idx < 5) { const int e = 5 * a + idx; j = e < 18 ? e : e + 12; }
  else j = 18 + 3 * a + (idx - 5);
  return (j << 3) | xcd;
}

struct P {
  const u16 *x, *la, *aim, *creA, *cimA, *ldtA, *dskA;
  const u16 *lre, *lim, *bre, *bim, *cre, *cim, *ldtB, *dskB, *gw;
  char* ws;
  float* out;
};

constexpr int G_NB = 4, G_STAGE = 32768;
constexpr int G_E = 153536;
constexpr int G_PF = G_E + 4096;
constexpr int G_L = G_PF + 4096;
constexpr int G_X = G_L + 1024;
constexpr int G_GO = G_X + 1024;
constexpr int G_T = G_NB * G_STAGE;
static_assert(G_GO + 4 <= 163840 && G_T + 4 * 32 * 144 <= G_E, "role G scratch");

DEV void g_loader(const P& p, LDS char* smem, int c, int g, int d0, int lw, int lane) {
  [[maybe_unused]] const int w = lw + 8;
  __builtin_amdgcn_s_setprio(3);
  GLB u32* ep = (GLB u32*)(p.ws + WS_EP + g * 256);
  GLB u64* gran = (GLB u64*)(p.ws + WS_GRAN) + (size_t)g * 16 * 128;
  const u32 tag = __hip_atomic_load(ep, RLX_AGENT) + 1u;
  u32 xo[4], bo[4];
#pragma unroll
  for (int i = 0; i < 4; ++i) {
    const int rho = 8 * (4 * lw + i) + (lane >> 3);
    const int hf = rho >> 6, m = (rho >> 4) & 3, r = rho & 15;
    const int tau = 64 * hf + 16 * (r >> 2) + 8 * (r & 1) + 2 * m + ((r >> 1) & 1);
    const int ch = (lane & 7) ^ (rho & 7);
    xo[i] = (u32)(128 * c + tau) * 1152u + ch * 16;
    const int pp = 64 * g + 16 * (rho >> 5) + r;
    bo[i] = (u32)pp * 1152u + ch * 16;
  }
  auto issue = [&](int s) {
    LDS char* buf = smem + (s & (G_NB - 1)) * G_STAGE + lw * 4096;
    int kb = g + s; kb = (kb >= 9 ? kb - 9 : kb) * 128;
#pragma unroll
    for (int i = 0; i < 4; ++i) dma16<0>((const char*)p.x + (xo[i] + kb), buf + i * 1024);
#pragma unroll
    for (int i = 0; i < 4; ++i) {
      const char* base = (const char*)((((4 * lw + i) >> 1) & 1) ? p.bim : p.bre);
      dma16<0>(base + (bo[i] + kb), buf + 16384 + i * 1024);
    }
  };
#pragma unroll
  for (int s = 0; s < G_NB - 1; ++s) issue(s);
#pragma unroll 1
  for (int s = 0; s < 6; ++s) { wait_vm<16>(); LSTAMP(0, 16 + s); bar(); issue(s + G_NB - 1); LSTAMP(0, 32 + s); }
  wait_vm<16>(); bar();
  wait_vm<8>(); bar();
  wait_vm<0>(); bar();
  STAMP(0, 1);
  if (lw < 2) {
    bar();
    u32 co[6];
#pragma unroll
    for (int i = 0; i < 6; ++i) {
      const int row = 8 * (6 * lw + i) + (lane >> 3), cp = lane & 7;
      co[i] = (u32)(d0 + row) * 1152u + ((cp ^ (row & 7)) << 4);
    }
    const int rot = ((d0 / 96) * 18) >> 3;
#pragma unroll 1
    for (int s = 0; s < 8; ++s) {
      LDS char* buf = smem + 32768 + s * 12288 + 6 * lw * 1024;
      const int ks = s + rot;
      const char* cb = ks < 9 ? (const char*)p.cre + ks * 128 : (const char*)p.cim + (ks - 9) * 128;
#pragma unroll
      for (int i = 0; i < 6; ++i) dma16<0>(cb + co[i], buf + i * 1024);
    }
    bar();
    return;
  }
  bar();
  STAMP(0, 2);
  if (lw == 3) {
    const f32x2 lv = *(LDS const f32x2*)(smem + G_L + lane * 8);
    const cf l16 = {lv[0], lv[1]};
    f32x2 ev[8];
#pragma unroll
    for (int s = 0; s < 8; ++s) ev[s] = *(LDS const f32x2*)(smem + G_E + (s * 64 + lane) * 8);
    cf tot = {0.f, 0.f};
    cf pre[8];
#pragma unroll
    for (int s = 0; s < 8; ++s) { pre[s] = tot; tot = cfma(l16, tot, cf{ev[s][0], ev[s][1]}); }
    if (c < 15) {
      __hip_atomic_store(gran + c * 128 + lane, ((u64)tag << 32) | __float_as_uint(tot.r), RLX_AGENT);
      __hip_atomic_store(gran + c * 128 + 64 + lane, ((u64)tag << 32) | __float_as_uint(tot.i), RLX_AGENT);
    }
#pragma unroll
    for (int s = 0; s < 8; ++s) *(LDS f32x2*)(smem + G_PF + (s * 64 + lane) * 8) = f32x2{pre[s].r, pre[s].i};
  }
  const int par = lw - 2;
  const f32x2 lv = *(LDS const f32x2*)(smem + G_L + 512 + lane * 8);
  const cf l128 = {lv[0], lv[1]};
  const cf l256 = cmul(l128, l128);
  cf acc = {0.f, 0.f};
  if (par < c) {
    const int nc = (c - par + 1) >> 1;
    u64 a[8], b[8];
    __builtin_amdgcn_s_sleep(GPOLL_DELAY);
    for (u32 spin = 0;; ++spin) {
      bool ok = true;
#pragma unroll
      for (int i = 0; i < 8; ++i) {
        const int cc = par + 2 * (i < nc ? i : 0);
        a[i] = __hip_atomic_load(gran + cc * 128 + lane, RLX_AGENT);
        b[i] = __hip_atomic_load(gran + cc * 128 + 64 + lane, RLX_AGENT);
      }
#pragma unroll
      for (int i = 0; i < 8; ++i) ok = ok && (u32)(a[i] >> 32) == tag && (u32)(b[i] >> 32) == tag;
      if (__all(ok) || spin > SPIN_CAP) break;
      __builtin_amdgcn_s_sleep(GPOLL_SLEEP);
    }
#pragma unroll
    for (int i = 0; i < 8; ++i)
      if (i < nc) acc = cfma(l256, acc, cf{__uint_as_float((u32)a[i]), __uint_as_float((u32)b[i])});
    if (c - 1 - (par + 2 * (nc - 1))) acc = cmul(acc, l128);
  }
  *(LDS f32x2*)(smem + G_X + (par * 64 + lane) * 8) = f32x2{acc.r, acc.i};
  STAMP(0, 3);
  bar();
  if (c == 15 && lw == 2 && lane == 0) __hip_atomic_store(ep, tag, RLX_AGENT);
}

template <int HF>
DEV void g_compute(const P& p, LDS char* smem, int c, int g, int w, int lane, u32& pf) {
  const int pq = w & 3, col = lane & 15, q = lane >> 4;
  const int pl = 16 * pq + col, pp = 64 * g + pl;
  cf lam, coef2;
  {
    const float lre = bf2f(p.lre[pp]), lim = bf2f(p.lim[pp]);
    const float dt = rbf(expf(bf2f(p.ldtB[pp])));
    const float zr = dt * lre, zi = dt * lim;
    const float er = expf(zr);
    float sn, cs;
    sincosf(zi, &sn, &cs);
    lam = {er * cs, er * sn};
    const float nr = lam.r - 1.0f, ni = lam.i;
    const float den = 1.0f / (lre * lre + lim * lim);
    coef2 = {2.0f * (nr * lre + ni * lim) * den, 2.0f * (ni * lre - nr * lim) * den};
  }
  cf l8 = cmul(lam, lam); l8 = cmul(l8, l8); l8 = cmul(l8, l8);
  const cf l16 = cmul(l8, l8);
  const cf l32 = cmul(l16, l16), l64 = cmul(l32, l32);
  if (HF == 0 && q == 0) {
    const cf l128 = cmul(l64, l64);
    *(LDS f32x2*)(smem + G_L + pl * 8) = f32x2{l16.r, l16.i};
    *(LDS f32x2*)(smem + G_L + 512 + pl * 8) = f32x2{l128.r, l128.i};
  }
  if (HF == 0 && w == 0 && lane == 0) *(LDS u32*)(smem + G_GO) = 0;
  cf pw = HF ? l64 : cf{1.f, 0.f};
  { const cf t = cmul(pw, l16); if (q & 1) pw = t; }
  { const cf t = cmul(pw, l32); if (q & 2) pw = t; }
  asm volatile("" : "+v"(lam.r), "+v"(lam.i), "+v"(coef2.r), "+v"(coef2.i), "+v"(l8.r), "+v"(l8.i), "+v"(pw.r), "+v"(pw.i));
  f32x4 are[4], aim[4];
#pragma unroll
  for (int m = 0; m < 4; ++m) { are[m] = f32x4{0, 0, 0, 0}; aim[m] = f32x4{0, 0, 0, 0}; }
  const int o0 = col * 128 + (((0 + q) ^ (col & 7)) << 4);
  const int o1 = col * 128 + (((4 + q) ^ (col & 7)) << 4);
  bf16x8 br[2], bi[2], a[2][4];
  auto read_frags = [&](int s) {
    LDS const char* buf = smem + (s & (G_NB - 1)) * G_STAGE;
    LDS const char* ab = buf + HF * 8192;
    LDS const char* bb = buf + 16384 + pq * 4096;
#pragma unroll
    for (int ks = 0; ks < 2; ++ks) {
      const int o = ks ? o1 : o0;
      br[ks] = lds_frag(bb + o); bi[ks] = lds_frag(bb + 2048 + o);
#pragma unroll
      for (int m = 0; m < 4; ++m) a[ks][m] = lds_frag(ab + m * 2048 + o);
    }
  };
  auto mma = [&]() {
#pragma unroll
    for (int ks = 0; ks < 2; ++ks)
#pragma unroll
      for (int m = 0; m < 4; ++m) {
        are[m] = mfma16(a[ks][m], br[ks], are[m]);
        aim[m] = mfma16(a[ks][m], bi[ks], aim[m]);
      }
  };
  STAMP(0, 1);
#pragma unroll
  for (int s = 0; s < 9; ++s) {
    if (HF == 1 && s == XT_WAIT) {
      wait_vm<0>();
      if (lane < 16) __hip_atomic_store((GLB u64*)(p.ws + WS_FL) + c * DM + 64 * g + 16 * pq + lane, FLAG_SET, RLX_AGENT);
    }
    bar();
    if (s == 0) { STAMP(0, 2); CSTAMP(0, 60); }
    if (HF == 1 && s > 0) { mma(); __builtin_amdgcn_sched_barrier(0); }
    if (HF == 1 && s == 0) {
      const int rr = 4 * q + (col >> 2);
      LDS const char* tb = smem + rr * 128 + (((2 * pq + ((col & 3) >> 1)) ^ (rr & 7)) << 4) + (col & 1) * 8;
      LDS char* sc = smem + G_T + pq * (32 * 144);
#pragma unroll
      for (int hf = 0; hf < 2; ++hf) {
        u32x2 t[4];
#pragma unroll
        for (int m = 0; m < 4; ++m) t[m] = __builtin_bit_cast(u32x2, tr_read(tb + hf * 8192 + m * 2048));
        u32x4 oa, ob;
#pragma unroll
        for (int m = 0; m < 4; ++m) {
          oa[m] = __builtin_amdgcn_perm(t[m][1], t[m][0], 0x05040100u);
          ob[m] = __builtin_amdgcn_perm(t[m][1], t[m][0], 0x07060302u);
        }
        *(LDS u32x4*)(sc + hf * (16 * 144) + col * 144 + q * 32) = oa;
        *(LDS u32x4*)(sc + hf * (16 * 144) + col * 144 + q * 32 + 16) = ob;
      }
      wave_fence();
      const __amdgpu_buffer_rsrc_t rs = mk_rsrc(p.ws + WS_XT, DM * SL * 2);
#pragma unroll
      for (int u = 0; u < 4; ++u) {
        const int row = (lane >> 3) + 8 * (u & 1), hf = u >> 1;
        const u32x4 v = *(LDS const u32x4*)(sc + hf * (16 * 144) + row * 144 + (lane & 7) * 16);
        __builtin_amdgcn_raw_buffer_store_b128(v, rs, ((64 * g + 16 * pq + row) * SL + 128 * c + 64 * hf) * 2 + (lane & 7) * 16, 0, SC1);
      }
      __builtin_amdgcn_sched_barrier(0);
    }
    read_frags(s);
    __builtin_amdgcn_sched_barrier(0);
    if (HF == 0) { mma(); __builtin_amdgcn_sched_barrier(0); }
  }
  if (HF == 1) mma();
  __builtin_amdgcn_sched_barrier(0);
  STAMP(0, 3); CSTAMP(0, 61);
  const f32x2 Lr = {lam.r, lam.r}, Li = {lam.i, lam.i};
  f32x2 er = {0.f, 0.f}, ei = {0.f, 0.f};
#pragma unroll
  for (int t = 0; t < 8; ++t) {
    const f32x2 br_ = (t & 1) ? hi2(are[t >> 1]) : lo2(are[t >> 1]);
    const f32x2 bi_ = (t & 1) ? hi2(aim[t >> 1]) : lo2(aim[t >> 1]);
    const f32x2 nr = fma2(-Li, ei, fma2(Lr, er, br_));
    ei = fma2(Li, er, fma2(Lr, ei, bi_));
    er = nr;
  }
  const cf eA = {er[0], ei[0]};
  const cf e = cfma(l8, eA, cf{er[1], ei[1]});
  const int seg = 4 * HF + q;
  *(LDS f32x2*)(smem + G_E + (seg * 64 + pl) * 8) = f32x2{e.r, e.i};
  STAMP(0, 4);
  bar();
  STAMP(0, 5);
  {
#pragma unroll
    for (int m = 0; m < 4; ++m) asm volatile("" : "+v"(are[m]), "+v"(aim[m]));
    const f32x2 Cr = {coef2.r, coef2.r}, Ci = {coef2.i, coef2.i};
#pragma unroll
    for (int m = 0; m < 4; ++m) {
      const f32x2 r0 = lo2(are[m]), r1 = hi2(are[m]), i0 = lo2(aim[m]), i1 = hi2(aim[m]);
      are[m] = cat2(fma2(-Ci, i0, Cr * r0), fma2(-Ci, i1, Cr * r1));
      aim[m] = cat2(fma2(-Ci, r0, -Cr * i0), fma2(-Ci, r1, -Cr * i1));
      asm volatile("" : "+v"(are[m]), "+v"(aim[m]));
    }
  }
  cf eAc = cmul(coef2, eA), pwc = cmul(coef2, pw);
  asm volatile("" : "+v"(eAc.r), "+v"(eAc.i), "+v"(pwc.r), "+v"(pwc.i));
  STAMP(0, 7);
  bar();
  STAMP(0, 8);
  cf cin;
  {
    f32x2 v[2];
#pragma unroll
    for (int i = 0; i < 2; ++i) v[i] = *(LDS const f32x2*)(smem + G_X + (i * 64 + pl) * 8);
    const f32x2 pf = *(LDS const f32x2*)(smem + G_PF + (seg * 64 + pl) * 8);
    const f32x2 S = v[0] + v[1];
    cin = cfma(pwc, cf{S[0], S[1]}, cmul(coef2, cf{pf[0], pf[1]}));
  }
  const cf cinB = cfma(l8, cin, eAc);
  er = f32x2{cin.r, cinB.r}; ei = f32x2{-cin.i, -cinB.i};
  f32x2 hr[8], hi[8];
#pragma unroll
  for (int t = 0; t < 8; ++t) {
    const f32x2 br_ = (t & 1) ? hi2(are[t >> 1]) : lo2(are[t >> 1]);
    const f32x2 bi_ = (t & 1) ? hi2(aim[t >> 1]) : lo2(aim[t >> 1]);
    const f32x2 nr = fma2(Li, ei, fma2(Lr, er, br_));
    ei = fma2(-Li, er, fma2(Lr, ei, bi_));
    er = nr;
    hr[t] = er; hi[t] = ei;
  }
  u32x4 o[4];
#pragma unroll
  for (int t = 0; t < 4; ++t) {
    o[0][t] = pack_bf16(hr[2 * t][0], hr[2 * t + 1][0]);
    o[1][t] = pack_bf16(hr[2 * t][1], hr[2 * t + 1][1]);
    o[2][t] = pack_bf16(hi[2 * t][0], hi[2 * t + 1][0]);
    o[3][t] = pack_bf16(hi[2 * t][1], hi[2 * t + 1][1]);
  }
  STAMP(0, 9);
  {
    const __amdgpu_buffer_rsrc_t rs = mk_rsrc(p.ws + WS_HB, 2 * DM * SL * 2);
    const int hb = (((c * 2 + HF) * 2) * 9 + g) * 8192 + pq * 2048;
    const int p0 = (16 * q + col) * 16, p1 = 1024 + (16 * q + (col ^ 4)) * 16;
    __builtin_amdgcn_raw_buffer_store_b128(o[0], rs, hb + p0, 0, SC1);
    __builtin_amdgcn_raw_buffer_store_b128(o[1], rs, hb + p1, 0, SC1);
    __builtin_amdgcn_raw_buffer_store_b128(o[2], rs, hb + 9 * 8192 + p0, 0, SC1);
    __builtin_amdgcn_raw_buffer_store_b128(o[3], rs, hb + 9 * 8192 + p1, 0, SC1);
    wait_vm<0>();
    STAMP(0, 10);
    if (lane < 8)
      __hip_atomic_store((GLB u64*)(p.ws + WS_IN) + tile_block(2 * c + HF, lane) * 64 + IN_H + g * 4 + pq, FLAG_SET, RLX_AGENT);
    bar();
  }
}

constexpr int S_CPY = 4160;
constexpr int S_X = 0;
constexpr int S_ST = 6 * 4 * S_CPY;
constexpr int S_Z = S_ST;
constexpr int S_ZSZ = (256 + SL) * 2;
constexpr int S_T = S_ST + 12 * 4608;
constexpr int S_TSZ = 1312;
constexpr int S_END = S_T + 6 * S_TSZ;
constexpr int S_CNT = 32768 + 8192;

DEV c2 cexp_k(float zl, float th, float tl, float k) {
  const float mag = __builtin_amdgcn_exp2f(zl * k);
  const float t = fmaf(tl, k, __builtin_amdgcn_fractf(th * k));
  return c2{mag * __builtin_amdgcn_cosf(t), mag * __builtin_amdgcn_sinf(t)};
}
#define PKM(d, a, b) "v_pk_mul_f32 " d ", " a ", " b " op_sel:[0,0] op_sel_hi:[0,1]\n\t"
#define PKF(d, a, b) "v_pk_fma_f32 " d ", " a ", " b ", " d " op_sel:[1,1,0] op_sel_hi:[1,0,1] neg_lo:[1,0,0]\n\t"
DEV void octet(LDS char* dst, c2 s0, c2 m1, c2 m2) {
  c2 v1, v2, v3, t0, t1, t2, t3;
  asm(PKM("%0", "%7", "%8") PKM("%3", "%7", "%9") PKF("%0", "%7", "%8") PKF("%3", "%7", "%9")
      PKM("%1", "%0", "%8") PKM("%4", "%0", "%9") PKF("%1", "%0", "%8") PKF("%4", "%0", "%9")
      PKM("%2", "%1", "%8") PKM("%5", "%1", "%9") PKF("%2", "%1", "%8") PKF("%5", "%1", "%9")
      PKM("%6", "%2", "%9") PKF("%6", "%2", "%9")
      : "=&v"(v1), "=&v"(v2), "=&v"(v3), "=&v"(t0), "=&v"(t1), "=&v"(t2), "=&v"(t3) : "v"(s0), "v"(m1), "v"(m2));
  *(LDS u32*)(dst + 0 * 288) = pack_bf16(s0[0], s0[1]);
  *(LDS u32*)(dst + 1 * 288) = pack_bf16(v1[0], v1[1]);
  *(LDS u32*)(dst + 2 * 288) = pack_bf16(v2[0], v2[1]);
  *(LDS u32*)(dst + 3 * 288) = pack_bf16(v3[0], v3[1]);
  *(LDS u32*)(dst + 4 * 288) = pack_bf16(t0[0], t0[1]);
  *(LDS u32*)(dst + 5 * 288) = pack_bf16(t1[0], t1[1]);
  *(LDS u32*)(dst + 6 * 288) = pack_bf16(t2[0], t2[1]);
  *(LDS u32*)(dst + 7 * 288) = pack_bf16(t3[0], t3[1]);
}
DEV u32 dup_lo(u32 a) { return __builtin_amdgcn_perm(a, a, 0x01000100u); }
DEV u32 dup_hi(u32 a) { return __builtin_amdgcn_perm(a, a, 0x03020302u); }
DEV u32 hi_lo(u32 a, u32 b) { return __builtin_amdgcn_alignbit(b, a, 16); }

DEV void role_s(const P& p, LDS char* smem, int sg, int w, int lane) {
  const int tid = w * 64 + lane;
  const int ch = w >> 1, hh = w & 1, d = 6 * sg + ch;
  const int n = d * 128 + 64 * hh + lane;
  const u16 r_ldt = p.ldtA[d], r_la = p.la[n], r_aim = p.aim[n], r_cre = p.creA[n], r_cim = p.cimA[n];
  u32 wb[2] = {0, 0};
  __builtin_amdgcn_sched_barrier(0);
#pragma unroll
  for (int i = 0; i < 2; ++i) {
    const int ln = i * 768 + tid, all = (sg % 12) * 864 + ln;
    if (ln < 864) wb[i] = *(const GLB u32*)((const char*)(all < 5184 ? p.bre : p.bim) + (size_t)(all < 5184 ? all : all - 5184) * 128);
  }
  __builtin_amdgcn_sched_barrier(0);
  STAMP(0, 1);
  f32x4 tab[3];
  [[maybe_unused]] u32 wc[2] = {0, 0};
  LDS char* st = smem + S_ST + w * 4608;
  {
    const float L2E = 1.44269504088896341f;
    const float dt = rbf(__builtin_amdgcn_exp2f(bf2f(r_ldt) * L2E));
    const float ar = -rbf(__builtin_amdgcn_exp2f(bf2f(r_la) * L2E));
    const float zl = ar * dt * L2E, zi = bf2f(r_aim) * dt;
    const float th = zi * 0.15915494309189535f;
    const float tl = fmaf(zi, 0.15915494309189535f, -th) + zi * 4.6017384e-10f;
    const c2 C = {2.0f * (bf2f(r_cre) * dt), 2.0f * (bf2f(r_cim) * dt)};
    const c2 w1 = cexp_k(zl, th, tl, 1.f), w8 = cexp_k(zl, th, tl, 8.f), w128 = cexp_k(zl, th, tl, 128.f);
    c2 w2, w4, w16, w32, w64, w256, w512, w1024, w2048;
    asm(PKM("%0", "%9", "%9") PKM("%2", "%10", "%10") PKM("%5", "%11", "%11") PKF("%0", "%9", "%9") PKF("%2", "%10", "%10") PKF("%5", "%11", "%11")
        PKM("%1", "%0", "%0") PKM("%3", "%2", "%2") PKM("%6", "%5", "%5") PKF("%1", "%0", "%0") PKF("%3", "%2", "%2") PKF("%6", "%5", "%5")
        PKM("%4", "%3", "%3") PKM("%7", "%6", "%6") PKF("%4", "%3", "%3") PKF("%7", "%6", "%6")
        PKM("%8", "%7", "%7") PKF("%8", "%7", "%7")
        : "=&v"(w2), "=&v"(w4), "=&v"(w16), "=&v"(w32), "=&v"(w64), "=&v"(w256), "=&v"(w512), "=&v"(w1024), "=&v"(w2048)
        : "v"(w1), "v"(w8), "v"(w128));
    c2 C128, C256, C512, C1024, C2048;
    asm(PKM("%0", "%5", "%6") PKM("%1", "%5", "%7") PKM("%2", "%5", "%8") PKM("%3", "%5", "%9") PKM("%4", "%5", "%10")
        PKF("%0", "%5", "%6") PKF("%1", "%5", "%7") PKF("%2", "%5", "%8") PKF("%3", "%5", "%9") PKF("%4", "%5", "%10")
        : "=&v"(C128), "=&v"(C256), "=&v"(C512), "=&v"(C1024), "=&v"(C2048)
        : "v"(C), "v"(w128), "v"(w256), "v"(w512), "v"(w1024), "v"(w2048));
#pragma unroll
    for (int i = 0; i < 2; ++i) {
      const int ln = i * 768 + tid, all = (sg % 12) * 864 + ln;
      if (ln < 864) wc[i] = *(const GLB u32*)((const char*)(all < 5184 ? p.cre : p.cim) + (size_t)(all < 5184 ? all : all - 5184) * 128);
    }
    STAMP(0, 2);
    LDS char* wr = st + 4 * lane;
    LDS const char* rd = st + (lane & 15) * 288 + (lane >> 4) * 16;
    bf16x8 B[4];
    const c2 w8c = {w8[0], -w8[1]}, w32c = {w32[0], -w32[1]}, w64c = {w64[0], -w64[1]};
    octet(wr, c2{1.f, 0.f}, w8c, w32c);
    octet(wr + 8 * 288, w64c, w8c, w32c);
    wave_fence();
#pragma unroll
    for (int ks = 0; ks < 4; ++ks) B[ks] = lds_frag(rd + 64 * ks);
    wave_fence();
#pragma unroll
    for (int mt = 0; mt < 3; ++mt) {
      if (mt == 0) { octet(wr, C, w1, w4); octet(wr + 8 * 288, C128, w1, w4); }
      if (mt == 1) { octet(wr, C256, w2, w128); octet(wr + 8 * 288, C512, w128, w4); }
      if (mt == 2) { octet(wr, C1024, w128, w512); *(LDS u32*)(wr + 8 * 288) = pack_bf16(C2048[0], C2048[1]); }
      wave_fence();
      f32x4 acc = {0, 0, 0, 0};
      bf16x8 A[4];
#pragma unroll
      for (int ks = 0; ks < 4; ++ks) A[ks] = lds_frag(rd + 64 * ks);
      __builtin_amdgcn_sched_barrier(0);
#pragma unroll
      for (int ks = 0; ks < 4; ++ks) acc = mfma16(A[ks], B[ks], acc);
      __builtin_amdgcn_sched_barrier(0);
      wave_fence();
      tab[mt] = acc;
    }
  }
  const int b = lane & 15, q = lane >> 4;
#pragma unroll
  for (int mt = 0; mt < 3; ++mt) *(LDS f32x4*)(st + mt * 1024 + lane * 16) = tab[mt];
  STAMP(0, 3);
  bar();
  {
    LDS const char* ot = smem + S_ST + (w ^ 1) * 4608 + lane * 16;
    LDS char* T = smem + S_T + ch * S_TSZ;
    if (hh == 0) {
      const f32x4 v0 = tab[0] + *(LDS const f32x4*)(ot), v2 = tab[2] + *(LDS const f32x4*)(ot + 2048);
      *(LDS u32x2*)(T + 2 * (128 * (q >> 1) + 8 * b + 4 * (q & 1))) = u32x2{pack_bf16(v0[0], v0[1]), pack_bf16(v0[2], v0[3])};
      const u32 a0 = pack_bf16(v2[0], v2[1]), a1 = pack_bf16(v2[2], v2[3]);
      if (q < 2) {
        LDS char* t3 = T + 2 * (512 + 64 * q + b);
        *(LDS u16*)(t3) = (u16)a0; *(LDS u16*)(t3 + 32) = (u16)(a0 >> 16);
        *(LDS u16*)(t3 + 64) = (u16)a1; *(LDS u16*)(t3 + 96) = (u16)(a1 >> 16);
      } else if (lane == 32) *(LDS u16*)(T + 2 * 640) = (u16)a0;
    } else {
      const f32x4 v1 = tab[1] + *(LDS const f32x4*)(ot + 1024);
      const u32 a0 = pack_bf16(v1[0], v1[1]), a1 = pack_bf16(v1[2], v1[3]);
      if (q < 2) *(LDS u32x2*)(T + 2 * (256 + 64 * q + 4 * b)) = u32x2{a0, a1};
      else {
        LDS char* t2 = T + 2 * (384 + 2 * b + (q - 2));
        *(LDS u16*)(t2) = (u16)a0; *(LDS u16*)(t2 + 64) = (u16)(a0 >> 16);
        *(LDS u16*)(t2 + 128) = (u16)a1; *(LDS u16*)(t2 + 192) = (u16)(a1 >> 16);
      }
    }
  }
  bar();
  STAMP(0, 4);
  u32x4 uv[2];
  {
    GLB u64* fl = (GLB u64*)(p.ws + WS_FL) + (8 * hh + (lane & 7)) * DM + d;
    asm volatile("" :: "v"(wc[0]), "v"(wc[1]), "v"(wb[0]), "v"(wb[1]));
    wait_flags(fl, true);
    const __amdgpu_buffer_rsrc_t rs = mk_rsrc(p.ws + WS_XT, DM * SL * 2);
#pragma unroll
    for (int v = 0; v < 2; ++v) uv[v] = __builtin_amdgcn_raw_buffer_load_b128(rs, (d * SL + 1024 * hh + 8 * (lane + 64 * v)) * 2, 0, SC1);
    if (lane < 8) __hip_atomic_store(fl, 0ull, RLX_AGENT);
  }
  STAMP(0, 5);
  {
    LDS const char* T = smem + S_T + ch * S_TSZ;
    u32 o[10];
    const u32 e512 = *(LDS const u16*)(T + 2 * 512);
    o[8] = o[9] = e512 | (e512 << 16);
    if (hh == 0) {
      const int off = lane < 16 ? 32 * lane : (lane < 32 ? 256 + 16 * lane : 512 + 8 * lane);
      u32x2 W[4];
#pragma unroll
      for (int i = 0; i < 4; ++i) W[i] = *(LDS const u32x2*)(T + off + 8 * i);
      if (lane < 16) {
#pragma unroll
        for (int i = 0; i < 8; ++i) o[i] = W[i >> 1][i & 1];
      } else if (lane < 32) {
#pragma unroll
        for (int i = 0; i < 4; ++i) {
          const u32 a = W[i >> 1][i & 1], nx = W[(i + 1) >> 1][(i + 1) & 1];
          o[2 * i] = dup_lo(a);
          o[2 * i + 1] = hi_lo(a, nx);
        }
      } else {
        const u32 w0 = W[0][0], w1 = W[0][1], w2 = W[1][0];
        o[0] = dup_lo(w0); o[1] = w0; o[2] = dup_hi(w0); o[3] = dup_lo(w1);
        o[4] = o[3]; o[5] = w1; o[6] = dup_hi(w1); o[7] = dup_lo(w2);
      }
    } else {
      const u32 w0 = *(LDS const u32*)(T + 1024 + 4 * lane), w1 = *(LDS const u32*)(T + 1028 + 4 * lane);
      o[0] = o[1] = dup_lo(w0); o[2] = w0; o[3] = o[4] = o[5] = dup_hi(w0); o[6] = o[7] = dup_lo(w1);
    }
    o[8] = (u32)__builtin_amdgcn_update_dpp((int)o[8], (int)o[0], 0x130, 0xf, 0xf, false);
    o[9] = (u32)__builtin_amdgcn_update_dpp((int)o[9], (int)o[1], 0x130, 0xf, 0xf, false);
    u32 a[9];
#pragma unroll
    for (int i = 0; i < 9; ++i) a[i] = hi_lo(o[i], o[i + 1]);
    LDS char* xb = smem + S_X + ch * 4 * S_CPY + 32 + 2 * (1024 * hh + 16 * lane);
    *(LDS u32x4*)(xb) = u32x4{o[0], o[1], o[2], o[3]};
    *(LDS u32x4*)(xb + 16) = u32x4{o[4], o[5], o[6], o[7]};
    *(LDS u32x4*)(xb + S_CPY) = u32x4{a[0], a[1], a[2], a[3]};
    *(LDS u32x4*)(xb + S_CPY + 16) = u32x4{a[4], a[5], a[6], a[7]};
    *(LDS u32x4*)(xb + 2 * S_CPY) = u32x4{o[1], o[2], o[3], o[4]};
    *(LDS u32x4*)(xb + 2 * S_CPY + 16) = u32x4{o[5], o[6], o[7], o[8]};
    *(LDS u32x4*)(xb + 3 * S_CPY) = u32x4{a[1], a[2], a[3], a[4]};
    *(LDS u32x4*)(xb + 3 * S_CPY + 16) = u32x4{a[5], a[6], a[7], a[8]};
    if (hh == 0 && lane == 0) {
      LDS char* hb = smem + S_X + ch * 4 * S_CPY;
      const u32x4 z = {0, 0, 0, 0};
      *(LDS u32x4*)(hb) = z;
      *(LDS u32x4*)(hb + 16) = z;
      *(LDS u32x4*)(hb + S_CPY) = z;
      *(LDS u32x4*)(hb + S_CPY + 16) = u32x4{0, 0, 0, o[0] << 16};
      *(LDS u32x4*)(hb + 2 * S_CPY) = z;
      *(LDS u32x4*)(hb + 2 * S_CPY + 16) = u32x4{0, 0, 0, o[0]};
      *(LDS u32x4*)(hb + 3 * S_CPY) = z;
      *(LDS u32x4*)(hb + 3 * S_CPY + 16) = u32x4{0, 0, o[0] << 16, a[0]};
    }
    if (tid < 6 * 32) *(LDS u32x4*)(smem + S_Z + (tid >> 5) * S_ZSZ + (tid & 31) * 16) = u32x4{0, 0, 0, 0};
    STAMP(0, 6);
    LDS char* z = smem + S_Z + ch * S_ZSZ + 512 + (1024 * hh + 8 * lane) * 2;
    *(LDS u32x4*)z = uv[0];
    *(LDS u32x4*)(z + 1024) = uv[1];
  }
  bar();
  STAMP(0, 7);
  f32x4 acc[8];
#pragma unroll
  for (int i = 0; i < 8; ++i) acc[i] = f32x4{0, 0, 0, 0};
  {
    const int kq = q, qq = b >> 2, pp = b & 3;
    LDS const char* fb = smem + S_X + (ch * 4 + ((4 - qq) & 3)) * S_CPY +
                         2 * (16 + 16 * (kq >> 1) - 8 * (kq & 1) + 4 * pp - (qq ? 4 : 0)) + 64 * hh;
    LDS const char* gb = smem + S_Z + ch * S_ZSZ + 512 + 32 * (b - (kq >> 1)) + 16 * (kq & 1) - 64 * hh;
#pragma unroll 1
    for (int it = 0; it < 4; ++it) {
      bf16x8 F[8], G[8];
#pragma unroll
      for (int s = 0; s < 8; ++s) {
        F[s] = tr_frag(fb + 512 * s, fb + 512 * s - 8);
        G[s] = lds_frag(gb + 512 * s);
      }
      __builtin_amdgcn_sched_barrier(0);
#pragma unroll
      for (int t = 0; t < 8; ++t)
#pragma unroll
        for (int s = 0; s <= t; ++s) acc[t] = mfma16(F[s], G[t - s], acc[t]);
      fb += 128;
      gb -= 128;
    }
  }
  __builtin_amdgcn_sched_barrier(0);
  STAMP(0, 8);
  bar();
  {
    LDS char* ex = smem + S_X + 49152 + w * 4096 + lane * 16;
#pragma unroll
    for (int i = 0; i < 4; ++i) {
      const f32x4 lo = acc[i], hi = acc[4 + i];
      *(LDS f32x4*)(ex + i * 1024) = hh ? +lo : +hi;
    }
    if (tid == 0) *(LDS u32*)(smem + S_CNT) = 0;
  }
  bar();
  u32x2 yo[4];
  {
    LDS const char* ey = smem + S_X + 49152 + (w ^ 1) * 4096 + lane * 16;
#pragma unroll
    for (int i = 0; i < 4; ++i) {
      const f32x4 lo = acc[i], hi = acc[4 + i];
      const f32x4 y = (hh ? +hi : +lo) + *(LDS const f32x4*)(ey + i * 1024);
      yo[i] = u32x2{pack_bf16(y[0], y[1]), pack_bf16(y[2], y[3])};
    }
  }
  bar();
  {
    const __amdgpu_buffer_rsrc_t rs = mk_rsrc(p.ws + WS_AT, DM * SL * 2);
    const int dst = (d * SL + 1024 * hh + 16 * (lane & 15) + 4 * (lane >> 4)) * 2;
#pragma unroll
    for (int i = 0; i < 4; ++i) __builtin_amdgcn_raw_buffer_store_b64(yo[i], rs, dst + 512 * i, 0, SC1);
  }
  STAMP(0, 9);
}
DEV void s_signal(const P& p, LDS char* smem, int sg, int lane) {
  u32 old = 0;
  if (lane == 0) old = __hip_atomic_fetch_add((LDS u32*)(smem + S_CNT), 1u, __ATOMIC_RELAXED, __HIP_MEMORY_SCOPE_WORKGROUP);
  if (__builtin_amdgcn_readfirstlane(old) == 11) {
    const int lt = lane & 31, second = lane >> 5;
    const int dd = 6 * sg + (second ? 5 : 0);
    const int idx = dd < 480 ? dd / 96 : 5 + (dd - 480) / 32;
    const int dfirst = idx < 5 ? 96 * idx : 480 + 32 * (idx - 5);
    __hip_atomic_store((GLB u64*)(p.ws + WS_IN) + tile_block(lt, idx) * 64 + IN_A + (sg - dfirst / 6), FLAG_SET, RLX_AGENT);
  }
}

DEV void role_gate(const P& p, int rb, int w, int lane, u64 pc) {
  u32 pf0 = 0, pf1 = 0;
  const int tid = w * 64 + lane;
  const int sub = lane & 7;
  u32x4 wv[9];
#pragma unroll
  for (int i = 0; i < 9; ++i) wv[i] = *(const u32x4*)((const char*)p.gw + (i * 8 + sub) * 16);
  for (int r = w * 8 + (lane >> 3); r < 128; r += 96) {
    const int l = rb * 128 + r;
    float s = 0.f;
    u32x4 xv[9];
#pragma unroll
    for (int i = 0; i < 9; ++i) xv[i] = *(const u32x4*)((const char*)p.x + (size_t)l * 1152 + (i * 8 + sub) * 16);
#pragma unroll
    for (int i = 0; i < 9; ++i) {
#pragma unroll
      for (int k = 0; k < 4; ++k) {
        const u32 pr = pack_bf16(bflo(xv[i][k]) * bflo(wv[i][k]), bfhi(xv[i][k]) * bfhi(wv[i][k]));
        s += bflo(pr);
        s += bfhi(pr);
      }
    }
    s += __shfl_xor(s, 1); s += __shfl_xor(s, 2); s += __shfl_xor(s, 4);
    const float gv = rbf(1.0f / (1.0f + expf(-rbf(s))));
    if (sub == 0)
      __hip_atomic_store((GLB u64*)(p.ws + WS_G) + l, ((u64)__float_as_uint(rbf(1.0f - gv)) << 32) | __float_as_uint(gv), RLX_AGENT);
  }
  asm volatile("" :: "v"(pf0), "v"(pf1));
  wait_vm<0>();
  bar();
  if (w == 0 && lane < 16)
    __hip_atomic_store((GLB u64*)(p.ws + WS_IN) + tile_block(2 * rb + (lane >> 3), lane & 7) * 64 + IN_G, FLAG_SET, RLX_AGENT);
}

constexpr int H_C = 32768, H_CS = 12288;
DEV int h_slot(int i) { return i < 4 ? i * 8192 : 131072 + (i - 4) * 8192; }
constexpr int H_AT = H_C + 2 * H_CS;
constexpr int H_XT = H_C + 3 * H_CS;
constexpr int H_G = H_C + 4 * H_CS;
constexpr int H_OUT = H_C + 5 * H_CS;
constexpr int K_LDS = 163840;

constexpr int h_wait(int D, int NDT) {
  const int G = 4 + 2 * NDT;
  switch (D) {
    case 0: return 4;
    case 1: return 8;
    case 2: return 4 + G;
    case 3: case 4: return 2 * G;
    case 5: case 6: return 2 * G + 1;
    case 7: return G + NDT;
    default: return NDT;
  }
}
template <int D, int NDT, class F> DEV void h_steps(F&& after, u64& fv, char* wsp) {
  if constexpr (D == 7) asm volatile("s_waitcnt vmcnt(%1)" : "+v"(fv) : "n"(h_wait(D, NDT)) : "memory");
  else wait_vm<h_wait(D, NDT)>();
  LSTAMP2(1, 16 + D);
  bar();
  after(std::integral_constant<int, D>{});
  if constexpr (D < 8) h_steps<D + 1, NDT>(after, fv, wsp);
}
DEV int p2_rot(int d0) { const int idx = d0 < 480 ? d0 / 96 : 5 + (d0 - 480) / 32; return (idx * 18) >> 3; }

template <int NDT, class F>
DEV void p2_loader(const P& p, LDS char* smem, int lt, int d0, int lw, int lane, bool scratch_barrier, F&& pre_poll) {
  __builtin_amdgcn_s_setprio(3);
  const char* hb = p.ws + WS_HB + (size_t)lt * 18 * 8192 + (2 * lw) * 1024 + lane * 16;
  constexpr int W = 32 * NDT;
  const int l0 = 64 * lt, rot = p2_rot(d0);
  u32 co[NDT];
#pragma unroll
  for (int i = 0; i < NDT; ++i) {
    const int row = 8 * (NDT * lw + i) + (lane >> 3), cp = lane & 7;
    co[i] = (u32)(d0 + row) * 1152u + ((cp ^ (row & 7)) << 4);
  }
  auto issue_c = [&](int s) {
    LDS char* buf = smem + H_C + (s & 7) * H_CS + NDT * lw * 1024;
    int ks = s + rot; ks = ks >= 18 ? ks - 18 : ks;
    const char* cb = ks < 9 ? (const char*)p.cre + ks * 128 : (const char*)p.cim + (ks - 9) * 128;
#pragma unroll
    for (int i = 0; i < NDT; ++i) dma16<0>(cb + co[i], buf + i * 1024);
  };
  auto issue_h = [&](int s) {
    LDS char* buf = smem + h_slot(s & 7) + 2 * lw * 1024;
    int ks = s + rot; ks = ks >= 18 ? ks - 18 : ks;
#pragma unroll
    for (int i = 0; i < 2; ++i) dma16<SC1>(hb + ks * 8192 + i * 1024, buf + i * 1024);
  };
  GLB u64* inb = (GLB u64*)(p.ws + WS_IN) + blockIdx.x * 64;
  const int nA = (d0 + W - 1) / 6 - d0 / 6 + 1;
  const bool need = lane == IN_G || (lane >= IN_A && lane < IN_A + nA);
  if (!scratch_barrier) {
#pragma unroll 1
    for (int s = 0; s < 8; ++s) issue_c(s);
  }
  pre_poll();
  { [[maybe_unused]] const int w = lw + 8; STAMP(1, 1); }
  if (scratch_barrier) bar();
  wait_flags(inb + IN_H + (lane < 36 ? lane : 0), true);
  { [[maybe_unused]] const int w = lw + 8; STAMP(1, 2); }
#pragma unroll
  for (int s = 0; s < 4; ++s) issue_h(s);
  { [[maybe_unused]] const int w = lw + 8; STAMP(1, 3); }
  u64 fv = 0;
  bool late = false;
  auto issue_at = [&]() {
#pragma unroll
    for (int i = 0; i < NDT; ++i) {
      const int pc = NDT * lw + i;
      dma16<SC1>(p.ws + WS_AT + (size_t)(d0 + 8 * pc + (lane >> 3)) * 4096 + l0 * 2 + (lane & 7) * 16, smem + H_AT + pc * 1024);
    }
  };
  auto issue_g = [&]() { if (lane < 8) dma16<SC1>(p.ws + WS_G + (size_t)l0 * 8 + lw * 128 + lane * 16, smem + H_G + lw * 128); };
  h_steps<0, NDT>([&](auto D_) {
    constexpr int D = decltype(D_)::value;
    if constexpr (D == 0) { issue_h(4); issue_h(5); issue_h(6); issue_h(7); }
    if constexpr (D >= 1 && D <= 5) { issue_h(2 * D + 6); issue_h(2 * D + 7); issue_c(2 * D + 6); issue_c(2 * D + 7); }
    if constexpr (D == 4)
      asm volatile("global_load_dwordx2 %0, %1, off sc1" : "=v"(fv) : "v"(inb + lane) : "memory");
    if constexpr (D == 6) {
#pragma unroll
      for (int i = 0; i < NDT; ++i) {
        const int pc = NDT * lw + i, id = pc * 64 + lane, row = id / (4 * NDT), qq = id - row * (4 * NDT);
        dma16<0>((const char*)p.x + (size_t)(l0 + row) * 1152 + d0 * 2 + qq * 16, smem + H_XT + pc * 1024);
      }
    }
    if constexpr (D == 7) {
      late = !__all(!need || fv == FLAG_SET);
      if (!late) { issue_g(); issue_at(); }
    }
  }, fv, p.ws);
  if (late) { wait_flags(inb + lane, need); issue_g(); issue_at(); }
  wait_vm<0>();
  bar();
}

template <int NDT>
DEV void p2_compute(const P& p, LDS char* smem, int lt0, int d0, int w, int lane, const u32x2 (&dsk)[6]) {
  const int lh = (w >> 1) & 1, kh = w & 1, dg = w >> 2;
  const int r = lane & 15, kq = lane >> 4;
  const int lt = 2 * lh + kh, l0 = 64 * lt0;
  constexpr int W = 32 * NDT;
  u32x2 da[NDT], db[NDT];
#pragma unroll
  for (int t = 0; t < NDT; ++t) {
    const int d = d0 + 16 * (NDT * dg + t) + 4 * kq;
    da[t] = *(const u32x2*)(p.dskA + d);
    db[t] = *(const u32x2*)(p.dskB + d);
  }
  GLB u64* inb = (GLB u64*)(p.ws + WS_IN) + blockIdx.x * 64;

  f32x4 acc[2][NDT];
#pragma unroll
  for (int i = 0; i < 2; ++i)
#pragma unroll
    for (int t = 0; t < NDT; ++t) acc[i][t] = f32x4{0, 0, 0, 0};
  int ho[2];
#pragma unroll
  for (int u = 0; u < 2; ++u) {
    const int k = 32 * kh + 8 * kq + 4 * u + (r >> 2), i = (r & 3) >> 1;
    ho[u] = (2 * (k >> 4) + i) * 1024 + (32 * lh + ((k & 15) ^ (4 * i))) * 16 + (r & 1) * 8;
  }
  const int co = H_C + (16 * NDT * dg + r) * 128 + (((4 * kh + kq) ^ (r & 7)) << 4);
  bf16x8 hb[2][2][2], ca[2][2][NDT];
  auto rd = [&](auto SET_, int D) {
    constexpr int SET = decltype(SET_)::value;
#pragma unroll
    for (int u = 0; u < 2; ++u) {
      const int s = 2 * D + u;
      LDS const char* hbuf = smem + h_slot(s & 7);
#pragma unroll
      for (int i = 0; i < 2; ++i) hb[SET][u][i] = tr_frag(hbuf + ho[0] + i * 256, hbuf + ho[1] + i * 256);
      LDS const char* cbuf = smem + (s & 7) * H_CS;
#pragma unroll
      for (int t = 0; t < NDT; ++t) ca[SET][u][t] = lds_frag(cbuf + co + t * 2048);
    }
    __builtin_amdgcn_sched_barrier(0);
  };
  auto mma = [&](auto SET_) {
    constexpr int SET = decltype(SET_)::value;
#pragma unroll
    for (int u = 0; u < 2; ++u)
#pragma unroll
      for (int t = 0; t < NDT; ++t) {
        acc[0][t] = mfma16(ca[SET][u][t], hb[SET][u][0], acc[0][t]);
        acc[1][t] = mfma16(ca[SET][u][t], hb[SET][u][1], acc[1][t]);
      }
    __builtin_amdgcn_sched_barrier(0);
  };
  using S0 = std::integral_constant<int, 0>;
  using S1 = std::integral_constant<int, 1>;
  STAMP(1, 1);
  bar();
  STAMP(1, 2);
  rd(S0{}, 0);
#pragma unroll 1
  for (int D = 1; D < 9; D += 2) {
    bar(); rd(S1{}, D); mma(S0{});
    bar(); rd(S0{}, D + 1); mma(S1{});
  }
  mma(S0{});
  __builtin_amdgcn_sched_barrier(0);
  STAMP(1, 3);
  bar();
  if (w == 0) __hip_atomic_store(inb + lane, 0ull, RLX_AGENT);
  LDS char* ex = smem + w * (NDT * 1024) + lane * 16;
#pragma unroll
  for (int t = 0; t < NDT; ++t) {
    const f32x4 a0 = acc[0][t], a1 = acc[1][t];
    *(LDS f32x4*)(ex + t * 1024) = kh ? +a0 : +a1;
  }
  bar();
  LDS const char* ey = smem + (w ^ 1) * (NDT * 1024) + lane * 16;
  constexpr int OSTR = W * 4 + 16;
  const f32x2 gg = *(LDS const f32x2*)(smem + H_G + (16 * lt + r) * 8);
#pragma unroll
  for (int t = 0; t < NDT; ++t) {
    const int dt = NDT * dg + t;
    const f32x4 o = *(LDS const f32x4*)(ey + t * 1024);
    const f32x4 a0 = acc[0][t], a1 = acc[1][t];
    const f32x4 y = (kh ? +a1 : +a0) + o;
    const s16x4 av = tr_read(smem + H_AT + (16 * dt + 4 * kq + (r >> 2)) * 128 + lt * 32 + (r & 3) * 8);
    const u32x2 xv = *(LDS const u32x2*)(smem + H_XT + (16 * lt + r) * (W * 2) + (16 * dt + 4 * kq) * 2);
    f32x4 res;
#pragma unroll
    for (int i = 0; i < 4; ++i) {
      const u32 xw = xv[i >> 1], aw = da[t][i >> 1], bw = db[t][i >> 1];
      const float xf = (i & 1) ? bfhi(xw) : bflo(xw);
      const float sa = (i & 1) ? bfhi(aw) : bflo(aw);
      const float sb = (i & 1) ? bfhi(bw) : bflo(bw);
      const float yc = bf2f((u16)av[i]);
      const float a = rbf(yc + rbf(xf * sa));
      const float b = y[i] + rbf(xf * sb);
      res[i] = rbf(gg[0] * a) + gg[1] * b;
    }
    *(LDS f32x4*)(smem + H_OUT + (16 * lt + r) * OSTR + (16 * dt + 4 * kq) * 4) = res;
  }
  STAMP(1, 4);
  bar();
#pragma unroll
  for (int it = 0; it < NDT; ++it) {
    const int id = it * 512 + w * 64 + lane;
    const int row = id / (8 * NDT), pc = id - row * (8 * NDT);
    const f32x4 v = *(LDS const f32x4*)(smem + H_OUT + row * OSTR + pc * 16);
    __builtin_amdgcn_raw_buffer_store_b128(__builtin_bit_cast(u32x4, v), mk_rsrc(p.out, SL * DM * 4), ((l0 + row) * DM + d0 + pc * 4) * 4, 0, OUT_AUX);
  }
}

template <int NDT>
DEV void phase2(const P& p, LDS char* smem, int lt, int d0, int w, int lane, bool scratch_barrier, int sg, const u32x2 (&dsk)[6]) {
  STAMP(1, 0);
  if (w >= 8) {
    p2_loader<NDT>(p, smem, lt, d0, w - 8, lane, scratch_barrier, [&]() { if (NDT == 1) { wait_vm<8>(); s_signal(p, smem, sg, lane); } });
  } else {
    if (NDT == 1) { wait_vm<0>(); s_signal(p, smem, sg, lane); }
    p2_compute<NDT>(p, smem, lt, d0, w, lane, dsk);
  }
  STAMP(1, 15);
}

__global__ __launch_bounds__(768, 3) void gated_mix_s4d_s5(P p) {
  extern __shared__ __attribute__((aligned(16))) char smem_raw[];
  LDS char* smem = (LDS char*)smem_raw;
  u64 pc;
  asm volatile("s_getpc_b64 %0" : "=s"(pc));
  u32 pf = 0;
#define PF_TOP 312
  if (threadIdx.x < PF_TOP && blockIdx.x >= 144)
    asm volatile("global_load_dword %0, %1, off" : "=v"(pf) : "v"((pc & ~255ull) + threadIdx.x * 128));
  const int w = __builtin_amdgcn_readfirstlane(threadIdx.x >> 6), lane = threadIdx.x & 63;
  const int xcd = blockIdx.x & 7, j = blockIdx.x >> 3;
  u32x2 dsk[6];
#pragma unroll
  for (int i = 0; i < 6; ++i) dsk[i] = u32x2{0, 0};
  STAMP(0, 0);
  if (j >= 18 && j < 30) {
    const int sg = 12 * xcd + (j - 18);
    role_s(p, smem, sg, w, lane);
    STAMP(0, 15);
    phase2<1>(p, smem, 4 * xcd + (j - 18) / 3, 480 + 32 * ((j - 18) % 3), w, lane, false, sg, dsk);
  } else {
    const bool isg = j < 18;
    if (isg) {
      const int c = 2 * xcd + j / 9, g = j % 9;
      if (w >= 8) g_loader(p, smem, c, g, 96 * (j % 5), w - 8, lane);
      else if (w < 4) g_compute<0>(p, smem, c, g, w, lane, pf);
      else g_compute<1>(p, smem, c, g, w, lane, pf);
    } else {
      role_gate(p, 2 * xcd + (j - 30), w, lane, pc);
    }
    STAMP(0, 15);
    const int e = isg ? j : j - 12;
    phase2<3>(p, smem, 4 * xcd + e / 5, 96 * (e % 5), w, lane, isg, 0, dsk);
  }
  asm volatile("s_waitcnt vmcnt(0)" :: "v"(pf));
}

extern "C" void kernel_launch(void* const* d_in, const int* in_sizes, int n_in, void* d_out, int out_size,
                              void* d_ws, size_t ws_size, hipStream_t stream) {
  P a;
  a.x = (const u16*)d_in[0]; a.la = (const u16*)d_in[1]; a.aim = (const u16*)d_in[2];
  a.creA = (const u16*)d_in[3]; a.cimA = (const u16*)d_in[4]; a.ldtA = (const u16*)d_in[5]; a.dskA = (const u16*)d_in[6];
  a.lre = (const u16*)d_in[8]; a.lim = (const u16*)d_in[9]; a.bre = (const u16*)d_in[10]; a.bim = (const u16*)d_in[11];
  a.cre = (const u16*)d_in[12]; a.cim = (const u16*)d_in[13]; a.ldtB = (const u16*)d_in[14];
  a.dskB = (const u16*)d_in[15]; a.gw = (const u16*)d_in[16];
  a.ws = (char*)d_ws + WS_OFF; a.out = (float*)d_out;
  (void)hipFuncSetAttribute((const void*)gated_mix_s4d_s5, hipFuncAttributeMaxDynamicSharedMemorySize, K_LDS);
  gated_mix_s4d_s5<<<256, 768, K_LDS, stream>>>(a);
}
```

```cpp
#define WARM_BAR 1
#define PF_NOT_G 1
#include <hip/hip_runtime.h>
#include <hip/hip_bf16.h>
#include <stdint.h>
#include <type_traits>

typedef unsigned short u16;
typedef unsigned int u32;
typedef unsigned long long u64;
typedef __attribute__((ext_vector_type(8))) __bf16 bf16x8;
typedef __attribute__((ext_vector_type(2))) __bf16 bf16x2;
typedef __attribute__((ext_vector_type(4))) short s16x4;
typedef __attribute__((ext_vector_type(4))) float f32x4;
typedef __attribute__((ext_vector_type(2))) float f32x2;
typedef __attribute__((ext_vector_type(4))) unsigned u32x4;
typedef __attribute__((ext_vector_type(2))) unsigned u32x2;
#define LDS __attribute__((address_space(3)))
#define GLB __attribute__((address_space(1)))
#define DEV __device__ __forceinline__
#define RLX_AGENT __ATOMIC_RELAXED, __HIP_MEMORY_SCOPE_AGENT

constexpr int SL = 2048;
constexpr int DM = 576;

constexpr size_t WS_HB = 0;
constexpr size_t WS_AT = WS_HB + (size_t)2 * DM * SL * 2;
constexpr size_t WS_G = WS_AT + (size_t)DM * SL * 2;
constexpr size_t WS_GRAN = WS_G + (size_t)SL * 8;
constexpr size_t WS_EP = WS_GRAN + (size_t)9 * 16 * 2 * 64 * 8;
constexpr size_t WS_FL = WS_EP + 4096;
constexpr size_t WS_XT = WS_FL + (size_t)16 * DM * 8;
constexpr size_t WS_IN = WS_XT + (size_t)DM * SL * 2;
constexpr u64 FLAG_SET = 0x9E3779B97F4A7C15ull;
constexpr u32 SPIN_CAP = 1u << 17;
constexpr int IN_H = 0, IN_G = 36, IN_A = 40;

constexpr size_t WS_ST = (size_t)16 << 20;
#define STAMP(kern, idx) do {} while (0)
#define CSTAMP(kern, idx) do {} while (0)
#define LSTAMP(kern, idx) do {} while (0)
#define LSTAMP2(kern, idx) do {} while (0)

DEV float bf2f(u16 v) { return __uint_as_float((u32)v << 16); }
DEV float bflo(u32 v) { return __uint_as_float(v << 16); }
DEV float bfhi(u32 v) { return __uint_as_float(v & 0xffff0000u); }
DEV u32 pack_bf16(float lo, float hi) {
  f32x2 v = {lo, hi};
  bf16x2 b = __builtin_convertvector(v, bf16x2);
  return __builtin_bit_cast(u32, b);
}
DEV float rbf(float x) { return bflo(pack_bf16(x, x)); }
template <int N> DEV void wait_vm() { asm volatile("s_waitcnt vmcnt(%0)" ::"n"(N) : "memory"); }
DEV void bar() { asm volatile("s_waitcnt lgkmcnt(0)\n\ts_barrier" ::: "memory"); }
DEV void wave_fence() { __builtin_amdgcn_fence(__ATOMIC_ACQ_REL, "wavefront"); }
template <int AUX> DEV void dma16(const void* src, LDS char* dst) {
  __builtin_amdgcn_global_load_lds((const GLB u32*)src, (LDS u32*)dst, 16, 0, AUX);
}
constexpr int SC1 = 16;
#define GPOLL_SLEEP 1
#define GPOLL_DELAY 24
#define POLL_SLEEP 1
#define OUT_AUX 16
#define WS_OFF 0
#define XT_WAIT 4
DEV bf16x8 lds_frag(LDS const char* p) { return *(LDS const bf16x8*)p; }
DEV s16x4 tr_read(LDS const char* a) { return __builtin_amdgcn_ds_read_tr16_b64_v4i16((LDS s16x4*)a); }
DEV bf16x8 tr_frag(LDS const char* a, LDS const char* b) {
  const s16x4 lo = tr_read(a), hi = tr_read(b);
  return __builtin_bit_cast(bf16x8, __builtin_shufflevector(lo, hi, 0, 1, 2, 3, 4, 5, 6, 7));
}
DEV f32x4 mfma16(bf16x8 a, bf16x8 b, f32x4 c) { return __builtin_amdgcn_mfma_f32_16x16x32_bf16(a, b, c, 0, 0, 0); }
DEV __amdgpu_buffer_rsrc_t mk_rsrc(const void* base, int bytes) {
  return __builtin_amdgcn_make_buffer_rsrc(const_cast<void*>(base), (short)0, bytes, 0x00020000);
}
struct cf { float r, i; };
DEV cf cmul(cf a, cf b) { return {a.r * b.r - a.i * b.i, a.r * b.i + a.i * b.r}; }
DEV cf cfma(cf a, cf b, cf c) { return {fmaf(a.r, b.r, fmaf(-a.i, b.i, c.r)), fmaf(a.r, b.i, fmaf(a.i, b.r, c.i))}; }
typedef f32x2 c2;
DEV c2 cm2(c2 a, c2 b) {
  c2 t, r;
  asm("v_pk_mul_f32 %0, %1, %2 op_sel:[0,0] op_sel_hi:[0,1]" : "=v"(t) : "v"(a), "v"(b));
  asm("v_pk_fma_f32 %0, %1, %2, %3 op_sel:[1,1,0] op_sel_hi:[1,0,1] neg_lo:[1,0,0]" : "=v"(r) : "v"(a), "v"(b), "v"(t));
  return r;
}
DEV f32x2 fma2(f32x2 a, f32x2 b, f32x2 c) { return __builtin_elementwise_fma(a, b, c); }
DEV f32x2 lo2(f32x4 v) { return __builtin_shufflevector(v, v, 0, 1); }
DEV f32x2 hi2(f32x4 v) { return __builtin_shufflevector(v, v, 2, 3); }
DEV f32x4 cat2(f32x2 a, f32x2 b) { return __builtin_shufflevector(a, b, 0, 1, 2, 3); }
DEV void wait_flags(GLB u64* word, bool need) {
  for (u32 spin = 0;; ++spin) {
    const u64 v = __hip_atomic_load(word, RLX_AGENT);
    if (__all(!need || v == FLAG_SET) || spin > SPIN_CAP) break;
    __builtin_amdgcn_s_sleep(POLL_SLEEP);
  }
}
DEV int tile_block(int lt, int idx) {
  const int xcd = lt >> 2, a = lt & 3;
  int j;
  if (idx < 5) { const int e = 5 * a + idx; j = e < 18 ? e : e + 12; }
  else j = 18 + 3 * a + (idx - 5);
  return (j << 3) | xcd;
}

struct P {
  const u16 *x, *la, *aim, *creA, *cimA, *ldtA, *dskA;
  const u16 *lre, *lim, *bre, *bim, *cre, *cim, *ldtB, *dskB, *gw;
  char* ws;
  float* out;
};

constexpr int G_NB = 4, G_STAGE = 32768;
constexpr int G_E = 153536;
constexpr int G_PF = G_E + 4096;
constexpr int G_L = G_PF + 4096;
constexpr int G_X = G_L + 1024;
constexpr int G_GO = G_X + 1024;
constexpr int G_T = G_NB * G_STAGE;
static_assert(G_GO + 4 <= 163840 && G_T + 4 * 32 * 144 <= G_E, "role G scratch");

DEV void g_loader(const P& p, LDS char* smem, int c, int g, int d0, int lw, int lane) {
  [[maybe_unused]] const int w = lw + 8;
  __builtin_amdgcn_s_setprio(3);
  GLB u32* ep = (GLB u32*)(p.ws + WS_EP + g * 256);
  GLB u64* gran = (GLB u64*)(p.ws + WS_GRAN) + (size_t)g * 16 * 128;
  const u32 tag = __hip_atomic_load(ep, RLX_AGENT) + 1u;
  u32 xo[4], bo[4];
#pragma unroll
  for (int i = 0; i < 4; ++i) {
    const int rho = 8 * (4 * lw + i) + (lane >> 3);
    const int hf = rho >> 6, m = (rho >> 4) & 3, r = rho & 15;
    const int tau = 64 * hf + 16 * (r >> 2) + 8 * (r & 1) + 2 * m + ((r >> 1) & 1);
    const int ch = (lane & 7) ^ (rho & 7);
    xo[i] = (u32)(128 * c + tau) * 1152u + ch * 16;
    const int pp = 64 * g + 16 * (rho >> 5) + r;
    bo[i] = (u32)pp * 1152u + ch * 16;
  }
  auto issue = [&](int s) {
    LDS char* buf = smem + (s & (G_NB - 1)) * G_STAGE + lw * 4096;
    int kb = g + s; kb = (kb >= 9 ? kb - 9 : kb) * 128;
#pragma unroll
    for (int i = 0; i < 4; ++i) dma16<0>((const char*)p.x + (xo[i] + kb), buf + i * 1024);
#pragma unroll
    for (int i = 0; i < 4; ++i) {
      const char* base = (const char*)((((4 * lw + i) >> 1) & 1) ? p.bim : p.bre);
      dma16<0>(base + (bo[i] + kb), buf + 16384 + i * 1024);
    }
  };
#pragma unroll
  for (int s = 0; s < G_NB - 1; ++s) issue(s);
#pragma unroll 1
  for (int s = 0; s < 6; ++s) { wait_vm<16>(); LSTAMP(0, 16 + s); bar(); issue(s + G_NB - 1); LSTAMP(0, 32 + s); }
  wait_vm<16>(); bar();
  wait_vm<8>(); bar();
  wait_vm<0>(); bar();
  STAMP(0, 1);
  if (lw < 2) {
    bar();
    u32 co[6];
#pragma unroll
    for (int i = 0; i < 6; ++i) {
      const int row = 8 * (6 * lw + i) + (lane >> 3), cp = lane & 7;
      co[i] = (u32)(d0 + row) * 1152u + ((cp ^ (row & 7)) << 4);
    }
    const int rot = ((d0 / 96) * 18) >> 3;
#pragma unroll 1
    for (int s = 0; s < 8; ++s) {
      LDS char* buf = smem + 32768 + s * 12288 + 6 * lw * 1024;
      const int ks = s + rot;
      const char* cb = ks < 9 ? (const char*)p.cre + ks * 128 : (const char*)p.cim + (ks - 9) * 128;
#pragma unroll
      for (int i = 0; i < 6; ++i) dma16<0>(cb + co[i], buf + i * 1024);
    }
    bar();
    return;
  }
  bar();
  STAMP(0, 2);
  if (lw == 3) {
    const f32x2 lv = *(LDS const f32x2*)(smem + G_L + lane * 8);
    const cf l16 = {lv[0], lv[1]};
    f32x2 ev[8];
#pragma unroll
    for (int s = 0; s < 8; ++s) ev[s] = *(LDS const f32x2*)(smem + G_E + (s * 64 + lane) * 8);
    cf tot = {0.f, 0.f};
    cf pre[8];
#pragma unroll
    for (int s = 0; s < 8; ++s) { pre[s] = tot; tot = cfma(l16, tot, cf{ev[s][0], ev[s][1]}); }
    if (c < 15) {
      __hip_atomic_store(gran + c * 128 + lane, ((u64)tag << 32) | __float_as_uint(tot.r), RLX_AGENT);
      __hip_atomic_store(gran + c * 128 + 64 + lane, ((u64)tag << 32) | __float_as_uint(tot.i), RLX_AGENT);
    }
#pragma unroll
    for (int s = 0; s < 8; ++s) *(LDS f32x2*)(smem + G_PF + (s * 64 + lane) * 8) = f32x2{pre[s].r, pre[s].i};
  }
  const int par = lw - 2;
  const f32x2 lv = *(LDS const f32x2*)(smem + G_L + 512 + lane * 8);
  const cf l128 = {lv[0], lv[1]};
  const cf l256 = cmul(l128, l128);
  cf acc = {0.f, 0.f};
  if (par < c) {
    const int nc = (c - par + 1) >> 1;
    u64 a[8], b[8];
    __builtin_amdgcn_s_sleep(GPOLL_DELAY);
    for (u32 spin = 0;; ++spin) {
      bool ok = true;
#pragma unroll
      for (int i = 0; i < 8; ++i) {
        const int cc = par + 2 * (i < nc ? i : 0);
        a[i] = __hip_atomic_load(gran + cc * 128 + lane, RLX_AGENT);
        b[i] = __hip_atomic_load(gran + cc * 128 + 64 + lane, RLX_AGENT);
      }
#pragma unroll
      for (int i = 0; i < 8; ++i) ok = ok && (u32)(a[i] >> 32) == tag && (u32)(b[i] >> 32) == tag;
      if (__all(ok) || spin > SPIN_CAP) break;
      __builtin_amdgcn_s_sleep(GPOLL_SLEEP);
    }
#pragma unroll
    for (int i = 0; i < 8; ++i)
      if (i < nc) acc = cfma(l256, acc, cf{__uint_as_float((u32)a[i]), __uint_as_float((u32)b[i])});
    if (c - 1 - (par + 2 * (nc - 1))) acc = cmul(acc, l128);
  }
  *(LDS f32x2*)(smem + G_X + (par * 64 + lane) * 8) = f32x2{acc.r, acc.i};
  STAMP(0, 3);
  bar();
  if (c == 15 && lw == 2 && lane == 0) __hip_atomic_store(ep, tag, RLX_AGENT);
}

template <int HF>
DEV void g_compute(const P& p, LDS char* smem, int c, int g, int w, int lane, u32& pf) {
  const int pq = w & 3, col = lane & 15, q = lane >> 4;
  const int pl = 16 * pq + col, pp = 64 * g + pl;
  cf lam, coef2;
  {
    const float lre = bf2f(p.lre[pp]), lim = bf2f(p.lim[pp]);
    const float dt = rbf(expf(bf2f(p.ldtB[pp])));
    const float zr = dt * lre, zi = dt * lim;
    const float er = expf(zr);
    float sn, cs;
    sincosf(zi, &sn, &cs);
    lam = {er * cs, er * sn};
    const float nr = lam.r - 1.0f, ni = lam.i;
    const float den = 1.0f / (lre * lre + lim * lim);
    coef2 = {2.0f * (nr * lre + ni * lim) * den, 2.0f * (ni * lre - nr * lim) * den};
  }
  cf l8 = cmul(lam, lam); l8 = cmul(l8, l8); l8 = cmul(l8, l8);
  const cf l16 = cmul(l8, l8);
  const cf l32 = cmul(l16, l16), l64 = cmul(l32, l32);
  if (HF == 0 && q == 0) {
    const cf l128 = cmul(l64, l64);
    *(LDS f32x2*)(smem + G_L + pl * 8) = f32x2{l16.r, l16.i};
    *(LDS f32x2*)(smem + G_L + 512 + pl * 8) = f32x2{l128.r, l128.i};
  }
  if (HF == 0 && w == 0 && lane == 0) *(LDS u32*)(smem + G_GO) = 0;
  cf pw = HF ? l64 : cf{1.f, 0.f};
  { const cf t = cmul(pw, l16); if (q & 1) pw = t; }
  { const cf t = cmul(pw, l32); if (q & 2) pw = t; }
  asm volatile("" : "+v"(lam.r), "+v"(lam.i), "+v"(coef2.r), "+v"(coef2.i), "+v"(l8.r), "+v"(l8.i), "+v"(pw.r), "+v"(pw.i));
  f32x4 are[4], aim[4];
#pragma unroll
  for (int m = 0; m < 4; ++m) { are[m] = f32x4{0, 0, 0, 0}; aim[m] = f32x4{0, 0, 0, 0}; }
  const int o0 = col * 128 + (((0 + q) ^ (col & 7)) << 4);
  const int o1 = col * 128 + (((4 + q) ^ (col & 7)) << 4);
  bf16x8 br[2], bi[2], a[2][4];
  auto read_frags = [&](int s) {
    LDS const char* buf = smem + (s & (G_NB - 1)) * G_STAGE;
    LDS const char* ab = buf + HF * 8192;
    LDS const char* bb = buf + 16384 + pq * 4096;
#pragma unroll
    for (int ks = 0; ks < 2; ++ks) {
      const int o = ks ? o1 : o0;
      br[ks] = lds_frag(bb + o); bi[ks] = lds_frag(bb + 2048 + o);
#pragma unroll
      for (int m = 0; m < 4; ++m) a[ks][m] = lds_frag(ab + m * 2048 + o);
    }
  };
  auto mma = [&]() {
#pragma unroll
    for (int ks = 0; ks < 2; ++ks)
#pragma unroll
      for (int m = 0; m < 4; ++m) {
        are[m] = mfma16(a[ks][m], br[ks], are[m]);
        aim[m] = mfma16(a[ks][m], bi[ks], aim[m]);
      }
  };
  STAMP(0, 1);
#pragma unroll
  for (int s = 0; s < 9; ++s) {
    if (HF == 1 && s == XT_WAIT) {
      wait_vm<0>();
      if (lane < 16) __hip_atomic_store((GLB u64*)(p.ws + WS_FL) + c * DM + 64 * g + 16 * pq + lane, FLAG_SET, RLX_AGENT);
    }
    bar();
    if (s == 0) { STAMP(0, 2); CSTAMP(0, 60); }
    if (HF == 1 && s > 0) { mma(); __builtin_amdgcn_sched_barrier(0); }
    if (HF == 1 && s == 0) {
      const int rr = 4 * q + (col >> 2);
      LDS const char* tb = smem + rr * 128 + (((2 * pq + ((col & 3) >> 1)) ^ (rr & 7)) << 4) + (col & 1) * 8;
      LDS char* sc = smem + G_T + pq * (32 * 144);
#pragma unroll
      for (int hf = 0; hf < 2; ++hf) {
        u32x2 t[4];
#pragma unroll
        for (int m = 0; m < 4; ++m) t[m] = __builtin_bit_cast(u32x2, tr_read(tb + hf * 8192 + m * 2048));
        u32x4 oa, ob;
#pragma unroll
        for (int m = 0; m < 4; ++m) {
          oa[m] = __builtin_amdgcn_perm(t[m][1], t[m][0], 0x05040100u);
          ob[m] = __builtin_amdgcn_perm(t[m][1], t[m][0], 0x07060302u);
        }
        *(LDS u32x4*)(sc + hf * (16 * 144) + col * 144 + q * 32) = oa;
        *(LDS u32x4*)(sc + hf * (16 * 144) + col * 144 + q * 32 + 16) = ob;
      }
      wave_fence();
      const __amdgpu_buffer_rsrc_t rs = mk_rsrc(p.ws + WS_XT, DM * SL * 2);
#pragma unroll
      for (int u = 0; u < 4; ++u) {
        const int row = (lane >> 3) + 8 * (u & 1), hf = u >> 1;
        const u32x4 v = *(LDS const u32x4*)(sc + hf * (16 * 144) + row * 144 + (lane & 7) * 16);
        __builtin_amdgcn_raw_buffer_store_b128(v, rs, ((64 * g + 16 * pq + row) * SL + 128 * c + 64 * hf) * 2 + (lane & 7) * 16, 0, SC1);
      }
      __builtin_amdgcn_sched_barrier(0);
    }
    read_frags(s);
    __builtin_amdgcn_sched_barrier(0);
    if (HF == 0) { mma(); __builtin_amdgcn_sched_barrier(0); }
  }
  if (HF == 1) mma();
  __builtin_amdgcn_sched_barrier(0);
  STAMP(0, 3); CSTAMP(0, 61);
  const f32x2 Lr = {lam.r, lam.r}, Li = {lam.i, lam.i};
  f32x2 er = {0.f, 0.f}, ei = {0.f, 0.f};
#pragma unroll
  for (int t = 0; t < 8; ++t) {
    const f32x2 br_ = (t & 1) ? hi2(are[t >> 1]) : lo2(are[t >> 1]);
    const f32x2 bi_ = (t & 1) ? hi2(aim[t >> 1]) : lo2(aim[t >> 1]);
    const f32x2 nr = fma2(-Li, ei, fma2(Lr, er, br_));
    ei = fma2(Li, er, fma2(Lr, ei, bi_));
    er = nr;
  }
  const cf eA = {er[0], ei[0]};
  const cf e = cfma(l8, eA, cf{er[1], ei[1]});
  const int seg = 4 * HF + q;
  *(LDS f32x2*)(smem + G_E + (seg * 64 + pl) * 8) = f32x2{e.r, e.i};
  STAMP(0, 4);
  bar();
  STAMP(0, 5);
  {
#pragma unroll
    for (int m = 0; m < 4; ++m) asm volatile("" : "+v"(are[m]), "+v"(aim[m]));
    const f32x2 Cr = {coef2.r, coef2.r}, Ci = {coef2.i, coef2.i};
#pragma unroll
    for (int m = 0; m < 4; ++m) {
      const f32x2 r0 = lo2(are[m]), r1 = hi2(are[m]), i0 = lo2(aim[m]), i1 = hi2(aim[m]);
      are[m] = cat2(fma2(-Ci, i0, Cr * r0), fma2(-Ci, i1, Cr * r1));
      aim[m] = cat2(fma2(-Ci, r0, -Cr * i0), fma2(-Ci, r1, -Cr * i1));
      asm volatile("" : "+v"(are[m]), "+v"(aim[m]));
    }
  }
  cf eAc = cmul(coef2, eA), pwc = cmul(coef2, pw);
  asm volatile("" : "+v"(eAc.r), "+v"(eAc.i), "+v"(pwc.r), "+v"(pwc.i));
  STAMP(0, 7);
  bar();
  STAMP(0, 8);
  cf cin;
  {
    f32x2 v[2];
#pragma unroll
    for (int i = 0; i < 2; ++i) v[i] = *(LDS const f32x2*)(smem + G_X + (i * 64 + pl) * 8);
    const f32x2 pf = *(LDS const f32x2*)(smem + G_PF + (seg * 64 + pl) * 8);
    const f32x2 S = v[0] + v[1];
    cin = cfma(pwc, cf{S[0], S[1]}, cmul(coef2, cf{pf[0], pf[1]}));
  }
  const cf cinB = cfma(l8, cin, eAc);
  er = f32x2{cin.r, cinB.r}; ei = f32x2{-cin.i, -cinB.i};
  f32x2 hr[8], hi[8];
#pragma unroll
  for (int t = 0; t < 8; ++t) {
    const f32x2 br_ = (t & 1) ? hi2(are[t >> 1]) : lo2(are[t >> 1]);
    const f32x2 bi_ = (t & 1) ? hi2(aim[t >> 1]) : lo2(aim[t >> 1]);
    const f32x2 nr = fma2(Li, ei, fma2(Lr, er, br_));
    ei = fma2(-Li, er, fma2(Lr, ei, bi_));
    er = nr;
    hr[t] = er; hi[t] = ei;
  }
  u32x4 o[4];
#pragma unroll
  for (int t = 0; t < 4; ++t) {
    o[0][t] = pack_bf16(hr[2 * t][0], hr[2 * t + 1][0]);
    o[1][t] = pack_bf16(hr[2 * t][1], hr[2 * t + 1][1]);
    o[2][t] = pack_bf16(hi[2 * t][0], hi[2 * t + 1][0]);
    o[3][t] = pack_bf16(hi[2 * t][1], hi[2 * t + 1][1]);
  }
  STAMP(0, 9);
  {
    const __amdgpu_buffer_rsrc_t rs = mk_rsrc(p.ws + WS_HB, 2 * DM * SL * 2);
    const int hb = (((c * 2 + HF) * 2) * 9 + g) * 8192 + pq * 2048;
    const int p0 = (16 * q + col) * 16, p1 = 1024 + (16 * q + (col ^ 4)) * 16;
    __builtin_amdgcn_raw_buffer_store_b128(o[0], rs, hb + p0, 0, SC1);
    __builtin_amdgcn_raw_buffer_store_b128(o[1], rs, hb + p1, 0, SC1);
    __builtin_amdgcn_raw_buffer_store_b128(o[2], rs, hb + 9 * 8192 + p0, 0, SC1);
    __builtin_amdgcn_raw_buffer_store_b128(o[3], rs, hb + 9 * 8192 + p1, 0, SC1);
    wait_vm<0>();
    STAMP(0, 10);
    if (lane < 8)
      __hip_atomic_store((GLB u64*)(p.ws + WS_IN) + tile_block(2 * c + HF, lane) * 64 + IN_H + g * 4 + pq, FLAG_SET, RLX_AGENT);
    bar();
  }
}

constexpr int S_CPY = 4160;
constexpr int S_X = 0;
constexpr int S_ST = 6 * 4 * S_CPY;
constexpr int S_Z = S_ST;
constexpr int S_ZSZ = (256 + SL) * 2;
constexpr int S_T = S_ST + 12 * 4608;
constexpr int S_TSZ = 1312;
constexpr int S_END = S_T + 6 * S_TSZ;
constexpr int S_CNT = 32768 + 8192;

DEV c2 cexp_k(float zl, float th, float tl, float k) {
  const float mag = __builtin_amdgcn_exp2f(zl * k);
  const float t = fmaf(tl, k, __builtin_amdgcn_fractf(th * k));
  return c2{mag * __builtin_amdgcn_cosf(t), mag * __builtin_amdgcn_sinf(t)};
}
#define PKM(d, a, b) "v_pk_mul_f32 " d ", " a ", " b " op_sel:[0,0] op_sel_hi:[0,1]\n\t"
#define PKF(d, a, b) "v_pk_fma_f32 " d ", " a ", " b ", " d " op_sel:[1,1,0] op_sel_hi:[1,0,1] neg_lo:[1,0,0]\n\t"
DEV void octet(LDS char* dst, c2 s0, c2 m1, c2 m2) {
  c2 v1, v2, v3, t0, t1, t2, t3;
  asm(PKM("%0", "%7", "%8") PKM("%3", "%7", "%9") PKF("%0", "%7", "%8") PKF("%3", "%7", "%9")
      PKM("%1", "%0", "%8") PKM("%4", "%0", "%9") PKF("%1", "%0", "%8") PKF("%4", "%0", "%9")
      PKM("%2", "%1", "%8") PKM("%5", "%1", "%9") PKF("%2", "%1", "%8") PKF("%5", "%1", "%9")
      PKM("%6", "%2", "%9") PKF("%6", "%2", "%9")
      : "=&v"(v1), "=&v"(v2), "=&v"(v3), "=&v"(t0), "=&v"(t1), "=&v"(t2), "=&v"(t3) : "v"(s0), "v"(m1), "v"(m2));
  *(LDS u32*)(dst + 0 * 288) = pack_bf16(s0[0], s0[1]);
  *(LDS u32*)(dst + 1 * 288) = pack_bf16(v1[0], v1[1]);
  *(LDS u32*)(dst + 2 * 288) = pack_bf16(v2[0], v2[1]);
  *(LDS u32*)(dst + 3 * 288) = pack_bf16(v3[0], v3[1]);
  *(LDS u32*)(dst + 4 * 288) = pack_bf16(t0[0], t0[1]);
  *(LDS u32*)(dst + 5 * 288) = pack_bf16(t1[0], t1[1]);
  *(LDS u32*)(dst + 6 * 288) = pack_bf16(t2[0], t2[1]);
  *(LDS u32*)(dst + 7 * 288) = pack_bf16(t3[0], t3[1]);
}
DEV u32 dup_lo(u32 a) { return __builtin_amdgcn_perm(a, a, 0x01000100u); }
DEV u32 dup_hi(u32 a) { return __builtin_amdgcn_perm(a, a, 0x03020302u); }
DEV u32 hi_lo(u32 a, u32 b) { return __builtin_amdgcn_alignbit(b, a, 16); }

DEV void role_s(const P& p, LDS char* smem, int sg, int w, int lane) {
  const int tid = w * 64 + lane;
  const int ch = w >> 1, hh = w & 1, d = 6 * sg + ch;
  const int n = d * 128 + 64 * hh + lane;
  const u16 r_ldt = p.ldtA[d], r_la = p.la[n], r_aim = p.aim[n], r_cre = p.creA[n], r_cim = p.cimA[n];
  u32 wb[2] = {0, 0};
  __builtin_amdgcn_sched_barrier(0);
  __builtin_amdgcn_s_barrier();
#pragma unroll
  for (int i = 0; i < 2; ++i) {
    const int ln = i * 768 + tid, all = (sg % 12) * 864 + ln;
    if (ln < 864) wb[i] = *(const GLB u32*)((const char*)(all < 5184 ? p.bre : p.bim) + (size_t)(all < 5184 ? all : all - 5184) * 128);
  }
  __builtin_amdgcn_sched_barrier(0);
  STAMP(0, 1);
  f32x4 tab[3];
  [[maybe_unused]] u32 wc[2] = {0, 0};
  LDS char* st = smem + S_ST + w * 4608;
  {
    const float L2E = 1.44269504088896341f;
    const float dt = rbf(__builtin_amdgcn_exp2f(bf2f(r_ldt) * L2E));
    const float ar = -rbf(__builtin_amdgcn_exp2f(bf2f(r_la) * L2E));
    const float zl = ar * dt * L2E, zi = bf2f(r_aim) * dt;
    const float th = zi * 0.15915494309189535f;
    const float tl = fmaf(zi, 0.15915494309189535f, -th) + zi * 4.6017384e-10f;
    const c2 C = {2.0f * (bf2f(r_cre) * dt), 2.0f * (bf2f(r_cim) * dt)};
    const c2 w1 = cexp_k(zl, th, tl, 1.f), w8 = cexp_k(zl, th, tl, 8.f), w128 = cexp_k(zl, th, tl, 128.f);
    c2 w2, w4, w16, w32, w64, w256, w512, w1024, w2048;
    asm(PKM("%0", "%9", "%9") PKM("%2", "%10", "%10") PKM("%5", "%11", "%11") PKF("%0", "%9", "%9") PKF("%2", "%10", "%10") PKF("%5", "%11", "%11")
        PKM("%1", "%0", "%0") PKM("%3", "%2", "%2") PKM("%6", "%5", "%5") PKF("%1", "%0", "%0") PKF("%3", "%2", "%2") PKF("%6", "%5", "%5")
        PKM("%4", "%3", "%3") PKM("%7", "%6", "%6") PKF("%4", "%3", "%3") PKF("%7", "%6", "%6")
        PKM("%8", "%7", "%7") PKF("%8", "%7", "%7")
        : "=&v"(w2), "=&v"(w4), "=&v"(w16), "=&v"(w32), "=&v"(w64), "=&v"(w256), "=&v"(w512), "=&v"(w1024), "=&v"(w2048)
        : "v"(w1), "v"(w8), "v"(w128));
    c2 C128, C256, C512, C1024, C2048;
    asm(PKM("%0", "%5", "%6") PKM("%1", "%5", "%7") PKM("%2", "%5", "%8") PKM("%3", "%5", "%9") PKM("%4", "%5", "%10")
        PKF("%0", "%5", "%6") PKF("%1", "%5", "%7") PKF("%2", "%5", "%8") PKF("%3", "%5", "%9") PKF("%4", "%5", "%10")
        : "=&v"(C128), "=&v"(C256), "=&v"(C512), "=&v"(C1024), "=&v"(C2048)
        : "v"(C), "v"(w128), "v"(w256), "v"(w512), "v"(w1024), "v"(w2048));
#pragma unroll
    for (int i = 0; i < 2; ++i) {
      const int ln = i * 768 + tid, all = (sg % 12) * 864 + ln;
      if (ln < 864) wc[i] = *(const GLB u32*)((const char*)(all < 5184 ? p.cre : p.cim) + (size_t)(all < 5184 ? all : all - 5184) * 128);
    }
    STAMP(0, 2);
    LDS char* wr = st + 4 * lane;
    LDS const char* rd = st + (lane & 15) * 288 + (lane >> 4) * 16;
    bf16x8 B[4];
    const c2 w8c = {w8[0], -w8[1]}, w32c = {w32[0], -w32[1]}, w64c = {w64[0], -w64[1]};
    octet(wr, c2{1.f, 0.f}, w8c, w32c);
    octet(wr + 8 * 288, w64c, w8c, w32c);
    wave_fence();
#pragma unroll
    for (int ks = 0; ks < 4; ++ks) B[ks] = lds_frag(rd + 64 * ks);
    wave_fence();
#pragma unroll
    for (int mt = 0; mt < 3; ++mt) {
      if (mt == 0) { octet(wr, C, w1, w4); octet(wr + 8 * 288, C128, w1, w4); }
      if (mt == 1) { octet(wr, C256, w2, w128); octet(wr + 8 * 288, C512, w128, w4); }
      if (mt == 2) { octet(wr, C1024, w128, w512); *(LDS u32*)(wr + 8 * 288) = pack_bf16(C2048[0], C2048[1]); }
      wave_fence();
      f32x4 acc = {0, 0, 0, 0};
      bf16x8 A[4];
#pragma unroll
      for (int ks = 0; ks < 4; ++ks) A[ks] = lds_frag(rd + 64 * ks);
      __builtin_amdgcn_sched_barrier(0);
#pragma unroll
      for (int ks = 0; ks < 4; ++ks) acc = mfma16(A[ks], B[ks], acc);
      __builtin_amdgcn_sched_barrier(0);
      wave_fence();
      tab[mt] = acc;
    }
  }
  const int b = lane & 15, q = lane >> 4;
#pragma unroll
  for (int mt = 0; mt < 3; ++mt) *(LDS f32x4*)(st + mt * 1024 + lane * 16) = tab[mt];
  STAMP(0, 3);
  bar();
  {
    LDS const char* ot = smem + S_ST + (w ^ 1) * 4608 + lane * 16;
    LDS char* T = smem + S_T + ch * S_TSZ;
    if (hh == 0) {
      const f32x4 v0 = tab[0] + *(LDS const f32x4*)(ot), v2 = tab[2] + *(LDS const f32x4*)(ot + 2048);
      *(LDS u32x2*)(T + 2 * (128 * (q >> 1) + 8 * b + 4 * (q & 1))) = u32x2{pack_bf16(v0[0], v0[1]), pack_bf16(v0[2], v0[3])};
      const u32 a0 = pack_bf16(v2[0], v2[1]), a1 = pack_bf16(v2[2], v2[3]);
      if (q < 2) {
        LDS char* t3 = T + 2 * (512 + 64 * q + b);
        *(LDS u16*)(t3) = (u16)a0; *(LDS u16*)(t3 + 32) = (u16)(a0 >> 16);
        *(LDS u16*)(t3 + 64) = (u16)a1; *(LDS u16*)(t3 + 96) = (u16)(a1 >> 16);
      } else if (lane == 32) *(LDS u16*)(T + 2 * 640) = (u16)a0;
    } else {
      const f32x4 v1 = tab[1] + *(LDS const f32x4*)(ot + 1024);
      const u32 a0 = pack_bf16(v1[0], v1[1]), a1 = pack_bf16(v1[2], v1[3]);
      if (q < 2) *(LDS u32x2*)(T + 2 * (256 + 64 * q + 4 * b)) = u32x2{a0, a1};
      else {
        LDS char* t2 = T + 2 * (384 + 2 * b + (q - 2));
        *(LDS u16*)(t2) = (u16)a0; *(LDS u16*)(t2 + 64) = (u16)(a0 >> 16);
        *(LDS u16*)(t2 + 128) = (u16)a1; *(LDS u16*)(t2 + 192) = (u16)(a1 >> 16);
      }
    }
  }
  bar();
  STAMP(0, 4);
  u32x4 uv[2];
  {
    GLB u64* fl = (GLB u64*)(p.ws + WS_FL) + (8 * hh + (lane & 7)) * DM + d;
    asm volatile("" :: "v"(wc[0]), "v"(wc[1]), "v"(wb[0]), "v"(wb[1]));
    wait_flags(fl, true);
    const __amdgpu_buffer_rsrc_t rs = mk_rsrc(p.ws + WS_XT, DM * SL * 2);
#pragma unroll
    for (int v = 0; v < 2; ++v) uv[v] = __builtin_amdgcn_raw_buffer_load_b128(rs, (d * SL + 1024 * hh + 8 * (lane + 64 * v)) * 2, 0, SC1);
    if (lane < 8) __hip_atomic_store(fl, 0ull, RLX_AGENT);
  }
  STAMP(0, 5);
  {
    LDS const char* T = smem + S_T + ch * S_TSZ;
    u32 o[10];
    const u32 e512 = *(LDS const u16*)(T + 2 * 512);
    o[8] = o[9] = e512 | (e512 << 16);
    if (hh == 0) {
      const int off = lane < 16 ? 32 * lane : (lane < 32 ? 256 + 16 * lane : 512 + 8 * lane);
      u32x2 W[4];
#pragma unroll
      for (int i = 0; i < 4; ++i) W[i] = *(LDS const u32x2*)(T + off + 8 * i);
      if (lane < 16) {
#pragma unroll
        for (int i = 0; i < 8; ++i) o[i] = W[i >> 1][i & 1];
      } else if (lane < 32) {
#pragma unroll
        for (int i = 0; i < 4; ++i) {
          const u32 a = W[i >> 1][i & 1], nx = W[(i + 1) >> 1][(i + 1) & 1];
          o[2 * i] = dup_lo(a);
          o[2 * i + 1] = hi_lo(a, nx);
        }
      } else {
        const u32 w0 = W[0][0], w1 = W[0][1], w2 = W[1][0];
        o[0] = dup_lo(w0); o[1] = w0; o[2] = dup_hi(w0); o[3] = dup_lo(w1);
        o[4] = o[3]; o[5] = w1; o[6] = dup_hi(w1); o[7] = dup_lo(w2);
      }
    } else {
      const u32 w0 = *(LDS const u32*)(T + 1024 + 4 * lane), w1 = *(LDS const u32*)(T + 1028 + 4 * lane);
      o[0] = o[1] = dup_lo(w0); o[2] = w0; o[3] = o[4] = o[5] = dup_hi(w0); o[6] = o[7] = dup_lo(w1);
    }
    o[8] = (u32)__builtin_amdgcn_update_dpp((int)o[8], (int)o[0], 0x130, 0xf, 0xf, false);
    o[9] = (u32)__builtin_amdgcn_update_dpp((int)o[9], (int)o[1], 0x130, 0xf, 0xf, false);
    u32 a[9];
#pragma unroll
    for (int i = 0; i < 9; ++i) a[i] = hi_lo(o[i], o[i + 1]);
    LDS char* xb = smem + S_X + ch * 4 * S_CPY + 32 + 2 * (1024 * hh + 16 * lane);
    *(LDS u32x4*)(xb) = u32x4{o[0], o[1], o[2], o[3]};
    *(LDS u32x4*)(xb + 16) = u32x4{o[4], o[5], o[6], o[7]};
    *(LDS u32x4*)(xb + S_CPY) = u32x4{a[0], a[1], a[2], a[3]};
    *(LDS u32x4*)(xb + S_CPY + 16) = u32x4{a[4], a[5], a[6], a[7]};
    *(LDS u32x4*)(xb + 2 * S_CPY) = u32x4{o[1], o[2], o[3], o[4]};
    *(LDS u32x4*)(xb + 2 * S_CPY + 16) = u32x4{o[5], o[6], o[7], o[8]};
    *(LDS u32x4*)(xb + 3 * S_CPY) = u32x4{a[1], a[2], a[3], a[4]};
    *(LDS u32x4*)(xb + 3 * S_CPY + 16) = u32x4{a[5], a[6], a[7], a[8]};
    if (hh == 0 && lane == 0) {
      LDS char* hb = smem + S_X + ch * 4 * S_CPY;
      const u32x4 z = {0, 0, 0, 0};
      *(LDS u32x4*)(hb) = z;
      *(LDS u32x4*)(hb + 16) = z;
      *(LDS u32x4*)(hb + S_CPY) = z;
      *(LDS u32x4*)(hb + S_CPY + 16) = u32x4{0, 0, 0, o[0] << 16};
      *(LDS u32x4*)(hb + 2 * S_CPY) = z;
      *(LDS u32x4*)(hb + 2 * S_CPY + 16) = u32x4{0, 0, 0, o[0]};
      *(LDS u32x4*)(hb + 3 * S_CPY) = z;
      *(LDS u32x4*)(hb + 3 * S_CPY + 16) = u32x4{0, 0, o[0] << 16, a[0]};
    }
    if (tid < 6 * 32) *(LDS u32x4*)(smem + S_Z + (tid >> 5) * S_ZSZ + (tid & 31) * 16) = u32x4{0, 0, 0, 0};
    STAMP(0, 6);
    LDS char* z = smem + S_Z + ch * S_ZSZ + 512 + (1024 * hh + 8 * lane) * 2;
    *(LDS u32x4*)z = uv[0];
    *(LDS u32x4*)(z + 1024) = uv[1];
  }
  bar();
  STAMP(0, 7);
  f32x4 acc[8];
#pragma unroll
  for (int i = 0; i < 8; ++i) acc[i] = f32x4{0, 0, 0, 0};
  {
    const int kq = q, qq = b >> 2, pp = b & 3;
    LDS const char* fb = smem + S_X + (ch * 4 + ((4 - qq) & 3)) * S_CPY +
                         2 * (16 + 16 * (kq >> 1) - 8 * (kq & 1) + 4 * pp - (qq ? 4 : 0)) + 64 * hh;
    LDS const char* gb = smem + S_Z + ch * S_ZSZ + 512 + 32 * (b - (kq >> 1)) + 16 * (kq & 1) - 64 * hh;
#pragma unroll 1
    for (int it = 0; it < 4; ++it) {
      bf16x8 F[8], G[8];
#pragma unroll
      for (int s = 0; s < 8; ++s) {
        F[s] = tr_frag(fb + 512 * s, fb + 512 * s - 8);
        G[s] = lds_frag(gb + 512 * s);
      }
      __builtin_amdgcn_sched_barrier(0);
#pragma unroll
      for (int t = 0; t < 8; ++t)
#pragma unroll
        for (int s = 0; s <= t; ++s) acc[t] = mfma16(F[s], G[t - s], acc[t]);
      fb += 128;
      gb -= 128;
    }
  }
  __builtin_amdgcn_sched_barrier(0);
  STAMP(0, 8);
  bar();
  {
    LDS char* ex = smem + S_X + 49152 + w * 4096 + lane * 16;
#pragma unroll
    for (int i = 0; i < 4; ++i) {
      const f32x4 lo = acc[i], hi = acc[4 + i];
      *(LDS f32x4*)(ex + i * 1024) = hh ? +lo : +hi;
    }
    if (tid == 0) *(LDS u32*)(smem + S_CNT) = 0;
  }
  bar();
  u32x2 yo[4];
  {
    LDS const char* ey = smem + S_X + 49152 + (w ^ 1) * 4096 + lane * 16;
#pragma unroll
    for (int i = 0; i < 4; ++i) {
      const f32x4 lo = acc[i], hi = acc[4 + i];
      const f32x4 y = (hh ? +hi : +lo) + *(LDS const f32x4*)(ey + i * 1024);
      yo[i] = u32x2{pack_bf16(y[0], y[1]), pack_bf16(y[2], y[3])};
    }
  }
  bar();
  {
    const __amdgpu_buffer_rsrc_t rs = mk_rsrc(p.ws + WS_AT, DM * SL * 2);
    const int dst = (d * SL + 1024 * hh + 16 * (lane & 15) + 4 * (lane >> 4)) * 2;
#pragma unroll
    for (int i = 0; i < 4; ++i) __builtin_amdgcn_raw_buffer_store_b64(yo[i], rs, dst + 512 * i, 0, SC1);
  }
  STAMP(0, 9);
}
DEV void s_signal(const P& p, LDS char* smem, int sg, int lane) {
  u32 old = 0;
  if (lane == 0) old = __hip_atomic_fetch_add((LDS u32*)(smem + S_CNT), 1u, __ATOMIC_RELAXED, __HIP_MEMORY_SCOPE_WORKGROUP);
  if (__builtin_amdgcn_readfirstlane(old) == 11) {
    const int lt = lane & 31, second = lane >> 5;
    const int dd = 6 * sg + (second ? 5 : 0);
    const int idx = dd < 480 ? dd / 96 : 5 + (dd - 480) / 32;
    const int dfirst = idx < 5 ? 96 * idx : 480 + 32 * (idx - 5);
    __hip_atomic_store((GLB u64*)(p.ws + WS_IN) + tile_block(lt, idx) * 64 + IN_A + (sg - dfirst / 6), FLAG_SET, RLX_AGENT);
  }
}

DEV void role_gate(const P& p, int rb, int w, int lane, u64 pc) {
  u32 pf0 = 0, pf1 = 0;
  const int tid = w * 64 + lane;
  const int sub = lane & 7;
  u32x4 wv[9];
#pragma unroll
  for (int i = 0; i < 9; ++i) wv[i] = *(const u32x4*)((const char*)p.gw + (i * 8 + sub) * 16);
  for (int r = w * 8 + (lane >> 3); r < 128; r += 96) {
    const int l = rb * 128 + r;
    float s = 0.f;
    u32x4 xv[9];
#pragma unroll
    for (int i = 0; i < 9; ++i) xv[i] = *(const u32x4*)((const char*)p.x + (size_t)l * 1152 + (i * 8 + sub) * 16);
#pragma unroll
    for (int i = 0; i < 9; ++i) {
#pragma unroll
      for (int k = 0; k < 4; ++k) {
        const u32 pr = pack_bf16(bflo(xv[i][k]) * bflo(wv[i][k]), bfhi(xv[i][k]) * bfhi(wv[i][k]));
        s += bflo(pr);
        s += bfhi(pr);
      }
    }
    s += __shfl_xor(s, 1); s += __shfl_xor(s, 2); s += __shfl_xor(s, 4);
    const float gv = rbf(1.0f / (1.0f + expf(-rbf(s))));
    if (sub == 0)
      __hip_atomic_store((GLB u64*)(p.ws + WS_G) + l, ((u64)__float_as_uint(rbf(1.0f - gv)) << 32) | __float_as_uint(gv), RLX_AGENT);
  }
  asm volatile("" :: "v"(pf0), "v"(pf1));
  wait_vm<0>();
  bar();
  if (w == 0 && lane < 16)
    __hip_atomic_store((GLB u64*)(p.ws + WS_IN) + tile_block(2 * rb + (lane >> 3), lane & 7) * 64 + IN_G, FLAG_SET, RLX_AGENT);
}

constexpr int H_C = 32768, H_CS = 12288;
DEV int h_slot(int i) { return i < 4 ? i * 8192 : 131072 + (i - 4) * 8192; }
constexpr int H_AT = H_C + 2 * H_CS;
constexpr int H_XT = H_C + 3 * H_CS;
constexpr int H_G = H_C + 4 * H_CS;
constexpr int H_OUT = H_C + 5 * H_CS;
constexpr int K_LDS = 163840;

constexpr int h_wait(int D, int NDT) {
  const int G = 4 + 2 * NDT;
  switch (D) {
    case 0: return 4;
    case 1: return 8;
    case 2: return 4 + G;
    case 3: case 4: return 2 * G;
    case 5: case 6: return 2 * G + 1;
    case 7: return G + NDT;
    default: return NDT;
  }
}
template <int D, int NDT, class F> DEV void h_steps(F&& after, u64& fv, char* wsp) {
  if constexpr (D == 7) asm volatile("s_waitcnt vmcnt(%1)" : "+v"(fv) : "n"(h_wait(D, NDT)) : "memory");
  else wait_vm<h_wait(D, NDT)>();
  LSTAMP2(1, 16 + D);
  bar();
  after(std::integral_constant<int, D>{});
  if constexpr (D < 8) h_steps<D + 1, NDT>(after, fv, wsp);
}
DEV int p2_rot(int d0) { const int idx = d0 < 480 ? d0 / 96 : 5 + (d0 - 480) / 32; return (idx * 18) >> 3; }

template <int NDT, class F>
DEV void p2_loader(const P& p, LDS char* smem, int lt, int d0, int lw, int lane, bool scratch_barrier, F&& pre_poll) {
  __builtin_amdgcn_s_setprio(3);
  const char* hb = p.ws + WS_HB + (size_t)lt * 18 * 8192 + (2 * lw) * 1024 + lane * 16;
  constexpr int W = 32 * NDT;
  const int l0 = 64 * lt, rot = p2_rot(d0);
  u32 co[NDT];
#pragma unroll
  for (int i = 0; i < NDT; ++i) {
    const int row = 8 * (NDT * lw + i) + (lane >> 3), cp = lane & 7;
    co[i] = (u32)(d0 + row) * 1152u + ((cp ^ (row & 7)) << 4);
  }
  auto issue_c = [&](int s) {
    LDS char* buf = smem + H_C + (s & 7) * H_CS + NDT * lw * 1024;
    int ks = s + rot; ks = ks >= 18 ? ks - 18 : ks;
    const char* cb = ks < 9 ? (const char*)p.cre + ks * 128 : (const char*)p.cim + (ks - 9) * 128;
#pragma unroll
    for (int i = 0; i < NDT; ++i) dma16<0>(cb + co[i], buf + i * 1024);
  };
  auto issue_h = [&](int s) {
    LDS char* buf = smem + h_slot(s & 7) + 2 * lw * 1024;
    int ks = s + rot; ks = ks >= 18 ? ks - 18 : ks;
#pragma unroll
    for (int i = 0; i < 2; ++i) dma16<SC1>(hb + ks * 8192 + i * 1024, buf + i * 1024);
  };
  GLB u64* inb = (GLB u64*)(p.ws + WS_IN) + blockIdx.x * 64;
  const int nA = (d0 + W - 1) / 6 - d0 / 6 + 1;
  const bool need = lane == IN_G || (lane >= IN_A && lane < IN_A + nA);
  if (!scratch_barrier) {
#pragma unroll 1
    for (int s = 0; s < 8; ++s) issue_c(s);
  }
  pre_poll();
  { [[maybe_unused]] const int w = lw + 8; STAMP(1, 1); }
  if (scratch_barrier) bar();
  wait_flags(inb + IN_H + (lane < 36 ? lane : 0), true);
  { [[maybe_unused]] const int w = lw + 8; STAMP(1, 2); }
#pragma unroll
  for (int s = 0; s < 4; ++s) issue_h(s);
  { [[maybe_unused]] const int w = lw + 8; STAMP(1, 3); }
  u64 fv = 0;
  bool late = false;
  auto issue_at = [&]() {
#pragma unroll
    for (int i = 0; i < NDT; ++i) {
      const int pc = NDT * lw + i;
      dma16<SC1>(p.ws + WS_AT + (size_t)(d0 + 8 * pc + (lane >> 3)) * 4096 + l0 * 2 + (lane & 7) * 16, smem + H_AT + pc * 1024);
    }
  };
  auto issue_g = [&]() { if (lane < 8) dma16<SC1>(p.ws + WS_G + (size_t)l0 * 8 + lw * 128 + lane * 16, smem + H_G + lw * 128); };
  h_steps<0, NDT>([&](auto D_) {
    constexpr int D = decltype(D_)::value;
    if constexpr (D == 0) { issue_h(4); issue_h(5); issue_h(6); issue_h(7); }
    if constexpr (D >= 1 && D <= 5) { issue_h(2 * D + 6); issue_h(2 * D + 7); issue_c(2 * D + 6); issue_c(2 * D + 7); }
    if constexpr (D == 4)
      asm volatile("global_load_dwordx2 %0, %1, off sc1" : "=v"(fv) : "v"(inb + lane) : "memory");
    if constexpr (D == 6) {
#pragma unroll
      for (int i = 0; i < NDT; ++i) {
        const int pc = NDT * lw + i, id = pc * 64 + lane, row = id / (4 * NDT), qq = id - row * (4 * NDT);
        dma16<0>((const char*)p.x + (size_t)(l0 + row) * 1152 + d0 * 2 + qq * 16, smem + H_XT + pc * 1024);
      }
    }
    if constexpr (D == 7) {
      late = !__all(!need || fv == FLAG_SET);
      if (!late) { issue_g(); issue_at(); }
    }
  }, fv, p.ws);
  if (late) { wait_flags(inb + lane, need); issue_g(); issue_at(); }
  wait_vm<0>();
  bar();
}

template <int NDT>
DEV void p2_compute(const P& p, LDS char* smem, int lt0, int d0, int w, int lane, const u32x2 (&dsk)[6]) {
  const int lh = (w >> 1) & 1, kh = w & 1, dg = w >> 2;
  const int r = lane & 15, kq = lane >> 4;
  const int lt = 2 * lh + kh, l0 = 64 * lt0;
  constexpr int W = 32 * NDT;
  u32x2 da[NDT], db[NDT];
#pragma unroll
  for (int t = 0; t < NDT; ++t) {
    const int d = d0 + 16 * (NDT * dg + t) + 4 * kq;
    da[t] = *(const u32x2*)(p.dskA + d);
    db[t] = *(const u32x2*)(p.dskB + d);
  }
  GLB u64* inb = (GLB u64*)(p.ws + WS_IN) + blockIdx.x * 64;

  f32x4 acc[2][NDT];
#pragma unroll
  for (int i = 0; i < 2; ++i)
#pragma unroll
    for (int t = 0; t < NDT; ++t) acc[i][t] = f32x4{0, 0, 0, 0};
  int ho[2];
#pragma unroll
  for (int u = 0; u < 2; ++u) {
    const int k = 32 * kh + 8 * kq + 4 * u + (r >> 2), i = (r & 3) >> 1;
    ho[u] = (2 * (k >> 4) + i) * 1024 + (32 * lh + ((k & 15) ^ (4 * i))) * 16 + (r & 1) * 8;
  }
  const int co = H_C + (16 * NDT * dg + r) * 128 + (((4 * kh + kq) ^ (r & 7)) << 4);
  bf16x8 hb[2][2][2], ca[2][2][NDT];
  auto rd = [&](auto SET_, int D) {
    constexpr int SET = decltype(SET_)::value;
#pragma unroll
    for (int u = 0; u < 2; ++u) {
      const int s = 2 * D + u;
      LDS const char* hbuf = smem + h_slot(s & 7);
#pragma unroll
      for (int i = 0; i < 2; ++i) hb[SET][u][i] = tr_frag(hbuf + ho[0] + i * 256, hbuf + ho[1] + i * 256);
      LDS const char* cbuf = smem + (s & 7) * H_CS;
#pragma unroll
      for (int t = 0; t < NDT; ++t) ca[SET][u][t] = lds_frag(cbuf + co + t * 2048);
    }
    __builtin_amdgcn_sched_barrier(0);
  };
  auto mma = [&](auto SET_) {
    constexpr int SET = decltype(SET_)::value;
#pragma unroll
    for (int u = 0; u < 2; ++u)
#pragma unroll
      for (int t = 0; t < NDT; ++t) {
        acc[0][t] = mfma16(ca[SET][u][t], hb[SET][u][0], acc[0][t]);
        acc[1][t] = mfma16(ca[SET][u][t], hb[SET][u][1], acc[1][t]);
      }
    __builtin_amdgcn_sched_barrier(0);
  };
  using S0 = std::integral_constant<int, 0>;
  using S1 = std::integral_constant<int, 1>;
  STAMP(1, 1);
  bar();
  STAMP(1, 2);
  rd(S0{}, 0);
#pragma unroll 1
  for (int D = 1; D < 9; D += 2) {
    bar(); rd(S1{}, D); mma(S0{});
    bar(); rd(S0{}, D + 1); mma(S1{});
  }
  mma(S0{});
  __builtin_amdgcn_sched_barrier(0);
  STAMP(1, 3);
  bar();
  if (w == 0) __hip_atomic_store(inb + lane, 0ull, RLX_AGENT);
  LDS char* ex = smem + w * (NDT * 1024) + lane * 16;
#pragma unroll
  for (int t = 0; t < NDT; ++t) {
    const f32x4 a0 = acc[0][t], a1 = acc[1][t];
    *(LDS f32x4*)(ex + t * 1024) = kh ? +a0 : +a1;
  }
  bar();
  LDS const char* ey = smem + (w ^ 1) * (NDT * 1024) + lane * 16;
  constexpr int OSTR = W * 4 + 16;
  const f32x2 gg = *(LDS const f32x2*)(smem + H_G + (16 * lt + r) * 8);
#pragma unroll
  for (int t = 0; t < NDT; ++t) {
    const int dt = NDT * dg + t;
    const f32x4 o = *(LDS const f32x4*)(ey + t * 1024);
    const f32x4 a0 = acc[0][t], a1 = acc[1][t];
    const f32x4 y = (kh ? +a1 : +a0) + o;
    const s16x4 av = tr_read(smem + H_AT + (16 * dt + 4 * kq + (r >> 2)) * 128 + lt * 32 + (r & 3) * 8);
    const u32x2 xv = *(LDS const u32x2*)(smem + H_XT + (16 * lt + r) * (W * 2) + (16 * dt + 4 * kq) * 2);
    f32x4 res;
#pragma unroll
    for (int i = 0; i < 4; ++i) {
      const u32 xw = xv[i >> 1], aw = da[t][i >> 1], bw = db[t][i >> 1];
      const float xf = (i & 1) ? bfhi(xw) : bflo(xw);
      const float sa = (i & 1) ? bfhi(aw) : bflo(aw);
      const float sb = (i & 1) ? bfhi(bw) : bflo(bw);
      const float yc = bf2f((u16)av[i]);
      const float a = rbf(yc + rbf(xf * sa));
      const float b = y[i] + rbf(xf * sb);
      res[i] = rbf(gg[0] * a) + gg[1] * b;
    }
    *(LDS f32x4*)(smem + H_OUT + (16 * lt + r) * OSTR + (16 * dt + 4 * kq) * 4) = res;
  }
  STAMP(1, 4);
  bar();
#pragma unroll
  for (int it = 0; it < NDT; ++it) {
    const int id = it * 512 + w * 64 + lane;
    const int row = id / (8 * NDT), pc = id - row * (8 * NDT);
    const f32x4 v = *(LDS const f32x4*)(smem + H_OUT + row * OSTR + pc * 16);
    __builtin_amdgcn_raw_buffer_store_b128(__builtin_bit_cast(u32x4, v), mk_rsrc(p.out, SL * DM * 4), ((l0 + row) * DM + d0 + pc * 4) * 4, 0, OUT_AUX);
  }
}

template <int NDT>
DEV void phase2(const P& p, LDS char* smem, int lt, int d0, int w, int lane, bool scratch_barrier, int sg, const u32x2 (&dsk)[6]) {
  STAMP(1, 0);
  if (w >= 8) {
    p2_loader<NDT>(p, smem, lt, d0, w - 8, lane, scratch_barrier, [&]() {
      if (NDT == 1) { wait_vm<8>(); s_signal(p, smem, sg, lane); }
    });
  } else {
    if (NDT == 1) { wait_vm<0>(); s_signal(p, smem, sg, lane); }
    p2_compute<NDT>(p, smem, lt, d0, w, lane, dsk);
  }
  STAMP(1, 15);
}

__global__ __launch_bounds__(768, 3) void gated_mix_s4d_s5(P p) {
  extern __shared__ __attribute__((aligned(16))) char smem_raw[];
  LDS char* smem = (LDS char*)smem_raw;
  u64 pc;
  asm volatile("s_getpc_b64 %0" : "=s"(pc));
  u32 pf = 0;
#define PF_TOP 312
  if (threadIdx.x < PF_TOP && blockIdx.x >= 144)
    asm volatile("global_load_dword %0, %1, off" : "=v"(pf) : "v"((pc & ~255ull) + threadIdx.x * 128));
  const int w = __builtin_amdgcn_readfirstlane(threadIdx.x >> 6), lane = threadIdx.x & 63;
  const int xcd = blockIdx.x & 7, j = blockIdx.x >> 3;
  u32x2 dsk[6];
#pragma unroll
  for (int i = 0; i < 6; ++i) dsk[i] = u32x2{0, 0};
  STAMP(0, 0);
  if (j >= 18 && j < 30) {
    const int sg = 12 * xcd + (j - 18);
    role_s(p, smem, sg, w, lane);
    STAMP(0, 15);
    phase2<1>(p, smem, 4 * xcd + (j - 18) / 3, 480 + 32 * ((j - 18) % 3), w, lane, false, sg, dsk);
  } else {
    const bool isg = j < 18;
    if (isg) {
      const int c = 2 * xcd + j / 9, g = j % 9;
      if (w >= 8) g_loader(p, smem, c, g, 96 * (j % 5), w - 8, lane);
      else if (w < 4) g_compute<0>(p, smem, c, g, w, lane, pf);
      else g_compute<1>(p, smem, c, g, w, lane, pf);
    } else {
      role_gate(p, 2 * xcd + (j - 30), w, lane, pc);
    }
    STAMP(0, 15);
    const int e = isg ? j : j - 12;
    phase2<3>(p, smem, 4 * xcd + e / 5, 96 * (e % 5), w, lane, isg, 0, dsk);
  }
  asm volatile("s_waitcnt vmcnt(0)" :: "v"(pf));
}

extern "C" void kernel_launch(void* const* d_in, const int* in_sizes, int n_in, void* d_out, int out_size,
                              void* d_ws, size_t ws_size, hipStream_t stream) {
  P a;
  a.x = (const u16*)d_in[0]; a.la = (const u16*)d_in[1]; a.aim = (const u16*)d_in[2];
  a.creA = (const u16*)d_in[3]; a.cimA = (const u16*)d_in[4]; a.ldtA = (const u16*)d_in[5]; a.dskA = (const u16*)d_in[6];
  a.lre = (const u16*)d_in[8]; a.lim = (const u16*)d_in[9]; a.bre = (const u16*)d_in[10]; a.bim = (const u16*)d_in[11];
  a.cre = (const u16*)d_in[12]; a.cim = (const u16*)d_in[13]; a.ldtB = (const u16*)d_in[14];
  a.dskB = (const u16*)d_in[15]; a.gw = (const u16*)d_in[16];
  a.ws = (char*)d_ws + WS_OFF; a.out = (float*)d_out;
  (void)hipFuncSetAttribute((const void*)gated_mix_s4d_s5, hipFuncAttributeMaxDynamicSharedMemorySize, K_LDS);
  gated_mix_s4d_s5<<<256, 768, K_LDS, stream>>>(a);
}
```

```cpp
#define SIG_FIRST 1
#define PF_NOT_G 1
#include <hip/hip_runtime.h>
#include <hip/hip_bf16.h>
#include <stdint.h>
#include <type_traits>

typedef unsigned short u16;
typedef unsigned int u32;
typedef unsigned long long u64;
typedef __attribute__((ext_vector_type(8))) __bf16 bf16x8;
typedef __attribute__((ext_vector_type(2))) __bf16 bf16x2;
typedef __attribute__((ext_vector_type(4))) short s16x4;
typedef __attribute__((ext_vector_type(4))) float f32x4;
typedef __attribute__((ext_vector_type(2))) float f32x2;
typedef __attribute__((ext_vector_type(4))) unsigned u32x4;
typedef __attribute__((ext_vector_type(2))) unsigned u32x2;
#define LDS __attribute__((address_space(3)))
#define GLB __attribute__((address_space(1)))
#define DEV __device__ __forceinline__
#define RLX_AGENT __ATOMIC_RELAXED, __HIP_MEMORY_SCOPE_AGENT

constexpr int SL = 2048;
constexpr int DM = 576;

constexpr size_t WS_HB = 0;
constexpr size_t WS_AT = WS_HB + (size_t)2 * DM * SL * 2;
constexpr size_t WS_G = WS_AT + (size_t)DM * SL * 2;
constexpr size_t WS_GRAN = WS_G + (size_t)SL * 8;
constexpr size_t WS_EP = WS_GRAN + (size_t)9 * 16 * 2 * 64 * 8;
constexpr size_t WS_FL = WS_EP + 4096;
constexpr size_t WS_XT = WS_FL + (size_t)16 * DM * 8;
constexpr size_t WS_IN = WS_XT + (size_t)DM * SL * 2;
constexpr u64 FLAG_SET = 0x9E3779B97F4A7C15ull;
constexpr u32 SPIN_CAP = 1u << 17;
constexpr int IN_H = 0, IN_G = 36, IN_A = 40;

constexpr size_t WS_ST = (size_t)16 << 20;
#define STAMP(kern, idx) do {} while (0)
#define CSTAMP(kern, idx) do {} while (0)
#define LSTAMP(kern, idx) do {} while (0)
#define LSTAMP2(kern, idx) do {} while (0)

DEV float bf2f(u16 v) { return __uint_as_float((u32)v << 16); }
DEV float bflo(u32 v) { return __uint_as_float(v << 16); }
DEV float bfhi(u32 v) { return __uint_as_float(v & 0xffff0000u); }
DEV u32 pack_bf16(float lo, float hi) {
  f32x2 v = {lo, hi};
  bf16x2 b = __builtin_convertvector(v, bf16x2);
  return __builtin_bit_cast(u32, b);
}
DEV float rbf(float x) { return bflo(pack_bf16(x, x)); }
template <int N> DEV void wait_vm() { asm volatile("s_waitcnt vmcnt(%0)" ::"n"(N) : "memory"); }
DEV void bar() { asm volatile("s_waitcnt lgkmcnt(0)\n\ts_barrier" ::: "memory"); }
DEV void wave_fence() { __builtin_amdgcn_fence(__ATOMIC_ACQ_REL, "wavefront"); }
template <int AUX> DEV void dma16(const void* src, LDS char* dst) {
  __builtin_amdgcn_global_load_lds((const GLB u32*)src, (LDS u32*)dst, 16, 0, AUX);
}
constexpr int SC1 = 16;
#define GPOLL_SLEEP 1
#define GPOLL_DELAY 24
#define POLL_SLEEP 1
#define OUT_AUX 16
#define WS_OFF 0
#define XT_WAIT 4
DEV bf16x8 lds_frag(LDS const char* p) { return *(LDS const bf16x8*)p; }
DEV s16x4 tr_read(LDS const char* a) { return __builtin_amdgcn_ds_read_tr16_b64_v4i16((LDS s16x4*)a); }
DEV bf16x8 tr_frag(LDS const char* a, LDS const char* b) {
  const s16x4 lo = tr_read(a), hi = tr_read(b);
  return __builtin_bit_cast(bf16x8, __builtin_shufflevector(lo, hi, 0, 1, 2, 3, 4, 5, 6, 7));
}
DEV f32x4 mfma16(bf16x8 a, bf16x8 b, f32x4 c) { return __builtin_amdgcn_mfma_f32_16x16x32_bf16(a, b, c, 0, 0, 0); }
DEV __amdgpu_buffer_rsrc_t mk_rsrc(const void* base, int bytes) {
  return __builtin_amdgcn_make_buffer_rsrc(const_cast<void*>(base), (short)0, bytes, 0x00020000);
}
struct cf { float r, i; };
DEV cf cmul(cf a, cf b) { return {a.r * b.r - a.i * b.i, a.r * b.i + a.i * b.r}; }
DEV cf cfma(cf a, cf b, cf c) { return {fmaf(a.r, b.r, fmaf(-a.i, b.i, c.r)), fmaf(a.r, b.i, fmaf(a.i, b.r, c.i))}; }
typedef f32x2 c2;
DEV c2 cm2(c2 a, c2 b) {
  c2 t, r;
  asm("v_pk_mul_f32 %0, %1, %2 op_sel:[0,0] op_sel_hi:[0,1]" : "=v"(t) : "v"(a), "v"(b));
  asm("v_pk_fma_f32 %0, %1, %2, %3 op_sel:[1,1,0] op_sel_hi:[1,0,1] neg_lo:[1,0,0]" : "=v"(r) : "v"(a), "v"(b), "v"(t));
  return r;
}
DEV f32x2 fma2(f32x2 a, f32x2 b, f32x2 c) { return __builtin_elementwise_fma(a, b, c); }
DEV f32x2 lo2(f32x4 v) { return __builtin_shufflevector(v, v, 0, 1); }
DEV f32x2 hi2(f32x4 v) { return __builtin_shufflevector(v, v, 2, 3); }
DEV f32x4 cat2(f32x2 a, f32x2 b) { return __builtin_shufflevector(a, b, 0, 1, 2, 3); }
DEV void wait_flags(GLB u64* word, bool need) {
  for (u32 spin = 0;; ++spin) {
    const u64 v = __hip_atomic_load(word, RLX_AGENT);
    if (__all(!need || v == FLAG_SET) || spin > SPIN_CAP) break;
    __builtin_amdgcn_s_sleep(POLL_SLEEP);
  }
}
DEV int tile_block(int lt, int idx) {
  const int xcd = lt >> 2, a = lt & 3;
  int j;
  if (idx < 5) { const int e = 5 * a + idx; j = e < 18 ? e : e + 12; }
  else j = 18 + 3 * a + (idx - 5);
  return (j << 3) | xcd;
}

struct P {
  const u16 *x, *la, *aim, *creA, *cimA, *ldtA, *dskA;
  const u16 *lre, *lim, *bre, *bim, *cre, *cim, *ldtB, *dskB, *gw;
  char* ws;
  float* out;
};

constexpr int G_NB = 4, G_STAGE = 32768;
constexpr int G_E = 153536;
constexpr int G_PF = G_E + 4096;
constexpr int G_L = G_PF + 4096;
constexpr int G_X = G_L + 1024;
constexpr int G_GO = G_X + 1024;
constexpr int G_T = G_NB * G_STAGE;
static_assert(G_GO + 4 <= 163840 && G_T + 4 * 32 * 144 <= G_E, "role G scratch");

DEV void g_loader(const P& p, LDS char* smem, int c, int g, int d0, int lw, int lane) {
  [[maybe_unused]] const int w = lw + 8;
  __builtin_amdgcn_s_setprio(3);
  GLB u32* ep = (GLB u32*)(p.ws + WS_EP + g * 256);
  GLB u64* gran = (GLB u64*)(p.ws + WS_GRAN) + (size_t)g * 16 * 128;
  const u32 tag = __hip_atomic_load(ep, RLX_AGENT) + 1u;
  u32 xo[4], bo[4];
#pragma unroll
  for (int i = 0; i < 4; ++i) {
    const int rho = 8 * (4 * lw + i) + (lane >> 3);
    const int hf = rho >> 6, m = (rho >> 4) & 3, r = rho & 15;
    const int tau = 64 * hf + 16 * (r >> 2) + 8 * (r & 1) + 2 * m + ((r >> 1) & 1);
    const int ch = (lane & 7) ^ (rho & 7);
    xo[i] = (u32)(128 * c + tau) * 1152u + ch * 16;
    const int pp = 64 * g + 16 * (rho >> 5) + r;
    bo[i] = (u32)pp * 1152u + ch * 16;
  }
  auto issue = [&](int s) {
    LDS char* buf = smem + (s & (G_NB - 1)) * G_STAGE + lw * 4096;
    int kb = g + s; kb = (kb >= 9 ? kb - 9 : kb) * 128;
#pragma unroll
    for (int i = 0; i < 4; ++i) dma16<0>((const char*)p.x + (xo[i] + kb), buf + i * 1024);
#pragma unroll
    for (int i = 0; i < 4; ++i) {
      const char* base = (const char*)((((4 * lw + i) >> 1) & 1) ? p.bim : p.bre);
      dma16<0>(base + (bo[i] + kb), buf + 16384 + i * 1024);
    }
  };
#pragma unroll
  for (int s = 0; s < G_NB - 1; ++s) issue(s);
#pragma unroll 1
  for (int s = 0; s < 6; ++s) { wait_vm<16>(); LSTAMP(0, 16 + s); bar(); issue(s + G_NB - 1); LSTAMP(0, 32 + s); }
  wait_vm<16>(); bar();
  wait_vm<8>(); bar();
  wait_vm<0>(); bar();
  STAMP(0, 1);
  if (lw < 2) {
    bar();
    u32 co[6];
#pragma unroll
    for (int i = 0; i < 6; ++i) {
      const int row = 8 * (6 * lw + i) + (lane >> 3), cp = lane & 7;
      co[i] = (u32)(d0 + row) * 1152u + ((cp ^ (row & 7)) << 4);
    }
    const int rot = ((d0 / 96) * 18) >> 3;
#pragma unroll 1
    for (int s = 0; s < 8; ++s) {
      LDS char* buf = smem + 32768 + s * 12288 + 6 * lw * 1024;
      const int ks = s + rot;
      const char* cb = ks < 9 ? (const char*)p.cre + ks * 128 : (const char*)p.cim + (ks - 9) * 128;
#pragma unroll
      for (int i = 0; i < 6; ++i) dma16<0>(cb + co[i], buf + i * 1024);
    }
    bar();
    return;
  }
  bar();
  STAMP(0, 2);
  if (lw == 3) {
    const f32x2 lv = *(LDS const f32x2*)(smem + G_L + lane * 8);
    const cf l16 = {lv[0], lv[1]};
    f32x2 ev[8];
#pragma unroll
    for (int s = 0; s < 8; ++s) ev[s] = *(LDS const f32x2*)(smem + G_E + (s * 64 + lane) * 8);
    cf tot = {0.f, 0.f};
    cf pre[8];
#pragma unroll
    for (int s = 0; s < 8; ++s) { pre[s] = tot; tot = cfma(l16, tot, cf{ev[s][0], ev[s][1]}); }
    if (c < 15) {
      __hip_atomic_store(gran + c * 128 + lane, ((u64)tag << 32) | __float_as_uint(tot.r), RLX_AGENT);
      __hip_atomic_store(gran + c * 128 + 64 + lane, ((u64)tag << 32) | __float_as_uint(tot.i), RLX_AGENT);
    }
#pragma unroll
    for (int s = 0; s < 8; ++s) *(LDS f32x2*)(smem + G_PF + (s * 64 + lane) * 8) = f32x2{pre[s].r, pre[s].i};
  }
  const int par = lw - 2;
  const f32x2 lv = *(LDS const f32x2*)(smem + G_L + 512 + lane * 8);
  const cf l128 = {lv[0], lv[1]};
  const cf l256 = cmul(l128, l128);
  cf acc = {0.f, 0.f};
  if (par < c) {
    const int nc = (c - par + 1) >> 1;
    u64 a[8], b[8];
    __builtin_amdgcn_s_sleep(GPOLL_DELAY);
    for (u32 spin = 0;; ++spin) {
      bool ok = true;
#pragma unroll
      for (int i = 0; i < 8; ++i) {
        const int cc = par + 2 * (i < nc ? i : 0);
        a[i] = __hip_atomic_load(gran + cc * 128 + lane, RLX_AGENT);
        b[i] = __hip_atomic_load(gran + cc * 128 + 64 + lane, RLX_AGENT);
      }
#pragma unroll
      for (int i = 0; i < 8; ++i) ok = ok && (u32)(a[i] >> 32) == tag && (u32)(b[i] >> 32) == tag;
      if (__all(ok) || spin > SPIN_CAP) break;
      __builtin_amdgcn_s_sleep(GPOLL_SLEEP);
    }
#pragma unroll
    for (int i = 0; i < 8; ++i)
      if (i < nc) acc = cfma(l256, acc, cf{__uint_as_float((u32)a[i]), __uint_as_float((u32)b[i])});
    if (c - 1 - (par + 2 * (nc - 1))) acc = cmul(acc, l128);
  }
  *(LDS f32x2*)(smem + G_X + (par * 64 + lane) * 8) = f32x2{acc.r, acc.i};
  STAMP(0, 3);
  bar();
  if (c == 15 && lw == 2 && lane == 0) __hip_atomic_store(ep, tag, RLX_AGENT);
}

template <int HF>
DEV void g_compute(const P& p, LDS char* smem, int c, int g, int w, int lane, u32& pf) {
  const int pq = w & 3, col = lane & 15, q = lane >> 4;
  const int pl = 16 * pq + col, pp = 64 * g + pl;
  cf lam, coef2;
  {
    const float lre = bf2f(p.lre[pp]), lim = bf2f(p.lim[pp]);
    const float dt = rbf(expf(bf2f(p.ldtB[pp])));
    const float zr = dt * lre, zi = dt * lim;
    const float er = expf(zr);
    float sn, cs;
    sincosf(zi, &sn, &cs);
    lam = {er * cs, er * sn};
    const float nr = lam.r - 1.0f, ni = lam.i;
    const float den = 1.0f / (lre * lre + lim * lim);
    coef2 = {2.0f * (nr * lre + ni * lim) * den, 2.0f * (ni * lre - nr * lim) * den};
  }
  cf l8 = cmul(lam, lam); l8 = cmul(l8, l8); l8 = cmul(l8, l8);
  const cf l16 = cmul(l8, l8);
  const cf l32 = cmul(l16, l16), l64 = cmul(l32, l32);
  if (HF == 0 && q == 0) {
    const cf l128 = cmul(l64, l64);
    *(LDS f32x2*)(smem + G_L + pl * 8) = f32x2{l16.r, l16.i};
    *(LDS f32x2*)(smem + G_L + 512 + pl * 8) = f32x2{l128.r, l128.i};
  }
  if (HF == 0 && w == 0 && lane == 0) *(LDS u32*)(smem + G_GO) = 0;
  cf pw = HF ? l64 : cf{1.f, 0.f};
  { const cf t = cmul(pw, l16); if (q & 1) pw = t; }
  { const cf t = cmul(pw, l32); if (q & 2) pw = t; }
  asm volatile("" : "+v"(lam.r), "+v"(lam.i), "+v"(coef2.r), "+v"(coef2.i), "+v"(l8.r), "+v"(l8.i), "+v"(pw.r), "+v"(pw.i));
  f32x4 are[4], aim[4];
#pragma unroll
  for (int m = 0; m < 4; ++m) { are[m] = f32x4{0, 0, 0, 0}; aim[m] = f32x4{0, 0, 0, 0}; }
  const int o0 = col * 128 + (((0 + q) ^ (col & 7)) << 4);
  const int o1 = col * 128 + (((4 + q) ^ (col & 7)) << 4);
  bf16x8 br[2], bi[2], a[2][4];
  auto read_frags = [&](int s) {
    LDS const char* buf = smem + (s & (G_NB - 1)) * G_STAGE;
    LDS const char* ab = buf + HF * 8192;
    LDS const char* bb = buf + 16384 + pq * 4096;
#pragma unroll
    for (int ks = 0; ks < 2; ++ks) {
      const int o = ks ? o1 : o0;
      br[ks] = lds_frag(bb + o); bi[ks] = lds_frag(bb + 2048 + o);
#pragma unroll
      for (int m = 0; m < 4; ++m) a[ks][m] = lds_frag(ab + m * 2048 + o);
    }
  };
  auto mma = [&]() {
#pragma unroll
    for (int ks = 0; ks < 2; ++ks)
#pragma unroll
      for (int m = 0; m < 4; ++m) {
        are[m] = mfma16(a[ks][m], br[ks], are[m]);
        aim[m] = mfma16(a[ks][m], bi[ks], aim[m]);
      }
  };
  STAMP(0, 1);
#pragma unroll
  for (int s = 0; s < 9; ++s) {
    if (HF == 1 && s == XT_WAIT) {
      wait_vm<0>();
      if (lane < 16) __hip_atomic_store((GLB u64*)(p.ws + WS_FL) + c * DM + 64 * g + 16 * pq + lane, FLAG_SET, RLX_AGENT);
    }
    bar();
    if (s == 0) { STAMP(0, 2); CSTAMP(0, 60); }
    if (HF == 1 && s > 0) { mma(); __builtin_amdgcn_sched_barrier(0); }
    if (HF == 1 && s == 0) {
      const int rr = 4 * q + (col >> 2);
      LDS const char* tb = smem + rr * 128 + (((2 * pq + ((col & 3) >> 1)) ^ (rr & 7)) << 4) + (col & 1) * 8;
      LDS char* sc = smem + G_T + pq * (32 * 144);
#pragma unroll
      for (int hf = 0; hf < 2; ++hf) {
        u32x2 t[4];
#pragma unroll
        for (int m = 0; m < 4; ++m) t[m] = __builtin_bit_cast(u32x2, tr_read(tb + hf * 8192 + m * 2048));
        u32x4 oa, ob;
#pragma unroll
        for (int m = 0; m < 4; ++m) {
          oa[m] = __builtin_amdgcn_perm(t[m][1], t[m][0], 0x05040100u);
          ob[m] = __builtin_amdgcn_perm(t[m][1], t[m][0], 0x07060302u);
        }
        *(LDS u32x4*)(sc + hf * (16 * 144) + col * 144 + q * 32) = oa;
        *(LDS u32x4*)(sc + hf * (16 * 144) + col * 144 + q * 32 + 16) = ob;
      }
      wave_fence();
      const __amdgpu_buffer_rsrc_t rs = mk_rsrc(p.ws + WS_XT, DM * SL * 2);
#pragma unroll
      for (int u = 0; u < 4; ++u) {
        const int row = (lane >> 3) + 8 * (u & 1), hf = u >> 1;
        const u32x4 v = *(LDS const u32x4*)(sc + hf * (16 * 144) + row * 144 + (lane & 7) * 16);
        __builtin_amdgcn_raw_buffer_store_b128(v, rs, ((64 * g + 16 * pq + row) * SL + 128 * c + 64 * hf) * 2 + (lane & 7) * 16, 0, SC1);
      }
      __builtin_amdgcn_sched_barrier(0);
    }
    read_frags(s);
    __builtin_amdgcn_sched_barrier(0);
    if (HF == 0) { mma(); __builtin_amdgcn_sched_barrier(0); }
  }
  if (HF == 1) mma();
  __builtin_amdgcn_sched_barrier(0);
  STAMP(0, 3); CSTAMP(0, 61);
  const f32x2 Lr = {lam.r, lam.r}, Li = {lam.i, lam.i};
  f32x2 er = {0.f, 0.f}, ei = {0.f, 0.f};
#pragma unroll
  for (int t = 0; t < 8; ++t) {
    const f32x2 br_ = (t & 1) ? hi2(are[t >> 1]) : lo2(are[t >> 1]);
    const f32x2 bi_ = (t & 1) ? hi2(aim[t >> 1]) : lo2(aim[t >> 1]);
    const f32x2 nr = fma2(-Li, ei, fma2(Lr, er, br_));
    ei = fma2(Li, er, fma2(Lr, ei, bi_));
    er = nr;
  }
  const cf eA = {er[0], ei[0]};
  const cf e = cfma(l8, eA, cf{er[1], ei[1]});
  const int seg = 4 * HF + q;
  *(LDS f32x2*)(smem + G_E + (seg * 64 + pl) * 8) = f32x2{e.r, e.i};
  STAMP(0, 4);
  bar();
  STAMP(0, 5);
  {
#pragma unroll
    for (int m = 0; m < 4; ++m) asm volatile("" : "+v"(are[m]), "+v"(aim[m]));
    const f32x2 Cr = {coef2.r, coef2.r}, Ci = {coef2.i, coef2.i};
#pragma unroll
    for (int m = 0; m < 4; ++m) {
      const f32x2 r0 = lo2(are[m]), r1 = hi2(are[m]), i0 = lo2(aim[m]), i1 = hi2(aim[m]);
      are[m] = cat2(fma2(-Ci, i0, Cr * r0), fma2(-Ci, i1, Cr * r1));
      aim[m] = cat2(fma2(-Ci, r0, -Cr * i0), fma2(-Ci, r1, -Cr * i1));
      asm volatile("" : "+v"(are[m]), "+v"(aim[m]));
    }
  }
  cf eAc = cmul(coef2, eA), pwc = cmul(coef2, pw);
  asm volatile("" : "+v"(eAc.r), "+v"(eAc.i), "+v"(pwc.r), "+v"(pwc.i));
  STAMP(0, 7);
  bar();
  STAMP(0, 8);
  cf cin;
  {
    f32x2 v[2];
#pragma unroll
    for (int i = 0; i < 2; ++i) v[i] = *(LDS const f32x2*)(smem + G_X + (i * 64 + pl) * 8);
    const f32x2 pf = *(LDS const f32x2*)(smem + G_PF + (seg * 64 + pl) * 8);
    const f32x2 S = v[0] + v[1];
    cin = cfma(pwc, cf{S[0], S[1]}, cmul(coef2, cf{pf[0], pf[1]}));
  }
  const cf cinB = cfma(l8, cin, eAc);
  er = f32x2{cin.r, cinB.r}; ei = f32x2{-cin.i, -cinB.i};
  f32x2 hr[8], hi[8];
#pragma unroll
  for (int t = 0; t < 8; ++t) {
    const f32x2 br_ = (t & 1) ? hi2(are[t >> 1]) : lo2(are[t >> 1]);
    const f32x2 bi_ = (t & 1) ? hi2(aim[t >> 1]) : lo2(aim[t >> 1]);
    const f32x2 nr = fma2(Li, ei, fma2(Lr, er, br_));
    ei = fma2(-Li, er, fma2(Lr, ei, bi_));
    er = nr;
    hr[t] = er; hi[t] = ei;
  }
  u32x4 o[4];
#pragma unroll
  for (int t = 0; t < 4; ++t) {
    o[0][t] = pack_bf16(hr[2 * t][0], hr[2 * t + 1][0]);
    o[1][t] = pack_bf16(hr[2 * t][1], hr[2 * t + 1][1]);
    o[2][t] = pack_bf16(hi[2 * t][0], hi[2 * t + 1][0]);
    o[3][t] = pack_bf16(hi[2 * t][1], hi[2 * t + 1][1]);
  }
  STAMP(0, 9);
  {
    const __amdgpu_buffer_rsrc_t rs = mk_rsrc(p.ws + WS_HB, 2 * DM * SL * 2);
    const int hb = (((c * 2 + HF) * 2) * 9 + g) * 8192 + pq * 2048;
    const int p0 = (16 * q + col) * 16, p1 = 1024 + (16 * q + (col ^ 4)) * 16;
    __builtin_amdgcn_raw_buffer_store_b128(o[0], rs, hb + p0, 0, SC1);
    __builtin_amdgcn_raw_buffer_store_b128(o[1], rs, hb + p1, 0, SC1);
    __builtin_amdgcn_raw_buffer_store_b128(o[2], rs, hb + 9 * 8192 + p0, 0, SC1);
    __builtin_amdgcn_raw_buffer_store_b128(o[3], rs, hb + 9 * 8192 + p1, 0, SC1);
    wait_vm<0>();
    STAMP(0, 10);
    if (lane < 8)
      __hip_atomic_store((GLB u64*)(p.ws + WS_IN) + tile_block(2 * c + HF, lane) * 64 + IN_H + g * 4 + pq, FLAG_SET, RLX_AGENT);
    bar();
  }
}

constexpr int S_CPY = 4160;
constexpr int S_X = 0;
constexpr int S_ST = 6 * 4 * S_CPY;
constexpr int S_Z = S_ST;
constexpr int S_ZSZ = (256 + SL) * 2;
constexpr int S_T = S_ST + 12 * 4608;
constexpr int S_TSZ = 1312;
constexpr int S_END = S_T + 6 * S_TSZ;
constexpr int S_CNT = 32768 + 8192;

DEV c2 cexp_k(float zl, float th, float tl, float k) {
  const float mag = __builtin_amdgcn_exp2f(zl * k);
  const float t = fmaf(tl, k, __builtin_amdgcn_fractf(th * k));
  return c2{mag * __builtin_amdgcn_cosf(t), mag * __builtin_amdgcn_sinf(t)};
}
#define PKM(d, a, b) "v_pk_mul_f32 " d ", " a ", " b " op_sel:[0,0] op_sel_hi:[0,1]\n\t"
#define PKF(d, a, b) "v_pk_fma_f32 " d ", " a ", " b ", " d " op_sel:[1,1,0] op_sel_hi:[1,0,1] neg_lo:[1,0,0]\n\t"
DEV void octet(LDS char* dst, c2 s0, c2 m1, c2 m2) {
  c2 v1, v2, v3, t0, t1, t2, t3;
  asm(PKM("%0", "%7", "%8") PKM("%3", "%7", "%9") PKF("%0", "%7", "%8") PKF("%3", "%7", "%9")
      PKM("%1", "%0", "%8") PKM("%4", "%0", "%9") PKF("%1", "%0", "%8") PKF("%4", "%0", "%9")
      PKM("%2", "%1", "%8") PKM("%5", "%1", "%9") PKF("%2", "%1", "%8") PKF("%5", "%1", "%9")
      PKM("%6", "%2", "%9") PKF("%6", "%2", "%9")
      : "=&v"(v1), "=&v"(v2), "=&v"(v3), "=&v"(t0), "=&v"(t1), "=&v"(t2), "=&v"(t3) : "v"(s0), "v"(m1), "v"(m2));
  *(LDS u32*)(dst + 0 * 288) = pack_bf16(s0[0], s0[1]);
  *(LDS u32*)(dst + 1 * 288) = pack_bf16(v1[0], v1[1]);
  *(LDS u32*)(dst + 2 * 288) = pack_bf16(v2[0], v2[1]);
  *(LDS u32*)(dst + 3 * 288) = pack_bf16(v3[0], v3[1]);
  *(LDS u32*)(dst + 4 * 288) = pack_bf16(t0[0], t0[1]);
  *(LDS u32*)(dst + 5 * 288) = pack_bf16(t1[0], t1[1]);
  *(LDS u32*)(dst + 6 * 288) = pack_bf16(t2[0], t2[1]);
  *(LDS u32*)(dst + 7 * 288) = pack_bf16(t3[0], t3[1]);
}
DEV u32 dup_lo(u32 a) { return __builtin_amdgcn_perm(a, a, 0x01000100u); }
DEV u32 dup_hi(u32 a) { return __builtin_amdgcn_perm(a, a, 0x03020302u); }
DEV u32 hi_lo(u32 a, u32 b) { return __builtin_amdgcn_alignbit(b, a, 16); }

DEV void role_s(const P& p, LDS char* smem, int sg, int w, int lane) {
  const int tid = w * 64 + lane;
  const int ch = w >> 1, hh = w & 1, d = 6 * sg + ch;
  const int n = d * 128 + 64 * hh + lane;
  const u16 r_ldt = p.ldtA[d], r_la = p.la[n], r_aim = p.aim[n], r_cre = p.creA[n], r_cim = p.cimA[n];
  u32 wb[2] = {0, 0};
  __builtin_amdgcn_sched_barrier(0);
#pragma unroll
  for (int i = 0; i < 2; ++i) {
    const int ln = i * 768 + tid, all = (sg % 12) * 864 + ln;
    if (ln < 864) wb[i] = *(const GLB u32*)((const char*)(all < 5184 ? p.bre : p.bim) + (size_t)(all < 5184 ? all : all - 5184) * 128);
  }
  __builtin_amdgcn_sched_barrier(0);
  STAMP(0, 1);
  f32x4 tab[3];
  [[maybe_unused]] u32 wc[2] = {0, 0};
  LDS char* st = smem + S_ST + w * 4608;
  {
    const float L2E = 1.44269504088896341f;
    const float dt = rbf(__builtin_amdgcn_exp2f(bf2f(r_ldt) * L2E));
    const float ar = -rbf(__builtin_amdgcn_exp2f(bf2f(r_la) * L2E));
    const float zl = ar * dt * L2E, zi = bf2f(r_aim) * dt;
    const float th = zi * 0.15915494309189535f;
    const float tl = fmaf(zi, 0.15915494309189535f, -th) + zi * 4.6017384e-10f;
    const c2 C = {2.0f * (bf2f(r_cre) * dt), 2.0f * (bf2f(r_cim) * dt)};
    const c2 w1 = cexp_k(zl, th, tl, 1.f), w8 = cexp_k(zl, th, tl, 8.f), w128 = cexp_k(zl, th, tl, 128.f);
    c2 w2, w4, w16, w32, w64, w256, w512, w1024, w2048;
    asm(PKM("%0", "%9", "%9") PKM("%2", "%10", "%10") PKM("%5", "%11", "%11") PKF("%0", "%9", "%9") PKF("%2", "%10", "%10") PKF("%5", "%11", "%11")
        PKM("%1", "%0", "%0") PKM("%3", "%2", "%2") PKM("%6", "%5", "%5") PKF("%1", "%0", "%0") PKF("%3", "%2", "%2") PKF("%6", "%5", "%5")
        PKM("%4", "%3", "%3") PKM("%7", "%6", "%6") PKF("%4", "%3", "%3") PKF("%7", "%6", "%6")
        PKM("%8", "%7", "%7") PKF("%8", "%7", "%7")
        : "=&v"(w2), "=&v"(w4), "=&v"(w16), "=&v"(w32), "=&v"(w64), "=&v"(w256), "=&v"(w512), "=&v"(w1024), "=&v"(w2048)
        : "v"(w1), "v"(w8), "v"(w128));
    c2 C128, C256, C512, C1024, C2048;
    asm(PKM("%0", "%5", "%6") PKM("%1", "%5", "%7") PKM("%2", "%5", "%8") PKM("%3", "%5", "%9") PKM("%4", "%5", "%10")
        PKF("%0", "%5", "%6") PKF("%1", "%5", "%7") PKF("%2", "%5", "%8") PKF("%3", "%5", "%9") PKF("%4", "%5", "%10")
        : "=&v"(C128), "=&v"(C256), "=&v"(C512), "=&v"(C1024), "=&v"(C2048)
        : "v"(C), "v"(w128), "v"(w256), "v"(w512), "v"(w1024), "v"(w2048));
#pragma unroll
    for (int i = 0; i < 2; ++i) {
      const int ln = i * 768 + tid, all = (sg % 12) * 864 + ln;
      if (ln < 864) wc[i] = *(const GLB u32*)((const char*)(all < 5184 ? p.cre : p.cim) + (size_t)(all < 5184 ? all : all - 5184) * 128);
    }
    STAMP(0, 2);
    LDS char* wr = st + 4 * lane;
    LDS const char* rd = st + (lane & 15) * 288 + (lane >> 4) * 16;
    bf16x8 B[4];
    const c2 w8c = {w8[0], -w8[1]}, w32c = {w32[0], -w32[1]}, w64c = {w64[0], -w64[1]};
    octet(wr, c2{1.f, 0.f}, w8c, w32c);
    octet(wr + 8 * 288, w64c, w8c, w32c);
    wave_fence();
#pragma unroll
    for (int ks = 0; ks < 4; ++ks) B[ks] = lds_frag(rd + 64 * ks);
    wave_fence();
#pragma unroll
    for (int mt = 0; mt < 3; ++mt) {
      if (mt == 0) { octet(wr, C, w1, w4); octet(wr + 8 * 288, C128, w1, w4); }
      if (mt == 1) { octet(wr, C256, w2, w128); octet(wr + 8 * 288, C512, w128, w4); }
      if (mt == 2) { octet(wr, C1024, w128, w512); *(LDS u32*)(wr + 8 * 288) = pack_bf16(C2048[0], C2048[1]); }
      wave_fence();
      f32x4 acc = {0, 0, 0, 0};
      bf16x8 A[4];
#pragma unroll
      for (int ks = 0; ks < 4; ++ks) A[ks] = lds_frag(rd + 64 * ks);
      __builtin_amdgcn_sched_barrier(0);
#pragma unroll
      for (int ks = 0; ks < 4; ++ks) acc = mfma16(A[ks], B[ks], acc);
      __builtin_amdgcn_sched_barrier(0);
      wave_fence();
      tab[mt] = acc;
    }
  }
  const int b = lane & 15, q = lane >> 4;
#pragma unroll
  for (int mt = 0; mt < 3; ++mt) *(LDS f32x4*)(st + mt * 1024 + lane * 16) = tab[mt];
  STAMP(0, 3);
  bar();
  {
    LDS const char* ot = smem + S_ST + (w ^ 1) * 4608 + lane * 16;
    LDS char* T = smem + S_T + ch * S_TSZ;
    if (hh == 0) {
      const f32x4 v0 = tab[0] + *(LDS const f32x4*)(ot), v2 = tab[2] + *(LDS const f32x4*)(ot + 2048);
      *(LDS u32x2*)(T + 2 * (128 * (q >> 1) + 8 * b + 4 * (q & 1))) = u32x2{pack_bf16(v0[0], v0[1]), pack_bf16(v0[2], v0[3])};
      const u32 a0 = pack_bf16(v2[0], v2[1]), a1 = pack_bf16(v2[2], v2[3]);
      if (q < 2) {
        LDS char* t3 = T + 2 * (512 + 64 * q + b);
        *(LDS u16*)(t3) = (u16)a0; *(LDS u16*)(t3 + 32) = (u16)(a0 >> 16);
        *(LDS u16*)(t3 + 64) = (u16)a1; *(LDS u16*)(t3 + 96) = (u16)(a1 >> 16);
      } else if (lane == 32) *(LDS u16*)(T + 2 * 640) = (u16)a0;
    } else {
      const f32x4 v1 = tab[1] + *(LDS const f32x4*)(ot + 1024);
      const u32 a0 = pack_bf16(v1[0], v1[1]), a1 = pack_bf16(v1[2], v1[3]);
      if (q < 2) *(LDS u32x2*)(T + 2 * (256 + 64 * q + 4 * b)) = u32x2{a0, a1};
      else {
        LDS char* t2 = T + 2 * (384 + 2 * b + (q - 2));
        *(LDS u16*)(t2) = (u16)a0; *(LDS u16*)(t2 + 64) = (u16)(a0 >> 16);
        *(LDS u16*)(t2 + 128) = (u16)a1; *(LDS u16*)(t2 + 192) = (u16)(a1 >> 16);
      }
    }
  }
  bar();
  STAMP(0, 4);
  u32x4 uv[2];
  {
    GLB u64* fl = (GLB u64*)(p.ws + WS_FL) + (8 * hh + (lane & 7)) * DM + d;
    asm volatile("" :: "v"(wc[0]), "v"(wc[1]), "v"(wb[0]), "v"(wb[1]));
    wait_flags(fl, true);
    const __amdgpu_buffer_rsrc_t rs = mk_rsrc(p.ws + WS_XT, DM * SL * 2);
#pragma unroll
    for (int v = 0; v < 2; ++v) uv[v] = __builtin_amdgcn_raw_buffer_load_b128(rs, (d * SL + 1024 * hh + 8 * (lane + 64 * v)) * 2, 0, SC1);
    if (lane < 8) __hip_atomic_store(fl, 0ull, RLX_AGENT);
  }
  STAMP(0, 5);
  {
    LDS const char* T = smem + S_T + ch * S_TSZ;
    u32 o[10];
    const u32 e512 = *(LDS const u16*)(T + 2 * 512);
    o[8] = o[9] = e512 | (e512 << 16);
    if (hh == 0) {
      const int off = lane < 16 ? 32 * lane : (lane < 32 ? 256 + 16 * lane : 512 + 8 * lane);
      u32x2 W[4];
#pragma unroll
      for (int i = 0; i < 4; ++i) W[i] = *(LDS const u32x2*)(T + off + 8 * i);
      if (lane < 16) {
#pragma unroll
        for (int i = 0; i < 8; ++i) o[i] = W[i >> 1][i & 1];
      } else if (lane < 32) {
#pragma unroll
        for (int i = 0; i < 4; ++i) {
          const u32 a = W[i >> 1][i & 1], nx = W[(i + 1) >> 1][(i + 1) & 1];
          o[2 * i] = dup_lo(a);
          o[2 * i + 1] = hi_lo(a, nx);
        }
      } else {
        const u32 w0 = W[0][0], w1 = W[0][1], w2 = W[1][0];
        o[0] = dup_lo(w0); o[1] = w0; o[2] = dup_hi(w0); o[3] = dup_lo(w1);
        o[4] = o[3]; o[5] = w1; o[6] = dup_hi(w1); o[7] = dup_lo(w2);
      }
    } else {
      const u32 w0 = *(LDS const u32*)(T + 1024 + 4 * lane), w1 = *(LDS const u32*)(T + 1028 + 4 * lane);
      o[0] = o[1] = dup_lo(w0); o[2] = w0; o[3] = o[4] = o[5] = dup_hi(w0); o[6] = o[7] = dup_lo(w1);
    }
    o[8] = (u32)__builtin_amdgcn_update_dpp((int)o[8], (int)o[0], 0x130, 0xf, 0xf, false);
    o[9] = (u32)__builtin_amdgcn_update_dpp((int)o[9], (int)o[1], 0x130, 0xf, 0xf, false);
    u32 a[9];
#pragma unroll
    for (int i = 0; i < 9; ++i) a[i] = hi_lo(o[i], o[i + 1]);
    LDS char* xb = smem + S_X + ch * 4 * S_CPY + 32 + 2 * (1024 * hh + 16 * lane);
    *(LDS u32x4*)(xb) = u32x4{o[0], o[1], o[2], o[3]};
    *(LDS u32x4*)(xb + 16) = u32x4{o[4], o[5], o[6], o[7]};
    *(LDS u32x4*)(xb + S_CPY) = u32x4{a[0], a[1], a[2], a[3]};
    *(LDS u32x4*)(xb + S_CPY + 16) = u32x4{a[4], a[5], a[6], a[7]};
    *(LDS u32x4*)(xb + 2 * S_CPY) = u32x4{o[1], o[2], o[3], o[4]};
    *(LDS u32x4*)(xb + 2 * S_CPY + 16) = u32x4{o[5], o[6], o[7], o[8]};
    *(LDS u32x4*)(xb + 3 * S_CPY) = u32x4{a[1], a[2], a[3], a[4]};
    *(LDS u32x4*)(xb + 3 * S_CPY + 16) = u32x4{a[5], a[6], a[7], a[8]};
    if (hh == 0 && lane == 0) {
      LDS char* hb = smem + S_X + ch * 4 * S_CPY;
      const u32x4 z = {0, 0, 0, 0};
      *(LDS u32x4*)(hb) = z;
      *(LDS u32x4*)(hb + 16) = z;
      *(LDS u32x4*)(hb + S_CPY) = z;
      *(LDS u32x4*)(hb + S_CPY + 16) = u32x4{0, 0, 0, o[0] << 16};
      *(LDS u32x4*)(hb + 2 * S_CPY) = z;
      *(LDS u32x4*)(hb + 2 * S_CPY + 16) = u32x4{0, 0, 0, o[0]};
      *(LDS u32x4*)(hb + 3 * S_CPY) = z;
      *(LDS u32x4*)(hb + 3 * S_CPY + 16) = u32x4{0, 0, o[0] << 16, a[0]};
    }
    if (tid < 6 * 32) *(LDS u32x4*)(smem + S_Z + (tid >> 5) * S_ZSZ + (tid & 31) * 16) = u32x4{0, 0, 0, 0};
    STAMP(0, 6);
    LDS char* z = smem + S_Z + ch * S_ZSZ + 512 + (1024 * hh + 8 * lane) * 2;
    *(LDS u32x4*)z = uv[0];
    *(LDS u32x4*)(z + 1024) = uv[1];
  }
  bar();
  STAMP(0, 7);
  f32x4 acc[8];
#pragma unroll
  for (int i = 0; i < 8; ++i) acc[i] = f32x4{0, 0, 0, 0};
  {
    const int kq = q, qq = b >> 2, pp = b & 3;
    LDS const char* fb = smem + S_X + (ch * 4 + ((4 - qq) & 3)) * S_CPY +
                         2 * (16 + 16 * (kq >> 1) - 8 * (kq & 1) + 4 * pp - (qq ? 4 : 0)) + 64 * hh;
    LDS const char* gb = smem + S_Z + ch * S_ZSZ + 512 + 32 * (b - (kq >> 1)) + 16 * (kq & 1) - 64 * hh;
#pragma unroll 1
    for (int it = 0; it < 4; ++it) {
      bf16x8 F[8], G[8];
#pragma unroll
      for (int s = 0; s < 8; ++s) {
        F[s] = tr_frag(fb + 512 * s, fb + 512 * s - 8);
        G[s] = lds_frag(gb + 512 * s);
      }
      __builtin_amdgcn_sched_barrier(0);
#pragma unroll
      for (int t = 0; t < 8; ++t)
#pragma unroll
        for (int s = 0; s <= t; ++s) acc[t] = mfma16(F[s], G[t - s], acc[t]);
      fb += 128;
      gb -= 128;
    }
  }
  __builtin_amdgcn_sched_barrier(0);
  STAMP(0, 8);
  bar();
  {
    LDS char* ex = smem + S_X + 49152 + w * 4096 + lane * 16;
#pragma unroll
    for (int i = 0; i < 4; ++i) {
      const f32x4 lo = acc[i], hi = acc[4 + i];
      *(LDS f32x4*)(ex + i * 1024) = hh ? +lo : +hi;
    }
    if (tid == 0) *(LDS u32*)(smem + S_CNT) = 0;
  }
  bar();
  u32x2 yo[4];
  {
    LDS const char* ey = smem + S_X + 49152 + (w ^ 1) * 4096 + lane * 16;
#pragma unroll
    for (int i = 0; i < 4; ++i) {
      const f32x4 lo = acc[i], hi = acc[4 + i];
      const f32x4 y = (hh ? +hi : +lo) + *(LDS const f32x4*)(ey + i * 1024);
      yo[i] = u32x2{pack_bf16(y[0], y[1]), pack_bf16(y[2], y[3])};
    }
  }
  bar();
  {
    const __amdgpu_buffer_rsrc_t rs = mk_rsrc(p.ws + WS_AT, DM * SL * 2);
    const int dst = (d * SL + 1024 * hh + 16 * (lane & 15) + 4 * (lane >> 4)) * 2;
#pragma unroll
    for (int i = 0; i < 4; ++i) __builtin_amdgcn_raw_buffer_store_b64(yo[i], rs, dst + 512 * i, 0, SC1);
  }
  STAMP(0, 9);
}
DEV void s_signal(const P& p, LDS char* smem, int sg, int lane) {
  u32 old = 0;
  if (lane == 0) old = __hip_atomic_fetch_add((LDS u32*)(smem + S_CNT), 1u, __ATOMIC_RELAXED, __HIP_MEMORY_SCOPE_WORKGROUP);
  if (__builtin_amdgcn_readfirstlane(old) == 11) {
    const int lt = lane & 31, second = lane >> 5;
    const int dd = 6 * sg + (second ? 5 : 0);
    const int idx = dd < 480 ? dd / 96 : 5 + (dd - 480) / 32;
    const int dfirst = idx < 5 ? 96 * idx : 480 + 32 * (idx - 5);
    __hip_atomic_store((GLB u64*)(p.ws + WS_IN) + tile_block(lt, idx) * 64 + IN_A + (sg - dfirst / 6), FLAG_SET, RLX_AGENT);
  }
}

DEV void role_gate(const P& p, int rb, int w, int lane, u64 pc) {
  u32 pf0 = 0, pf1 = 0;
  const int tid = w * 64 + lane;
  const int sub = lane & 7;
  u32x4 wv[9];
#pragma unroll
  for (int i = 0; i < 9; ++i) wv[i] = *(const u32x4*)((const char*)p.gw + (i * 8 + sub) * 16);
  for (int r = w * 8 + (lane >> 3); r < 128; r += 96) {
    const int l = rb * 128 + r;
    float s = 0.f;
    u32x4 xv[9];
#pragma unroll
    for (int i = 0; i < 9; ++i) xv[i] = *(const u32x4*)((const char*)p.x + (size_t)l * 1152 + (i * 8 + sub) * 16);
#pragma unroll
    for (int i = 0; i < 9; ++i) {
#pragma unroll
      for (int k = 0; k < 4; ++k) {
        const u32 pr = pack_bf16(bflo(xv[i][k]) * bflo(wv[i][k]), bfhi(xv[i][k]) * bfhi(wv[i][k]));
        s += bflo(pr);
        s += bfhi(pr);
      }
    }
    s += __shfl_xor(s, 1); s += __shfl_xor(s, 2); s += __shfl_xor(s, 4);
    const float gv = rbf(1.0f / (1.0f + expf(-rbf(s))));
    if (sub == 0)
      __hip_atomic_store((GLB u64*)(p.ws + WS_G) + l, ((u64)__float_as_uint(rbf(1.0f - gv)) << 32) | __float_as_uint(gv), RLX_AGENT);
  }
  asm volatile("" :: "v"(pf0), "v"(pf1));
  wait_vm<0>();
  bar();
  if (w == 0 && lane < 16)
    __hip_atomic_store((GLB u64*)(p.ws + WS_IN) + tile_block(2 * rb + (lane >> 3), lane & 7) * 64 + IN_G, FLAG_SET, RLX_AGENT);
}

constexpr int H_C = 32768, H_CS = 12288;
DEV int h_slot(int i) { return i < 4 ? i * 8192 : 131072 + (i - 4) * 8192; }
constexpr int H_AT = H_C + 2 * H_CS;
constexpr int H_XT = H_C + 3 * H_CS;
constexpr int H_G = H_C + 4 * H_CS;
constexpr int H_OUT = H_C + 5 * H_CS;
constexpr int K_LDS = 163840;

constexpr int h_wait(int D, int NDT) {
  const int G = 4 + 2 * NDT;
  switch (D) {
    case 0: return 4;
    case 1: return 8;
    case 2: return 4 + G;
    case 3: case 4: return 2 * G;
    case 5: case 6: return 2 * G + 1;
    case 7: return G + NDT;
    default: return NDT;
  }
}
template <int D, int NDT, class F> DEV void h_steps(F&& after, u64& fv, char* wsp) {
  if constexpr (D == 7) asm volatile("s_waitcnt vmcnt(%1)" : "+v"(fv) : "n"(h_wait(D, NDT)) : "memory");
  else wait_vm<h_wait(D, NDT)>();
  LSTAMP2(1, 16 + D);
  bar();
  after(std::integral_constant<int, D>{});
  if constexpr (D < 8) h_steps<D + 1, NDT>(after, fv, wsp);
}
DEV int p2_rot(int d0) { const int idx = d0 < 480 ? d0 / 96 : 5 + (d0 - 480) / 32; return (idx * 18) >> 3; }

template <int NDT, class F>
DEV void p2_loader(const P& p, LDS char* smem, int lt, int d0, int lw, int lane, bool scratch_barrier, F&& pre_poll) {
  __builtin_amdgcn_s_setprio(3);
  const char* hb = p.ws + WS_HB + (size_t)lt * 18 * 8192 + (2 * lw) * 1024 + lane * 16;
  constexpr int W = 32 * NDT;
  const int l0 = 64 * lt, rot = p2_rot(d0);
  u32 co[NDT];
#pragma unroll
  for (int i = 0; i < NDT; ++i) {
    const int row = 8 * (NDT * lw + i) + (lane >> 3), cp = lane & 7;
    co[i] = (u32)(d0 + row) * 1152u + ((cp ^ (row & 7)) << 4);
  }
  auto issue_c = [&](int s) {
    LDS char* buf = smem + H_C + (s & 7) * H_CS + NDT * lw * 1024;
    int ks = s + rot; ks = ks >= 18 ? ks - 18 : ks;
    const char* cb = ks < 9 ? (const char*)p.cre + ks * 128 : (const char*)p.cim + (ks - 9) * 128;
#pragma unroll
    for (int i = 0; i < NDT; ++i) dma16<0>(cb + co[i], buf + i * 1024);
  };
  auto issue_h = [&](int s) {
    LDS char* buf = smem + h_slot(s & 7) + 2 * lw * 1024;
    int ks = s + rot; ks = ks >= 18 ? ks - 18 : ks;
#pragma unroll
    for (int i = 0; i < 2; ++i) dma16<SC1>(hb + ks * 8192 + i * 1024, buf + i * 1024);
  };
  GLB u64* inb = (GLB u64*)(p.ws + WS_IN) + blockIdx.x * 64;
  const int nA = (d0 + W - 1) / 6 - d0 / 6 + 1;
  const bool need = lane == IN_G || (lane >= IN_A && lane < IN_A + nA);
  pre_poll();
  if (!scratch_barrier) {
#pragma unroll 1
    for (int s = 0; s < 8; ++s) issue_c(s);
  }
  { [[maybe_unused]] const int w = lw + 8; STAMP(1, 1); }
  if (scratch_barrier) bar();
  wait_flags(inb + IN_H + (lane < 36 ? lane : 0), true);
  { [[maybe_unused]] const int w = lw + 8; STAMP(1, 2); }
#pragma unroll
  for (int s = 0; s < 4; ++s) issue_h(s);
  { [[maybe_unused]] const int w = lw + 8; STAMP(1, 3); }
  u64 fv = 0;
  bool late = false;
  auto issue_at = [&]() {
#pragma unroll
    for (int i = 0; i < NDT; ++i) {
      const int pc = NDT * lw + i;
      dma16<SC1>(p.ws + WS_AT + (size_t)(d0 + 8 * pc + (lane >> 3)) * 4096 + l0 * 2 + (lane & 7) * 16, smem + H_AT + pc * 1024);
    }
  };
  auto issue_g = [&]() { if (lane < 8) dma16<SC1>(p.ws + WS_G + (size_t)l0 * 8 + lw * 128 + lane * 16, smem + H_G + lw * 128); };
  h_steps<0, NDT>([&](auto D_) {
    constexpr int D = decltype(D_)::value;
    if constexpr (D == 0) { issue_h(4); issue_h(5); issue_h(6); issue_h(7); }
    if constexpr (D >= 1 && D <= 5) { issue_h(2 * D + 6); issue_h(2 * D + 7); issue_c(2 * D + 6); issue_c(2 * D + 7); }
    if constexpr (D == 4)
      asm volatile("global_load_dwordx2 %0, %1, off sc1" : "=v"(fv) : "v"(inb + lane) : "memory");
    if constexpr (D == 6) {
#pragma unroll
      for (int i = 0; i < NDT; ++i) {
        const int pc = NDT * lw + i, id = pc * 64 + lane, row = id / (4 * NDT), qq = id - row * (4 * NDT);
        dma16<0>((const char*)p.x + (size_t)(l0 + row) * 1152 + d0 * 2 + qq * 16, smem + H_XT + pc * 1024);
      }
    }
    if constexpr (D == 7) {
      late = !__all(!need || fv == FLAG_SET);
      if (!late) { issue_g(); issue_at(); }
    }
  }, fv, p.ws);
  if (late) { wait_flags(inb + lane, need); issue_g(); issue_at(); }
  wait_vm<0>();
  bar();
}

template <int NDT>
DEV void p2_compute(const P& p, LDS char* smem, int lt0, int d0, int w, int lane, const u32x2 (&dsk)[6]) {
  const int lh = (w >> 1) & 1, kh = w & 1, dg = w >> 2;
  const int r = lane & 15, kq = lane >> 4;
  const int lt = 2 * lh + kh, l0 = 64 * lt0;
  constexpr int W = 32 * NDT;
  u32x2 da[NDT], db[NDT];
#pragma unroll
  for (int t = 0; t < NDT; ++t) {
    const int d = d0 + 16 * (NDT * dg + t) + 4 * kq;
    da[t] = *(const u32x2*)(p.dskA + d);
    db[t] = *(const u32x2*)(p.dskB + d);
  }
  GLB u64* inb = (GLB u64*)(p.ws + WS_IN) + blockIdx.x * 64;

  f32x4 acc[2][NDT];
#pragma unroll
  for (int i = 0; i < 2; ++i)
#pragma unroll
    for (int t = 0; t < NDT; ++t) acc[i][t] = f32x4{0, 0, 0, 0};
  int ho[2][2];
#pragma unroll
  for (int u = 0; u < 2; ++u) {
    const int k = 32 * kh + 8 * kq + 4 * u + (r >> 2), i = (r & 3) >> 1;
    const int o = (2 * (k >> 4) + i) * 1024 + (32 * lh + ((k & 15) ^ (4 * i))) * 16 + (r & 1) * 8;
    ho[0][u] = o + 256 * kh;
    ho[1][u] = o + 256 * (kh ^ 1);
  }
  const int co = H_C + (16 * NDT * dg + r) * 128 + (((4 * kh + kq) ^ (r & 7)) << 4);
  bf16x8 hb[2][2][2], ca[2][2][NDT];
  auto rd = [&](auto SET_, int D) {
    constexpr int SET = decltype(SET_)::value;
#pragma unroll
    for (int u = 0; u < 2; ++u) {
      const int s = 2 * D + u;
      LDS const char* hbuf = smem + h_slot(s & 7);
#pragma unroll
      for (int i = 0; i < 2; ++i) hb[SET][u][i] = tr_frag(hbuf + ho[i][0], hbuf + ho[i][1]);
      LDS const char* cbuf = smem + (s & 7) * H_CS;
#pragma unroll
      for (int t = 0; t < NDT; ++t) ca[SET][u][t] = lds_frag(cbuf + co + t * 2048);
    }
    __builtin_amdgcn_sched_barrier(0);
  };
  auto mma = [&](auto SET_) {
    constexpr int SET = decltype(SET_)::value;
#pragma unroll
    for (int u = 0; u < 2; ++u)
#pragma unroll
      for (int t = 0; t < NDT; ++t) {
        acc[0][t] = mfma16(ca[SET][u][t], hb[SET][u][0], acc[0][t]);
        acc[1][t] = mfma16(ca[SET][u][t], hb[SET][u][1], acc[1][t]);
      }
    __builtin_amdgcn_sched_barrier(0);
  };
  int orow[NDT], opc[NDT];
#pragma unroll
  for (int it = 0; it < NDT; ++it) {
    const int id = it * 512 + w * 64 + lane;
    orow[it] = id / (8 * NDT); opc[it] = id - orow[it] * (8 * NDT);
    asm volatile("" : "+v"(orow[it]), "+v"(opc[it]));
  }
  using S0 = std::integral_constant<int, 0>;
  using S1 = std::integral_constant<int, 1>;
  STAMP(1, 1);
  bar();
  STAMP(1, 2);
  rd(S0{}, 0);
#pragma unroll 1
  for (int D = 1; D < 9; D += 2) {
    bar(); rd(S1{}, D); mma(S0{});
    bar(); rd(S0{}, D + 1); mma(S1{});
  }
  mma(S0{});
  __builtin_amdgcn_sched_barrier(0);
  STAMP(1, 3);
  bar();
  if (w == 0) __hip_atomic_store(inb + lane, 0ull, RLX_AGENT);
  LDS char* ex = smem + w * (NDT * 1024) + lane * 16;
#pragma unroll
  for (int t = 0; t < NDT; ++t) *(LDS f32x4*)(ex + t * 1024) = acc[1][t];
  bar();
  LDS const char* ey = smem + (w ^ 1) * (NDT * 1024) + lane * 16;
  constexpr int OSTR = W * 4 + 16;
  const f32x2 gg = *(LDS const f32x2*)(smem + H_G + (16 * lt + r) * 8);
  const f32x2 g2 = {gg[0], gg[0]}, om2 = {gg[1], gg[1]};
  auto up = [](u32 v) { return f32x2{bflo(v), bfhi(v)}; };
  auto rb2 = [&](f32x2 v) { return up(pack_bf16(v[0], v[1])); };
#pragma unroll
  for (int t = 0; t < NDT; ++t) {
    const int dt = NDT * dg + t;
    const f32x4 y = acc[0][t] + *(LDS const f32x4*)(ey + t * 1024);
    const u32x2 av = __builtin_bit_cast(u32x2, tr_read(smem + H_AT + (16 * dt + 4 * kq + (r >> 2)) * 128 + lt * 32 + (r & 3) * 8));
    const u32x2 xv = *(LDS const u32x2*)(smem + H_XT + (16 * lt + r) * (W * 2) + (16 * dt + 4 * kq) * 2);
    f32x2 res[2];
#pragma unroll
    for (int h = 0; h < 2; ++h) {
      const f32x2 xf = up(xv[h]);
      const f32x2 a = rb2(up(av[h]) + rb2(xf * up(da[t][h])));
      const f32x2 b = (h ? hi2(y) : lo2(y)) + rb2(xf * up(db[t][h]));
      res[h] = fma2(om2, b, rb2(g2 * a));
    }
    *(LDS f32x4*)(smem + H_OUT + (16 * lt + r) * OSTR + (16 * dt + 4 * kq) * 4) = cat2(res[0], res[1]);
  }
  STAMP(1, 4);
  bar();
  const __amdgpu_buffer_rsrc_t rso = mk_rsrc(p.out, SL * DM * 4);
#pragma unroll
  for (int it = 0; it < NDT; ++it) {
    const f32x4 v = *(LDS const f32x4*)(smem + H_OUT + orow[it] * OSTR + opc[it] * 16);
    __builtin_amdgcn_raw_buffer_store_b128(__builtin_bit_cast(u32x4, v), rso, ((l0 + orow[it]) * DM + d0 + opc[it] * 4) * 4, 0, OUT_AUX);
  }
}

template <int NDT>
DEV void phase2(const P& p, LDS char* smem, int lt, int d0, int w, int lane, bool scratch_barrier, int sg, const u32x2 (&dsk)[6]) {
  STAMP(1, 0);
  if (w >= 8) {
    p2_loader<NDT>(p, smem, lt, d0, w - 8, lane, scratch_barrier, [&]() {
      if (NDT == 1) { wait_vm<0>(); s_signal(p, smem, sg, lane); }
    });
  } else {
    if (NDT == 1) { wait_vm<0>(); s_signal(p, smem, sg, lane); }
    p2_compute<NDT>(p, smem, lt, d0, w, lane, dsk);
  }
  STAMP(1, 15);
}

__global__ __launch_bounds__(768, 3) void gated_mix_s4d_s5(P p) {
  extern __shared__ __attribute__((aligned(16))) char smem_raw[];
  LDS char* smem = (LDS char*)smem_raw;
  u64 pc;
  asm volatile("s_getpc_b64 %0" : "=s"(pc));
  u32 pf = 0;
#define PF_TOP 312
  if (threadIdx.x < PF_TOP && blockIdx.x >= 144)
    asm volatile("global_load_dword %0, %1, off" : "=v"(pf) : "v"((pc & ~255ull) + threadIdx.x * 128));
  const int w = __builtin_amdgcn_readfirstlane(threadIdx.x >> 6), lane = threadIdx.x & 63;
  const int xcd = blockIdx.x & 7, j = blockIdx.x >> 3;
  u32x2 dsk[6];
#pragma unroll
  for (int i = 0; i < 6; ++i) dsk[i] = u32x2{0, 0};
  STAMP(0, 0);
  if (j >= 18 && j < 30) {
    const int sg = 12 * xcd + (j - 18);
    role_s(p, smem, sg, w, lane);
    STAMP(0, 15);
    phase2<1>(p, smem, 4 * xcd + (j - 18) / 3, 480 + 32 * ((j - 18) % 3), w, lane, false, sg, dsk);
  } else {
    const bool isg = j < 18;
    if (isg) {
      const int c = 2 * xcd + j / 9, g = j % 9;
      if (w >= 8) g_loader(p, smem, c, g, 96 * (j % 5), w - 8, lane);
      else if (w < 4) g_compute<0>(p, smem, c, g, w, lane, pf);
      else g_compute<1>(p, smem, c, g, w, lane, pf);
    } else {
      role_gate(p, 2 * xcd + (j - 30), w, lane, pc);
    }
    STAMP(0, 15);
    const int e = isg ? j : j - 12;
    phase2<3>(p, smem, 4 * xcd + e / 5, 96 * (e % 5), w, lane, isg, 0, dsk);
  }
  asm volatile("s_waitcnt vmcnt(0)" :: "v"(pf));
}

extern "C" void kernel_launch(void* const* d_in, const int* in_sizes, int n_in, void* d_out, int out_size,
                              void* d_ws, size_t ws_size, hipStream_t stream) {
  P a;
  a.x = (const u16*)d_in[0]; a.la = (const u16*)d_in[1]; a.aim = (const u16*)d_in[2];
  a.creA = (const u16*)d_in[3]; a.cimA = (const u16*)d_in[4]; a.ldtA = (const u16*)d_in[5]; a.dskA = (const u16*)d_in[6];
  a.lre = (const u16*)d_in[8]; a.lim = (const u16*)d_in[9]; a.bre = (const u16*)d_in[10]; a.bim = (const u16*)d_in[11];
  a.cre = (const u16*)d_in[12]; a.cim = (const u16*)d_in[13]; a.ldtB = (const u16*)d_in[14];
  a.dskB = (const u16*)d_in[15]; a.gw = (const u16*)d_in[16];
  a.ws = (char*)d_ws + WS_OFF; a.out = (float*)d_out;
  (void)hipFuncSetAttribute((const void*)gated_mix_s4d_s5, hipFuncAttributeMaxDynamicSharedMemorySize, K_LDS);
  gated_mix_s4d_s5<<<256, 768, K_LDS, stream>>>(a);
}
```

```cpp
#define HOLD_AT 14
#define SIG_FIRST 1
#define PF_NOT_G 1
#include <hip/hip_runtime.h>
#include <hip/hip_bf16.h>
#include <stdint.h>
#include <type_traits>

typedef unsigned short u16;
typedef unsigned int u32;
typedef unsigned long long u64;
typedef __attribute__((ext_vector_type(8))) __bf16 bf16x8;
typedef __attribute__((ext_vector_type(2))) __bf16 bf16x2;
typedef __attribute__((ext_vector_type(4))) short s16x4;
typedef __attribute__((ext_vector_type(4))) float f32x4;
typedef __attribute__((ext_vector_type(2))) float f32x2;
typedef __attribute__((ext_vector_type(4))) unsigned u32x4;
typedef __attribute__((ext_vector_type(2))) unsigned u32x2;
#define LDS __attribute__((address_space(3)))
#define GLB __attribute__((address_space(1)))
#define DEV __device__ __forceinline__
#define RLX_AGENT __ATOMIC_RELAXED, __HIP_MEMORY_SCOPE_AGENT

constexpr int SL = 2048;
constexpr int DM = 576;

constexpr size_t WS_HB = 0;
constexpr size_t WS_AT = WS_HB + (size_t)2 * DM * SL * 2;
constexpr size_t WS_G = WS_AT + (size_t)DM * SL * 2;
constexpr size_t WS_GRAN = WS_G + (size_t)SL * 8;
constexpr size_t WS_EP = WS_GRAN + (size_t)9 * 16 * 2 * 64 * 8;
constexpr size_t WS_FL = WS_EP + 4096;
constexpr size_t WS_XT = WS_FL + (size_t)16 * DM * 8;
constexpr size_t WS_IN = WS_XT + (size_t)DM * SL * 2;
constexpr u64 FLAG_SET = 0x9E3779B97F4A7C15ull;
constexpr u32 SPIN_CAP = 1u << 17;
constexpr int IN_H = 0, IN_G = 36, IN_A = 40;

constexpr size_t WS_ST = (size_t)16 << 20;
#define STAMP(kern, idx) do {} while (0)
#define CSTAMP(kern, idx) do {} while (0)
#define LSTAMP(kern, idx) do {} while (0)
#define LSTAMP2(kern, idx) do {} while (0)

#define HOLD(id) do { if (HOLD_AT == (id)) { while (__builtin_amdgcn_s_memrealtime() - g_tb < 4000) __builtin_amdgcn_s_sleep(2); } } while (0)
DEV float bf2f(u16 v) { return __uint_as_float((u32)v << 16); }
DEV float bflo(u32 v) { return __uint_as_float(v << 16); }
DEV float bfhi(u32 v) { return __uint_as_float(v & 0xffff0000u); }
DEV u32 pack_bf16(float lo, float hi) {
  f32x2 v = {lo, hi};
  bf16x2 b = __builtin_convertvector(v, bf16x2);
  return __builtin_bit_cast(u32, b);
}
DEV float rbf(float x) { return bflo(pack_bf16(x, x)); }
template <int N> DEV void wait_vm() { asm volatile("s_waitcnt vmcnt(%0)" ::"n"(N) : "memory"); }
DEV void bar() { asm volatile("s_waitcnt lgkmcnt(0)\n\ts_barrier" ::: "memory"); }
DEV void wave_fence() { __builtin_amdgcn_fence(__ATOMIC_ACQ_REL, "wavefront"); }
template <int AUX> DEV void dma16(const void* src, LDS char* dst) {
  __builtin_amdgcn_global_load_lds((const GLB u32*)src, (LDS u32*)dst, 16, 0, AUX);
}
constexpr int SC1 = 16;
#define GPOLL_SLEEP 1
#define GPOLL_DELAY 24
#define POLL_SLEEP 1
#define OUT_AUX 16
#define WS_OFF 0
#define XT_WAIT 4
DEV bf16x8 lds_frag(LDS const char* p) { return *(LDS const bf16x8*)p; }
DEV s16x4 tr_read(LDS const char* a) { return __builtin_amdgcn_ds_read_tr16_b64_v4i16((LDS s16x4*)a); }
DEV bf16x8 tr_frag(LDS const char* a, LDS const char* b) {
  const s16x4 lo = tr_read(a), hi = tr_read(b);
  return __builtin_bit_cast(bf16x8, __builtin_shufflevector(lo, hi, 0, 1, 2, 3, 4, 5, 6, 7));
}
DEV f32x4 mfma16(bf16x8 a, bf16x8 b, f32x4 c) { return __builtin_amdgcn_mfma_f32_16x16x32_bf16(a, b, c, 0, 0, 0); }
DEV __amdgpu_buffer_rsrc_t mk_rsrc(const void* base, int bytes) {
  return __builtin_amdgcn_make_buffer_rsrc(const_cast<void*>(base), (short)0, bytes, 0x00020000);
}
struct cf { float r, i; };
DEV cf cmul(cf a, cf b) { return {a.r * b.r - a.i * b.i, a.r * b.i + a.i * b.r}; }
DEV cf cfma(cf a, cf b, cf c) { return {fmaf(a.r, b.r, fmaf(-a.i, b.i, c.r)), fmaf(a.r, b.i, fmaf(a.i, b.r, c.i))}; }
typedef f32x2 c2;
DEV c2 cm2(c2 a, c2 b) {
  c2 t, r;
  asm("v_pk_mul_f32 %0, %1, %2 op_sel:[0,0] op_sel_hi:[0,1]" : "=v"(t) : "v"(a), "v"(b));
  asm("v_pk_fma_f32 %0, %1, %2, %3 op_sel:[1,1,0] op_sel_hi:[1,0,1] neg_lo:[1,0,0]" : "=v"(r) : "v"(a), "v"(b), "v"(t));
  return r;
}
DEV f32x2 fma2(f32x2 a, f32x2 b, f32x2 c) { return __builtin_elementwise_fma(a, b, c); }
DEV f32x2 lo2(f32x4 v) { return __builtin_shufflevector(v, v, 0, 1); }
DEV f32x2 hi2(f32x4 v) { return __builtin_shufflevector(v, v, 2, 3); }
DEV f32x4 cat2(f32x2 a, f32x2 b) { return __builtin_shufflevector(a, b, 0, 1, 2, 3); }
DEV void wait_flags(GLB u64* word, bool need) {
  for (u32 spin = 0;; ++spin) {
    const u64 v = __hip_atomic_load(word, RLX_AGENT);
    if (__all(!need || v == FLAG_SET) || spin > SPIN_CAP) break;
    __builtin_amdgcn_s_sleep(POLL_SLEEP);
  }
}
DEV int tile_block(int lt, int idx) {
  const int xcd = lt >> 2, a = lt & 3;
  int j;
  if (idx < 5) { const int e = 5 * a + idx; j = e < 18 ? e : e + 12; }
  else j = 18 + 3 * a + (idx - 5);
  return (j << 3) | xcd;
}

struct P {
  const u16 *x, *la, *aim, *creA, *cimA, *ldtA, *dskA;
  const u16 *lre, *lim, *bre, *bim, *cre, *cim, *ldtB, *dskB, *gw;
  char* ws;
  float* out;
};

constexpr int G_NB = 4, G_STAGE = 32768;
constexpr int G_E = 153536;
constexpr int G_PF = G_E + 4096;
constexpr int G_L = G_PF + 4096;
constexpr int G_X = G_L + 1024;
constexpr int G_GO = G_X + 1024;
constexpr int G_T = G_NB * G_STAGE;
static_assert(G_GO + 4 <= 163840 && G_T + 4 * 32 * 144 <= G_E, "role G scratch");

DEV void g_loader(const P& p, LDS char* smem, int c, int g, int d0, int lw, int lane) {
  [[maybe_unused]] const int w = lw + 8;
  __builtin_amdgcn_s_setprio(3);
  GLB u32* ep = (GLB u32*)(p.ws + WS_EP + g * 256);
  GLB u64* gran = (GLB u64*)(p.ws + WS_GRAN) + (size_t)g * 16 * 128;
  const u32 tag = __hip_atomic_load(ep, RLX_AGENT) + 1u;
  u32 xo[4], bo[4];
#pragma unroll
  for (int i = 0; i < 4; ++i) {
    const int rho = 8 * (4 * lw + i) + (lane >> 3);
    const int hf = rho >> 6, m = (rho >> 4) & 3, r = rho & 15;
    const int tau = 64 * hf + 16 * (r >> 2) + 8 * (r & 1) + 2 * m + ((r >> 1) & 1);
    const int ch = (lane & 7) ^ (rho & 7);
    xo[i] = (u32)(128 * c + tau) * 1152u + ch * 16;
    const int pp = 64 * g + 16 * (rho >> 5) + r;
    bo[i] = (u32)pp * 1152u + ch * 16;
  }
  auto issue = [&](int s) {
    LDS char* buf = smem + (s & (G_NB - 1)) * G_STAGE + lw * 4096;
    int kb = g + s; kb = (kb >= 9 ? kb - 9 : kb) * 128;
#pragma unroll
    for (int i = 0; i < 4; ++i) dma16<0>((const char*)p.x + (xo[i] + kb), buf + i * 1024);
#pragma unroll
    for (int i = 0; i < 4; ++i) {
      const char* base = (const char*)((((4 * lw + i) >> 1) & 1) ? p.bim : p.bre);
      dma16<0>(base + (bo[i] + kb), buf + 16384 + i * 1024);
    }
  };
#pragma unroll
  for (int s = 0; s < G_NB - 1; ++s) issue(s);
#pragma unroll 1
  for (int s = 0; s < 6; ++s) { wait_vm<16>(); LSTAMP(0, 16 + s); bar(); issue(s + G_NB - 1); LSTAMP(0, 32 + s); }
  wait_vm<16>(); bar();
  wait_vm<8>(); bar();
  wait_vm<0>(); bar();
  STAMP(0, 1);
  if (lw < 2) {
    bar();
    u32 co[6];
#pragma unroll
    for (int i = 0; i < 6; ++i) {
      const int row = 8 * (6 * lw + i) + (lane >> 3), cp = lane & 7;
      co[i] = (u32)(d0 + row) * 1152u + ((cp ^ (row & 7)) << 4);
    }
    const int rot = ((d0 / 96) * 18) >> 3;
#pragma unroll 1
    for (int s = 0; s < 8; ++s) {
      LDS char* buf = smem + 32768 + s * 12288 + 6 * lw * 1024;
      const int ks = s + rot;
      const char* cb = ks < 9 ? (const char*)p.cre + ks * 128 : (const char*)p.cim + (ks - 9) * 128;
#pragma unroll
      for (int i = 0; i < 6; ++i) dma16<0>(cb + co[i], buf + i * 1024);
    }
    bar();
    return;
  }
  bar();
  STAMP(0, 2);
  if (lw == 3) {
    const f32x2 lv = *(LDS const f32x2*)(smem + G_L + lane * 8);
    const cf l16 = {lv[0], lv[1]};
    f32x2 ev[8];
#pragma unroll
    for (int s = 0; s < 8; ++s) ev[s] = *(LDS const f32x2*)(smem + G_E + (s * 64 + lane) * 8);
    cf tot = {0.f, 0.f};
    cf pre[8];
#pragma unroll
    for (int s = 0; s < 8; ++s) { pre[s] = tot; tot = cfma(l16, tot, cf{ev[s][0], ev[s][1]}); }
    if (c < 15) {
      __hip_atomic_store(gran + c * 128 + lane, ((u64)tag << 32) | __float_as_uint(tot.r), RLX_AGENT);
      __hip_atomic_store(gran + c * 128 + 64 + lane, ((u64)tag << 32) | __float_as_uint(tot.i), RLX_AGENT);
    }
#pragma unroll
    for (int s = 0; s < 8; ++s) *(LDS f32x2*)(smem + G_PF + (s * 64 + lane) * 8) = f32x2{pre[s].r, pre[s].i};
  }
  const int par = lw - 2;
  const f32x2 lv = *(LDS const f32x2*)(smem + G_L + 512 + lane * 8);
  const cf l128 = {lv[0], lv[1]};
  const cf l256 = cmul(l128, l128);
  cf acc = {0.f, 0.f};
  if (par < c) {
    const int nc = (c - par + 1) >> 1;
    u64 a[8], b[8];
    __builtin_amdgcn_s_sleep(GPOLL_DELAY);
    for (u32 spin = 0;; ++spin) {
      bool ok = true;
#pragma unroll
      for (int i = 0; i < 8; ++i) {
        const int cc = par + 2 * (i < nc ? i : 0);
        a[i] = __hip_atomic_load(gran + cc * 128 + lane, RLX_AGENT);
        b[i] = __hip_atomic_load(gran + cc * 128 + 64 + lane, RLX_AGENT);
      }
#pragma unroll
      for (int i = 0; i < 8; ++i) ok = ok && (u32)(a[i] >> 32) == tag && (u32)(b[i] >> 32) == tag;
      if (__all(ok) || spin > SPIN_CAP) break;
      __builtin_amdgcn_s_sleep(GPOLL_SLEEP);
    }
#pragma unroll
    for (int i = 0; i < 8; ++i)
      if (i < nc) acc = cfma(l256, acc, cf{__uint_as_float((u32)a[i]), __uint_as_float((u32)b[i])});
    if (c - 1 - (par + 2 * (nc - 1))) acc = cmul(acc, l128);
  }
  *(LDS f32x2*)(smem + G_X + (par * 64 + lane) * 8) = f32x2{acc.r, acc.i};
  STAMP(0, 3);
  bar();
  if (c == 15 && lw == 2 && lane == 0) __hip_atomic_store(ep, tag, RLX_AGENT);
}

template <int HF>
DEV void g_compute(const P& p, LDS char* smem, int c, int g, int w, int lane, u32& pf) {
  const int pq = w & 3, col = lane & 15, q = lane >> 4;
  const int pl = 16 * pq + col, pp = 64 * g + pl;
  cf lam, coef2;
  {
    const float lre = bf2f(p.lre[pp]), lim = bf2f(p.lim[pp]);
    const float dt = rbf(expf(bf2f(p.ldtB[pp])));
    const float zr = dt * lre, zi = dt * lim;
    const float er = expf(zr);
    float sn, cs;
    sincosf(zi, &sn, &cs);
    lam = {er * cs, er * sn};
    const float nr = lam.r - 1.0f, ni = lam.i;
    const float den = 1.0f / (lre * lre + lim * lim);
    coef2 = {2.0f * (nr * lre + ni * lim) * den, 2.0f * (ni * lre - nr * lim) * den};
  }
  cf l8 = cmul(lam, lam); l8 = cmul(l8, l8); l8 = cmul(l8, l8);
  const cf l16 = cmul(l8, l8);
  const cf l32 = cmul(l16, l16), l64 = cmul(l32, l32);
  if (HF == 0 && q == 0) {
    const cf l128 = cmul(l64, l64);
    *(LDS f32x2*)(smem + G_L + pl * 8) = f32x2{l16.r, l16.i};
    *(LDS f32x2*)(smem + G_L + 512 + pl * 8) = f32x2{l128.r, l128.i};
  }
  if (HF == 0 && w == 0 && lane == 0) *(LDS u32*)(smem + G_GO) = 0;
  cf pw = HF ? l64 : cf{1.f, 0.f};
  { const cf t = cmul(pw, l16); if (q & 1) pw = t; }
  { const cf t = cmul(pw, l32); if (q & 2) pw = t; }
  asm volatile("" : "+v"(lam.r), "+v"(lam.i), "+v"(coef2.r), "+v"(coef2.i), "+v"(l8.r), "+v"(l8.i), "+v"(pw.r), "+v"(pw.i));
  f32x4 are[4], aim[4];
#pragma unroll
  for (int m = 0; m < 4; ++m) { are[m] = f32x4{0, 0, 0, 0}; aim[m] = f32x4{0, 0, 0, 0}; }
  const int o0 = col * 128 + (((0 + q) ^ (col & 7)) << 4);
  const int o1 = col * 128 + (((4 + q) ^ (col & 7)) << 4);
  bf16x8 br[2], bi[2], a[2][4];
  auto read_frags = [&](int s) {
    LDS const char* buf = smem + (s & (G_NB - 1)) * G_STAGE;
    LDS const char* ab = buf + HF * 8192;
    LDS const char* bb = buf + 16384 + pq * 4096;
#pragma unroll
    for (int ks = 0; ks < 2; ++ks) {
      const int o = ks ? o1 : o0;
      br[ks] = lds_frag(bb + o); bi[ks] = lds_frag(bb + 2048 + o);
#pragma unroll
      for (int m = 0; m < 4; ++m) a[ks][m] = lds_frag(ab + m * 2048 + o);
    }
  };
  auto mma = [&]() {
#pragma unroll
    for (int ks = 0; ks < 2; ++ks)
#pragma unroll
      for (int m = 0; m < 4; ++m) {
        are[m] = mfma16(a[ks][m], br[ks], are[m]);
        aim[m] = mfma16(a[ks][m], bi[ks], aim[m]);
      }
  };
  STAMP(0, 1);
#pragma unroll
  for (int s = 0; s < 9; ++s) {
    if (HF == 1 && s == XT_WAIT) {
      wait_vm<0>();
      if (lane < 16) __hip_atomic_store((GLB u64*)(p.ws + WS_FL) + c * DM + 64 * g + 16 * pq + lane, FLAG_SET, RLX_AGENT);
    }
    bar();
    if (s == 0) { STAMP(0, 2); CSTAMP(0, 60); }
    if (HF == 1 && s > 0) { mma(); __builtin_amdgcn_sched_barrier(0); }
    if (HF == 1 && s == 0) {
      const int rr = 4 * q + (col >> 2);
      LDS const char* tb = smem + rr * 128 + (((2 * pq + ((col & 3) >> 1)) ^ (rr & 7)) << 4) + (col & 1) * 8;
      LDS char* sc = smem + G_T + pq * (32 * 144);
#pragma unroll
      for (int hf = 0; hf < 2; ++hf) {
        u32x2 t[4];
#pragma unroll
        for (int m = 0; m < 4; ++m) t[m] = __builtin_bit_cast(u32x2, tr_read(tb + hf * 8192 + m * 2048));
        u32x4 oa, ob;
#pragma unroll
        for (int m = 0; m < 4; ++m) {
          oa[m] = __builtin_amdgcn_perm(t[m][1], t[m][0], 0x05040100u);
          ob[m] = __builtin_amdgcn_perm(t[m][1], t[m][0], 0x07060302u);
        }
        *(LDS u32x4*)(sc + hf * (16 * 144) + col * 144 + q * 32) = oa;
        *(LDS u32x4*)(sc + hf * (16 * 144) + col * 144 + q * 32 + 16) = ob;
      }
      wave_fence();
      const __amdgpu_buffer_rsrc_t rs = mk_rsrc(p.ws + WS_XT, DM * SL * 2);
#pragma unroll
      for (int u = 0; u < 4; ++u) {
        const int row = (lane >> 3) + 8 * (u & 1), hf = u >> 1;
        const u32x4 v = *(LDS const u32x4*)(sc + hf * (16 * 144) + row * 144 + (lane & 7) * 16);
        __builtin_amdgcn_raw_buffer_store_b128(v, rs, ((64 * g + 16 * pq + row) * SL + 128 * c + 64 * hf) * 2 + (lane & 7) * 16, 0, SC1);
      }
      __builtin_amdgcn_sched_barrier(0);
    }
    read_frags(s);
    __builtin_amdgcn_sched_barrier(0);
    if (HF == 0) { mma(); __builtin_amdgcn_sched_barrier(0); }
  }
  if (HF == 1) mma();
  __builtin_amdgcn_sched_barrier(0);
  STAMP(0, 3); CSTAMP(0, 61);
  const f32x2 Lr = {lam.r, lam.r}, Li = {lam.i, lam.i};
  f32x2 er = {0.f, 0.f}, ei = {0.f, 0.f};
#pragma unroll
  for (int t = 0; t < 8; ++t) {
    const f32x2 br_ = (t & 1) ? hi2(are[t >> 1]) : lo2(are[t >> 1]);
    const f32x2 bi_ = (t & 1) ? hi2(aim[t >> 1]) : lo2(aim[t >> 1]);
    const f32x2 nr = fma2(-Li, ei, fma2(Lr, er, br_));
    ei = fma2(Li, er, fma2(Lr, ei, bi_));
    er = nr;
  }
  const cf eA = {er[0], ei[0]};
  const cf e = cfma(l8, eA, cf{er[1], ei[1]});
  const int seg = 4 * HF + q;
  *(LDS f32x2*)(smem + G_E + (seg * 64 + pl) * 8) = f32x2{e.r, e.i};
  STAMP(0, 4);
  bar();
  STAMP(0, 5);
  {
#pragma unroll
    for (int m = 0; m < 4; ++m) asm volatile("" : "+v"(are[m]), "+v"(aim[m]));
    const f32x2 Cr = {coef2.r, coef2.r}, Ci = {coef2.i, coef2.i};
#pragma unroll
    for (int m = 0; m < 4; ++m) {
      const f32x2 r0 = lo2(are[m]), r1 = hi2(are[m]), i0 = lo2(aim[m]), i1 = hi2(aim[m]);
      are[m] = cat2(fma2(-Ci, i0, Cr * r0), fma2(-Ci, i1, Cr * r1));
      aim[m] = cat2(fma2(-Ci, r0, -Cr * i0), fma2(-Ci, r1, -Cr * i1));
      asm volatile("" : "+v"(are[m]), "+v"(aim[m]));
    }
  }
  cf eAc = cmul(coef2, eA), pwc = cmul(coef2, pw);
  asm volatile("" : "+v"(eAc.r), "+v"(eAc.i), "+v"(pwc.r), "+v"(pwc.i));
  STAMP(0, 7);
  bar();
  STAMP(0, 8);
  cf cin;
  {
    f32x2 v[2];
#pragma unroll
    for (int i = 0; i < 2; ++i) v[i] = *(LDS const f32x2*)(smem + G_X + (i * 64 + pl) * 8);
    const f32x2 pf = *(LDS const f32x2*)(smem + G_PF + (seg * 64 + pl) * 8);
    const f32x2 S = v[0] + v[1];
    cin = cfma(pwc, cf{S[0], S[1]}, cmul(coef2, cf{pf[0], pf[1]}));
  }
  const cf cinB = cfma(l8, cin, eAc);
  er = f32x2{cin.r, cinB.r}; ei = f32x2{-cin.i, -cinB.i};
  f32x2 hr[8], hi[8];
#pragma unroll
  for (int t = 0; t < 8; ++t) {
    const f32x2 br_ = (t & 1) ? hi2(are[t >> 1]) : lo2(are[t >> 1]);
    const f32x2 bi_ = (t & 1) ? hi2(aim[t >> 1]) : lo2(aim[t >> 1]);
    const f32x2 nr = fma2(Li, ei, fma2(Lr, er, br_));
    ei = fma2(-Li, er, fma2(Lr, ei, bi_));
    er = nr;
    hr[t] = er; hi[t] = ei;
  }
  u32x4 o[4];
#pragma unroll
  for (int t = 0; t < 4; ++t) {
    o[0][t] = pack_bf16(hr[2 * t][0], hr[2 * t + 1][0]);
    o[1][t] = pack_bf16(hr[2 * t][1], hr[2 * t + 1][1]);
    o[2][t] = pack_bf16(hi[2 * t][0], hi[2 * t + 1][0]);
    o[3][t] = pack_bf16(hi[2 * t][1], hi[2 * t + 1][1]);
  }
  STAMP(0, 9);
  {
    const __amdgpu_buffer_rsrc_t rs = mk_rsrc(p.ws + WS_HB, 2 * DM * SL * 2);
    const int hb = (((c * 2 + HF) * 2) * 9 + g) * 8192 + pq * 2048;
    const int p0 = (16 * q + col) * 16, p1 = 1024 + (16 * q + (col ^ 4)) * 16;
    __builtin_amdgcn_raw_buffer_store_b128(o[0], rs, hb + p0, 0, SC1);
    __builtin_amdgcn_raw_buffer_store_b128(o[1], rs, hb + p1, 0, SC1);
    __builtin_amdgcn_raw_buffer_store_b128(o[2], rs, hb + 9 * 8192 + p0, 0, SC1);
    __builtin_amdgcn_raw_buffer_store_b128(o[3], rs, hb + 9 * 8192 + p1, 0, SC1);
    wait_vm<0>();
    STAMP(0, 10);
    if (lane < 8)
      __hip_atomic_store((GLB u64*)(p.ws + WS_IN) + tile_block(2 * c + HF, lane) * 64 + IN_H + g * 4 + pq, FLAG_SET, RLX_AGENT);
    bar();
  }
}

constexpr int S_CPY = 4160;
constexpr int S_X = 0;
constexpr int S_ST = 6 * 4 * S_CPY;
constexpr int S_Z = S_ST;
constexpr int S_ZSZ = (256 + SL) * 2;
constexpr int S_T = S_ST + 12 * 4608;
constexpr int S_TSZ = 1312;
constexpr int S_END = S_T + 6 * S_TSZ;
constexpr int S_CNT = 32768 + 8192;

DEV c2 cexp_k(float zl, float th, float tl, float k) {
  const float mag = __builtin_amdgcn_exp2f(zl * k);
  const float t = fmaf(tl, k, __builtin_amdgcn_fractf(th * k));
  return c2{mag * __builtin_amdgcn_cosf(t), mag * __builtin_amdgcn_sinf(t)};
}
#define PKM(d, a, b) "v_pk_mul_f32 " d ", " a ", " b " op_sel:[0,0] op_sel_hi:[0,1]\n\t"
#define PKF(d, a, b) "v_pk_fma_f32 " d ", " a ", " b ", " d " op_sel:[1,1,0] op_sel_hi:[1,0,1] neg_lo:[1,0,0]\n\t"
DEV void octet(LDS char* dst, c2 s0, c2 m1, c2 m2) {
  c2 v1, v2, v3, t0, t1, t2, t3;
  asm(PKM("%0", "%7", "%8") PKM("%3", "%7", "%9") PKF("%0", "%7", "%8") PKF("%3", "%7", "%9")
      PKM("%1", "%0", "%8") PKM("%4", "%0", "%9") PKF("%1", "%0", "%8") PKF("%4", "%0", "%9")
      PKM("%2", "%1", "%8") PKM("%5", "%1", "%9") PKF("%2", "%1", "%8") PKF("%5", "%1", "%9")
      PKM("%6", "%2", "%9") PKF("%6", "%2", "%9")
      : "=&v"(v1), "=&v"(v2), "=&v"(v3), "=&v"(t0), "=&v"(t1), "=&v"(t2), "=&v"(t3) : "v"(s0), "v"(m1), "v"(m2));
  *(LDS u32*)(dst + 0 * 288) = pack_bf16(s0[0], s0[1]);
  *(LDS u32*)(dst + 1 * 288) = pack_bf16(v1[0], v1[1]);
  *(LDS u32*)(dst + 2 * 288) = pack_bf16(v2[0], v2[1]);
  *(LDS u32*)(dst + 3 * 288) = pack_bf16(v3[0], v3[1]);
  *(LDS u32*)(dst + 4 * 288) = pack_bf16(t0[0], t0[1]);
  *(LDS u32*)(dst + 5 * 288) = pack_bf16(t1[0], t1[1]);
  *(LDS u32*)(dst + 6 * 288) = pack_bf16(t2[0], t2[1]);
  *(LDS u32*)(dst + 7 * 288) = pack_bf16(t3[0], t3[1]);
}
DEV u32 dup_lo(u32 a) { return __builtin_amdgcn_perm(a, a, 0x01000100u); }
DEV u32 dup_hi(u32 a) { return __builtin_amdgcn_perm(a, a, 0x03020302u); }
DEV u32 hi_lo(u32 a, u32 b) { return __builtin_amdgcn_alignbit(b, a, 16); }

DEV void role_s(const P& p, LDS char* smem, int sg, int w, int lane, u64 g_tb) {
  const int tid = w * 64 + lane;
  const int ch = w >> 1, hh = w & 1, d = 6 * sg + ch;
  const int n = d * 128 + 64 * hh + lane;
  const u16 r_ldt = p.ldtA[d], r_la = p.la[n], r_aim = p.aim[n], r_cre = p.creA[n], r_cim = p.cimA[n];
  u32 wb[2] = {0, 0};
  __builtin_amdgcn_sched_barrier(0);
#pragma unroll
  for (int i = 0; i < 2; ++i) {
    const int ln = i * 768 + tid, all = (sg % 12) * 864 + ln;
    if (ln < 864) wb[i] = *(const GLB u32*)((const char*)(all < 5184 ? p.bre : p.bim) + (size_t)(all < 5184 ? all : all - 5184) * 128);
  }
  __builtin_amdgcn_sched_barrier(0);
  STAMP(0, 1);
  f32x4 tab[3];
  [[maybe_unused]] u32 wc[2] = {0, 0};
  LDS char* st = smem + S_ST + w * 4608;
  {
    const float L2E = 1.44269504088896341f;
    const float dt = rbf(__builtin_amdgcn_exp2f(bf2f(r_ldt) * L2E));
    const float ar = -rbf(__builtin_amdgcn_exp2f(bf2f(r_la) * L2E));
    const float zl = ar * dt * L2E, zi = bf2f(r_aim) * dt;
    const float th = zi * 0.15915494309189535f;
    const float tl = fmaf(zi, 0.15915494309189535f, -th) + zi * 4.6017384e-10f;
    const c2 C = {2.0f * (bf2f(r_cre) * dt), 2.0f * (bf2f(r_cim) * dt)};
    const c2 w1 = cexp_k(zl, th, tl, 1.f), w8 = cexp_k(zl, th, tl, 8.f), w128 = cexp_k(zl, th, tl, 128.f);
    c2 w2, w4, w16, w32, w64, w256, w512, w1024, w2048;
    asm(PKM("%0", "%9", "%9") PKM("%2", "%10", "%10") PKM("%5", "%11", "%11") PKF("%0", "%9", "%9") PKF("%2", "%10", "%10") PKF("%5", "%11", "%11")
        PKM("%1", "%0", "%0") PKM("%3", "%2", "%2") PKM("%6", "%5", "%5") PKF("%1", "%0", "%0") PKF("%3", "%2", "%2") PKF("%6", "%5", "%5")
        PKM("%4", "%3", "%3") PKM("%7", "%6", "%6") PKF("%4", "%3", "%3") PKF("%7", "%6", "%6")
        PKM("%8", "%7", "%7") PKF("%8", "%7", "%7")
        : "=&v"(w2), "=&v"(w4), "=&v"(w16), "=&v"(w32), "=&v"(w64), "=&v"(w256), "=&v"(w512), "=&v"(w1024), "=&v"(w2048)
        : "v"(w1), "v"(w8), "v"(w128));
    c2 C128, C256, C512, C1024, C2048;
    asm(PKM("%0", "%5", "%6") PKM("%1", "%5", "%7") PKM("%2", "%5", "%8") PKM("%3", "%5", "%9") PKM("%4", "%5", "%10")
        PKF("%0", "%5", "%6") PKF("%1", "%5", "%7") PKF("%2", "%5", "%8") PKF("%3", "%5", "%9") PKF("%4", "%5", "%10")
        : "=&v"(C128), "=&v"(C256), "=&v"(C512), "=&v"(C1024), "=&v"(C2048)
        : "v"(C), "v"(w128), "v"(w256), "v"(w512), "v"(w1024), "v"(w2048));
#pragma unroll
    for (int i = 0; i < 2; ++i) {
      const int ln = i * 768 + tid, all = (sg % 12) * 864 + ln;
      if (ln < 864) wc[i] = *(const GLB u32*)((const char*)(all < 5184 ? p.cre : p.cim) + (size_t)(all < 5184 ? all : all - 5184) * 128);
    }
    STAMP(0, 2);
    HOLD(11);
    LDS char* wr = st + 4 * lane;
    LDS const char* rd = st + (lane & 15) * 288 + (lane >> 4) * 16;
    bf16x8 B[4];
    const c2 w8c = {w8[0], -w8[1]}, w32c = {w32[0], -w32[1]}, w64c = {w64[0], -w64[1]};
    octet(wr, c2{1.f, 0.f}, w8c, w32c);
    octet(wr + 8 * 288, w64c, w8c, w32c);
    wave_fence();
#pragma unroll
    for (int ks = 0; ks < 4; ++ks) B[ks] = lds_frag(rd + 64 * ks);
    wave_fence();
#pragma unroll
    for (int mt = 0; mt < 3; ++mt) {
      if (mt == 0) { octet(wr, C, w1, w4); octet(wr + 8 * 288, C128, w1, w4); }
      if (mt == 1) { octet(wr, C256, w2, w128); octet(wr + 8 * 288, C512, w128, w4); }
      if (mt == 2) { octet(wr, C1024, w128, w512); *(LDS u32*)(wr + 8 * 288) = pack_bf16(C2048[0], C2048[1]); }
      wave_fence();
      f32x4 acc = {0, 0, 0, 0};
      bf16x8 A[4];
#pragma unroll
      for (int ks = 0; ks < 4; ++ks) A[ks] = lds_frag(rd + 64 * ks);
      __builtin_amdgcn_sched_barrier(0);
#pragma unroll
      for (int ks = 0; ks < 4; ++ks) acc = mfma16(A[ks], B[ks], acc);
      __builtin_amdgcn_sched_barrier(0);
      wave_fence();
      tab[mt] = acc;
    }
  }
  const int b = lane & 15, q = lane >> 4;
#pragma unroll
  for (int mt = 0; mt < 3; ++mt) *(LDS f32x4*)(st + mt * 1024 + lane * 16) = tab[mt];
  STAMP(0, 3);
  bar();
  {
    LDS const char* ot = smem + S_ST + (w ^ 1) * 4608 + lane * 16;
    LDS char* T = smem + S_T + ch * S_TSZ;
    if (hh == 0) {
      const f32x4 v0 = tab[0] + *(LDS const f32x4*)(ot), v2 = tab[2] + *(LDS const f32x4*)(ot + 2048);
      *(LDS u32x2*)(T + 2 * (128 * (q >> 1) + 8 * b + 4 * (q & 1))) = u32x2{pack_bf16(v0[0], v0[1]), pack_bf16(v0[2], v0[3])};
      const u32 a0 = pack_bf16(v2[0], v2[1]), a1 = pack_bf16(v2[2], v2[3]);
      if (q < 2) {
        LDS char* t3 = T + 2 * (512 + 64 * q + b);
        *(LDS u16*)(t3) = (u16)a0; *(LDS u16*)(t3 + 32) = (u16)(a0 >> 16);
        *(LDS u16*)(t3 + 64) = (u16)a1; *(LDS u16*)(t3 + 96) = (u16)(a1 >> 16);
      } else if (lane == 32) *(LDS u16*)(T + 2 * 640) = (u16)a0;
    } else {
      const f32x4 v1 = tab[1] + *(LDS const f32x4*)(ot + 1024);
      const u32 a0 = pack_bf16(v1[0], v1[1]), a1 = pack_bf16(v1[2], v1[3]);
      if (q < 2) *(LDS u32x2*)(T + 2 * (256 + 64 * q + 4 * b)) = u32x2{a0, a1};
      else {
        LDS char* t2 = T + 2 * (384 + 2 * b + (q - 2));
        *(LDS u16*)(t2) = (u16)a0; *(LDS u16*)(t2 + 64) = (u16)(a0 >> 16);
        *(LDS u16*)(t2 + 128) = (u16)a1; *(LDS u16*)(t2 + 192) = (u16)(a1 >> 16);
      }
    }
  }
  bar();
  STAMP(0, 4);
  HOLD(12);
  u32x4 uv[2];
  {
    GLB u64* fl = (GLB u64*)(p.ws + WS_FL) + (8 * hh + (lane & 7)) * DM + d;
    asm volatile("" :: "v"(wc[0]), "v"(wc[1]), "v"(wb[0]), "v"(wb[1]));
    wait_flags(fl, true);
    const __amdgpu_buffer_rsrc_t rs = mk_rsrc(p.ws + WS_XT, DM * SL * 2);
#pragma unroll
    for (int v = 0; v < 2; ++v) uv[v] = __builtin_amdgcn_raw_buffer_load_b128(rs, (d * SL + 1024 * hh + 8 * (lane + 64 * v)) * 2, 0, SC1);
    if (lane < 8) __hip_atomic_store(fl, 0ull, RLX_AGENT);
  }
  STAMP(0, 5);
  {
    LDS const char* T = smem + S_T + ch * S_TSZ;
    u32 o[10];
    const u32 e512 = *(LDS const u16*)(T + 2 * 512);
    o[8] = o[9] = e512 | (e512 << 16);
    if (hh == 0) {
      const int off = lane < 16 ? 32 * lane : (lane < 32 ? 256 + 16 * lane : 512 + 8 * lane);
      u32x2 W[4];
#pragma unroll
      for (int i = 0; i < 4; ++i) W[i] = *(LDS const u32x2*)(T + off + 8 * i);
      if (lane < 16) {
#pragma unroll
        for (int i = 0; i < 8; ++i) o[i] = W[i >> 1][i & 1];
      } else if (lane < 32) {
#pragma unroll
        for (int i = 0; i < 4; ++i) {
          const u32 a = W[i >> 1][i & 1], nx = W[(i + 1) >> 1][(i + 1) & 1];
          o[2 * i] = dup_lo(a);
          o[2 * i + 1] = hi_lo(a, nx);
        }
      } else {
        const u32 w0 = W[0][0], w1 = W[0][1], w2 = W[1][0];
        o[0] = dup_lo(w0); o[1] = w0; o[2] = dup_hi(w0); o[3] = dup_lo(w1);
        o[4] = o[3]; o[5] = w1; o[6] = dup_hi(w1); o[7] = dup_lo(w2);
      }
    } else {
      const u32 w0 = *(LDS const u32*)(T + 1024 + 4 * lane), w1 = *(LDS const u32*)(T + 1028 + 4 * lane);
      o[0] = o[1] = dup_lo(w0); o[2] = w0; o[3] = o[4] = o[5] = dup_hi(w0); o[6] = o[7] = dup_lo(w1);
    }
    o[8] = (u32)__builtin_amdgcn_update_dpp((int)o[8], (int)o[0], 0x130, 0xf, 0xf, false);
    o[9] = (u32)__builtin_amdgcn_update_dpp((int)o[9], (int)o[1], 0x130, 0xf, 0xf, false);
    u32 a[9];
#pragma unroll
    for (int i = 0; i < 9; ++i) a[i] = hi_lo(o[i], o[i + 1]);
    LDS char* xb = smem + S_X + ch * 4 * S_CPY + 32 + 2 * (1024 * hh + 16 * lane);
    *(LDS u32x4*)(xb) = u32x4{o[0], o[1], o[2], o[3]};
    *(LDS u32x4*)(xb + 16) = u32x4{o[4], o[5], o[6], o[7]};
    *(LDS u32x4*)(xb + S_CPY) = u32x4{a[0], a[1], a[2], a[3]};
    *(LDS u32x4*)(xb + S_CPY + 16) = u32x4{a[4], a[5], a[6], a[7]};
    *(LDS u32x4*)(xb + 2 * S_CPY) = u32x4{o[1], o[2], o[3], o[4]};
    *(LDS u32x4*)(xb + 2 * S_CPY + 16) = u32x4{o[5], o[6], o[7], o[8]};
    *(LDS u32x4*)(xb + 3 * S_CPY) = u32x4{a[1], a[2], a[3], a[4]};
    *(LDS u32x4*)(xb + 3 * S_CPY + 16) = u32x4{a[5], a[6], a[7], a[8]};
    if (hh == 0 && lane == 0) {
      LDS char* hb = smem + S_X + ch * 4 * S_CPY;
      const u32x4 z = {0, 0, 0, 0};
      *(LDS u32x4*)(hb) = z;
      *(LDS u32x4*)(hb + 16) = z;
      *(LDS u32x4*)(hb + S_CPY) = z;
      *(LDS u32x4*)(hb + S_CPY + 16) = u32x4{0, 0, 0, o[0] << 16};
      *(LDS u32x4*)(hb + 2 * S_CPY) = z;
      *(LDS u32x4*)(hb + 2 * S_CPY + 16) = u32x4{0, 0, 0, o[0]};
      *(LDS u32x4*)(hb + 3 * S_CPY) = z;
      *(LDS u32x4*)(hb + 3 * S_CPY + 16) = u32x4{0, 0, o[0] << 16, a[0]};
    }
    if (tid < 6 * 32) *(LDS u32x4*)(smem + S_Z + (tid >> 5) * S_ZSZ + (tid & 31) * 16) = u32x4{0, 0, 0, 0};
    STAMP(0, 6);
    LDS char* z = smem + S_Z + ch * S_ZSZ + 512 + (1024 * hh + 8 * lane) * 2;
    *(LDS u32x4*)z = uv[0];
    *(LDS u32x4*)(z + 1024) = uv[1];
  }
  bar();
  STAMP(0, 7);
  HOLD(13);
  f32x4 acc[8];
#pragma unroll
  for (int i = 0; i < 8; ++i) acc[i] = f32x4{0, 0, 0, 0};
  {
    const int kq = q, qq = b >> 2, pp = b & 3;
    LDS const char* fb = smem + S_X + (ch * 4 + ((4 - qq) & 3)) * S_CPY +
                         2 * (16 + 16 * (kq >> 1) - 8 * (kq & 1) + 4 * pp - (qq ? 4 : 0)) + 64 * hh;
    LDS const char* gb = smem + S_Z + ch * S_ZSZ + 512 + 32 * (b - (kq >> 1)) + 16 * (kq & 1) - 64 * hh;
#pragma unroll 1
    for (int it = 0; it < 4; ++it) {
      bf16x8 F[8], G[8];
#pragma unroll
      for (int s = 0; s < 8; ++s) {
        F[s] = tr_frag(fb + 512 * s, fb + 512 * s - 8);
        G[s] = lds_frag(gb + 512 * s);
      }
      __builtin_amdgcn_sched_barrier(0);
#pragma unroll
      for (int t = 0; t < 8; ++t)
#pragma unroll
        for (int s = 0; s <= t; ++s) acc[t] = mfma16(F[s], G[t - s], acc[t]);
      fb += 128;
      gb -= 128;
    }
  }
  __builtin_amdgcn_sched_barrier(0);
  STAMP(0, 8);
  HOLD(14);
  bar();
  {
    LDS char* ex = smem + S_X + 49152 + w * 4096 + lane * 16;
#pragma unroll
    for (int i = 0; i < 4; ++i) {
      const f32x4 lo = acc[i], hi = acc[4 + i];
      *(LDS f32x4*)(ex + i * 1024) = hh ? +lo : +hi;
    }
    if (tid == 0) *(LDS u32*)(smem + S_CNT) = 0;
  }
  bar();
  u32x2 yo[4];
  {
    LDS const char* ey = smem + S_X + 49152 + (w ^ 1) * 4096 + lane * 16;
#pragma unroll
    for (int i = 0; i < 4; ++i) {
      const f32x4 lo = acc[i], hi = acc[4 + i];
      const f32x4 y = (hh ? +hi : +lo) + *(LDS const f32x4*)(ey + i * 1024);
      yo[i] = u32x2{pack_bf16(y[0], y[1]), pack_bf16(y[2], y[3])};
    }
  }
  bar();
  {
    const __amdgpu_buffer_rsrc_t rs = mk_rsrc(p.ws + WS_AT, DM * SL * 2);
    const int dst = (d * SL + 1024 * hh + 16 * (lane & 15) + 4 * (lane >> 4)) * 2;
#pragma unroll
    for (int i = 0; i < 4; ++i) __builtin_amdgcn_raw_buffer_store_b64(yo[i], rs, dst + 512 * i, 0, SC1);
  }
  STAMP(0, 9);
}
DEV void s_signal(const P& p, LDS char* smem, int sg, int lane) {
  u32 old = 0;
  if (lane == 0) old = __hip_atomic_fetch_add((LDS u32*)(smem + S_CNT), 1u, __ATOMIC_RELAXED, __HIP_MEMORY_SCOPE_WORKGROUP);
  if (__builtin_amdgcn_readfirstlane(old) == 11) {
    const int lt = lane & 31, second = lane >> 5;
    const int dd = 6 * sg + (second ? 5 : 0);
    const int idx = dd < 480 ? dd / 96 : 5 + (dd - 480) / 32;
    const int dfirst = idx < 5 ? 96 * idx : 480 + 32 * (idx - 5);
    __hip_atomic_store((GLB u64*)(p.ws + WS_IN) + tile_block(lt, idx) * 64 + IN_A + (sg - dfirst / 6), FLAG_SET, RLX_AGENT);
  }
}

DEV void role_gate(const P& p, int rb, int w, int lane, u64 pc) {
  u32 pf0 = 0, pf1 = 0;
  const int tid = w * 64 + lane;
  const int sub = lane & 7;
  u32x4 wv[9];
#pragma unroll
  for (int i = 0; i < 9; ++i) wv[i] = *(const u32x4*)((const char*)p.gw + (i * 8 + sub) * 16);
  for (int r = w * 8 + (lane >> 3); r < 128; r += 96) {
    const int l = rb * 128 + r;
    float s = 0.f;
    u32x4 xv[9];
#pragma unroll
    for (int i = 0; i < 9; ++i) xv[i] = *(const u32x4*)((const char*)p.x + (size_t)l * 1152 + (i * 8 + sub) * 16);
#pragma unroll
    for (int i = 0; i < 9; ++i) {
#pragma unroll
      for (int k = 0; k < 4; ++k) {
        const u32 pr = pack_bf16(bflo(xv[i][k]) * bflo(wv[i][k]), bfhi(xv[i][k]) * bfhi(wv[i][k]));
        s += bflo(pr);
        s += bfhi(pr);
      }
    }
    s += __shfl_xor(s, 1); s += __shfl_xor(s, 2); s += __shfl_xor(s, 4);
    const float gv = rbf(1.0f / (1.0f + expf(-rbf(s))));
    if (sub == 0)
      __hip_atomic_store((GLB u64*)(p.ws + WS_G) + l, ((u64)__float_as_uint(rbf(1.0f - gv)) << 32) | __float_as_uint(gv), RLX_AGENT);
  }
  asm volatile("" :: "v"(pf0), "v"(pf1));
  wait_vm<0>();
  bar();
  if (w == 0 && lane < 16)
    __hip_atomic_store((GLB u64*)(p.ws + WS_IN) + tile_block(2 * rb + (lane >> 3), lane & 7) * 64 + IN_G, FLAG_SET, RLX_AGENT);
}

constexpr int H_C = 32768, H_CS = 12288;
DEV int h_slot(int i) { return i < 4 ? i * 8192 : 131072 + (i - 4) * 8192; }
constexpr int H_AT = H_C + 2 * H_CS;
constexpr int H_XT = H_C + 3 * H_CS;
constexpr int H_G = H_C + 4 * H_CS;
constexpr int H_OUT = H_C + 5 * H_CS;
constexpr int K_LDS = 163840;

constexpr int h_wait(int D, int NDT) {
  const int G = 4 + 2 * NDT;
  switch (D) {
    case 0: return 4;
    case 1: return 8;
    case 2: return 4 + G;
    case 3: case 4: return 2 * G;
    case 5: case 6: return 2 * G + 1;
    case 7: return G + NDT;
    default: return NDT;
  }
}
template <int D, int NDT, class F> DEV void h_steps(F&& after, u64& fv, char* wsp) {
  if constexpr (D == 7) asm volatile("s_waitcnt vmcnt(%1)" : "+v"(fv) : "n"(h_wait(D, NDT)) : "memory");
  else wait_vm<h_wait(D, NDT)>();
  LSTAMP2(1, 16 + D);
  bar();
  after(std::integral_constant<int, D>{});
  if constexpr (D < 8) h_steps<D + 1, NDT>(after, fv, wsp);
}
DEV int p2_rot(int d0) { const int idx = d0 < 480 ? d0 / 96 : 5 + (d0 - 480) / 32; return (idx * 18) >> 3; }

template <int NDT, class F>
DEV void p2_loader(const P& p, LDS char* smem, int lt, int d0, int lw, int lane, bool scratch_barrier, F&& pre_poll) {
  __builtin_amdgcn_s_setprio(3);
  const char* hb = p.ws + WS_HB + (size_t)lt * 18 * 8192 + (2 * lw) * 1024 + lane * 16;
  constexpr int W = 32 * NDT;
  const int l0 = 64 * lt, rot = p2_rot(d0);
  u32 co[NDT];
#pragma unroll
  for (int i = 0; i < NDT; ++i) {
    const int row = 8 * (NDT * lw + i) + (lane >> 3), cp = lane & 7;
    co[i] = (u32)(d0 + row) * 1152u + ((cp ^ (row & 7)) << 4);
  }
  auto issue_c = [&](int s) {
    LDS char* buf = smem + H_C + (s & 7) * H_CS + NDT * lw * 1024;
    int ks = s + rot; ks = ks >= 18 ? ks - 18 : ks;
    const char* cb = ks < 9 ? (const char*)p.cre + ks * 128 : (const char*)p.cim + (ks - 9) * 128;
#pragma unroll
    for (int i = 0; i < NDT; ++i) dma16<0>(cb + co[i], buf + i * 1024);
  };
  auto issue_h = [&](int s) {
    LDS char* buf = smem + h_slot(s & 7) + 2 * lw * 1024;
    int ks = s + rot; ks = ks >= 18 ? ks - 18 : ks;
#pragma unroll
    for (int i = 0; i < 2; ++i) dma16<SC1>(hb + ks * 8192 + i * 1024, buf + i * 1024);
  };
  GLB u64* inb = (GLB u64*)(p.ws + WS_IN) + blockIdx.x * 64;
  const int nA = (d0 + W - 1) / 6 - d0 / 6 + 1;
  const bool need = lane == IN_G || (lane >= IN_A && lane < IN_A + nA);
  pre_poll();
  if (!scratch_barrier) {
#pragma unroll 1
    for (int s = 0; s < 8; ++s) issue_c(s);
  }
  { [[maybe_unused]] const int w = lw + 8; STAMP(1, 1); }
  if (scratch_barrier) bar();
  wait_flags(inb + IN_H + (lane < 36 ? lane : 0), true);
  { [[maybe_unused]] const int w = lw + 8; STAMP(1, 2); }
#pragma unroll
  for (int s = 0; s < 4; ++s) issue_h(s);
  { [[maybe_unused]] const int w = lw + 8; STAMP(1, 3); }
  u64 fv = 0;
  bool late = false;
  auto issue_at = [&]() {
#pragma unroll
    for (int i = 0; i < NDT; ++i) {
      const int pc = NDT * lw + i;
      dma16<SC1>(p.ws + WS_AT + (size_t)(d0 + 8 * pc + (lane >> 3)) * 4096 + l0 * 2 + (lane & 7) * 16, smem + H_AT + pc * 1024);
    }
  };
  auto issue_g = [&]() { if (lane < 8) dma16<SC1>(p.ws + WS_G + (size_t)l0 * 8 + lw * 128 + lane * 16, smem + H_G + lw * 128); };
  h_steps<0, NDT>([&](auto D_) {
    constexpr int D = decltype(D_)::value;
    if constexpr (D == 0) { issue_h(4); issue_h(5); issue_h(6); issue_h(7); }
    if constexpr (D >= 1 && D <= 5) { issue_h(2 * D + 6); issue_h(2 * D + 7); issue_c(2 * D + 6); issue_c(2 * D + 7); }
    if constexpr (D == 4)
      asm volatile("global_load_dwordx2 %0, %1, off sc1" : "=v"(fv) : "v"(inb + lane) : "memory");
    if constexpr (D == 6) {
#pragma unroll
      for (int i = 0; i < NDT; ++i) {
        const int pc = NDT * lw + i, id = pc * 64 + lane, row = id / (4 * NDT), qq = id - row * (4 * NDT);
        dma16<0>((const char*)p.x + (size_t)(l0 + row) * 1152 + d0 * 2 + qq * 16, smem + H_XT + pc * 1024);
      }
    }
    if constexpr (D == 7) {
      late = !__all(!need || fv == FLAG_SET);
      if (!late) { issue_g(); issue_at(); }
    }
  }, fv, p.ws);
  if (late) { wait_flags(inb + lane, need); issue_g(); issue_at(); }
  wait_vm<0>();
  bar();
}

template <int NDT>
DEV void p2_compute(const P& p, LDS char* smem, int lt0, int d0, int w, int lane, const u32x2 (&dsk)[6]) {
  const int lh = (w >> 1) & 1, kh = w & 1, dg = w >> 2;
  const int r = lane & 15, kq = lane >> 4;
  const int lt = 2 * lh + kh, l0 = 64 * lt0;
  constexpr int W = 32 * NDT;
  u32x2 da[NDT], db[NDT];
#pragma unroll
  for (int t = 0; t < NDT; ++t) {
    const int d = d0 + 16 * (NDT * dg + t) + 4 * kq;
    da[t] = *(const u32x2*)(p.dskA + d);
    db[t] = *(const u32x2*)(p.dskB + d);
  }
  GLB u64* inb = (GLB u64*)(p.ws + WS_IN) + blockIdx.x * 64;

  f32x4 acc[2][NDT];
#pragma unroll
  for (int i = 0; i < 2; ++i)
#pragma unroll
    for (int t = 0; t < NDT; ++t) acc[i][t] = f32x4{0, 0, 0, 0};
  int ho[2][2];
#pragma unroll
  for (int u = 0; u < 2; ++u) {
    const int k = 32 * kh + 8 * kq + 4 * u + (r >> 2), i = (r & 3) >> 1;
    const int o = (2 * (k >> 4) + i) * 1024 + (32 * lh + ((k & 15) ^ (4 * i))) * 16 + (r & 1) * 8;
    ho[0][u] = o + 256 * kh;
    ho[1][u] = o + 256 * (kh ^ 1);
  }
  const int co = H_C + (16 * NDT * dg + r) * 128 + (((4 * kh + kq) ^ (r & 7)) << 4);
  bf16x8 hb[2][2][2], ca[2][2][NDT];
  auto rd = [&](auto SET_, int D) {
    constexpr int SET = decltype(SET_)::value;
#pragma unroll
    for (int u = 0; u < 2; ++u) {
      const int s = 2 * D + u;
      LDS const char* hbuf = smem + h_slot(s & 7);
#pragma unroll
      for (int i = 0; i < 2; ++i) hb[SET][u][i] = tr_frag(hbuf + ho[i][0], hbuf + ho[i][1]);
      LDS const char* cbuf = smem + (s & 7) * H_CS;
#pragma unroll
      for (int t = 0; t < NDT; ++t) ca[SET][u][t] = lds_frag(cbuf + co + t * 2048);
    }
    __builtin_amdgcn_sched_barrier(0);
  };
  auto mma = [&](auto SET_) {
    constexpr int SET = decltype(SET_)::value;
#pragma unroll
    for (int u = 0; u < 2; ++u)
#pragma unroll
      for (int t = 0; t < NDT; ++t) {
        acc[0][t] = mfma16(ca[SET][u][t], hb[SET][u][0], acc[0][t]);
        acc[1][t] = mfma16(ca[SET][u][t], hb[SET][u][1], acc[1][t]);
      }
    __builtin_amdgcn_sched_barrier(0);
  };
  int orow[NDT], opc[NDT];
#pragma unroll
  for (int it = 0; it < NDT; ++it) {
    const int id = it * 512 + w * 64 + lane;
    orow[it] = id / (8 * NDT); opc[it] = id - orow[it] * (8 * NDT);
    asm volatile("" : "+v"(orow[it]), "+v"(opc[it]));
  }
  using S0 = std::integral_constant<int, 0>;
  using S1 = std::integral_constant<int, 1>;
  STAMP(1, 1);
  bar();
  STAMP(1, 2);
  rd(S0{}, 0);
#pragma unroll 1
  for (int D = 1; D < 9; D += 2) {
    bar(); rd(S1{}, D); mma(S0{});
    bar(); rd(S0{}, D + 1); mma(S1{});
  }
  mma(S0{});
  __builtin_amdgcn_sched_barrier(0);
  STAMP(1, 3);
  bar();
  if (w == 0) __hip_atomic_store(inb + lane, 0ull, RLX_AGENT);
  LDS char* ex = smem + w * (NDT * 1024) + lane * 16;
#pragma unroll
  for (int t = 0; t < NDT; ++t) *(LDS f32x4*)(ex + t * 1024) = acc[1][t];
  bar();
  LDS const char* ey = smem + (w ^ 1) * (NDT * 1024) + lane * 16;
  constexpr int OSTR = W * 4 + 16;
  const f32x2 gg = *(LDS const f32x2*)(smem + H_G + (16 * lt + r) * 8);
  const f32x2 g2 = {gg[0], gg[0]}, om2 = {gg[1], gg[1]};
  auto up = [](u32 v) { return f32x2{bflo(v), bfhi(v)}; };
  auto rb2 = [&](f32x2 v) { return up(pack_bf16(v[0], v[1])); };
#pragma unroll
  for (int t = 0; t < NDT; ++t) {
    const int dt = NDT * dg + t;
    const f32x4 y = acc[0][t] + *(LDS const f32x4*)(ey + t * 1024);
    const u32x2 av = __builtin_bit_cast(u32x2, tr_read(smem + H_AT + (16 * dt + 4 * kq + (r >> 2)) * 128 + lt * 32 + (r & 3) * 8));
    const u32x2 xv = *(LDS const u32x2*)(smem + H_XT + (16 * lt + r) * (W * 2) + (16 * dt + 4 * kq) * 2);
    f32x2 res[2];
#pragma unroll
    for (int h = 0; h < 2; ++h) {
      const f32x2 xf = up(xv[h]);
      const f32x2 a = rb2(up(av[h]) + rb2(xf * up(da[t][h])));
      const f32x2 b = (h ? hi2(y) : lo2(y)) + rb2(xf * up(db[t][h]));
      res[h] = fma2(om2, b, rb2(g2 * a));
    }
    *(LDS f32x4*)(smem + H_OUT + (16 * lt + r) * OSTR + (16 * dt + 4 * kq) * 4) = cat2(res[0], res[1]);
  }
  STAMP(1, 4);
  bar();
  const __amdgpu_buffer_rsrc_t rso = mk_rsrc(p.out, SL * DM * 4);
#pragma unroll
  for (int it = 0; it < NDT; ++it) {
    const f32x4 v = *(LDS const f32x4*)(smem + H_OUT + orow[it] * OSTR + opc[it] * 16);
    __builtin_amdgcn_raw_buffer_store_b128(__builtin_bit_cast(u32x4, v), rso, ((l0 + orow[it]) * DM + d0 + opc[it] * 4) * 4, 0, OUT_AUX);
  }
}

template <int NDT>
DEV void phase2(const P& p, LDS char* smem, int lt, int d0, int w, int lane, bool scratch_barrier, int sg, const u32x2 (&dsk)[6]) {
  STAMP(1, 0);
  if (w >= 8) {
    p2_loader<NDT>(p, smem, lt, d0, w - 8, lane, scratch_barrier, [&]() {
      if (NDT == 1) { wait_vm<0>(); s_signal(p, smem, sg, lane); }
    });
  } else {
    if (NDT == 1) { wait_vm<0>(); s_signal(p, smem, sg, lane); }
    p2_compute<NDT>(p, smem, lt, d0, w, lane, dsk);
  }
  STAMP(1, 15);
}

__global__ __launch_bounds__(768, 3) void gated_mix_s4d_s5(P p) {
  extern __shared__ __attribute__((aligned(16))) char smem_raw[];
  LDS char* smem = (LDS char*)smem_raw;
  u64 pc;
  asm volatile("s_getpc_b64 %0" : "=s"(pc));
  u32 pf = 0;
  const u64 g_tb = __builtin_amdgcn_s_memrealtime();
#define PF_TOP 312
  if (threadIdx.x < PF_TOP && blockIdx.x >= 144)
    asm volatile("global_load_dword %0, %1, off" : "=v"(pf) : "v"((pc & ~255ull) + threadIdx.x * 128));
  const int w = __builtin_amdgcn_readfirstlane(threadIdx.x >> 6), lane = threadIdx.x & 63;
  const int xcd = blockIdx.x & 7, j = blockIdx.x >> 3;
  u32x2 dsk[6];
#pragma unroll
  for (int i = 0; i < 6; ++i) dsk[i] = u32x2{0, 0};
  STAMP(0, 0);
  if (j >= 18 && j < 30) {
    const int sg = 12 * xcd + (j - 18);
    role_s(p, smem, sg, w, lane, g_tb);
    STAMP(0, 15);
    phase2<1>(p, smem, 4 * xcd + (j - 18) / 3, 480 + 32 * ((j - 18) % 3), w, lane, false, sg, dsk);
  } else {
    const bool isg = j < 18;
    if (isg) {
      const int c = 2 * xcd + j / 9, g = j % 9;
      if (w >= 8) g_loader(p, smem, c, g, 96 * (j % 5), w - 8, lane);
      else if (w < 4) g_compute<0>(p, smem, c, g, w, lane, pf);
      else g_compute<1>(p, smem, c, g, w, lane, pf);
    } else {
      role_gate(p, 2 * xcd + (j - 30), w, lane, pc);
    }
    STAMP(0, 15);
    const int e = isg ? j : j - 12;
    phase2<3>(p, smem, 4 * xcd + e / 5, 96 * (e % 5), w, lane, isg, 0, dsk);
  }
  asm volatile("s_waitcnt vmcnt(0)" :: "v"(pf));
}

extern "C" void kernel_launch(void* const* d_in, const int* in_sizes, int n_in, void* d_out, int out_size,
                              void* d_ws, size_t ws_size, hipStream_t stream) {
  P a;
  a.x = (const u16*)d_in[0]; a.la = (const u16*)d_in[1]; a.aim = (const u16*)d_in[2];
  a.creA = (const u16*)d_in[3]; a.cimA = (const u16*)d_in[4]; a.ldtA = (const u16*)d_in[5]; a.dskA = (const u16*)d_in[6];
  a.lre = (const u16*)d_in[8]; a.lim = (const u16*)d_in[9]; a.bre = (const u16*)d_in[10]; a.bim = (const u16*)d_in[11];
  a.cre = (const u16*)d_in[12]; a.cim = (const u16*)d_in[13]; a.ldtB = (const u16*)d_in[14];
  a.dskB = (const u16*)d_in[15]; a.gw = (const u16*)d_in[16];
  a.ws = (char*)d_ws + WS_OFF; a.out = (float*)d_out;
  (void)hipFuncSetAttribute((const void*)gated_mix_s4d_s5, hipFuncAttributeMaxDynamicSharedMemorySize, K_LDS);
  gated_mix_s4d_s5<<<256, 768, K_LDS, stream>>>(a);
}
```

```cpp
#define SIG_FIRST 1
#define PF_NOT_G 1
#include <hip/hip_runtime.h>
#include <hip/hip_bf16.h>
#include <stdint.h>
#include <type_traits>

typedef unsigned short u16;
typedef unsigned int u32;
typedef unsigned long long u64;
typedef __attribute__((ext_vector_type(8))) __bf16 bf16x8;
typedef __attribute__((ext_vector_type(2))) __bf16 bf16x2;
typedef __attribute__((ext_vector_type(4))) short s16x4;
typedef __attribute__((ext_vector_type(4))) float f32x4;
typedef __attribute__((ext_vector_type(2))) float f32x2;
typedef __attribute__((ext_vector_type(4))) unsigned u32x4;
typedef __attribute__((ext_vector_type(2))) unsigned u32x2;
#define LDS __attribute__((address_space(3)))
#define GLB __attribute__((address_space(1)))
#define DEV __device__ __forceinline__
#define RLX_AGENT __ATOMIC_RELAXED, __HIP_MEMORY_SCOPE_AGENT

constexpr int SL = 2048;
constexpr int DM = 576;

constexpr size_t WS_HB = 0;
constexpr size_t WS_AT = WS_HB + (size_t)2 * DM * SL * 2;
constexpr size_t WS_G = WS_AT + (size_t)DM * SL * 2;
constexpr size_t WS_GRAN = WS_G + (size_t)SL * 8;
constexpr size_t WS_EP = WS_GRAN + (size_t)9 * 16 * 2 * 64 * 8;
constexpr size_t WS_FL = WS_EP + 4096;
constexpr size_t WS_XT = WS_FL + (size_t)16 * DM * 8;
constexpr size_t WS_IN = WS_XT + (size_t)DM * SL * 2;
constexpr u64 FLAG_SET = 0x9E3779B97F4A7C15ull;
constexpr u32 SPIN_CAP = 1u << 17;
constexpr int IN_H = 0, IN_G = 36, IN_A = 40;

constexpr size_t WS_ST = (size_t)16 << 20;
#define STAMP(kern, idx) do {} while (0)
#define CSTAMP(kern, idx) do {} while (0)
#define LSTAMP(kern, idx) do {} while (0)
#define LSTAMP2(kern, idx) do {} while (0)

DEV float bf2f(u16 v) { return __uint_as_float((u32)v << 16); }
DEV float bflo(u32 v) { return __uint_as_float(v << 16); }
DEV float bfhi(u32 v) { return __uint_as_float(v & 0xffff0000u); }
DEV u32 pack_bf16(float lo, float hi) {
  f32x2 v = {lo, hi};
  bf16x2 b = __builtin_convertvector(v, bf16x2);
  return __builtin_bit_cast(u32, b);
}
DEV float rbf(float x) { return bflo(pack_bf16(x, x)); }
template <int N> DEV void wait_vm() { asm volatile("s_waitcnt vmcnt(%0)" ::"n"(N) : "memory"); }
DEV void bar() { asm volatile("s_waitcnt lgkmcnt(0)\n\ts_barrier" ::: "memory"); }
DEV void wave_fence() { __builtin_amdgcn_fence(__ATOMIC_ACQ_REL, "wavefront"); }
template <int AUX> DEV void dma16(const void* src, LDS char* dst) {
  __builtin_amdgcn_global_load_lds((const GLB u32*)src, (LDS u32*)dst, 16, 0, AUX);
}
constexpr int SC1 = 16;
#define GPOLL_SLEEP 1
#define GPOLL_DELAY 24
#define POLL_SLEEP 1
#define OUT_AUX 16
#define WS_OFF 0
#define XT_WAIT 4
DEV bf16x8 lds_frag(LDS const char* p) { return *(LDS const bf16x8*)p; }
DEV s16x4 tr_read(LDS const char* a) { return __builtin_amdgcn_ds_read_tr16_b64_v4i16((LDS s16x4*)a); }
DEV bf16x8 tr_frag(LDS const char* a, LDS const char* b) {
  const s16x4 lo = tr_read(a), hi = tr_read(b);
  return __builtin_bit_cast(bf16x8, __builtin_shufflevector(lo, hi, 0, 1, 2, 3, 4, 5, 6, 7));
}
DEV f32x4 mfma16(bf16x8 a, bf16x8 b, f32x4 c) { return __builtin_amdgcn_mfma_f32_16x16x32_bf16(a, b, c, 0, 0, 0); }
DEV __amdgpu_buffer_rsrc_t mk_rsrc(const void* base, int bytes) {
  return __builtin_amdgcn_make_buffer_rsrc(const_cast<void*>(base), (short)0, bytes, 0x00020000);
}
struct cf { float r, i; };
DEV cf cmul(cf a, cf b) { return {a.r * b.r - a.i * b.i, a.r * b.i + a.i * b.r}; }
DEV cf cfma(cf a, cf b, cf c) { return {fmaf(a.r, b.r, fmaf(-a.i, b.i, c.r)), fmaf(a.r, b.i, fmaf(a.i, b.r, c.i))}; }
typedef f32x2 c2;
DEV c2 cm2(c2 a, c2 b) {
  c2 t, r;
  asm("v_pk_mul_f32 %0, %1, %2 op_sel:[0,0] op_sel_hi:[0,1]" : "=v"(t) : "v"(a), "v"(b));
  asm("v_pk_fma_f32 %0, %1, %2, %3 op_sel:[1,1,0] op_sel_hi:[1,0,1] neg_lo:[1,0,0]" : "=v"(r) : "v"(a), "v"(b), "v"(t));
  return r;
}
DEV f32x2 fma2(f32x2 a, f32x2 b, f32x2 c) { return __builtin_elementwise_fma(a, b, c); }
DEV f32x2 lo2(f32x4 v) { return __builtin_shufflevector(v, v, 0, 1); }
DEV f32x2 hi2(f32x4 v) { return __builtin_shufflevector(v, v, 2, 3); }
DEV f32x4 cat2(f32x2 a, f32x2 b) { return __builtin_shufflevector(a, b, 0, 1, 2, 3); }
DEV void wait_flags(GLB u64* word, bool need) {
  for (u32 spin = 0;; ++spin) {
    const u64 v = __hip_atomic_load(word, RLX_AGENT);
    if (__all(!need || v == FLAG_SET) || spin > SPIN_CAP) break;
    __builtin_amdgcn_s_sleep(POLL_SLEEP);
  }
}
DEV int tile_block(int lt, int idx) {
  const int xcd = lt >> 2, a = lt & 3;
  int j;
  if (idx < 5) { const int e = 5 * a + idx; j = e < 18 ? e : e + 12; }
  else j = 18 + 3 * a + (idx - 5);
  return (j << 3) | xcd;
}

struct P {
  const u16 *x, *la, *aim, *creA, *cimA, *ldtA, *dskA;
  const u16 *lre, *lim, *bre, *bim, *cre, *cim, *ldtB, *dskB, *gw;
  char* ws;
  float* out;
};

constexpr int G_NB = 4, G_STAGE = 32768;
constexpr int G_E = 153536;
constexpr int G_PF = G_E + 4096;
constexpr int G_L = G_PF + 4096;
constexpr int G_X = G_L + 1024;
constexpr int G_GO = G_X + 1024;
constexpr int G_T = G_NB * G_STAGE;
static_assert(G_GO + 4 <= 163840 && G_T + 4 * 32 * 144 <= G_E, "role G scratch");

DEV void g_loader(const P& p, LDS char* smem, int c, int g, int d0, int lw, int lane) {
  [[maybe_unused]] const int w = lw + 8;
  __builtin_amdgcn_s_setprio(3);
  GLB u32* ep = (GLB u32*)(p.ws + WS_EP + g * 256);
  GLB u64* gran = (GLB u64*)(p.ws + WS_GRAN) + (size_t)g * 16 * 128;
  const u32 tag = __hip_atomic_load(ep, RLX_AGENT) + 1u;
  u32 xo[4], bo[4];
#pragma unroll
  for (int i = 0; i < 4; ++i) {
    const int rho = 8 * (4 * lw + i) + (lane >> 3);
    const int hf = rho >> 6, m = (rho >> 4) & 3, r = rho & 15;
    const int tau = 64 * hf + 16 * (r >> 2) + 8 * (r & 1) + 2 * m + ((r >> 1) & 1);
    const int ch = (lane & 7) ^ (rho & 7);
    xo[i] = (u32)(128 * c + tau) * 1152u + ch * 16;
    const int pp = 64 * g + 16 * (rho >> 5) + r;
    bo[i] = (u32)pp * 1152u + ch * 16;
  }
  auto issue = [&](int s) {
    LDS char* buf = smem + (s & (G_NB - 1)) * G_STAGE + lw * 4096;
    int kb = g + s; kb = (kb >= 9 ? kb - 9 : kb) * 128;
#pragma unroll
    for (int i = 0; i < 4; ++i) dma16<0>((const char*)p.x + (xo[i] + kb), buf + i * 1024);
#pragma unroll
    for (int i = 0; i < 4; ++i) {
      const char* base = (const char*)((((4 * lw + i) >> 1) & 1) ? p.bim : p.bre);
      dma16<0>(base + (bo[i] + kb), buf + 16384 + i * 1024);
    }
  };
#pragma unroll
  for (int s = 0; s < G_NB - 1; ++s) issue(s);
#pragma unroll 1
  for (int s = 0; s < 6; ++s) { wait_vm<16>(); LSTAMP(0, 16 + s); bar(); issue(s + G_NB - 1); LSTAMP(0, 32 + s); }
  wait_vm<16>(); bar();
  wait_vm<8>(); bar();
  wait_vm<0>(); bar();
  STAMP(0, 1);
  if (lw < 2) {
    bar();
    u32 co[6];
#pragma unroll
    for (int i = 0; i < 6; ++i) {
      const int row = 8 * (6 * lw + i) + (lane >> 3), cp = lane & 7;
      co[i] = (u32)(d0 + row) * 1152u + ((cp ^ (row & 7)) << 4);
    }
    const int rot = ((d0 / 96) * 18) >> 3;
#pragma unroll 1
    for (int s = 0; s < 8; ++s) {
      LDS char* buf = smem + 32768 + s * 12288 + 6 * lw * 1024;
      const int ks = s + rot;
      const char* cb = ks < 9 ? (const char*)p.cre + ks * 128 : (const char*)p.cim + (ks - 9) * 128;
#pragma unroll
      for (int i = 0; i < 6; ++i) dma16<0>(cb + co[i], buf + i * 1024);
    }
    bar();
    return;
  }
  bar();
  STAMP(0, 2);
  if (lw == 3) {
    const f32x2 lv = *(LDS const f32x2*)(smem + G_L + lane * 8);
    const cf l16 = {lv[0], lv[1]};
    f32x2 ev[8];
#pragma unroll
    for (int s = 0; s < 8; ++s) ev[s] = *(LDS const f32x2*)(smem + G_E + (s * 64 + lane) * 8);
    cf tot = {0.f, 0.f};
    cf pre[8];
#pragma unroll
    for (int s = 0; s < 8; ++s) { pre[s] = tot; tot = cfma(l16, tot, cf{ev[s][0], ev[s][1]}); }
    if (c < 15) {
      __hip_atomic_store(gran + c * 128 + lane, ((u64)tag << 32) | __float_as_uint(tot.r), RLX_AGENT);
      __hip_atomic_store(gran + c * 128 + 64 + lane, ((u64)tag << 32) | __float_as_uint(tot.i), RLX_AGENT);
    }
#pragma unroll
    for (int s = 0; s < 8; ++s) *(LDS f32x2*)(smem + G_PF + (s * 64 + lane) * 8) = f32x2{pre[s].r, pre[s].i};
  }
  const int par = lw - 2;
  const f32x2 lv = *(LDS const f32x2*)(smem + G_L + 512 + lane * 8);
  const cf l128 = {lv[0], lv[1]};
  const cf l256 = cmul(l128, l128);
  cf acc = {0.f, 0.f};
  if (par < c) {
    const int nc = (c - par + 1) >> 1;
    u64 a[8], b[8];
    __builtin_amdgcn_s_sleep(GPOLL_DELAY);
    for (u32 spin = 0;; ++spin) {
      bool ok = true;
#pragma unroll
      for (int i = 0; i < 8; ++i) {
        const int cc = par + 2 * (i < nc ? i : 0);
        a[i] = __hip_atomic_load(gran + cc * 128 + lane, RLX_AGENT);
        b[i] = __hip_atomic_load(gran + cc * 128 + 64 + lane, RLX_AGENT);
      }
#pragma unroll
      for (int i = 0; i < 8; ++i) ok = ok && (u32)(a[i] >> 32) == tag && (u32)(b[i] >> 32) == tag;
      if (__all(ok) || spin > SPIN_CAP) break;
      __builtin_amdgcn_s_sleep(GPOLL_SLEEP);
    }
#pragma unroll
    for (int i = 0; i < 8; ++i)
      if (i < nc) acc = cfma(l256, acc, cf{__uint_as_float((u32)a[i]), __uint_as_float((u32)b[i])});
    if (c - 1 - (par + 2 * (nc - 1))) acc = cmul(acc, l128);
  }
  *(LDS f32x2*)(smem + G_X + (par * 64 + lane) * 8) = f32x2{acc.r, acc.i};
  STAMP(0, 3);
  bar();
  if (c == 15 && lw == 2 && lane == 0) __hip_atomic_store(ep, tag, RLX_AGENT);
}

template <int HF>
DEV void g_compute(const P& p, LDS char* smem, int c, int g, int w, int lane, u32& pf) {
  const int pq = w & 3, col = lane & 15, q = lane >> 4;
  const int pl = 16 * pq + col, pp = 64 * g + pl;
  cf lam, coef2;
  {
    const float lre = bf2f(p.lre[pp]), lim = bf2f(p.lim[pp]);
    const float dt = rbf(expf(bf2f(p.ldtB[pp])));
    const float zr = dt * lre, zi = dt * lim;
    const float er = expf(zr);
    float sn, cs;
    sincosf(zi, &sn, &cs);
    lam = {er * cs, er * sn};
    const float nr = lam.r - 1.0f, ni = lam.i;
    const float den = 1.0f / (lre * lre + lim * lim);
    coef2 = {2.0f * (nr * lre + ni * lim) * den, 2.0f * (ni * lre - nr * lim) * den};
  }
  cf l8 = cmul(lam, lam); l8 = cmul(l8, l8); l8 = cmul(l8, l8);
  const cf l16 = cmul(l8, l8);
  const cf l32 = cmul(l16, l16), l64 = cmul(l32, l32);
  if (HF == 0 && q == 0) {
    const cf l128 = cmul(l64, l64);
    *(LDS f32x2*)(smem + G_L + pl * 8) = f32x2{l16.r, l16.i};
    *(LDS f32x2*)(smem + G_L + 512 + pl * 8) = f32x2{l128.r, l128.i};
  }
  if (HF == 0 && w == 0 && lane == 0) *(LDS u32*)(smem + G_GO) = 0;
  cf pw = HF ? l64 : cf{1.f, 0.f};
  { const cf t = cmul(pw, l16); if (q & 1) pw = t; }
  { const cf t = cmul(pw, l32); if (q & 2) pw = t; }
  asm volatile("" : "+v"(lam.r), "+v"(lam.i), "+v"(coef2.r), "+v"(coef2.i), "+v"(l8.r), "+v"(l8.i), "+v"(pw.r), "+v"(pw.i));
  f32x4 are[4], aim[4];
#pragma unroll
  for (int m = 0; m < 4; ++m) { are[m] = f32x4{0, 0, 0, 0}; aim[m] = f32x4{0, 0, 0, 0}; }
  const int o0 = col * 128 + (((0 + q) ^ (col & 7)) << 4);
  const int o1 = col * 128 + (((4 + q) ^ (col & 7)) << 4);
  bf16x8 br[2], bi[2], a[2][4];
  auto read_frags = [&](int s) {
    LDS const char* buf = smem + (s & (G_NB - 1)) * G_STAGE;
    LDS const char* ab = buf + HF * 8192;
    LDS const char* bb = buf + 16384 + pq * 4096;
#pragma unroll
    for (int ks = 0; ks < 2; ++ks) {
      const int o = ks ? o1 : o0;
      br[ks] = lds_frag(bb + o); bi[ks] = lds_frag(bb + 2048 + o);
#pragma unroll
      for (int m = 0; m < 4; ++m) a[ks][m] = lds_frag(ab + m * 2048 + o);
    }
  };
  auto mma = [&]() {
#pragma unroll
    for (int ks = 0; ks < 2; ++ks)
#pragma unroll
      for (int m = 0; m < 4; ++m) {
        are[m] = mfma16(a[ks][m], br[ks], are[m]);
        aim[m] = mfma16(a[ks][m], bi[ks], aim[m]);
      }
  };
  STAMP(0, 1);
#pragma unroll
  for (int s = 0; s < 9; ++s) {
    if (HF == 1 && s == XT_WAIT) {
      wait_vm<0>();
      if (lane < 16) __hip_atomic_store((GLB u64*)(p.ws + WS_FL) + c * DM + 64 * g + 16 * pq + lane, FLAG_SET, RLX_AGENT);
    }
    bar();
    if (s == 0) { STAMP(0, 2); CSTAMP(0, 60); }
    if (HF == 1 && s > 0) { mma(); __builtin_amdgcn_sched_barrier(0); }
    if (HF == 1 && s == 0) {
      const int rr = 4 * q + (col >> 2);
      LDS const char* tb = smem + rr * 128 + (((2 * pq + ((col & 3) >> 1)) ^ (rr & 7)) << 4) + (col & 1) * 8;
      LDS char* sc = smem + G_T + pq * (32 * 144);
#pragma unroll
      for (int hf = 0; hf < 2; ++hf) {
        u32x2 t[4];
#pragma unroll
        for (int m = 0; m < 4; ++m) t[m] = __builtin_bit_cast(u32x2, tr_read(tb + hf * 8192 + m * 2048));
        u32x4 oa, ob;
#pragma unroll
        for (int m = 0; m < 4; ++m) {
          oa[m] = __builtin_amdgcn_perm(t[m][1], t[m][0], 0x05040100u);
          ob[m] = __builtin_amdgcn_perm(t[m][1], t[m][0], 0x07060302u);
        }
        *(LDS u32x4*)(sc + hf * (16 * 144) + col * 144 + q * 32) = oa;
        *(LDS u32x4*)(sc + hf * (16 * 144) + col * 144 + q * 32 + 16) = ob;
      }
      wave_fence();
      const __amdgpu_buffer_rsrc_t rs = mk_rsrc(p.ws + WS_XT, DM * SL * 2);
#pragma unroll
      for (int u = 0; u < 4; ++u) {
        const int row = (lane >> 3) + 8 * (u & 1), hf = u >> 1;
        const u32x4 v = *(LDS const u32x4*)(sc + hf * (16 * 144) + row * 144 + (lane & 7) * 16);
        __builtin_amdgcn_raw_buffer_store_b128(v, rs, ((64 * g + 16 * pq + row) * SL + 128 * c + 64 * hf) * 2 + (lane & 7) * 16, 0, SC1);
      }
      __builtin_amdgcn_sched_barrier(0);
    }
    read_frags(s);
    __builtin_amdgcn_sched_barrier(0);
    if (HF == 0) { mma(); __builtin_amdgcn_sched_barrier(0); }
  }
  if (HF == 1) mma();
  __builtin_amdgcn_sched_barrier(0);
  STAMP(0, 3); CSTAMP(0, 61);
  const f32x2 Lr = {lam.r, lam.r}, Li = {lam.i, lam.i};
  f32x2 er = {0.f, 0.f}, ei = {0.f, 0.f};
#pragma unroll
  for (int t = 0; t < 8; ++t) {
    const f32x2 br_ = (t & 1) ? hi2(are[t >> 1]) : lo2(are[t >> 1]);
    const f32x2 bi_ = (t & 1) ? hi2(aim[t >> 1]) : lo2(aim[t >> 1]);
    const f32x2 nr = fma2(-Li, ei, fma2(Lr, er, br_));
    ei = fma2(Li, er, fma2(Lr, ei, bi_));
    er = nr;
  }
  const cf eA = {er[0], ei[0]};
  const cf e = cfma(l8, eA, cf{er[1], ei[1]});
  const int seg = 4 * HF + q;
  *(LDS f32x2*)(smem + G_E + (seg * 64 + pl) * 8) = f32x2{e.r, e.i};
  STAMP(0, 4);
  bar();
  STAMP(0, 5);
  {
#pragma unroll
    for (int m = 0; m < 4; ++m) asm volatile("" : "+v"(are[m]), "+v"(aim[m]));
    const f32x2 Cr = {coef2.r, coef2.r}, Ci = {coef2.i, coef2.i};
#pragma unroll
    for (int m = 0; m < 4; ++m) {
      const f32x2 r0 = lo2(are[m]), r1 = hi2(are[m]), i0 = lo2(aim[m]), i1 = hi2(aim[m]);
      are[m] = cat2(fma2(-Ci, i0, Cr * r0), fma2(-Ci, i1, Cr * r1));
      aim[m] = cat2(fma2(-Ci, r0, -Cr * i0), fma2(-Ci, r1, -Cr * i1));
      asm volatile("" : "+v"(are[m]), "+v"(aim[m]));
    }
  }
  cf eAc = cmul(coef2, eA), pwc = cmul(coef2, pw);
  asm volatile("" : "+v"(eAc.r), "+v"(eAc.i), "+v"(pwc.r), "+v"(pwc.i));
  STAMP(0, 7);
  bar();
  STAMP(0, 8);
  cf cin;
  {
    f32x2 v[2];
#pragma unroll
    for (int i = 0; i < 2; ++i) v[i] = *(LDS const f32x2*)(smem + G_X + (i * 64 + pl) * 8);
    const f32x2 pf = *(LDS const f32x2*)(smem + G_PF + (seg * 64 + pl) * 8);
    const f32x2 S = v[0] + v[1];
    cin = cfma(pwc, cf{S[0], S[1]}, cmul(coef2, cf{pf[0], pf[1]}));
  }
  const cf cinB = cfma(l8, cin, eAc);
  er = f32x2{cin.r, cinB.r}; ei = f32x2{-cin.i, -cinB.i};
  f32x2 hr[8], hi[8];
#pragma unroll
  for (int t = 0; t < 8; ++t) {
    const f32x2 br_ = (t & 1) ? hi2(are[t >> 1]) : lo2(are[t >> 1]);
    const f32x2 bi_ = (t & 1) ? hi2(aim[t >> 1]) : lo2(aim[t >> 1]);
    const f32x2 nr = fma2(Li, ei, fma2(Lr, er, br_));
    ei = fma2(-Li, er, fma2(Lr, ei, bi_));
    er = nr;
    hr[t] = er; hi[t] = ei;
  }
  u32x4 o[4];
#pragma unroll
  for (int t = 0; t < 4; ++t) {
    o[0][t] = pack_bf16(hr[2 * t][0], hr[2 * t + 1][0]);
    o[1][t] = pack_bf16(hr[2 * t][1], hr[2 * t + 1][1]);
    o[2][t] = pack_bf16(hi[2 * t][0], hi[2 * t + 1][0]);
    o[3][t] = pack_bf16(hi[2 * t][1], hi[2 * t + 1][1]);
  }
  STAMP(0, 9);
  {
    const __amdgpu_buffer_rsrc_t rs = mk_rsrc(p.ws + WS_HB, 2 * DM * SL * 2);
    const int hb = (((c * 2 + HF) * 2) * 9 + g) * 8192 + pq * 2048;
    const int p0 = (16 * q + col) * 16, p1 = 1024 + (16 * q + (col ^ 4)) * 16;
    __builtin_amdgcn_raw_buffer_store_b128(o[0], rs, hb + p0, 0, SC1);
    __builtin_amdgcn_raw_buffer_store_b128(o[1], rs, hb + p1, 0, SC1);
    __builtin_amdgcn_raw_buffer_store_b128(o[2], rs, hb + 9 * 8192 + p0, 0, SC1);
    __builtin_amdgcn_raw_buffer_store_b128(o[3], rs, hb + 9 * 8192 + p1, 0, SC1);
    wait_vm<0>();
    STAMP(0, 10);
    if (lane < 8)
      __hip_atomic_store((GLB u64*)(p.ws + WS_IN) + tile_block(2 * c + HF, lane) * 64 + IN_H + g * 4 + pq, FLAG_SET, RLX_AGENT);
    bar();
  }
}

constexpr int S_CPY = 4160;
constexpr int S_X = 0;
constexpr int S_ST = 6 * 4 * S_CPY;
constexpr int S_Z = S_ST;
constexpr int S_ZSZ = (256 + SL) * 2;
constexpr int S_T = S_ST + 12 * 4608;
constexpr int S_TSZ = 1312;
constexpr int S_END = S_T + 6 * S_TSZ;
constexpr int S_CNT = 32768 + 8192;

DEV c2 cexp_k(float zl, float th, float tl, float k) {
  const float mag = __builtin_amdgcn_exp2f(zl * k);
  const float t = fmaf(tl, k, __builtin_amdgcn_fractf(th * k));
  return c2{mag * __builtin_amdgcn_cosf(t), mag * __builtin_amdgcn_sinf(t)};
}
#define PKM(d, a, b) "v_pk_mul_f32 " d ", " a ", " b " op_sel:[0,0] op_sel_hi:[0,1]\n\t"
#define PKF(d, a, b) "v_pk_fma_f32 " d ", " a ", " b ", " d " op_sel:[1,1,0] op_sel_hi:[1,0,1] neg_lo:[1,0,0]\n\t"
DEV void octet(LDS char* dst, c2 s0, c2 m1, c2 m2) {
  c2 v1, v2, v3, t0, t1, t2, t3;
  asm(PKM("%0", "%7", "%8") PKM("%3", "%7", "%9") PKF("%0", "%7", "%8") PKF("%3", "%7", "%9")
      PKM("%1", "%0", "%8") PKM("%4", "%0", "%9") PKF("%1", "%0", "%8") PKF("%4", "%0", "%9")
      PKM("%2", "%1", "%8") PKM("%5", "%1", "%9") PKF("%2", "%1", "%8") PKF("%5", "%1", "%9")
      PKM("%6", "%2", "%9") PKF("%6", "%2", "%9")
      : "=&v"(v1), "=&v"(v2), "=&v"(v3), "=&v"(t0), "=&v"(t1), "=&v"(t2), "=&v"(t3) : "v"(s0), "v"(m1), "v"(m2));
  *(LDS u32*)(dst + 0 * 288) = pack_bf16(s0[0], s0[1]);
  *(LDS u32*)(dst + 1 * 288) = pack_bf16(v1[0], v1[1]);
  *(LDS u32*)(dst + 2 * 288) = pack_bf16(v2[0], v2[1]);
  *(LDS u32*)(dst + 3 * 288) = pack_bf16(v3[0], v3[1]);
  *(LDS u32*)(dst + 4 * 288) = pack_bf16(t0[0], t0[1]);
  *(LDS u32*)(dst + 5 * 288) = pack_bf16(t1[0], t1[1]);
  *(LDS u32*)(dst + 6 * 288) = pack_bf16(t2[0], t2[1]);
  *(LDS u32*)(dst + 7 * 288) = pack_bf16(t3[0], t3[1]);
}
DEV u32 dup_lo(u32 a) { return __builtin_amdgcn_perm(a, a, 0x01000100u); }
DEV u32 dup_hi(u32 a) { return __builtin_amdgcn_perm(a, a, 0x03020302u); }
DEV u32 hi_lo(u32 a, u32 b) { return __builtin_amdgcn_alignbit(b, a, 16); }

DEV void role_s(const P& p, LDS char* smem, int sg, int w, int lane) {
  const int tid = w * 64 + lane;
  const int ch = w >> 1, hh = w & 1, d = 6 * sg + ch;
  const int n = d * 128 + 64 * hh + lane;
  const u16 r_ldt = p.ldtA[d], r_la = p.la[n], r_aim = p.aim[n], r_cre = p.creA[n], r_cim = p.cimA[n];
  u32 wb[2] = {0, 0};
  __builtin_amdgcn_sched_barrier(0);
#pragma unroll
  for (int i = 0; i < 2; ++i) {
    const int ln = i * 768 + tid, all = (sg % 12) * 864 + ln;
    if (ln < 864) wb[i] = *(const GLB u32*)((const char*)(all < 5184 ? p.bre : p.bim) + (size_t)(all < 5184 ? all : all - 5184) * 128);
  }
  __builtin_amdgcn_sched_barrier(0);
  STAMP(0, 1);
  f32x4 tab[3];
  [[maybe_unused]] u32 wc[2] = {0, 0};
  LDS char* st = smem + S_ST + w * 4608;
  {
    const float L2E = 1.44269504088896341f;
    const float dt = rbf(__builtin_amdgcn_exp2f(bf2f(r_ldt) * L2E));
    const float ar = -rbf(__builtin_amdgcn_exp2f(bf2f(r_la) * L2E));
    const float zl = ar * dt * L2E, zi = bf2f(r_aim) * dt;
    const float th = zi * 0.15915494309189535f;
    const float tl = fmaf(zi, 0.15915494309189535f, -th) + zi * 4.6017384e-10f;
    const c2 C = {2.0f * (bf2f(r_cre) * dt), 2.0f * (bf2f(r_cim) * dt)};
    const c2 w1 = cexp_k(zl, th, tl, 1.f), w8 = cexp_k(zl, th, tl, 8.f), w128 = cexp_k(zl, th, tl, 128.f);
    c2 w2, w4, w16, w32, w64, w256, w512, w1024, w2048;
    asm(PKM("%0", "%9", "%9") PKM("%2", "%10", "%10") PKM("%5", "%11", "%11") PKF("%0", "%9", "%9") PKF("%2", "%10", "%10") PKF("%5", "%11", "%11")
        PKM("%1", "%0", "%0") PKM("%3", "%2", "%2") PKM("%6", "%5", "%5") PKF("%1", "%0", "%0") PKF("%3", "%2", "%2") PKF("%6", "%5", "%5")
        PKM("%4", "%3", "%3") PKM("%7", "%6", "%6") PKF("%4", "%3", "%3") PKF("%7", "%6", "%6")
        PKM("%8", "%7", "%7") PKF("%8", "%7", "%7")
        : "=&v"(w2), "=&v"(w4), "=&v"(w16), "=&v"(w32), "=&v"(w64), "=&v"(w256), "=&v"(w512), "=&v"(w1024), "=&v"(w2048)
        : "v"(w1), "v"(w8), "v"(w128));
    c2 C128, C256, C512, C1024, C2048;
    asm(PKM("%0", "%5", "%6") PKM("%1", "%5", "%7") PKM("%2", "%5", "%8") PKM("%3", "%5", "%9") PKM("%4", "%5", "%10")
        PKF("%0", "%5", "%6") PKF("%1", "%5", "%7") PKF("%2", "%5", "%8") PKF("%3", "%5", "%9") PKF("%4", "%5", "%10")
        : "=&v"(C128), "=&v"(C256), "=&v"(C512), "=&v"(C1024), "=&v"(C2048)
        : "v"(C), "v"(w128), "v"(w256), "v"(w512), "v"(w1024), "v"(w2048));
#pragma unroll
    for (int i = 0; i < 2; ++i) {
      const int ln = i * 768 + tid, all = (sg % 12) * 864 + ln;
      if (ln < 864) wc[i] = *(const GLB u32*)((const char*)(all < 5184 ? p.cre : p.cim) + (size_t)(all < 5184 ? all : all - 5184) * 128);
    }
    STAMP(0, 2);
    LDS char* wr = st + 4 * lane;
    LDS const char* rd = st + (lane & 15) * 288 + (lane >> 4) * 16;
    bf16x8 B[4];
    const c2 w8c = {w8[0], -w8[1]}, w32c = {w32[0], -w32[1]}, w64c = {w64[0], -w64[1]};
    octet(wr, c2{1.f, 0.f}, w8c, w32c);
    octet(wr + 8 * 288, w64c, w8c, w32c);
    wave_fence();
#pragma unroll
    for (int ks = 0; ks < 4; ++ks) B[ks] = lds_frag(rd + 64 * ks);
    wave_fence();
#pragma unroll
    for (int mt = 0; mt < 3; ++mt) {
      if (mt == 0) { octet(wr, C, w1, w4); octet(wr + 8 * 288, C128, w1, w4); }
      if (mt == 1) { octet(wr, C256, w2, w128); octet(wr + 8 * 288, C512, w128, w4); }
      if (mt == 2) { octet(wr, C1024, w128, w512); *(LDS u32*)(wr + 8 * 288) = pack_bf16(C2048[0], C2048[1]); }
      wave_fence();
      f32x4 acc = {0, 0, 0, 0};
      bf16x8 A[4];
#pragma unroll
      for (int ks = 0; ks < 4; ++ks) A[ks] = lds_frag(rd + 64 * ks);
      __builtin_amdgcn_sched_barrier(0);
#pragma unroll
      for (int ks = 0; ks < 4; ++ks) acc = mfma16(A[ks], B[ks], acc);
      __builtin_amdgcn_sched_barrier(0);
      wave_fence();
      tab[mt] = acc;
    }
  }
  const int b = lane & 15, q = lane >> 4;
#pragma unroll
  for (int mt = 0; mt < 3; ++mt) *(LDS f32x4*)(st + mt * 1024 + lane * 16) = tab[mt];
  STAMP(0, 3);
  bar();
  {
    LDS const char* ot = smem + S_ST + (w ^ 1) * 4608 + lane * 16;
    LDS char* T = smem + S_T + ch * S_TSZ;
    if (hh == 0) {
      const f32x4 v0 = tab[0] + *(LDS const f32x4*)(ot), v2 = tab[2] + *(LDS const f32x4*)(ot + 2048);
      *(LDS u32x2*)(T + 2 * (128 * (q >> 1) + 8 * b + 4 * (q & 1))) = u32x2{pack_bf16(v0[0], v0[1]), pack_bf16(v0[2], v0[3])};
      const u32 a0 = pack_bf16(v2[0], v2[1]), a1 = pack_bf16(v2[2], v2[3]);
      if (q < 2) {
        LDS char* t3 = T + 2 * (512 + 64 * q + b);
        *(LDS u16*)(t3) = (u16)a0; *(LDS u16*)(t3 + 32) = (u16)(a0 >> 16);
        *(LDS u16*)(t3 + 64) = (u16)a1; *(LDS u16*)(t3 + 96) = (u16)(a1 >> 16);
      } else if (lane == 32) *(LDS u16*)(T + 2 * 640) = (u16)a0;
    } else {
      const f32x4 v1 = tab[1] + *(LDS const f32x4*)(ot + 1024);
      const u32 a0 = pack_bf16(v1[0], v1[1]), a1 = pack_bf16(v1[2], v1[3]);
      if (q < 2) *(LDS u32x2*)(T + 2 * (256 + 64 * q + 4 * b)) = u32x2{a0, a1};
      else {
        LDS char* t2 = T + 2 * (384 + 2 * b + (q - 2));
        *(LDS u16*)(t2) = (u16)a0; *(LDS u16*)(t2 + 64) = (u16)(a0 >> 16);
        *(LDS u16*)(t2 + 128) = (u16)a1; *(LDS u16*)(t2 + 192) = (u16)(a1 >> 16);
      }
    }
  }
  bar();
  STAMP(0, 4);
  u32x4 uv[2];
  {
    GLB u64* fl = (GLB u64*)(p.ws + WS_FL) + (8 * hh + (lane & 7)) * DM + d;
    asm volatile("" :: "v"(wc[0]), "v"(wc[1]), "v"(wb[0]), "v"(wb[1]));
    wait_flags(fl, true);
    const __amdgpu_buffer_rsrc_t rs = mk_rsrc(p.ws + WS_XT, DM * SL * 2);
#pragma unroll
    for (int v = 0; v < 2; ++v) uv[v] = __builtin_amdgcn_raw_buffer_load_b128(rs, (d * SL + 1024 * hh + 8 * (lane + 64 * v)) * 2, 0, SC1);
    if (lane < 8) __hip_atomic_store(fl, 0ull, RLX_AGENT);
  }
  STAMP(0, 5);
  {
    LDS const char* T = smem + S_T + ch * S_TSZ;
    u32 o[10];
    const u32 e512 = *(LDS const u16*)(T + 2 * 512);
    o[8] = o[9] = e512 | (e512 << 16);
    if (hh == 0) {
      const int off = lane < 16 ? 32 * lane : (lane < 32 ? 256 + 16 * lane : 512 + 8 * lane);
      u32x2 W[4];
#pragma unroll
      for (int i = 0; i < 4; ++i) W[i] = *(LDS const u32x2*)(T + off + 8 * i);
      if (lane < 16) {
#pragma unroll
        for (int i = 0; i < 8; ++i) o[i] = W[i >> 1][i & 1];
      } else if (lane < 32) {
#pragma unroll
        for (int i = 0; i < 4; ++i) {
          const u32 a = W[i >> 1][i & 1], nx = W[(i + 1) >> 1][(i + 1) & 1];
          o[2 * i] = dup_lo(a);
          o[2 * i + 1] = hi_lo(a, nx);
        }
      } else {
        const u32 w0 = W[0][0], w1 = W[0][1], w2 = W[1][0];
        o[0] = dup_lo(w0); o[1] = w0; o[2] = dup_hi(w0); o[3] = dup_lo(w1);
        o[4] = o[3]; o[5] = w1; o[6] = dup_hi(w1); o[7] = dup_lo(w2);
      }
    } else {
      const u32 w0 = *(LDS const u32*)(T + 1024 + 4 * lane), w1 = *(LDS const u32*)(T + 1028 + 4 * lane);
      o[0] = o[1] = dup_lo(w0); o[2] = w0; o[3] = o[4] = o[5] = dup_hi(w0); o[6] = o[7] = dup_lo(w1);
    }
    o[8] = (u32)__builtin_amdgcn_update_dpp((int)o[8], (int)o[0], 0x130, 0xf, 0xf, false);
    o[9] = (u32)__builtin_amdgcn_update_dpp((int)o[9], (int)o[1], 0x130, 0xf, 0xf, false);
    u32 a[9];
#pragma unroll
    for (int i = 0; i < 9; ++i) a[i] = hi_lo(o[i], o[i + 1]);
    LDS char* xb = smem + S_X + ch * 4 * S_CPY + 32 + 2 * (1024 * hh + 16 * lane);
    *(LDS u32x4*)(xb) = u32x4{o[0], o[1], o[2], o[3]};
    *(LDS u32x4*)(xb + 16) = u32x4{o[4], o[5], o[6], o[7]};
    *(LDS u32x4*)(xb + S_CPY) = u32x4{a[0], a[1], a[2], a[3]};
    *(LDS u32x4*)(xb + S_CPY + 16) = u32x4{a[4], a[5], a[6], a[7]};
    *(LDS u32x4*)(xb + 2 * S_CPY) = u32x4{o[1], o[2], o[3], o[4]};
    *(LDS u32x4*)(xb + 2 * S_CPY + 16) = u32x4{o[5], o[6], o[7], o[8]};
    *(LDS u32x4*)(xb + 3 * S_CPY) = u32x4{a[1], a[2], a[3], a[4]};
    *(LDS u32x4*)(xb + 3 * S_CPY + 16) = u32x4{a[5], a[6], a[7], a[8]};
    if (hh == 0 && lane == 0) {
      LDS char* hb = smem + S_X + ch * 4 * S_CPY;
      const u32x4 z = {0, 0, 0, 0};
      *(LDS u32x4*)(hb) = z;
      *(LDS u32x4*)(hb + 16) = z;
      *(LDS u32x4*)(hb + S_CPY) = z;
      *(LDS u32x4*)(hb + S_CPY + 16) = u32x4{0, 0, 0, o[0] << 16};
      *(LDS u32x4*)(hb + 2 * S_CPY) = z;
      *(LDS u32x4*)(hb + 2 * S_CPY + 16) = u32x4{0, 0, 0, o[0]};
      *(LDS u32x4*)(hb + 3 * S_CPY) = z;
      *(LDS u32x4*)(hb + 3 * S_CPY + 16) = u32x4{0, 0, o[0] << 16, a[0]};
    }
    if (tid < 6 * 32) *(LDS u32x4*)(smem + S_Z + (tid >> 5) * S_ZSZ + (tid & 31) * 16) = u32x4{0, 0, 0, 0};
    STAMP(0, 6);
    LDS char* z = smem + S_Z + ch * S_ZSZ + 512 + (1024 * hh + 8 * lane) * 2;
    *(LDS u32x4*)z = uv[0];
    *(LDS u32x4*)(z + 1024) = uv[1];
  }
  bar();
  STAMP(0, 7);
  f32x4 acc[8];
#pragma unroll
  for (int i = 0; i < 8; ++i) acc[i] = f32x4{0, 0, 0, 0};
  {
    const int kq = q, qq = b >> 2, pp = b & 3;
    LDS const char* fb = smem + S_X + (ch * 4 + ((4 - qq) & 3)) * S_CPY +
                         2 * (16 + 16 * (kq >> 1) - 8 * (kq & 1) + 4 * pp - (qq ? 4 : 0)) + 64 * hh;
    LDS const char* gb = smem + S_Z + ch * S_ZSZ + 512 + 32 * (b - (kq >> 1)) + 16 * (kq & 1) - 64 * hh;
#pragma unroll 1
    for (int it = 0; it < 4; ++it) {
      bf16x8 F[8], G[8];
#pragma unroll
      for (int s = 0; s < 8; ++s) {
        F[s] = tr_frag(fb + 512 * s, fb + 512 * s - 8);
        G[s] = lds_frag(gb + 512 * s);
      }
      __builtin_amdgcn_sched_barrier(0);
#pragma unroll
      for (int t = 0; t < 8; ++t)
#pragma unroll
        for (int s = 0; s <= t; ++s) acc[t] = mfma16(F[s], G[t - s], acc[t]);
      fb += 128;
      gb -= 128;
    }
  }
  __builtin_amdgcn_sched_barrier(0);
  STAMP(0, 8);
  bar();
  {
    LDS char* ex = smem + S_X + 49152 + w * 4096 + lane * 16;
#pragma unroll
    for (int i = 0; i < 4; ++i) {
      const f32x4 lo = acc[i], hi = acc[4 + i];
      *(LDS f32x4*)(ex + i * 1024) = hh ? +lo : +hi;
    }
    if (tid == 0) *(LDS u32*)(smem + S_CNT) = 0;
  }
  bar();
  u32x2 yo[4];
  {
    LDS const char* ey = smem + S_X + 49152 + (w ^ 1) * 4096 + lane * 16;
#pragma unroll
    for (int i = 0; i < 4; ++i) {
      const f32x4 lo = acc[i], hi = acc[4 + i];
      const f32x4 y = (hh ? +hi : +lo) + *(LDS const f32x4*)(ey + i * 1024);
      yo[i] = u32x2{pack_bf16(y[0], y[1]), pack_bf16(y[2], y[3])};
    }
  }
  {
    const __amdgpu_buffer_rsrc_t rs = mk_rsrc(p.ws + WS_AT, DM * SL * 2);
    const int dst = (d * SL + 1024 * hh + 16 * (lane & 15) + 4 * (lane >> 4)) * 2;
#pragma unroll
    for (int i = 0; i < 4; ++i) __builtin_amdgcn_raw_buffer_store_b64(yo[i], rs, dst + 512 * i, 0, SC1);
  }
  bar();
  STAMP(0, 9);
}
DEV void s_signal(const P& p, LDS char* smem, int sg, int lane) {
  u32 old = 0;
  if (lane == 0) old = __hip_atomic_fetch_add((LDS u32*)(smem + S_CNT), 1u, __ATOMIC_RELAXED, __HIP_MEMORY_SCOPE_WORKGROUP);
  if (__builtin_amdgcn_readfirstlane(old) == 11) {
    const int lt = lane & 31, second = lane >> 5;
    const int dd = 6 * sg + (second ? 5 : 0);
    const int idx = dd < 480 ? dd / 96 : 5 + (dd - 480) / 32;
    const int dfirst = idx < 5 ? 96 * idx : 480 + 32 * (idx - 5);
    __hip_atomic_store((GLB u64*)(p.ws + WS_IN) + tile_block(lt, idx) * 64 + IN_A + (sg - dfirst / 6), FLAG_SET, RLX_AGENT);
  }
}

DEV void role_gate(const P& p, int rb, int w, int lane, u64 pc) {
  u32 pf0 = 0, pf1 = 0;
  const int tid = w * 64 + lane;
  const int sub = lane & 7;
  u32x4 wv[9];
#pragma unroll
  for (int i = 0; i < 9; ++i) wv[i] = *(const u32x4*)((const char*)p.gw + (i * 8 + sub) * 16);
  for (int r = w * 8 + (lane >> 3); r < 128; r += 96) {
    const int l = rb * 128 + r;
    float s = 0.f;
    u32x4 xv[9];
#pragma unroll
    for (int i = 0; i < 9; ++i) xv[i] = *(const u32x4*)((const char*)p.x + (size_t)l * 1152 + (i * 8 + sub) * 16);
#pragma unroll
    for (int i = 0; i < 9; ++i) {
#pragma unroll
      for (int k = 0; k < 4; ++k) {
        const u32 pr = pack_bf16(bflo(xv[i][k]) * bflo(wv[i][k]), bfhi(xv[i][k]) * bfhi(wv[i][k]));
        s += bflo(pr);
        s += bfhi(pr);
      }
    }
    s += __shfl_xor(s, 1); s += __shfl_xor(s, 2); s += __shfl_xor(s, 4);
    const float gv = rbf(1.0f / (1.0f + expf(-rbf(s))));
    if (sub == 0)
      __hip_atomic_store((GLB u64*)(p.ws + WS_G) + l, ((u64)__float_as_uint(rbf(1.0f - gv)) << 32) | __float_as_uint(gv), RLX_AGENT);
  }
  asm volatile("" :: "v"(pf0), "v"(pf1));
  wait_vm<0>();
  bar();
  if (w == 0 && lane < 16)
    __hip_atomic_store((GLB u64*)(p.ws + WS_IN) + tile_block(2 * rb + (lane >> 3), lane & 7) * 64 + IN_G, FLAG_SET, RLX_AGENT);
}

constexpr int H_C = 32768, H_CS = 12288;
DEV int h_slot(int i) { return i < 4 ? i * 8192 : 131072 + (i - 4) * 8192; }
constexpr int H_AT = H_C + 2 * H_CS;
constexpr int H_XT = H_C + 3 * H_CS;
constexpr int H_G = H_C + 4 * H_CS;
constexpr int H_OUT = H_C + 5 * H_CS;
constexpr int K_LDS = 163840;

constexpr int h_wait(int D, int NDT) {
  const int G = 4 + 2 * NDT;
  switch (D) {
    case 0: return 4;
    case 1: return 8;
    case 2: return 4 + G;
    case 3: case 4: return 2 * G;
    case 5: case 6: return 2 * G + 1;
    case 7: return G + NDT;
    default: return NDT;
  }
}
template <int D, int NDT, class F> DEV void h_steps(F&& after, u64& fv, char* wsp) {
  if constexpr (D == 7) asm volatile("s_waitcnt vmcnt(%1)" : "+v"(fv) : "n"(h_wait(D, NDT)) : "memory");
  else wait_vm<h_wait(D, NDT)>();
  LSTAMP2(1, 16 + D);
  bar();
  after(std::integral_constant<int, D>{});
  if constexpr (D < 7) h_steps<D + 1, NDT>(after, fv, wsp);
}
DEV int p2_rot(int d0) { const int idx = d0 < 480 ? d0 / 96 : 5 + (d0 - 480) / 32; return (idx * 18) >> 3; }

template <int NDT, class F>
DEV void p2_loader(const P& p, LDS char* smem, int lt, int d0, int lw, int lane, bool scratch_barrier, F&& pre_poll) {
  __builtin_amdgcn_s_setprio(3);
  const char* hb = p.ws + WS_HB + (size_t)lt * 18 * 8192 + (2 * lw) * 1024 + lane * 16;
  constexpr int W = 32 * NDT;
  const int l0 = 64 * lt, rot = p2_rot(d0);
  u32 co[NDT];
#pragma unroll
  for (int i = 0; i < NDT; ++i) {
    const int row = 8 * (NDT * lw + i) + (lane >> 3), cp = lane & 7;
    co[i] = (u32)(d0 + row) * 1152u + ((cp ^ (row & 7)) << 4);
  }
  auto issue_c = [&](int s) {
    LDS char* buf = smem + H_C + (s & 7) * H_CS + NDT * lw * 1024;
    int ks = s + rot; ks = ks >= 18 ? ks - 18 : ks;
    const char* cb = ks < 9 ? (const char*)p.cre + ks * 128 : (const char*)p.cim + (ks - 9) * 128;
#pragma unroll
    for (int i = 0; i < NDT; ++i) dma16<0>(cb + co[i], buf + i * 1024);
  };
  auto issue_h = [&](int s) {
    LDS char* buf = smem + h_slot(s & 7) + 2 * lw * 1024;
    int ks = s + rot; ks = ks >= 18 ? ks - 18 : ks;
#pragma unroll
    for (int i = 0; i < 2; ++i) dma16<SC1>(hb + ks * 8192 + i * 1024, buf + i * 1024);
  };
  GLB u64* inb = (GLB u64*)(p.ws + WS_IN) + blockIdx.x * 64;
  const int nA = (d0 + W - 1) / 6 - d0 / 6 + 1;
  const bool need = lane == IN_G || (lane >= IN_A && lane < IN_A + nA);
  pre_poll();
  if (!scratch_barrier) {
#pragma unroll 1
    for (int s = 0; s < 8; ++s) issue_c(s);
  }
  { [[maybe_unused]] const int w = lw + 8; STAMP(1, 1); }
  if (scratch_barrier) bar();
  wait_flags(inb + IN_H + (lane < 36 ? lane : 0), true);
  { [[maybe_unused]] const int w = lw + 8; STAMP(1, 2); }
#pragma unroll
  for (int s = 0; s < 4; ++s) issue_h(s);
  { [[maybe_unused]] const int w = lw + 8; STAMP(1, 3); }
  u64 fv = 0;
  auto issue_at = [&]() {
#pragma unroll
    for (int i = 0; i < NDT; ++i) {
      const int pc = NDT * lw + i;
      dma16<SC1>(p.ws + WS_AT + (size_t)(d0 + 8 * pc + (lane >> 3)) * 4096 + l0 * 2 + (lane & 7) * 16, smem + H_AT + pc * 1024);
    }
  };
  auto issue_g = [&]() { if (lane < 8) dma16<SC1>(p.ws + WS_G + (size_t)l0 * 8 + lw * 128 + lane * 16, smem + H_G + lw * 128); };
  h_steps<0, NDT>([&](auto D_) {
    constexpr int D = decltype(D_)::value;
    if constexpr (D == 0) { issue_h(4); issue_h(5); issue_h(6); issue_h(7); }
    if constexpr (D >= 1 && D <= 5) { issue_h(2 * D + 6); issue_h(2 * D + 7); issue_c(2 * D + 6); issue_c(2 * D + 7); }
    if constexpr (D == 4)
      asm volatile("global_load_dwordx2 %0, %1, off sc1" : "=v"(fv) : "v"(inb + lane) : "memory");
    if constexpr (D == 6) {
#pragma unroll
      for (int i = 0; i < NDT; ++i) {
        const int pc = NDT * lw + i, id = pc * 64 + lane, row = id / (4 * NDT), qq = id - row * (4 * NDT);
        dma16<0>((const char*)p.x + (size_t)(l0 + row) * 1152 + d0 * 2 + qq * 16, smem + H_XT + pc * 1024);
      }
    }
  }, fv, p.ws);
  if (__all(!need || fv == FLAG_SET)) {
    issue_g(); issue_at();
    wait_vm<NDT>();
    bar();
  } else {
    u64 fv2;
    asm volatile("global_load_dwordx2 %0, %1, off sc1" : "=v"(fv2) : "v"(inb + lane) : "memory");
    wait_vm<NDT>();
    bar();
    asm volatile("s_waitcnt vmcnt(0)" : "+v"(fv2) :: "memory");
    if (!__all(!need || fv2 == FLAG_SET)) wait_flags(inb + lane, need);
    issue_g(); issue_at();
  }
  wait_vm<0>();
  bar();
}

template <int NDT>
DEV void p2_compute(const P& p, LDS char* smem, int lt0, int d0, int w, int lane, const u32x2 (&dsk)[6]) {
  const int lh = (w >> 1) & 1, kh = w & 1, dg = w >> 2;
  const int r = lane & 15, kq = lane >> 4;
  const int lt = 2 * lh + kh, l0 = 64 * lt0;
  constexpr int W = 32 * NDT;
  u32x2 da[NDT], db[NDT];
#pragma unroll
  for (int t = 0; t < NDT; ++t) {
    const int d = d0 + 16 * (NDT * dg + t) + 4 * kq;
    da[t] = *(const u32x2*)(p.dskA + d);
    db[t] = *(const u32x2*)(p.dskB + d);
  }
  GLB u64* inb = (GLB u64*)(p.ws + WS_IN) + blockIdx.x * 64;

  f32x4 acc[2][NDT];
#pragma unroll
  for (int i = 0; i < 2; ++i)
#pragma unroll
    for (int t = 0; t < NDT; ++t) acc[i][t] = f32x4{0, 0, 0, 0};
  int ho[2][2];
#pragma unroll
  for (int u = 0; u < 2; ++u) {
    const int k = 32 * kh + 8 * kq + 4 * u + (r >> 2), i = (r & 3) >> 1;
    const int o = (2 * (k >> 4) + i) * 1024 + (32 * lh + ((k & 15) ^ (4 * i))) * 16 + (r & 1) * 8;
    ho[0][u] = o + 256 * kh;
    ho[1][u] = o + 256 * (kh ^ 1);
  }
  const int co = H_C + (16 * NDT * dg + r) * 128 + (((4 * kh + kq) ^ (r & 7)) << 4);
  bf16x8 hb[2][2][2], ca[2][2][NDT];
  auto rd = [&](auto SET_, int D) {
    constexpr int SET = decltype(SET_)::value;
#pragma unroll
    for (int u = 0; u < 2; ++u) {
      const int s = 2 * D + u;
      LDS const char* hbuf = smem + h_slot(s & 7);
#pragma unroll
      for (int i = 0; i < 2; ++i) hb[SET][u][i] = tr_frag(hbuf + ho[i][0], hbuf + ho[i][1]);
      LDS const char* cbuf = smem + (s & 7) * H_CS;
#pragma unroll
      for (int t = 0; t < NDT; ++t) ca[SET][u][t] = lds_frag(cbuf + co + t * 2048);
    }
    __builtin_amdgcn_sched_barrier(0);
  };
  auto mma = [&](auto SET_) {
    constexpr int SET = decltype(SET_)::value;
#pragma unroll
    for (int u = 0; u < 2; ++u)
#pragma unroll
      for (int t = 0; t < NDT; ++t) {
        acc[0][t] = mfma16(ca[SET][u][t], hb[SET][u][0], acc[0][t]);
        acc[1][t] = mfma16(ca[SET][u][t], hb[SET][u][1], acc[1][t]);
      }
    __builtin_amdgcn_sched_barrier(0);
  };
  int orow[NDT], opc[NDT];
#pragma unroll
  for (int it = 0; it < NDT; ++it) {
    const int id = it * 512 + w * 64 + lane;
    orow[it] = id / (8 * NDT); opc[it] = id - orow[it] * (8 * NDT);
    asm volatile("" : "+v"(orow[it]), "+v"(opc[it]));
  }
  using S0 = std::integral_constant<int, 0>;
  using S1 = std::integral_constant<int, 1>;
  STAMP(1, 1);
  bar();
  STAMP(1, 2);
  rd(S0{}, 0);
#pragma unroll 1
  for (int D = 1; D < 9; D += 2) {
    bar(); rd(S1{}, D); mma(S0{});
    bar(); rd(S0{}, D + 1); mma(S1{});
  }
  mma(S0{});
  __builtin_amdgcn_sched_barrier(0);
  STAMP(1, 3);
  bar();
  if (w == 0) __hip_atomic_store(inb + lane, 0ull, RLX_AGENT);
  LDS char* ex = smem + w * (NDT * 1024) + lane * 16;
#pragma unroll
  for (int t = 0; t < NDT; ++t) *(LDS f32x4*)(ex + t * 1024) = acc[1][t];
  bar();
  LDS const char* ey = smem + (w ^ 1) * (NDT * 1024) + lane * 16;
  constexpr int OSTR = W * 4 + 16;
  const f32x2 gg = *(LDS const f32x2*)(smem + H_G + (16 * lt + r) * 8);
  const f32x2 g2 = {gg[0], gg[0]}, om2 = {gg[1], gg[1]};
  auto up = [](u32 v) { return f32x2{bflo(v), bfhi(v)}; };
  auto rb2 = [&](f32x2 v) { return up(pack_bf16(v[0], v[1])); };
#pragma unroll
  for (int t = 0; t < NDT; ++t) {
    const int dt = NDT * dg + t;
    const f32x4 y = acc[0][t] + *(LDS const f32x4*)(ey + t * 1024);
    const u32x2 av = __builtin_bit_cast(u32x2, tr_read(smem + H_AT + (16 * dt + 4 * kq + (r >> 2)) * 128 + lt * 32 + (r & 3) * 8));
    const u32x2 xv = *(LDS const u32x2*)(smem + H_XT + (16 * lt + r) * (W * 2) + (16 * dt + 4 * kq) * 2);
    f32x2 res[2];
#pragma unroll
    for (int h = 0; h < 2; ++h) {
      const f32x2 xf = up(xv[h]);
      const f32x2 a = rb2(up(av[h]) + rb2(xf * up(da[t][h])));
      const f32x2 b = (h ? hi2(y) : lo2(y)) + rb2(xf * up(db[t][h]));
      res[h] = fma2(om2, b, rb2(g2 * a));
    }
    *(LDS f32x4*)(smem + H_OUT + (16 * lt + r) * OSTR + (16 * dt + 4 * kq) * 4) = cat2(res[0], res[1]);
  }
  STAMP(1, 4);
  bar();
  const __amdgpu_buffer_rsrc_t rso = mk_rsrc(p.out, SL * DM * 4);
#pragma unroll
  for (int it = 0; it < NDT; ++it) {
    const f32x4 v = *(LDS const f32x4*)(smem + H_OUT + orow[it] * OSTR + opc[it] * 16);
    __builtin_amdgcn_raw_buffer_store_b128(__builtin_bit_cast(u32x4, v), rso, ((l0 + orow[it]) * DM + d0 + opc[it] * 4) * 4, 0, OUT_AUX);
  }
}

template <int NDT>
DEV void phase2(const P& p, LDS char* smem, int lt, int d0, int w, int lane, bool scratch_barrier, int sg, const u32x2 (&dsk)[6]) {
  STAMP(1, 0);
  if (w >= 8) {
    p2_loader<NDT>(p, smem, lt, d0, w - 8, lane, scratch_barrier, [&]() {
      if (NDT == 1) { wait_vm<0>(); s_signal(p, smem, sg, lane); }
    });
  } else {
    if (NDT == 1) { wait_vm<0>(); s_signal(p, smem, sg, lane); }
    p2_compute<NDT>(p, smem, lt, d0, w, lane, dsk);
  }
  STAMP(1, 15);
}

__global__ __launch_bounds__(768, 3) void gated_mix_s4d_s5(P p) {
  extern __shared__ __attribute__((aligned(16))) char smem_raw[];
  LDS char* smem = (LDS char*)smem_raw;
  u64 pc;
  asm volatile("s_getpc_b64 %0" : "=s"(pc));
  u32 pf = 0;
#define PF_TOP 312
  if (threadIdx.x < PF_TOP && blockIdx.x >= 144)
    asm volatile("global_load_dword %0, %1, off" : "=v"(pf) : "v"((pc & ~255ull) + threadIdx.x * 128));
  const int w = __builtin_amdgcn_readfirstlane(threadIdx.x >> 6), lane = threadIdx.x & 63;
  const int xcd = blockIdx.x & 7, j = blockIdx.x >> 3;
  u32x2 dsk[6];
#pragma unroll
  for (int i = 0; i < 6; ++i) dsk[i] = u32x2{0, 0};
  STAMP(0, 0);
  if (j >= 18 && j < 30) {
    const int sg = 12 * xcd + (j - 18);
    role_s(p, smem, sg, w, lane);
    STAMP(0, 15);
    phase2<1>(p, smem, 4 * xcd + (j - 18) / 3, 480 + 32 * ((j - 18) % 3), w, lane, false, sg, dsk);
  } else {
    const bool isg = j < 18;
    if (isg) {
      const int c = 2 * xcd + j / 9, g = j % 9;
      if (w >= 8) g_loader(p, smem, c, g, 96 * (j % 5), w - 8, lane);
      else if (w < 4) g_compute<0>(p, smem, c, g, w, lane, pf);
      else g_compute<1>(p, smem, c, g, w, lane, pf);
    } else {
      role_gate(p, 2 * xcd + (j - 30), w, lane, pc);
    }
    STAMP(0, 15);
    const int e = isg ? j : j - 12;
    phase2<3>(p, smem, 4 * xcd + e / 5, 96 * (e % 5), w, lane, isg, 0, dsk);
  }
  asm volatile("s_waitcnt vmcnt(0)" :: "v"(pf));
}

extern "C" void kernel_launch(void* const* d_in, const int* in_sizes, int n_in, void* d_out, int out_size,
                              void* d_ws, size_t ws_size, hipStream_t stream) {
  P a;
  a.x = (const u16*)d_in[0]; a.la = (const u16*)d_in[1]; a.aim = (const u16*)d_in[2];
  a.creA = (const u16*)d_in[3]; a.cimA = (const u16*)d_in[4]; a.ldtA = (const u16*)d_in[5]; a.dskA = (const u16*)d_in[6];
  a.lre = (const u16*)d_in[8]; a.lim = (const u16*)d_in[9]; a.bre = (const u16*)d_in[10]; a.bim = (const u16*)d_in[11];
  a.cre = (const u16*)d_in[12]; a.cim = (const u16*)d_in[13]; a.ldtB = (const u16*)d_in[14];
  a.dskB = (const u16*)d_in[15]; a.gw = (const u16*)d_in[16];
  a.ws = (char*)d_ws + WS_OFF; a.out = (float*)d_out;
  (void)hipFuncSetAttribute((const void*)gated_mix_s4d_s5, hipFuncAttributeMaxDynamicSharedMemorySize, K_LDS);
  gated_mix_s4d_s5<<<256, 768, K_LDS, stream>>>(a);
}
```
